```python
import torch

def _softmax(x, axis=-1):
    return torch.softmax(x, dim=axis)
def _gelu(x):
    return torch.nn.functional.gelu(x, approximate="tanh")
def _silu(x):
    return torch.nn.functional.silu(x)
def _sigmoid(x):
    return torch.sigmoid(x)
def _layernorm(x, axis=-1, eps=1e-5):
    m = x.mean(dim=axis, keepdim=True)
    v = x.var(dim=axis, keepdim=True, unbiased=False)
    return (x - m) / torch.sqrt(v + eps)
def _scatter_add(tgt, idx, src, axis=0):
    return tgt.clone().index_add_(axis, idx, src)
def _segment_sum(src, seg, n):
    out = torch.zeros((n,) + src.shape[1:], dtype=src.dtype)
    return out.index_add_(0, seg, src)
def _pad(x, pw):
    flat = tuple(v for pair in reversed(pw) for v in pair)
    return torch.nn.functional.pad(x, flat)

kernel_name = 'sequential_preLN__sliding_block__glu_gated_glu__prompt'

def setup_inputs():

    _w = (lambda a: (lambda t: t.to(torch.bfloat16) if t.is_floating_point() else t)(torch.as_tensor(a)))
    def _A_setup():
        B, S, D = 1, 2048, 576
        return {
            "x": _w((lambda *s: torch.randn(*s, dtype=torch.bfloat16) * 0.05)(B, S, D)),
            "wqkv": _w((lambda *s: torch.randn(*s, dtype=torch.bfloat16) * 0.05)(D, 3 * D)),
            "wo": _w((lambda *s: torch.randn(*s, dtype=torch.bfloat16) * 0.05)(D, D)),
        }
    _A_in = _A_setup()

    def _B_setup():
        B, S, D, F = 1, 2048, 576, 4608
        return {
            "x": _w((lambda *s: torch.randn(*s, dtype=torch.bfloat16) * 0.05)(B, S, D)),
            "w_gate": _w((lambda *s: torch.randn(*s, dtype=torch.bfloat16) * 0.05)(D, F)),
            "w_up": _w((lambda *s: torch.randn(*s, dtype=torch.bfloat16) * 0.05)(D, F)),
            "w_down": _w((lambda *s: torch.randn(*s, dtype=torch.bfloat16) * 0.05)(F, D)),
        }
    _B_in = _B_setup()

    return {
        "A_x": _A_in["x"],
        "A_wqkv": _A_in["wqkv"],
        "A_wo": _A_in["wo"],
        "B_x": _B_in["x"],
        "B_w_gate": _B_in["w_gate"],
        "B_w_up": _B_in["w_up"],
        "B_w_down": _B_in["w_down"],
    }

def reference(A_x, A_wqkv, A_wo, B_x, B_w_gate, B_w_up, B_w_down):
    def _A_ref(x, wqkv, wo):
        B, S, D = x.shape
        H = 32; Dh = D // H; W = 256
        qkv = (lambda x, *d: x.permute(*d))((x @ wqkv).reshape(B, S, 3, H, Dh), 2, 0, 3, 1, 4)
        q, k, v = qkv[0], qkv[1], qkv[2]
        scores = torch.einsum("bhqd,bhkd->bhqk", q, k) * (Dh ** -0.5)
        idx = torch.arange(S)
        mask = (idx[:, None] - idx[None, :] >= 0) & (idx[:, None] - idx[None, :] < W)
        scores = torch.where(mask[None, None], scores, -1e9)
        p = _softmax(scores, axis=-1)
        o = torch.einsum("bhqk,bhkd->bhqd", p, v)
        return (lambda x, *d: x.permute(*d))(o, 0, 2, 1, 3).reshape(B, S, D) @ wo

    def _B_ref(x, w_gate, w_up, w_down):
        g = torch.einsum("bsd,df->bsf", x, w_gate)
        u = torch.einsum("bsd,df->bsf", x, w_up)
        h = _sigmoid(g) * u
        return torch.einsum("bsf,fd->bsd", h, w_down)

    x = A_x
    h = x + _A_ref(_layernorm(x, axis=-1), A_wqkv, A_wo)
    h2 = h + _B_ref(_layernorm(h, axis=-1), B_w_gate, B_w_up, B_w_down)
    return h2
```

```cpp
#include <hip/hip_runtime.h>
#include <hip/hip_bf16.h>
#include <stdint.h>

typedef __attribute__((ext_vector_type(8))) short bf16x8;
typedef __attribute__((ext_vector_type(4))) short s16x4;
typedef __attribute__((ext_vector_type(4))) float f32x4;
typedef __attribute__((ext_vector_type(16))) float f32x16;
typedef __attribute__((ext_vector_type(4))) unsigned u32x4;
typedef __attribute__((ext_vector_type(2))) unsigned u32x2;
typedef __attribute__((ext_vector_type(2))) __bf16 bf16x2_t;
typedef __attribute__((ext_vector_type(2))) float f32x2;
#define LDS_AS __attribute__((address_space(3)))
typedef LDS_AS char* lds_ptr;
#define DI __device__ __forceinline__

constexpr int SEQ = 2048, DM = 576, NH = 32, FF = 4608;
constexpr float LN_EPS = 1e-5f;
constexpr float QSCALE = 0.23570226039551584f * 1.4426950408889634f;

constexpr size_t WS_QM = 0;
constexpr size_t WS_KM = WS_QM + (size_t)32 * 64 * 1024;
constexpr size_t WS_QX = WS_KM + (size_t)32 * 64 * 1024;
constexpr size_t WS_KX = WS_QX + (size_t)16 * 2048 * 8;
constexpr size_t WS_V = WS_KX + (size_t)16 * 2048 * 8;
constexpr size_t WS_O = WS_V + (size_t)32 * 128 * 592 + 1024;
constexpr size_t WS_H = WS_O + (size_t)128 * 18 * 1024;
constexpr size_t WS_ST = WS_H + (size_t)128 * 18 * 1024;
constexpr size_t WS_CSP = WS_ST + (size_t)2048 * 80;
constexpr size_t WS_CS = WS_CSP + (size_t)8 * 9216 * 4;
constexpr size_t WS_HID = WS_CS + (size_t)9216 * 4;
constexpr size_t WS_END = WS_HID + (size_t)128 * 144 * 1024;
static_assert(WS_O % 1024 == 0 && WS_H % 1024 == 0 && WS_ST % 256 == 0 && WS_CSP % 256 == 0 && WS_HID % 256 == 0, "align");

#define STAMP_BEGIN
#define STAMP_MARK(k, i)
#define STAMP_END(k)

DI unsigned cvt_pk(float lo, float hi) {
  f32x2 v = {lo, hi};
  bf16x2_t r = __builtin_convertvector(v, bf16x2_t);
  return __builtin_bit_cast(unsigned, r);
}
DI float bf_lo(unsigned u) { return __builtin_bit_cast(float, u << 16); }
DI float bf_hi(unsigned u) { return __builtin_bit_cast(float, u & 0xffff0000u); }
DI float dot2(unsigned a, unsigned b, float c) {
  return __builtin_amdgcn_fdot2_f32_bf16(__builtin_bit_cast(bf16x2_t, a), __builtin_bit_cast(bf16x2_t, b), c, false);
}
template <int CTRL> DI float dpp_add(float v) {
  int t = __builtin_amdgcn_update_dpp(0, __builtin_bit_cast(int, v), CTRL, 0xf, 0xf, true);
  return v + __builtin_bit_cast(float, t);
}
DI float sum8(float v) {
  v = dpp_add<0xB1>(v);
  v = dpp_add<0x4E>(v);
  v = dpp_add<0x141>(v);
  return v;
}
DI void glds16(const void* sbase, unsigned voff, unsigned lds_dst) {
  asm volatile("s_mov_b32 m0, %2\n\ts_nop 3\n\tglobal_load_lds_dwordx4 %0, %1" ::"v"(voff), "s"(sbase), "s"(lds_dst) : "memory");
}
template <int N> DI void wait_bar() { asm volatile("s_waitcnt vmcnt(%0) lgkmcnt(0)\n\ts_barrier" ::"n"(N) : "memory"); }
template <int N> DI void wait_vm() { asm volatile("s_waitcnt vmcnt(%0)" ::"n"(N) : "memory"); }
DI void lds_bar() { asm volatile("s_waitcnt lgkmcnt(0)\n\ts_barrier" ::: "memory"); }
DI bf16x8 lds_r128(lds_ptr p) { return *(LDS_AS const bf16x8*)p; }
typedef short v4i16_t __attribute__((ext_vector_type(4)));
DI s16x4 lds_tr(lds_ptr p) { return __builtin_amdgcn_ds_read_tr16_b64_v4i16((LDS_AS v4i16_t*)p); }
DI bf16x8 cat8(s16x4 a, s16x4 b) { return __builtin_shufflevector(a, b, 0, 1, 2, 3, 4, 5, 6, 7); }
#define MFMA16(a, b, c) __builtin_amdgcn_mfma_f32_16x16x32_bf16(a, b, c, 0, 0, 0)
#define MFMA32(a, b, c) __builtin_amdgcn_mfma_f32_32x32x16_bf16(a, b, c, 0, 0, 0)
DI int xcd_remap(int b, int nwg) {
  int q = nwg >> 3, r = nwg & 7, x = b & 7, o = b >> 3;
  return (x < r ? x * (q + 1) : r * (q + 1) + (x - r) * q) + o;
}

constexpr int K1_A = 0, K1_W = 90 * 1024, K1_SLOT = 12288, K1_NSLOT = 5, K1_LDS = K1_W + K1_NSLOT * K1_SLOT;

template <bool ISV>
DI void k1_main(lds_ptr lds, unsigned lds0, const char* wsrc, unsigned wvoff, int wid, int lane, f32x4 (&acc)[5]) {
  const int g = lane >> 4, q = (lane & 15) >> 2, p = lane & 3, r = lane & 15;
  lds_ptr a0 = lds + K1_A + g * 256 + ((r ^ g) * 16);
  lds_ptr a1 = lds + K1_A + g * 256 + ((r ^ (4 + g)) * 16);
  const int rho0 = 16 * (g >> 1) + 4 * (g & 1) + q;
  const int cb = wid & 3;
  lds_ptr wb = lds + K1_W + (wid >> 2) * 4096 + rho0 * 128 + ((cb ^ ((rho0 >> 1) & 3)) * 32) + p * 8;
  const unsigned wdst = lds0 + K1_W + wid * 1024;
#pragma unroll
  for (int s = 0; s < 18; ++s) {
    if (s < 15) wait_bar<3>();
    else if (s == 15) wait_bar<2>();
    else if (s == 16) wait_bar<1>();
    else wait_bar<0>();
    if (s + 4 < 18) glds16(wsrc + (size_t)(s + 4) * 32 * 3456, wvoff, wdst + ((s + 4) % K1_NSLOT) * K1_SLOT);
    const int so = (s % K1_NSLOT) * K1_SLOT;
    bf16x8 w = cat8(lds_tr(wb + so), lds_tr(wb + so + 1024));
#pragma unroll
    for (int m = 0; m < 5; ++m) {
      bf16x8 a = lds_r128(((s & 1) ? a1 : a0) + (m * 18 + s) * 1024);
      acc[m] = ISV ? MFMA16(a, w, acc[m]) : MFMA16(w, a, acc[m]);
    }
  }
}

__global__ __launch_bounds__(768) void k1_ln_qkv(const char* __restrict__ x, const char* __restrict__ wqkv, char* __restrict__ ws) {
  extern __shared__ __attribute__((aligned(16))) char smem[];
  STAMP_BEGIN
  lds_ptr lds = (lds_ptr)smem;
  const unsigned lds0 = (unsigned)(uintptr_t)lds;
  const int tid = threadIdx.x, lane = tid & 63, wid = __builtin_amdgcn_readfirstlane(tid >> 6);
  const int tile = xcd_remap(blockIdx.x, 234);
  const int mt = tile / 9, ct = tile - mt * 9;
  const int m0 = min(mt * 80, SEQ - 80);
  const int row = tid >> 3, l8 = tid & 7;
  u32x4 xv[9];
  if (tid < 640) {
    const char* xp = x + (size_t)(m0 + row) * 1152 + l8 * 16;
#pragma unroll
    for (int i = 0; i < 9; ++i) xv[i] = *(const u32x4*)(xp + i * 128);
  }
  const int rho = 8 * (wid & 3) + (lane >> 3);
  const int gk = ((rho >> 4) & 1) * 2 + ((rho >> 2) & 1), tk = (rho >> 3) & 1, qk = rho & 3;
  const int krow = 8 * gk + 4 * tk + qk;
  const unsigned wvoff = krow * 3456 + (((lane & 7) ^ (((rho >> 1) & 3) << 1)) * 16);
  const char* wsrc = wqkv + (ct * 192 + (wid >> 2) * 64) * 2;
  const unsigned wdst = lds0 + K1_W + wid * 1024;
#pragma unroll
  for (int s = 0; s < 4; ++s) glds16(wsrc + (size_t)s * 32 * 3456, wvoff, wdst + s * K1_SLOT);
  if (tid < 640) {
    float s1 = 0.f, s2 = 0.f;
#pragma unroll
    for (int i = 0; i < 9; ++i)
#pragma unroll
      for (int j = 0; j < 4; ++j) { s1 = dot2(xv[i][j], 0x3f803f80u, s1); s2 = dot2(xv[i][j], xv[i][j], s2); }
    s1 = sum8(s1); s2 = sum8(s2);
    const float mean = s1 * (1.f / 576.f);
    const float var = fmaxf(s2 * (1.f / 576.f) - mean * mean, 0.f);
    const float rstd = __builtin_amdgcn_rsqf(var + LN_EPS);
    const float nb = -mean * rstd;
    lds_ptr wp = lds + K1_A + (row >> 4) * 18 * 1024 + (l8 >> 2) * 1024 + (l8 & 3) * 256 + (((row & 15) ^ l8) * 16);
#pragma unroll
    for (int i = 0; i < 9; ++i) {
      u32x4 o;
#pragma unroll
      for (int j = 0; j < 4; ++j) o[j] = cvt_pk(fmaf(bf_lo(xv[i][j]), rstd, nb), fmaf(bf_hi(xv[i][j]), rstd, nb));
      *(LDS_AS u32x4*)(wp + i * 2048) = o;
    }
  }
  f32x4 acc[5];
#pragma unroll
  for (int m = 0; m < 5; ++m) acc[m] = f32x4{0.f, 0.f, 0.f, 0.f};
  const int g = lane >> 4, r = lane & 15;
  if (ct >= 6) {
    k1_main<true>(lds, lds0, wsrc, wvoff, wid, lane, acc);
    const int c = (ct - 6) * 192 + wid * 16 + r;
    const int head = c / 18, d = c - head * 18;
    char* vp = ws + WS_V + (size_t)(head * 128 + (m0 >> 4)) * 592 + (g & 1) * 288 + d * 16 + (g >> 1) * 8;
#pragma unroll
    for (int m = 0; m < 5; ++m) {
      u32x2 o = {cvt_pk(acc[m][0], acc[m][1]), cvt_pk(acc[m][2], acc[m][3])};
      *(u32x2*)(vp + m * 592) = o;
    }
  } else {
    k1_main<false>(lds, lds0, wsrc, wvoff, wid, lane, acc);
    const bool isq = ct < 3;
    const float sc = isq ? QSCALE : 1.f;
    const int c = (isq ? ct : ct - 3) * 192 + wid * 16 + 4 * g;
    const int pair = c / 36, qi = (c - pair * 36) >> 2;
    const int head = 2 * pair + (qi > 4 ? 1 : 0), a = qi > 4 ? qi - 5 : qi;
    char* mainp = ws + (isq ? WS_QM : WS_KM) + (size_t)head * 65536 + (a >> 1) * 512 + (a & 1) * 8;
    char* xp = ws + (isq ? WS_QX : WS_KX) + (size_t)pair * 16384;
#pragma unroll
    for (int m = 0; m < 5; ++m) {
      const int rw = m0 + m * 16 + r;
      u32x2 o = {cvt_pk(acc[m][0] * sc, acc[m][1] * sc), cvt_pk(acc[m][2] * sc, acc[m][3] * sc)};
      char* dst = (qi == 4) ? xp + rw * 8 : mainp + (rw >> 5) * 1024 + (rw & 31) * 16;
      *(u32x2*)dst = o;
    }
  }
  STAMP_END(0);
}

constexpr int K2_KM = 0, K2_KX = 16384, K2_V = K2_KX + 4096, K2_CS = K2_V + 19 * 1024, K2_LDS = K2_CS + 14 * 288 * 4;

__global__ __launch_bounds__(512, 2) void k2_attn(char* __restrict__ ws, const char* __restrict__ wg, const char* __restrict__ wu) {
  extern __shared__ __attribute__((aligned(16))) char smem[];
  STAMP_BEGIN
  lds_ptr lds = (lds_ptr)smem;
  const unsigned lds0 = (unsigned)(uintptr_t)lds;
  const int tid = threadIdx.x, lane = tid & 63, wid = __builtin_amdgcn_readfirstlane(tid >> 6);
  const int hd = blockIdx.x >> 3, qb = blockIdx.x & 7;
  const int t = qb * 8 + wid;
  const int hi = lane >> 5, l31 = lane & 31;
  {
    const unsigned vo = lane * 16;
    const int kt0 = qb * 8 - 8;
#pragma unroll
    for (int i = 0; i < 2; ++i) {
      const int lt = wid * 2 + i;
      const int kt = max(kt0 + lt, 0);
      glds16(ws + WS_KM + (size_t)hd * 65536 + (size_t)kt * 1024, vo, lds0 + K2_KM + lt * 1024);
    }
    if (wid < 4) {
      const int k0 = max(qb * 256 - 256 + wid * 128, 0);
      glds16(ws + WS_KX + (size_t)(hd >> 1) * 16384 + (size_t)k0 * 8, vo, lds0 + K2_KX + wid * 1024);
    }
    const long vb0 = (long)(hd * 128 + qb * 16 - 16) * 592;
#pragma unroll
    for (int i = 0; i < 3; ++i) {
      const int pc = wid + 8 * i;
      if (pc < 19) {
        long off = vb0 + pc * 1024;
        unsigned v2 = vo;
        if (qb == 0) { off = (long)(hd * 128) * 592 + pc * 1024; v2 = (pc * 1024 + lane * 16 >= 9472) ? vo - 9472 + 65536 : vo + 65536; off -= 65536; }
        glds16(ws + WS_V + off, v2, lds0 + K2_V + pc * 1024);
      }
    }
  }
  bf16x8 q0 = *(const bf16x8*)(ws + WS_QM + (size_t)hd * 65536 + (size_t)t * 1024 + lane * 16);
  unsigned qx = *(const unsigned*)(ws + WS_QX + (size_t)(hd >> 1) * 16384 + (size_t)(t * 32 + l31) * 8 + (hd & 1) * 4);
  float cs[8];
  const int crg = tid / 36, cu = tid - crg * 36;
  {
#pragma unroll
    for (int j = 0; j < 8; ++j) cs[j] = 0.f;
    const char* wsrc = (hd < 16 ? wg : wu) + (size_t)(hd & 15) * 576 + cu * 16;
    u32x4 v[6];
#pragma unroll
    for (int i = 0; i < 6; ++i) {
      const int rr = crg + 14 * i;
      v[i] = u32x4{0, 0, 0, 0};
      if (tid < 504 && rr < 72) v[i] = *(const u32x4*)(wsrc + (size_t)(qb * 72 + rr) * 9216);
    }
#pragma unroll
    for (int i = 0; i < 6; ++i)
#pragma unroll
      for (int j = 0; j < 4; ++j) { cs[2 * j] += bf_lo(v[i][j]); cs[2 * j + 1] += bf_hi(v[i][j]); }
    if (tid < 504) {
      *(LDS_AS f32x4*)(lds + K2_CS + (crg * 288 + cu * 8) * 4) = f32x4{cs[0], cs[1], cs[2], cs[3]};
      *(LDS_AS f32x4*)(lds + K2_CS + (crg * 288 + cu * 8 + 4) * 4) = f32x4{cs[4], cs[5], cs[6], cs[7]};
    }
  }
  wait_bar<0>();
  if (tid < 32) *(LDS_AS u32x4*)(lds + K2_V + tid * 592 + 576) = u32x4{0x3f803f80u, 0x3f803f80u, 0x3f803f80u, 0x3f803f80u};
  if (tid < 288) {
    float s = 0.f;
#pragma unroll
    for (int i = 0; i < 14; ++i) s += *(LDS_AS float*)(lds + K2_CS + (i * 288 + tid) * 4);
    *(float*)(ws + WS_CSP + ((size_t)qb * 9216 + hd * 288 + tid) * 4) = s;
  }
  u32x4 q1u = {hi ? 0u : qx, 0u, 0u, 0u};
  bf16x8 q1 = __builtin_bit_cast(bf16x8, q1u);
  f32x16 S[9];
  lds_ptr kmp = lds + K2_KM + wid * 1024 + lane * 16;
  lds_ptr kxp = lds + K2_KX + (wid * 32 + l31) * 8 + (hd & 1) * 4;
#pragma unroll
  for (int i = 0; i < 9; ++i) {
    bf16x8 k0 = lds_r128(kmp + i * 1024);
    u32x4 k1u = {*(LDS_AS unsigned*)(kxp + i * 256), 0u, 0u, 0u};
    f32x16 z = {};
    S[i] = MFMA32(k0, q0, z);
    S[i] = MFMA32(__builtin_bit_cast(bf16x8, k1u), q1, S[i]);
  }
  lds_bar();
  const float NEG = -1e30f;
  if (qb == 0) {
#pragma unroll
    for (int i = 0; i < 8; ++i)
      if (wid + i < 8) {
#pragma unroll
        for (int r = 0; r < 16; ++r) S[i][r] = NEG;
      }
  }
  const int dq = l31 - 4 * hi;
#pragma unroll
  for (int r = 0; r < 16; ++r) S[0][r] = (((r & 3) + 8 * (r >> 2)) <= dq) ? S[8][r] : S[0][r];
  float mx = S[0][0];
#pragma unroll
  for (int i = 0; i < 8; ++i)
#pragma unroll
    for (int r = 0; r < 16; r += 2) mx = fmaxf(fmaxf(mx, S[i][r]), S[i][r + 1]);
  {
    auto rr = __builtin_amdgcn_permlane32_swap(__float_as_uint(mx), __float_as_uint(mx), false, false);
    mx = fmaxf(__uint_as_float(rr[0]), __uint_as_float(rr[1]));
  }
  lds_ptr vp = lds + K2_V + wid * 2 * 592 + (l31 < 18 ? hi * 288 + l31 * 16 : 576);
  f32x16 O = {};
#pragma unroll
  for (int i = 0; i < 8; ++i) {
    u32x4 P[2];
#pragma unroll
    for (int s = 0; s < 2; ++s)
#pragma unroll
      for (int j = 0; j < 4; ++j)
        P[s][j] = cvt_pk(__builtin_amdgcn_exp2f(S[i][8 * s + 2 * j] - mx), __builtin_amdgcn_exp2f(S[i][8 * s + 2 * j + 1] - mx));
    if (i == 0) {
#pragma unroll
      for (int s = 0; s < 2; ++s) {
        u32x4 P8, P0;
#pragma unroll
        for (int j = 0; j < 4; ++j) {
          const int r0 = 8 * s + 2 * j, r1 = r0 + 1;
          const unsigned mk = ((((r0 & 3) + 8 * (r0 >> 2)) <= dq) ? 0xffffu : 0u) | ((((r1 & 3) + 8 * (r1 >> 2)) <= dq) ? 0xffff0000u : 0u);
          P8[j] = P[s][j] & mk;
          P0[j] = P[s][j] & ~mk;
        }
        O = MFMA32(lds_r128(vp + s * 592), __builtin_bit_cast(bf16x8, P0), O);
        O = MFMA32(lds_r128(vp + (16 + s) * 592), __builtin_bit_cast(bf16x8, P8), O);
      }
    } else {
#pragma unroll
      for (int s = 0; s < 2; ++s) O = MFMA32(lds_r128(vp + (2 * i + s) * 592), __builtin_bit_cast(bf16x8, P[s]), O);
    }
  }
  const float inv = __builtin_amdgcn_rcpf(O[10]);
  const int row = t * 32 + l31;
  char* ob = ws + WS_O + (size_t)(row >> 4) * 18 * 1024 + (row & 15) * 16;
  u32x4 o = {cvt_pk(O[0] * inv, O[1] * inv), cvt_pk(O[2] * inv, O[3] * inv), cvt_pk(O[4] * inv, O[5] * inv), cvt_pk(O[6] * inv, O[7] * inv)};
  *(u32x4*)(ob + (hd >> 1) * 1024 + (2 * (hd & 1) + hi) * 256) = o;
  if (hi == 0) *(unsigned*)(ob + (16 + (hd >> 4)) * 1024 + ((hd >> 2) & 3) * 256 + (hd & 3) * 4) = cvt_pk(O[8] * inv, O[9] * inv);
  STAMP_END(1);
}

constexpr int T_SLOT = 18432, T_NSLOT = 8, T_LDS = T_SLOT * T_NSLOT;

template <int MODE>
__global__ __launch_bounds__(512, 2) void k_t80(const char* __restrict__ aimg, const char* __restrict__ w, const char* __restrict__ resid,
                                                 char* __restrict__ ws, char* __restrict__ out) {
  extern __shared__ __attribute__((aligned(16))) char smem[];
  STAMP_BEGIN
  lds_ptr lds = (lds_ptr)smem;
  const unsigned lds0 = (unsigned)(uintptr_t)lds;
  constexpr int NSB = MODE == 0 ? 1 : 8, NST = NSB * 9, KSTEPS = NSB * 18;
  const int tid = threadIdx.x, lane = tid & 63, wid = __builtin_amdgcn_readfirstlane(tid >> 6);
  const int tile = xcd_remap(blockIdx.x, 234);
  const int mt = tile / 9, nt = tile - mt * 9;
  const int m0 = min(mt * 80, SEQ - 80), rb0 = m0 >> 4;
  const int er = tid >> 3, ec = tid & 7;
  u32x4 res0, res1 = {0, 0, 0, 0};
  {
    const int col = nt * 64 + ec * 8;
    if (MODE == 0) {
      res0 = *(const u32x4*)(resid + (size_t)(m0 + er) * 1152 + col * 2);
      if (tid < 128) res1 = *(const u32x4*)(resid + (size_t)(m0 + 64 + er) * 1152 + col * 2);
    } else {
      const size_t co = (size_t)(col >> 5) * 1024 + ((col >> 3) & 3) * 256 + (er & 15) * 16;
      res0 = *(const u32x4*)(resid + (size_t)(rb0 + (er >> 4)) * 18432 + co);
      if (tid < 128) res1 = *(const u32x4*)(resid + (size_t)(rb0 + 4) * 18432 + co);
    }
  }
  if (MODE == 0 && blockIdx.x < 144 && tid < 64) {
    const int c = blockIdx.x * 64 + tid;
    float s = 0.f;
#pragma unroll
    for (int i = 0; i < 8; ++i) s += *(const float*)(ws + WS_CSP + ((size_t)i * 9216 + c) * 4);
    *(float*)(ws + WS_CS + (size_t)c * 4) = s;
  }
  const int ao0 = 1280 * wid + 16 * lane, ao1 = 1280 * wid + 1024 + 16 * (lane & 15);
  const unsigned avo0 = (ao0 >> 11) * (KSTEPS * 1024) + (ao0 & 2047);
  const unsigned avo1 = (ao1 >> 11) * (KSTEPS * 1024) + (ao1 & 2047);
  const char* asrc = aimg + (size_t)rb0 * KSTEPS * 1024;
  const int rho = 8 * wid + (lane >> 3);
  const int ks_ = rho >> 5, g_ = ((rho >> 4) & 1) * 2 + ((rho >> 2) & 1), t_ = (rho >> 3) & 1, q_ = rho & 3;
  int L1, L2;
  if (MODE == 0) { L1 = 36 * ks_ + 18 * (g_ >> 1) + 8 * t_ + 4 * (g_ & 1) + q_; L2 = 18 * (16 * ks_ + 4 * g_ + 2 * t_ + (q_ >> 1)) + 16 + (q_ & 1); }
  else { L1 = 32 * ks_ + 16 * t_ + 4 * g_ + q_; L2 = 288 * ks_ + 144 * (g_ >> 1) + 128 + 8 * (g_ & 1) + 4 * t_ + q_; }
  const unsigned csw = ((lane & 7) ^ (((rho >> 1) & 3) << 1)) * 16;
  const unsigned wvo1 = L1 * 1152 + csw, wvo2 = L2 * 1152 + csw;
  const char* wsrc = w + nt * 128;
  auto issue = [&](int n, int slot) {
    const int sb = n / 9, st = n - sb * 9;
    const unsigned dst = lds0 + slot * T_SLOT;
    glds16(asrc + (size_t)n * 2048, avo0, dst + 1280 * wid);
    if (lane < 16) glds16(asrc + (size_t)n * 2048, avo1, dst + 1280 * wid + 1024);
    const int b1 = MODE == 0 ? 72 * st : 144 * (st >> 1) + 64 * (st & 1);
    const int rowbase = sb * 576 + (st == 8 ? 0 : b1);
    glds16(wsrc + (size_t)rowbase * 1152, st == 8 ? wvo2 : wvo1, dst + 10240 + 1024 * wid);
  };
#pragma unroll
  for (int n = 0; n < 7; ++n) issue(n, n);
  const int kg = wid >> 2, nf = wid & 3;
  const int g = lane >> 4, q = (lane & 15) >> 2, p = lane & 3;
  lds_ptr ab = lds + kg * 1024 + lane * 16;
  const int rho0 = 32 * kg + 16 * (g >> 1) + 4 * (g & 1) + q;
  lds_ptr wb = lds + 10240 + rho0 * 128 + ((nf ^ ((rho0 >> 1) & 3)) * 32) + p * 8;
  f32x4 acc[5];
#pragma unroll
  for (int m = 0; m < 5; ++m) acc[m] = f32x4{0.f, 0.f, 0.f, 0.f};
  auto compute = [&](int slot) {
    const int so = slot * T_SLOT;
    bf16x8 wf = cat8(lds_tr(wb + so), lds_tr(wb + so + 1024));
#pragma unroll
    for (int m = 0; m < 5; ++m) acc[m] = MFMA16(wf, lds_r128(ab + so + m * 2048), acc[m]);
  };
  int s = 0;
  for (; s < NST - 7; ++s) {
    wait_bar<18>();
    issue(s + 7, (s + 7) & 7);
    compute(s & 7);
  }
  wait_bar<18>(); compute((NST - 7) & 7);
  wait_bar<15>(); compute((NST - 6) & 7);
  wait_bar<12>(); compute((NST - 5) & 7);
  wait_bar<9>();  compute((NST - 4) & 7);
  wait_bar<6>();  compute((NST - 3) & 7);
  wait_bar<3>();  compute((NST - 2) & 7);
  wait_bar<0>();  compute((NST - 1) & 7);
  lds_bar();
  {
    lds_ptr cw = lds + kg * (80 * 272) + (lane & 15) * 272 + (nf * 16 + 4 * g) * 4;
#pragma unroll
    for (int m = 0; m < 5; ++m) *(LDS_AS f32x4*)(cw + m * 16 * 272) = acc[m];
  }
  lds_bar();
#pragma unroll
  for (int pass = 0; pass < 2; ++pass) {
    if (pass == 1 && tid >= 128) break;
    const int rr = er + 64 * pass;
    lds_ptr cr = lds + rr * 272 + ec * 32;
    f32x4 a0 = *(LDS_AS f32x4*)(cr), a1 = *(LDS_AS f32x4*)(cr + 16);
    f32x4 b0 = *(LDS_AS f32x4*)(cr + 80 * 272), b1 = *(LDS_AS f32x4*)(cr + 80 * 272 + 16);
    const u32x4 rs = pass ? res1 : res0;
    a0 += b0; a1 += b1;
    u32x4 o;
    o[0] = cvt_pk(a0[0] + bf_lo(rs[0]), a0[1] + bf_hi(rs[0]));
    o[1] = cvt_pk(a0[2] + bf_lo(rs[1]), a0[3] + bf_hi(rs[1]));
    o[2] = cvt_pk(a1[0] + bf_lo(rs[2]), a1[1] + bf_hi(rs[2]));
    o[3] = cvt_pk(a1[2] + bf_lo(rs[3]), a1[3] + bf_hi(rs[3]));
    const int row = m0 + rr, col = nt * 64 + ec * 8;
    if (MODE == 0) {
      *(u32x4*)(ws + WS_H + (size_t)(row >> 4) * 18432 + (col >> 5) * 1024 + ((col >> 3) & 3) * 256 + (row & 15) * 16) = o;
      float s1 = 0.f, s2 = 0.f;
#pragma unroll
      for (int j = 0; j < 4; ++j) { s1 = dot2(o[j], 0x3f803f80u, s1); s2 = dot2(o[j], o[j], s2); }
      s1 = sum8(s1); s2 = sum8(s2);
      if (ec == 0) *(f32x2*)(ws + WS_ST + (size_t)row * 80 + nt * 8) = f32x2{s1, s2};
    } else {
      *(u32x4*)(out + (size_t)row * 1152 + col * 2) = o;
    }
  }
  STAMP_END(MODE == 0 ? 2 : 4);
}

constexpr int K4_WG = 16384, K4_WU = K4_WG + 9216 + 16, K4_SLOT = K4_WU + 9216, K4_NSLOT = 4;
constexpr int K4_ST = K4_SLOT * K4_NSLOT, K4_CS = K4_ST + 2048, K4_LDS = K4_CS + 2048;

__global__ __launch_bounds__(512, 2) void k4_gateup(const char* __restrict__ wgate, const char* __restrict__ wup, char* __restrict__ ws) {
  extern __shared__ __attribute__((aligned(16))) char smem[];
  STAMP_BEGIN
  lds_ptr lds = (lds_ptr)smem;
  const unsigned lds0 = (unsigned)(uintptr_t)lds;
  const int tid = threadIdx.x, lane = tid & 63, wid = __builtin_amdgcn_readfirstlane(tid >> 6);
  const int xcd = blockIdx.x & 7, idx = blockIdx.x >> 3;
  const int pn = 4 * (xcd >> 2) + (idx & 3), jt = 8 * (xcd & 3) + (idx >> 2);
  const int wm = wid & 3, wn = wid >> 2;
  f32x4 sp[5];
  float csv = 0.f;
  if (tid < 256) {
    const char* stp = ws + WS_ST + (size_t)(pn * 256 + tid) * 80;
#pragma unroll
    for (int i = 0; i < 4; ++i) sp[i] = *(const f32x4*)(stp + i * 16);
    f32x2 l = *(const f32x2*)(stp + 64);
    sp[4] = f32x4{l[0], l[1], 0.f, 0.f};
  }
  if (tid >= 224) {
    const int c = tid - 224;
    csv = *(const float*)(ws + WS_CS + (size_t)((c < 144 ? 0 : FF - 144) + jt * 144 + c) * 4);
  }
  const char* asrc = ws + WS_H + (size_t)(pn * 16 + 2 * wid) * 18432;
  const unsigned avo = lane * 16;
  const char* wsrc = (wn ? wup : wgate) + jt * 288;
  unsigned wvo[3];
#pragma unroll
  for (int i = 0; i < 3; ++i) {
    const int pc = i < 2 ? 2 * wm + i : 8;
    const int o = pc * 1024 + lane * 16;
    const int rho = o / 288, u = (o - rho * 288) >> 4;
    const int gk = ((rho >> 4) & 1) * 2 + ((rho >> 2) & 1), tk = (rho >> 3) & 1, qk = rho & 3;
    wvo[i] = (8 * gk + 4 * tk + qk) * 9216 + u * 16;
  }
  const unsigned wdst = lds0 + (wn ? K4_WU : K4_WG);
  auto issue = [&](int s, int slot) {
    const unsigned so = slot * K4_SLOT;
    glds16(asrc + s * 1024, avo, lds0 + so + wid * 2048);
    glds16(asrc + 18432 + s * 1024, avo, lds0 + so + wid * 2048 + 1024);
    const char* wp = wsrc + (size_t)s * 32 * 9216;
    glds16(wp, wvo[0], wdst + so + wm * 2048);
    glds16(wp, wvo[1], wdst + so + wm * 2048 + 1024);
    if (wm == 0) glds16(wp, wvo[2], wdst + so + 8192);
  };
  issue(0, 0); issue(1, 1); issue(2, 2);
  if (tid < 256) {
    const float s1 = sp[0][0] + sp[0][2] + sp[1][0] + sp[1][2] + sp[2][0] + sp[2][2] + sp[3][0] + sp[3][2] + sp[4][0];
    const float s2 = sp[0][1] + sp[0][3] + sp[1][1] + sp[1][3] + sp[2][1] + sp[2][3] + sp[3][1] + sp[3][3] + sp[4][1];
    const float mean = s1 * (1.f / 576.f);
    const float var = fmaxf(s2 * (1.f / 576.f) - mean * mean, 0.f);
    *(LDS_AS f32x2*)(lds + K4_ST + tid * 8) = f32x2{-mean, __builtin_amdgcn_rsqf(var + LN_EPS)};
  }
  if (tid >= 224) *(LDS_AS float*)(lds + K4_CS + (tid - 224) * 4) = csv;
  const int g = lane >> 4, q = (lane & 15) >> 2, p = lane & 3;
  lds_ptr ab = lds + wm * 4096 + lane * 16;
  const int rowp = (16 * (g >> 1) + 4 * (g & 1) + q) * 288;
  lds_ptr wb = lds + K4_WG + rowp + wn * 128 + p * 8;
  lds_ptr wmx = lds + K4_WG + rowp + 256 + wn * 16 + (p & 1) * 8 + (p >> 1) * (K4_WU - K4_WG);
  f32x4 acc[4][9];
#pragma unroll
  for (int m = 0; m < 4; ++m)
#pragma unroll
    for (int n = 0; n < 9; ++n) acc[m][n] = f32x4{0.f, 0.f, 0.f, 0.f};
  auto compute = [&](int slot) {
    const int so = slot * K4_SLOT;
    bf16x8 a[4];
#pragma unroll
    for (int m = 0; m < 4; ++m) a[m] = lds_r128(ab + so + m * 1024);
#pragma unroll
    for (int n = 0; n < 9; ++n) {
      lds_ptr bp = n < 4 ? wb + n * 32 : (n < 8 ? wb + (K4_WU - K4_WG) + (n - 4) * 32 : wmx);
      bf16x8 b = cat8(lds_tr(bp + so), lds_tr(bp + so + 2304));
#pragma unroll
      for (int m = 0; m < 4; ++m) acc[m][n] = MFMA16(b, a[m], acc[m][n]);
    }
  };
#pragma unroll 1
  for (int s = 0; s < 18; ++s) {
    if (s < 16) wait_bar<8>();
    else if (s == 16) wait_bar<4>();
    else wait_bar<0>();
    if (s < 15) issue(s + 3, (s + 3) & 3);
    compute(s & 3);
  }
  __builtin_amdgcn_sched_barrier(0);
  const int r = lane & 15;
  const float L2E = 1.4426950408889634f;
  char* hb = ws + WS_HID + (size_t)(pn * 16 + wm * 4) * (144 * 1024) + (size_t)(jt >> 2) * 18432;
  f32x2 st[4];
#pragma unroll
  for (int m = 0; m < 4; ++m) st[m] = *(LDS_AS f32x2*)(lds + K4_ST + (wm * 64 + m * 16 + r) * 8);
  auto glu = [&](float ag, float au, float cg, float cu, f32x2 s) -> float {
    const float tg = fmaf(s[0], cg, ag), tu = fmaf(s[0], cu, au);
    const float e = __builtin_amdgcn_exp2f(tg * (-L2E * s[1]));
    return tu * s[1] * __builtin_amdgcn_rcpf(1.f + e);
  };
#pragma unroll
  for (int kp = 0; kp < 2; ++kp) {
    f32x4 cg[2], cu[2];
#pragma unroll
    for (int h = 0; h < 2; ++h) {
      cg[h] = *(LDS_AS f32x4*)(lds + K4_CS + (wn * 64 + (2 * kp + h) * 16 + 4 * g) * 4);
      cu[h] = *(LDS_AS f32x4*)(lds + K4_CS + (144 + wn * 64 + (2 * kp + h) * 16 + 4 * g) * 4);
    }
#pragma unroll
    for (int m = 0; m < 4; ++m) {
      u32x4 o;
#pragma unroll
      for (int h = 0; h < 2; ++h) {
        const f32x4 ag = acc[m][2 * kp + h], au = acc[m][4 + 2 * kp + h];
        o[2 * h] = cvt_pk(glu(ag[0], au[0], cg[h][0], cu[h][0], st[m]), glu(ag[1], au[1], cg[h][1], cu[h][1], st[m]));
        o[2 * h + 1] = cvt_pk(glu(ag[2], au[2], cg[h][2], cu[h][2], st[m]), glu(ag[3], au[3], cg[h][3], cu[h][3], st[m]));
      }
      *(u32x4*)(hb + (size_t)m * (144 * 1024) + (4 * (jt & 3) + 2 * wn + kp) * 1024 + lane * 16) = o;
    }
  }
  {
    const f32x4 cg = *(LDS_AS f32x4*)(lds + K4_CS + (128 + 8 * wn + 4 * (g & 1)) * 4);
    const f32x4 cu = *(LDS_AS f32x4*)(lds + K4_CS + (144 + 128 + 8 * wn + 4 * (g & 1)) * 4);
#pragma unroll
    for (int mp = 0; mp < 2; ++mp) {
      f32x4 ag, au;
#pragma unroll
      for (int j = 0; j < 4; ++j) {
        auto rr = __builtin_amdgcn_permlane32_swap(__float_as_uint(acc[2 * mp][8][j]), __float_as_uint(acc[2 * mp + 1][8][j]), false, false);
        ag[j] = __uint_as_float(rr[0]);
        au[j] = __uint_as_float(rr[1]);
      }
      const int mh = lane >> 5;
      const f32x2 s = *(LDS_AS f32x2*)(lds + K4_ST + (wm * 64 + (2 * mp + mh) * 16 + r) * 8);
      u32x2 o = {cvt_pk(glu(ag[0], au[0], cg[0], cu[0], s), glu(ag[1], au[1], cg[1], cu[1], s)),
                 cvt_pk(glu(ag[2], au[2], cg[2], cu[2], s), glu(ag[3], au[3], cg[3], cu[3], s))};
      *(u32x2*)(hb + (size_t)(2 * mp + mh) * (144 * 1024) + (16 + ((jt >> 1) & 1)) * 1024 + (2 * (jt & 1) + wn) * 256 + r * 16 + (g & 1) * 8) = o;
    }
  }
  STAMP_END(3);
}

extern "C" void kernel_launch(void* const* d_in, const int* in_sizes, int n_in, void* d_out, int out_size, void* d_ws, size_t ws_size,
                              hipStream_t stream) {
  const char* x = (const char*)d_in[0];
  const char* wqkv = (const char*)d_in[1];
  const char* wo = (const char*)d_in[2];
  const char* wg = (const char*)d_in[4];
  const char* wu = (const char*)d_in[5];
  const char* wd = (const char*)d_in[6];
  char* ws = (char*)d_ws;
  (void)hipFuncSetAttribute((const void*)k1_ln_qkv, hipFuncAttributeMaxDynamicSharedMemorySize, K1_LDS);
  (void)hipFuncSetAttribute((const void*)k2_attn, hipFuncAttributeMaxDynamicSharedMemorySize, K2_LDS);
  (void)hipFuncSetAttribute((const void*)k_t80<0>, hipFuncAttributeMaxDynamicSharedMemorySize, T_LDS);
  (void)hipFuncSetAttribute((const void*)k_t80<1>, hipFuncAttributeMaxDynamicSharedMemorySize, T_LDS);
  (void)hipFuncSetAttribute((const void*)k4_gateup, hipFuncAttributeMaxDynamicSharedMemorySize, K4_LDS);
  k1_ln_qkv<<<234, 768, K1_LDS, stream>>>(x, wqkv, ws);
  k2_attn<<<256, 512, K2_LDS, stream>>>(ws, wg, wu);
  k_t80<0><<<234, 512, T_LDS, stream>>>(ws + WS_O, wo, x, ws, nullptr);
  k4_gateup<<<256, 512, K4_LDS, stream>>>(wg, wu, ws);
  k_t80<1><<<234, 512, T_LDS, stream>>>(ws + WS_HID, wd, ws + WS_H, ws, (char*)d_out);
}
```

```cpp
#include <hip/hip_runtime.h>
#include <hip/hip_bf16.h>
#include <stdint.h>
#include <utility>
#include <type_traits>

typedef __attribute__((ext_vector_type(8))) short bf16x8;
typedef __attribute__((ext_vector_type(4))) short s16x4;
typedef __attribute__((ext_vector_type(4))) float f32x4;
typedef __attribute__((ext_vector_type(16))) float f32x16;
typedef __attribute__((ext_vector_type(4))) unsigned u32x4;
typedef __attribute__((ext_vector_type(2))) unsigned u32x2;
typedef __attribute__((ext_vector_type(2))) __bf16 bf16x2_t;
typedef __attribute__((ext_vector_type(2))) float f32x2;
#define LDS_AS __attribute__((address_space(3)))
typedef LDS_AS char* lds_ptr;
#define DI __device__ __forceinline__

constexpr int SEQ = 2048, DM = 576, NH = 32, FF = 4608;
constexpr float LN_EPS = 1e-5f;
constexpr float QSCALE = 0.23570226039551584f * 1.4426950408889634f;

constexpr size_t WS_QM = 0;
constexpr size_t WS_KM = WS_QM + (size_t)32 * 64 * 1024;
constexpr size_t WS_QX = WS_KM + (size_t)32 * 64 * 1024;
constexpr size_t WS_KX = WS_QX + (size_t)16 * 2048 * 8;
constexpr size_t WS_V = WS_KX + (size_t)16 * 2048 * 8;
constexpr size_t WS_O = WS_V + (size_t)32 * 128 * 592 + 1024;
constexpr size_t WS_H = WS_O + (size_t)128 * 18 * 1024;
constexpr size_t WS_ST = WS_H + (size_t)128 * 18 * 1024;
constexpr size_t WS_CSP = WS_ST + (size_t)2048 * 80;
constexpr size_t WS_CS = WS_CSP + (size_t)8 * 9216 * 4;
constexpr size_t WS_HID = WS_CS + (size_t)9216 * 4;
constexpr size_t WS_END = WS_HID + (size_t)128 * 144 * 1024;
static_assert(WS_O % 1024 == 0 && WS_H % 1024 == 0 && WS_ST % 256 == 0 && WS_CSP % 256 == 0 && WS_HID % 256 == 0, "align");

#define STAMP_BEGIN
#define STAMP_MARK(k, i)
#define STAMP_MARKW(k, i, T)
#define STAMP_ENDW(k, T)
#define STAMP_CYC0
#define STAMP_CYC(k, i)
#define STAMP_END(k)

DI unsigned cvt_pk(float lo, float hi) {
  f32x2 v = {lo, hi};
  bf16x2_t r = __builtin_convertvector(v, bf16x2_t);
  return __builtin_bit_cast(unsigned, r);
}
DI float bf_lo(unsigned u) { return __builtin_bit_cast(float, u << 16); }
DI float bf_hi(unsigned u) { return __builtin_bit_cast(float, u & 0xffff0000u); }
DI float dot2(unsigned a, unsigned b, float c) {
  return __builtin_amdgcn_fdot2_f32_bf16(__builtin_bit_cast(bf16x2_t, a), __builtin_bit_cast(bf16x2_t, b), c, false);
}
template <int CTRL> DI float dpp_add(float v) {
  int t = __builtin_amdgcn_update_dpp(0, __builtin_bit_cast(int, v), CTRL, 0xf, 0xf, true);
  return v + __builtin_bit_cast(float, t);
}
DI float sum8(float v) {
  v = dpp_add<0xB1>(v);
  v = dpp_add<0x4E>(v);
  v = dpp_add<0x141>(v);
  return v;
}
DI void glds16(const void* sbase, unsigned voff, unsigned lds_dst) {
  asm volatile("s_mov_b32 m0, %2\n\ts_nop 3\n\tglobal_load_lds_dwordx4 %0, %1" ::"v"(voff), "s"(sbase), "s"(lds_dst) : "memory");
}
#define ST_AUX " sc1"
DI void st16(void* p, u32x4 v) { asm volatile("global_store_dwordx4 %0, %1, off" ST_AUX "\n\ts_nop 1" ::"v"(p), "v"(v) : "memory"); }
DI void st8(void* p, u32x2 v) { *(u32x2*)p = v; }
DI void st4(void* p, unsigned v) { *(unsigned*)p = v; }
DI void glds16_if0(int c, const void* sbase, unsigned voff, unsigned lds_dst) {
  asm volatile("s_cmp_lg_u32 %3, 0\n\ts_cbranch_scc1 1f\n\ts_mov_b32 m0, %2\n\ts_nop 1\n\tglobal_load_lds_dwordx4 %0, %1\n1:"
               ::"v"(voff), "s"(sbase), "s"(lds_dst), "s"(c) : "memory", "scc");
}
DI void touch_if0(int c, const void* sbase, unsigned voff, unsigned lds_dst) {
  asm volatile("s_cmp_lg_u32 %3, 0\n\ts_cbranch_scc1 1f\n\ts_mov_b32 m0, %2\n\ts_nop 1\n\tglobal_load_lds_dword %0, %1\n1:"
               ::"v"(voff), "s"(sbase), "s"(lds_dst), "s"(c) : "memory", "scc");
}
template <int N> DI void wait_bar() { asm volatile("s_waitcnt vmcnt(%0) lgkmcnt(0)\n\ts_barrier" ::"n"(N) : "memory"); }
template <int N> DI void wait_vm() { asm volatile("s_waitcnt vmcnt(%0)" ::"n"(N) : "memory"); }
DI void raw_bar() { asm volatile("s_barrier" ::: "memory"); }
DI void lds_bar() { asm volatile("s_waitcnt lgkmcnt(0)\n\ts_barrier" ::: "memory"); }
DI bf16x8 lds_r128(lds_ptr p) { return *(LDS_AS const bf16x8*)p; }
typedef short v4i16_t __attribute__((ext_vector_type(4)));
DI s16x4 lds_tr(lds_ptr p) { return __builtin_amdgcn_ds_read_tr16_b64_v4i16((LDS_AS v4i16_t*)p); }
DI bf16x8 cat8(s16x4 a, s16x4 b) { return __builtin_shufflevector(a, b, 0, 1, 2, 3, 4, 5, 6, 7); }
#define SB() __builtin_amdgcn_sched_barrier(0)
#define MFMA16(a, b, c) __builtin_amdgcn_mfma_f32_16x16x32_bf16(a, b, c, 0, 0, 0)
#define MFMA32(a, b, c) __builtin_amdgcn_mfma_f32_32x32x16_bf16(a, b, c, 0, 0, 0)
DI void mfma16_acc(f32x4& c, bf16x8 a, bf16x8 b) { asm volatile("v_mfma_f32_16x16x32_bf16 %0, %1, %2, %0" : "+v"(c) : "v"(a), "v"(b)); }
DI void mfma16_zero(f32x4& c, bf16x8 a, bf16x8 b) { asm volatile("v_mfma_f32_16x16x32_bf16 %0, %1, %2, 0" : "=&v"(c) : "v"(a), "v"(b)); }
template <class F, int... I> DI void for_seq_impl(F&& f, std::integer_sequence<int, I...>) { (f(std::integral_constant<int, I>{}), ...); }
template <int N, class F> DI void for_seq(F&& f) { for_seq_impl(f, std::make_integer_sequence<int, N>{}); }
DI int xcd_remap(int b, int nwg) {
  int q = nwg >> 3, r = nwg & 7, x = b & 7, o = b >> 3;
  return (x < r ? x * (q + 1) : r * (q + 1) + (x - r) * q) + o;
}

constexpr int K1_X = 0, K1_W = 40960, K1_SLOT = 12288, K1_ST = K1_W + K1_SLOT * 9, K1_CS = K1_W, K1_PF = K1_ST + 1024, K1_LDS = K1_PF + 1024;
constexpr int cmin(int a, int b) { return a < b ? a : b; }
constexpr int k1_nw(int kk) { return cmin(18, cmin(3 + 2 * kk, kk + 7)); }
constexpr int k1_nx(int kk) { return cmin(18, cmin(3 + 2 * kk, kk + 5)); }

DI void touch_lines(const char* base, unsigned voff, unsigned lds_dummy) {
  asm volatile("s_mov_b32 m0, %2\n\ts_nop 3\n\tglobal_load_lds_dword %0, %1" ::"v"(voff), "s"(base), "s"(lds_dummy) : "memory");
}

template <bool ISV, bool HI>
DI void k1_consumer(lds_ptr lds, int cb, int lane, char* ws, int ct, int m0) {
  const int g = lane >> 4, q = (lane & 15) >> 2, p = lane & 3, r = lane & 15;
  constexpr int NM = HI ? 2 : 3, MB = HI ? 3 : 0;
  lds_ptr a0 = lds + K1_X + MB * 2048 + (r >> 3) * 1024 + (r & 7) * 128 + ((g ^ (r >> 1)) * 16);
  lds_ptr a1 = lds + K1_X + MB * 2048 + (r >> 3) * 1024 + (r & 7) * 128 + (((4 + g) ^ (r >> 1)) * 16);
  const int rho0 = 16 * (g >> 1) + 4 * (g & 1) + q;
  lds_ptr wb = lds + K1_W + rho0 * 128 + ((cb ^ ((rho0 >> 1) & 3)) * 32) + p * 8;
  lds_ptr wb5 = wb + 5 * K1_SLOT;
  asm volatile("" : "+v"(wb), "+v"(wb5));
  const u32x4 onesu = {0x3f803f80u, 0x3f803f80u, 0x3f803f80u, 0x3f803f80u};
  const bf16x8 ones = __builtin_bit_cast(bf16x8, onesu);
  f32x4 acc[3][3];
#pragma unroll
  for (int m = 0; m < 3; ++m)
#pragma unroll
    for (int j = 0; j < 3; ++j) acc[m][j] = f32x4{0.f, 0.f, 0.f, 0.f};
  bf16x8 fA[2][3];
  s16x4 wl[2][3], wh[2][3];
  auto rd1 = [&](auto KK, auto II) {
    constexpr int kk = KK, i = II, bf = kk & 1, sl = kk % 9;
    if constexpr (i >= 2 && i <= 4) {
      if constexpr (i - 2 < NM) fA[bf][i - 2] = lds_r128(((kk & 1) ? a1 : a0) + ((kk >> 1) & 3) * 10240 + (i - 2) * 2048);
    } else {
      constexpr int j = i < 2 ? 0 : (i - 3) / 2, hh = i < 2 ? i : (i - 3) % 2;
      lds_ptr bp = (sl >= 5 ? wb5 + (sl - 5) * K1_SLOT : wb + sl * K1_SLOT) + j * 4096 + hh * 1024;
      if constexpr (hh == 0) wl[bf][j] = lds_tr(bp); else wh[bf][j] = lds_tr(bp);
    }
  };
  lds_bar();
  STAMP_MARKW(0, 2, 512);
  for_seq<9>([&](auto I) { rd1(std::integral_constant<int, 0>{}, I); });
  SB();
  for_seq<18>([&](auto KK) {
    constexpr int kk = KK, bf = kk & 1;
    if constexpr (kk < 17) { raw_bar(); SB(); }
    for_seq<9>([&](auto II) {
      constexpr int i = II, j = i / 3, m = i % 3;
      const bf16x8 wv = cat8(wl[bf][j], wh[bf][j]);
      const bf16x8 av = (m < NM) ? fA[bf][m] : ones;
      acc[m][j] = ISV ? MFMA16(av, wv, acc[m][j]) : MFMA16(wv, av, acc[m][j]);
      SB();
      if constexpr (kk < 17) { rd1(std::integral_constant<int, kk + 1>{}, II); SB(); }
    });
  });
  STAMP_MARKW(0, 4, 512);
  lds_ptr cx = lds + K1_CS + cb * 3072 + lane * 16;
  if (HI) {
#pragma unroll
    for (int j = 0; j < 3; ++j) *(LDS_AS f32x4*)(cx + j * 1024) = acc[2][j];
  }
  lds_bar();
  STAMP_MARKW(0, 5, 512);
  f32x4 cs[3];
#pragma unroll
  for (int j = 0; j < 3; ++j) cs[j] = HI ? acc[2][j] : *(LDS_AS f32x4*)(cx + j * 1024);
  if (ISV) {
    f32x4 st[NM][2];
#pragma unroll
    for (int m = 0; m < NM; ++m) {
      st[m][0] = *(LDS_AS f32x4*)(lds + K1_ST + ((MB + m) * 16 + 4 * g) * 8);
      st[m][1] = *(LDS_AS f32x4*)(lds + K1_ST + ((MB + m) * 16 + 4 * g) * 8 + 16);
    }
#pragma unroll
    for (int j = 0; j < 3; ++j) {
      const int c = (ct - 6) * 192 + j * 64 + cb * 16 + r;
      const int head = c / 18, d = c - head * 18;
      char* vb = ws + WS_V + (size_t)(head * 128 + (m0 >> 4) + MB) * 592;
      char* vp = vb + (g & 1) * 288 + d * 16 + (g >> 1) * 8;
      const float c0 = cs[j][0];
#pragma unroll
      for (int m = 0; m < NM; ++m) {
        u32x2 o = {cvt_pk(fmaf(st[m][0][0], c0, acc[m][j][0]) * st[m][0][1], fmaf(st[m][0][2], c0, acc[m][j][1]) * st[m][0][3]),
                   cvt_pk(fmaf(st[m][1][0], c0, acc[m][j][2]) * st[m][1][1], fmaf(st[m][1][2], c0, acc[m][j][3]) * st[m][1][3])};
        st8(vp + m * 592, o);
      }
      if (d == 17 && g < NM) *(u32x4*)(vb + g * 592 + 576) = onesu;
    }
  } else {
    const bool isq = ct < 3;
    const float sc = isq ? QSCALE : 1.f;
    f32x2 st[NM];
#pragma unroll
    for (int m = 0; m < NM; ++m) { st[m] = *(LDS_AS f32x2*)(lds + K1_ST + ((MB + m) * 16 + r) * 8); st[m][1] *= sc; }
#pragma unroll
    for (int j = 0; j < 3; ++j) {
      const int c = (isq ? ct : ct - 3) * 192 + j * 64 + cb * 16 + 4 * g;
      const int pair = c / 36, qi = (c - pair * 36) >> 2;
      const int head = 2 * pair + (qi > 4 ? 1 : 0), a = qi > 4 ? qi - 5 : qi;
      char* mainp = ws + (isq ? WS_QM : WS_KM) + (size_t)head * 65536 + (a >> 1) * 512 + (a & 1) * 8;
      char* xp = ws + (isq ? WS_QX : WS_KX) + (size_t)pair * 16384;
#pragma unroll
      for (int m = 0; m < NM; ++m) {
        const int rw = m0 + (MB + m) * 16 + r;
        u32x2 o = {cvt_pk(fmaf(st[m][0], cs[j][0], acc[m][j][0]) * st[m][1], fmaf(st[m][0], cs[j][1], acc[m][j][1]) * st[m][1]),
                   cvt_pk(fmaf(st[m][0], cs[j][2], acc[m][j][2]) * st[m][1], fmaf(st[m][0], cs[j][3], acc[m][j][3]) * st[m][1])};
        char* dst = (qi == 4) ? xp + rw * 8 : mainp + (rw >> 5) * 1024 + (rw & 31) * 16;
        st8(dst, o);
      }
    }
  }
}

__global__ __launch_bounds__(1024) void k1_ln_qkv(const char* __restrict__ x, const char* __restrict__ wqkv, char* __restrict__ ws) {
  extern __shared__ __attribute__((aligned(16))) char smem[];
  STAMP_BEGIN
  lds_ptr lds = (lds_ptr)smem;
  const unsigned lds0 = (unsigned)(uintptr_t)lds;
  const int tid = threadIdx.x, lane = tid & 63, wid = __builtin_amdgcn_readfirstlane(tid >> 6);
  const int tile = xcd_remap(blockIdx.x, 234);
  const int cgp = tile / 78, rem = tile - cgp * 78;
  const int mt = rem / 3, ct = cgp * 3 + (rem - mt * 3);
  const int m0 = min(mt * 80, SEQ - 80);
  if (wid < 3) {
    unsigned wv[4];
#pragma unroll
    for (int i = 0; i < 4; ++i) {
      const int rho = 8 * i + (lane >> 3);
      const int gk = ((rho >> 4) & 1) * 2 + ((rho >> 2) & 1), tk = (rho >> 3) & 1, qk = rho & 3;
      wv[i] = (8 * gk + 4 * tk + qk) * 3456 + (((lane & 7) ^ (((rho >> 1) & 3) << 1)) * 16);
    }
    const char* wsrc = wqkv + ct * 384 + wid * 128;
    const unsigned wdst = lds0 + K1_W + wid * 4096;
    for_seq<18>([&](auto KK) {
      constexpr int kk = KK;
#pragma unroll
      for (int n = (kk == 0 ? 0 : k1_nw(kk - 1)); n < k1_nw(kk); ++n)
#pragma unroll
        for (int i = 0; i < 4; ++i) glds16(wsrc + (size_t)n * 32 * 3456, wv[i], wdst + (n % 9) * K1_SLOT + i * 1024);
      wait_bar<(k1_nw(kk) - kk - 1) * 4>();
    });
    return;
  }
  if (wid == 3) {
    const unsigned v0 = (lane >> 3) * 1152 + (((lane & 7) ^ (lane >> 4)) * 16);
    const unsigned v1 = (lane >> 3) * 1152 + (((lane & 7) ^ (4 + (lane >> 4))) * 16);
    const char* xs = x + (size_t)m0 * 1152;
    for_seq<18>([&](auto KK) {
      constexpr int kk = KK;
#pragma unroll
      for (int h = (kk == 0 ? 0 : k1_nx(kk - 1)); h < k1_nx(kk); ++h)
#pragma unroll
        for (int i = 0; i < 5; ++i) {
          const int pc = (h & 1) * 5 + i;
          glds16(xs + pc * 9216 + (h >> 1) * 128, (pc & 1) ? v1 : v0, lds0 + K1_X + ((h >> 1) & 3) * 10240 + pc * 1024);
        }
      wait_bar<(k1_nx(kk) - 2 * (kk >> 1) - 2) * 5>();
    });
    return;
  }
  if (wid < 8) {
    const int j = wid - 4;
    {
      const int ln = blockIdx.x * 148 + j * 37 + lane;
      if (lane < 37) {
        if (ln < 15552) touch_lines(wqkv, ln * 128, lds0 + K1_PF + j * 256);
        else if (ln < 33984) touch_lines(x, (ln - 15552) * 128, lds0 + K1_PF + j * 256);
      }
    }
    lds_ptr p0 = lds + K1_X + j * 1024 + lane * 16, p2 = lds + K1_X + min(j + 8, 9) * 1024 + lane * 16;
    float s1[3] = {0.f, 0.f, 0.f}, s2[3] = {0.f, 0.f, 0.f};
#pragma unroll 1
    for (int s = 0; s < 9; ++s) {
      raw_bar();
      const int so = (s & 3) * 10240;
      u32x4 v[3] = {*(LDS_AS u32x4*)(p0 + so), *(LDS_AS u32x4*)(p0 + so + 4096), *(LDS_AS u32x4*)(p2 + so)};
      SB();
      raw_bar();
      SB();
#pragma unroll
      for (int i = 0; i < 3; ++i)
#pragma unroll
        for (int e = 0; e < 4; ++e) { s1[i] = dot2(v[i][e], 0x3f803f80u, s1[i]); s2[i] = dot2(v[i][e], v[i][e], s2[i]); }
      asm volatile("" : "+v"(s1[0]), "+v"(s1[1]), "+v"(s1[2]), "+v"(s2[0]), "+v"(s2[1]), "+v"(s2[2]));
    }
#pragma unroll
    for (int i = 0; i < 3; ++i) {
      const float a = sum8(s1[i]), b = sum8(s2[i]);
      const float mean = a * (1.f / 576.f);
      const float var = fmaxf(b * (1.f / 576.f) - mean * mean, 0.f);
      const int pc = i < 2 ? j + 4 * i : min(j + 8, 9);
      if ((lane & 7) == 0) *(LDS_AS f32x2*)(lds + K1_ST + (pc * 8 + (lane >> 3)) * 8) = f32x2{-mean, __builtin_amdgcn_rsqf(var + LN_EPS)};
    }
    lds_bar();
    return;
  }
  if (ct >= 6) {
    if (wid < 12) k1_consumer<true, false>(lds, wid - 8, lane, ws, ct, m0);
    else k1_consumer<true, true>(lds, wid - 12, lane, ws, ct, m0);
  } else {
    if (wid < 12) k1_consumer<false, false>(lds, wid - 8, lane, ws, ct, m0);
    else k1_consumer<false, true>(lds, wid - 12, lane, ws, ct, m0);
  }
  STAMP_ENDW(0, 512);
}

constexpr int K2_KM = 0, K2_KX = 16384, K2_V = K2_KX + 4096, K2_CS = K2_V + 19 * 1024, K2_LDS = K2_CS + 4 * 2048 + 1024;

template <bool FIRST>
DI void k2_core(lds_ptr lds, char* ws, int hd, int t, int wid, int lane, bf16x8 q0, unsigned qx) {
  const int hi = lane >> 5, l31 = lane & 31;
  u32x4 q1u = {hi ? 0u : qx, 0u, 0u, 0u};
  bf16x8 q1 = __builtin_bit_cast(bf16x8, q1u);
  f32x16 S[9];
  lds_ptr kmp = lds + K2_KM + wid * 1024 + lane * 16;
  lds_ptr kxp = lds + K2_KX + (wid * 32 + l31) * 8 + (hd & 1) * 4;
  const float NEG = -1e30f;
  const int dq = l31 - 4 * hi;
  float mx = NEG;
#pragma unroll
  for (int ii = 0; ii < 9; ++ii) {
    const int i = ii == 0 ? 8 : ii - 1;
    bf16x8 k0 = lds_r128(kmp + i * 1024);
    u32x4 k1u = {*(LDS_AS unsigned*)(kxp + i * 256), 0u, 0u, 0u};
    f32x16 z = {};
    S[i] = MFMA32(k0, q0, z);
    S[i] = MFMA32(__builtin_bit_cast(bf16x8, k1u), q1, S[i]);
    if (FIRST && i < 8 && wid + i < 8) {
#pragma unroll
      for (int r = 0; r < 16; ++r) S[i][r] = NEG;
    }
    if (i == 0) {
#pragma unroll
      for (int r = 0; r < 16; ++r) S[0][r] = (((r & 3) + 8 * (r >> 2)) <= dq) ? S[8][r] : S[0][r];
    }
    if (i < 8) {
#pragma unroll
      for (int r = 0; r < 16; r += 2) mx = fmaxf(fmaxf(mx, S[i][r]), S[i][r + 1]);
    }
  }
  {
    auto rr = __builtin_amdgcn_permlane32_swap(__float_as_uint(mx), __float_as_uint(mx), false, false);
    mx = fmaxf(__uint_as_float(rr[0]), __uint_as_float(rr[1]));
  }
  wait_bar<0>();
  lds_ptr vp = lds + K2_V + wid * 2 * 592 + (l31 < 18 ? hi * 288 + l31 * 16 : 576);
  f32x16 O = {};
#pragma unroll
  for (int i = 0; i < 8; ++i) {
    u32x4 P[2];
#pragma unroll
    for (int s = 0; s < 2; ++s)
#pragma unroll
      for (int j = 0; j < 4; ++j)
        P[s][j] = cvt_pk(__builtin_amdgcn_exp2f(S[i][8 * s + 2 * j] - mx), __builtin_amdgcn_exp2f(S[i][8 * s + 2 * j + 1] - mx));
    if (i == 0) {
#pragma unroll
      for (int s = 0; s < 2; ++s) {
        u32x4 P8, P0;
#pragma unroll
        for (int j = 0; j < 4; ++j) {
          const int r0 = 8 * s + 2 * j, r1 = r0 + 1;
          const unsigned mk = ((((r0 & 3) + 8 * (r0 >> 2)) <= dq) ? 0xffffu : 0u) | ((((r1 & 3) + 8 * (r1 >> 2)) <= dq) ? 0xffff0000u : 0u);
          P8[j] = P[s][j] & mk;
          P0[j] = P[s][j] & ~mk;
        }
        O = MFMA32(lds_r128(vp + s * 592), __builtin_bit_cast(bf16x8, P0), O);
        O = MFMA32(lds_r128(vp + (16 + s) * 592), __builtin_bit_cast(bf16x8, P8), O);
      }
    } else {
#pragma unroll
      for (int s = 0; s < 2; ++s) O = MFMA32(lds_r128(vp + (2 * i + s) * 592), __builtin_bit_cast(bf16x8, P[s]), O);
    }
  }
  const float inv = __builtin_amdgcn_rcpf(O[10]);
  const int row = t * 32 + l31;
  char* ob = ws + WS_O + (size_t)(row >> 4) * 18 * 1024 + (row & 15) * 16;
  u32x4 o = {cvt_pk(O[0] * inv, O[1] * inv), cvt_pk(O[2] * inv, O[3] * inv), cvt_pk(O[4] * inv, O[5] * inv), cvt_pk(O[6] * inv, O[7] * inv)};
  st16(ob + (hd >> 1) * 1024 + (2 * (hd & 1) + hi) * 256, o);
  if (hi == 0) st4(ob + (16 + (hd >> 4)) * 1024 + ((hd >> 2) & 3) * 256 + (hd & 3) * 4, cvt_pk(O[8] * inv, O[9] * inv));
}

__global__ __launch_bounds__(768) void k2_attn(char* __restrict__ ws, const char* __restrict__ wg, const char* __restrict__ wu,
                                               const char* __restrict__ wd) {
  extern __shared__ __attribute__((aligned(16))) char smem[];
  STAMP_BEGIN
  lds_ptr lds = (lds_ptr)smem;
  const unsigned lds0 = (unsigned)(uintptr_t)lds;
  const int tid = threadIdx.x, lane = tid & 63, wid = __builtin_amdgcn_readfirstlane(tid >> 6);
  const int hd = (blockIdx.x & 7) * 4 + ((blockIdx.x >> 3) & 3), qb = blockIdx.x >> 5;
  if (wid >= 8) {
    asm volatile("s_barrier" ::: "memory");
    const int sw = wid - 8, crg = lane / 9, cu = lane - crg * 9;
    if (sw == 0 && lane < 21 && blockIdx.x * 21 + lane < 5184) touch_lines(wd, (blockIdx.x * 21 + lane) * 128, lds0 + K2_CS + 8192);
    float cs[8];
#pragma unroll
    for (int j = 0; j < 8; ++j) cs[j] = 0.f;
    const char* wsrc = (hd < 16 ? wg : wu) + (size_t)(hd & 15) * 576 + sw * 144 + cu * 16;
    u32x4 v[11];
#pragma unroll
    for (int i = 0; i < 11; ++i) {
      const int rr = crg + 7 * i;
      v[i] = u32x4{0, 0, 0, 0};
      if (lane < 63 && rr < 72) v[i] = *(const u32x4*)(wsrc + (size_t)(qb * 72 + rr) * 9216);
    }
    SB();
    asm volatile("s_barrier" ::: "memory");
    SB();
#pragma unroll
    for (int i = 0; i < 11; ++i)
#pragma unroll
      for (int j = 0; j < 4; ++j) { cs[2 * j] += bf_lo(v[i][j]); cs[2 * j + 1] += bf_hi(v[i][j]); }
    lds_ptr cp = lds + K2_CS + sw * 2048;
    *(LDS_AS f32x4*)(cp + lane * 32) = f32x4{cs[0], cs[1], cs[2], cs[3]};
    *(LDS_AS f32x4*)(cp + lane * 32 + 16) = f32x4{cs[4], cs[5], cs[6], cs[7]};
    asm volatile("s_waitcnt lgkmcnt(0)" ::: "memory");
    if (lane < 36) {
      f32x2 s = {0.f, 0.f};
#pragma unroll
      for (int i = 0; i < 7; ++i) s += *(LDS_AS f32x2*)(cp + i * 288 + lane * 8);
      *(f32x2*)(ws + WS_CSP + ((size_t)qb * 9216 + hd * 288 + sw * 72 + lane * 2) * 4) = s;
    }
    return;
  }
  const int t = qb * 8 + wid;
  bf16x8 q0;
  unsigned qx;
  {
    const unsigned vo = lane * 16;
    const int kt0 = qb * 8 - 8;
    asm volatile("global_load_dwordx4 %0, %2, %3\n\tglobal_load_dword %1, %4, %5"
                 : "=&v"(q0), "=&v"(qx)
                 : "v"(vo), "s"(ws + WS_QM + (size_t)hd * 65536 + (size_t)t * 1024), "v"((lane & 31) * 8),
                   "s"(ws + WS_QX + (size_t)(hd >> 1) * 16384 + (size_t)t * 256 + (hd & 1) * 4)
                 : "memory");
#pragma unroll
    for (int i = 0; i < 2; ++i) {
      const int lt = wid * 2 + i;
      const int kt = max(kt0 + lt, 0);
      glds16(ws + WS_KM + (size_t)hd * 65536 + (size_t)kt * 1024, vo, lds0 + K2_KM + lt * 1024);
    }
    if (wid >= 4) {
      const int k0 = max(qb * 256 - 256 + (wid - 4) * 128, 0);
      glds16(ws + WS_KX + (size_t)(hd >> 1) * 16384 + (size_t)k0 * 8, vo, lds0 + K2_KX + (wid - 4) * 1024);
    }
    const long vb0 = (long)(hd * 128 + qb * 16 - 16) * 592;
#pragma unroll
    for (int i = 0; i < 3; ++i) {
      const int pc = wid + 8 * i;
      if (pc < 19) {
        long off = vb0 + pc * 1024;
        unsigned v2 = vo;
        if (qb == 0) { off = (long)(hd * 128) * 592 + pc * 1024; v2 = (pc * 1024 + lane * 16 >= 9472) ? vo - 9472 + 65536 : vo + 65536; off -= 65536; }
        glds16(ws + WS_V + off, v2, lds0 + K2_V + pc * 1024);
      }
    }
  }
  STAMP_MARK(1, 2);
  asm volatile("s_waitcnt vmcnt(2)\n\ts_barrier" : "+v"(q0), "+v"(qx)::"memory");
  STAMP_MARK(1, 3);
  if (qb == 0) k2_core<true>(lds, ws, hd, t, wid, lane, q0, qx);
  else k2_core<false>(lds, ws, hd, t, wid, lane, q0, qx);
  STAMP_MARK(1, 4);
  STAMP_END(1);
}

constexpr int T_SLOT = 24576, T_NSLOT = 6, T_LDS = T_SLOT * T_NSLOT;

template <int MODE>
__global__ __launch_bounds__(768) void k_t80(const char* __restrict__ aimg, const char* __restrict__ w, const char* __restrict__ resid,
                                             char* __restrict__ ws, char* __restrict__ out) {
  extern __shared__ __attribute__((aligned(16))) char smem[];
  STAMP_BEGIN
  lds_ptr lds = (lds_ptr)smem;
  const unsigned lds0 = (unsigned)(uintptr_t)lds;
  constexpr int NSB = MODE == 0 ? 1 : 8, KSTEPS = NSB * 18;
  const int tid = threadIdx.x, lane = tid & 63, wid = __builtin_amdgcn_readfirstlane(tid >> 6);
  const int tile = xcd_remap(blockIdx.x, 234);
  const int mt = tile / 9, nt = tile - mt * 9;
  const int m0 = min(mt * 80, SEQ - 80), rb0 = m0 >> 4;
  if (wid >= 10) {
    __builtin_amdgcn_s_setprio(2);
    const int par = wid - 10;
    unsigned va[4], vb[4];
#pragma unroll
    for (int i = 0; i < 4; ++i) {
      const int rho = 8 * i + (lane >> 3);
      const int g_ = ((rho >> 4) & 1) * 2 + ((rho >> 2) & 1), t_ = (rho >> 3) & 1, q_ = rho & 3;
      int La, Lb;
      if (MODE == 0) { La = 36 * par + 18 * (g_ >> 1) + 8 * t_ + 4 * (g_ & 1) + q_; Lb = 288 * par + 18 * (4 * g_ + 2 * t_ + (q_ >> 1)) + 16 + (q_ & 1); }
      else { La = 32 * par + 16 * t_ + 4 * g_ + q_; Lb = 288 * par + 144 * (g_ >> 1) + 128 + 8 * (g_ & 1) + 4 * t_ + q_; }
      const unsigned csw = ((lane & 7) ^ (((rho >> 1) & 3) << 1)) * 16;
      va[i] = La * 1152 + csw; vb[i] = Lb * 1152 + csw;
    }
    const char* wsrc = w + nt * 128;
    const unsigned dst0 = lds0 + par * 4096;
    auto issue = [&](int sb, int st, int slot) {
#pragma unroll
      for (int j = 0; j < 3; ++j) {
        const int kp = st * 3 + j;
        const int base = kp == 8 ? 0 : (MODE == 0 ? 72 * kp : 144 * (kp >> 1) + 64 * (kp & 1));
        const char* src = wsrc + (size_t)(sb * 576 + base) * 1152;
#pragma unroll
        for (int i = 0; i < 4; ++i) glds16(src, kp == 8 ? vb[i] : va[i], dst0 + slot * T_SLOT + j * 8192 + i * 1024);
      }
    };
    if (MODE == 0) {
      issue(0, 0, 0); issue(0, 1, 1);
      wait_bar<12>(); issue(0, 2, 2); wait_bar<12>(); wait_bar<0>();
    } else {
      issue(0, 0, 0); issue(0, 1, 1);
      wait_bar<12>();
      issue(0, 2, 2); issue(1, 0, 3); issue(1, 1, 4);
      wait_bar<36>(); issue(1, 2, 5);
      wait_bar<36>(); issue(2, 0, 0); issue(2, 1, 1);
#pragma unroll 1
      for (int sb = 1; sb < 6; ++sb) {
        const int o = (sb & 1) * 3;
        wait_bar<48>(); issue(sb + 1, 2, 5 - o);
        wait_bar<48>(); issue(sb + 2, 0, o);
        wait_bar<48>(); issue(sb + 2, 1, o + 1);
      }
      wait_bar<48>(); issue(7, 2, 5);
      wait_bar<48>(); wait_bar<36>();
      wait_bar<24>(); wait_bar<12>(); wait_bar<0>();
    }
    return;
  }
  const int mi = wid >> 1, kg = wid & 1;
  const int g = lane >> 4, q = (lane & 15) >> 2, p = lane & 3;
  const char* ap = aimg + (size_t)(rb0 + mi) * KSTEPS * 1024 + kg * 1024 + lane * 16;
  bf16x8 a[9];
#pragma unroll
  for (int i = 0; i < 9; ++i) a[i] = *(const bf16x8*)(ap + i * 2048);
  const int er = tid >> 3, ec = tid & 7;
  u32x4 res;
  {
    const int col = nt * 64 + ec * 8;
    if (MODE == 0) res = *(const u32x4*)(resid + (size_t)(m0 + er) * 1152 + col * 2);
    else res = *(const u32x4*)(resid + (size_t)(rb0 + (er >> 4)) * 18432 + (col >> 5) * 1024 + ((col >> 3) & 3) * 256 + (er & 15) * 16);
  }
  float csp[8];
  const bool do_cs = MODE == 0 && blockIdx.x < 72 && wid < 2;
  if (do_cs) {
#pragma unroll
    for (int i = 0; i < 8; ++i) csp[i] = *(const float*)(ws + WS_CSP + ((size_t)i * 9216 + blockIdx.x * 128 + tid) * 4);
  }
  const int rho0 = 16 * (g >> 1) + 4 * (g & 1) + q;
  lds_ptr wb[4];
#pragma unroll
  for (int n = 0; n < 4; ++n) wb[n] = lds + kg * 4096 + rho0 * 128 + ((n ^ ((rho0 >> 1) & 3)) * 32) + p * 8;
  f32x4 acc[4];
#pragma unroll
  for (int n = 0; n < 4; ++n) acc[n] = f32x4{0.f, 0.f, 0.f, 0.f};
  auto sblock = [&](int sb, bool more) {
    const int o = (sb & 1) * 3 * T_SLOT;
#pragma unroll
    for (int st = 0; st < 3; ++st) {
      lds_bar();
#pragma unroll
      for (int j = 0; j < 3; ++j) {
        bf16x8 wf[4];
#pragma unroll
        for (int n = 0; n < 4; ++n) wf[n] = cat8(lds_tr(wb[n] + o + st * T_SLOT + j * 8192), lds_tr(wb[n] + o + st * T_SLOT + j * 8192 + 1024));
#pragma unroll
        for (int n = 0; n < 4; ++n) acc[n] = MFMA16(wf[n], a[st * 3 + j], acc[n]);
        if (more) a[st * 3 + j] = *(const bf16x8*)(ap + (size_t)(sb + 1) * 18432 + (st * 3 + j) * 2048);
      }
      SB();
    }
  };
#pragma unroll 1
  for (int sb = 0; sb < NSB - 1; ++sb) sblock(sb, true);
  sblock(NSB - 1, false);
  STAMP_MARK(MODE == 0 ? 2 : 4, 3);
  if (do_cs) *(float*)(ws + WS_CS + (size_t)(blockIdx.x * 128 + tid) * 4) = ((csp[0] + csp[1]) + (csp[2] + csp[3])) + ((csp[4] + csp[5]) + (csp[6] + csp[7]));
  lds_bar();
  {
    lds_ptr cw = lds + kg * (80 * 272) + (mi * 16 + (lane & 15)) * 272 + 16 * g;
#pragma unroll
    for (int n = 0; n < 4; ++n) *(LDS_AS f32x4*)(cw + n * 64) = acc[n];
  }
  lds_bar();
  {
    lds_ptr cr = lds + er * 272 + ec * 32;
    f32x4 a0 = *(LDS_AS f32x4*)(cr), a1 = *(LDS_AS f32x4*)(cr + 16);
    f32x4 b0 = *(LDS_AS f32x4*)(cr + 80 * 272), b1 = *(LDS_AS f32x4*)(cr + 80 * 272 + 16);
    a0 += b0; a1 += b1;
    u32x4 o;
    o[0] = cvt_pk(a0[0] + bf_lo(res[0]), a0[1] + bf_hi(res[0]));
    o[1] = cvt_pk(a0[2] + bf_lo(res[1]), a0[3] + bf_hi(res[1]));
    o[2] = cvt_pk(a1[0] + bf_lo(res[2]), a1[1] + bf_hi(res[2]));
    o[3] = cvt_pk(a1[2] + bf_lo(res[3]), a1[3] + bf_hi(res[3]));
    const int row = m0 + er, col = nt * 64 + ec * 8;
    if (MODE == 0) {
      st16(ws + WS_H + (size_t)(row >> 4) * 18432 + (col >> 5) * 1024 + ((col >> 3) & 3) * 256 + (row & 15) * 16, o);
      float s1 = 0.f, s2 = 0.f;
#pragma unroll
      for (int j = 0; j < 4; ++j) { s1 = dot2(o[j], 0x3f803f80u, s1); s2 = dot2(o[j], o[j], s2); }
      s1 = sum8(s1); s2 = sum8(s2);
      if (ec == 0) *(f32x2*)(ws + WS_ST + (size_t)row * 80 + nt * 8) = f32x2{s1, s2};
    } else {
      *(u32x4*)(out + (size_t)row * 1152 + col * 2) = o;
    }
  }
  STAMP_END(MODE == 0 ? 2 : 4);
}

constexpr int K4_SLOT = 18432, K4_ST = K4_SLOT * 8, K4_CS = K4_ST + 2048, K4_PF = K4_CS + 2048, K4_LDS = K4_PF + 1024;

constexpr int k4_frag(int i) { return i < 16 ? ((i >> 2) * 2 + (i & 1) + ((i & 2) ? 9 : 0)) : (i == 16 ? 8 : 17); }
constexpr int k4_foff(int f) { return (f >= 9 ? 9216 + (f - 9) * 32 : f * 32); }
constexpr int K4_T0 = 13 * 18;
constexpr int k4_slab(int G) { return G < K4_T0 ? G / 18 : (G - K4_T0 < 80 ? 13 + ((G - K4_T0) % 20) / 4 : 13 + (G - K4_T0 - 80) / 2); }
constexpr int k4_pos(int G) { return G < K4_T0 ? G % 18 : (G - K4_T0 < 80 ? ((G - K4_T0) / 20) * 4 + ((G - K4_T0) & 3) : 16 + ((G - K4_T0) & 1)); }
constexpr int k4_op(int G) {
  const int s = G / 18, i = G % 18;
  if (s > 12) return 0;
  if (s < 3) {
    const int pos[10] = {0, 2, 4, 6, 7, 9, 11, 13, 15, 16};
    for (int k = 0; k < 10; ++k) if (pos[k] == i) return 1 + (2 * s + 2 + k / 5) * 8 + k % 5;
    return 0;
  }
  if (s == 3) {
    const int pos[8] = {0, 2, 4, 6, 8, 11, 14, 16};
    for (int k = 0; k < 8; ++k) if (pos[k] == i) return k < 5 ? 1 + 8 * 8 + k : 1 + 9 * 8 + (k - 3);
    return 0;
  }
  if (i == 1) return 1 + (s + 5) * 8;
  if (i == 4) return 1 + (s + 5) * 8 + 1;
  if (s + 6 > 17) return 0;
  if (i == 8) return 1 + (s + 6) * 8 + 2;
  if (i == 13) return 1 + (s + 6) * 8 + 3;
  if (i == 16) return 1 + (s + 6) * 8 + 4;
  return 0;
}
constexpr int k4_allow(int G, int need) {
  int n = (need == 0) ? 4 : 0;
  for (int g = 0; g < G; ++g) {
    const int op = k4_op(g);
    if (op == 0) continue;
    const int sl = (op - 1) >> 3, kind = (op - 1) & 7;
    if (sl <= need) n = 0;
    else if (kind != 4) ++n;
  }
  return n;
}
constexpr int k4_need(int G) {
  if (G + 5 >= 324 || G == 0) return 0;
  int mx = 0;
  for (int g = 0; g < G + 5; ++g) mx = k4_slab(g) > mx ? k4_slab(g) : mx;
  return k4_slab(G + 5) > mx ? k4_slab(G + 5) : 0;
}

__global__ __launch_bounds__(512, 2) void k4_gateup(const char* __restrict__ wgate, const char* __restrict__ wup, char* __restrict__ ws,
                                                    const char* __restrict__ wd) {
  extern __shared__ __attribute__((aligned(16))) char smem[];
  STAMP_BEGIN
  lds_ptr lds = (lds_ptr)smem;
  const unsigned lds0 = (unsigned)(uintptr_t)lds;
  const int tid = threadIdx.x, lane = tid & 63, wid = __builtin_amdgcn_readfirstlane(tid >> 6);
  const int xcd = blockIdx.x & 7, idx = blockIdx.x >> 3;
  const int pn = 4 * (xcd >> 2) + (idx & 3), jt = 8 * (xcd & 3) + (idx >> 2);
  const int wm = wid & 3, wn = wid >> 2;
  const char* asrc = ws + WS_H + (size_t)(pn * 16 + 2 * wid) * 18432;
  const unsigned avo = lane * 16, avo2 = lane * 16 + 18432;
  const char* wsrc = (wn ? wup : wgate) + jt * 288;
  unsigned wvo[3];
#pragma unroll
  for (int i = 0; i < 3; ++i) {
    const int pc = i < 2 ? 2 * wm + i : 8;
    const int o = pc * 1024 + lane * 16;
    const int rho = o / 288, u = (o - rho * 288) >> 4;
    const int gk = ((rho >> 4) & 1) * 2 + ((rho >> 2) & 1), tk = (rho >> 3) & 1, qk = rho & 3;
    wvo[i] = (8 * gk + 4 * tk + qk) * 9216 + u * 16;
  }
  const unsigned wdst = lds0 + wn * 9216;
  auto issue_w = [&](int s, int i) {
    const unsigned so = (s & 7) * K4_SLOT;
    const char* wp = wsrc + (size_t)s * 32 * 9216;
    if (i < 2) glds16(wp, wvo[i], wdst + so + wm * 2048 + i * 1024);
    else glds16_if0(wm, wp, wvo[2], wdst + so + 8192);
  };
  bf16x8 A[6][2];
  auto issue_a = [&](int s, bf16x8& dst, int m) {
    asm volatile("global_load_dwordx4 %0, %1, %2" : "=v"(dst) : "v"(m ? avo2 : avo), "s"(asrc + s * 1024) : "memory");
  };
  f32x4 sp0, sp1, sp2, sp3;
  f32x2 sp4;
  asm volatile("global_load_dwordx4 %0, %5, %6\n\tglobal_load_dwordx4 %1, %5, %6 offset:16\n\tglobal_load_dwordx4 %2, %5, %6 offset:32\n\t"
               "global_load_dwordx4 %3, %5, %6 offset:48\n\tglobal_load_dwordx2 %4, %5, %6 offset:64"
               : "=&v"(sp0), "=&v"(sp1), "=&v"(sp2), "=&v"(sp3), "=&v"(sp4)
               : "v"((lane & 31) * 80), "s"(ws + WS_ST + (size_t)(pn * 256 + wid * 32) * 80) : "memory");
  if (wm == 1 && lane < 36) glds16(ws + WS_CS + (size_t)(wn * FF + jt * 144) * 4, lane * 16, lds0 + K4_CS + wn * 576);
  for_seq<2>([&](auto S) { issue_w(S, 0); issue_w(S, 1); issue_w(S, 2); issue_a(S, A[S][0], 0); issue_a(S, A[S][1], 1); });
  {
    asm volatile("s_waitcnt vmcnt(8)" : "+v"(sp0), "+v"(sp1), "+v"(sp2), "+v"(sp3), "+v"(sp4)::"memory");
    const float s1 = sp0[0] + sp0[2] + sp1[0] + sp1[2] + sp2[0] + sp2[2] + sp3[0] + sp3[2] + sp4[0];
    const float s2 = sp0[1] + sp0[3] + sp1[1] + sp1[3] + sp2[1] + sp2[3] + sp3[1] + sp3[3] + sp4[1];
    const float mean = s1 * (1.f / 576.f);
    const float var = fmaxf(s2 * (1.f / 576.f) - mean * mean, 0.f);
    *(LDS_AS f32x2*)(lds + K4_ST + (wid * 32 + (lane & 31)) * 8) = f32x2{-mean, __builtin_amdgcn_rsqf(var + LN_EPS)};
    SB();
  }
  const int g = lane >> 4, q = (lane & 15) >> 2, p = lane & 3, r = lane & 15;
  lds_ptr wb = lds + (16 * (g >> 1) + 4 * (g & 1) + q) * 288 + p * 8;
  lds_ptr wbs[3] = {wb, wb + 3 * K4_SLOT, wb + 6 * K4_SLOT};
  asm volatile("" : "+v"(wbs[1]), "+v"(wbs[2]));
  lds_ptr csb = lds + K4_CS + 16 * g;
  asm volatile("" : "+v"(csb));
  f32x4 acc[2][18];
  asm volatile("s_waitcnt vmcnt(%2)\n\ts_barrier" : "+v"(A[0][0]), "+v"(A[0][1]) : "n"(k4_allow(0, 0)) : "memory");
  STAMP_MARK(3, 2);
  STAMP_CYC0
  s16x4 Bl[6], Bh[6];
  for_seq<5>([&](auto I) { Bl[I] = lds_tr(wb + k4_foff(k4_frag(I))); Bh[I] = lds_tr(wb + k4_foff(k4_frag(I)) + 2304); });
  SB();
  const float L2E = 1.4426950408889634f;
  char* hb = ws + WS_HID + (size_t)(pn * 16 + wid * 2) * (144 * 1024) + (size_t)(jt >> 2) * 18432;
  f32x2 st[2];
  float c1[2];
  auto glu = [&](float ag, float au, float cg, float cu, int m) -> float {
    const float tg = fmaf(st[m][0], cg, ag), tu = fmaf(st[m][0], cu, au);
    const float e = __builtin_amdgcn_exp2f(tg * c1[m]);
    return tu * st[m][1] * __builtin_amdgcn_rcpf(1.f + e);
  };
  f32x4 cg[2], cu[2];
  u32x4 ov;
  auto slice = [&](auto QQ, auto KK) {
    constexpr int Q = QQ, k = KK, m = k >> 2, d = k & 3, h = d >> 1, e = (d & 1) * 2;
    const f32x4 ag = acc[m][2 * Q + h], au = acc[m][9 + 2 * Q + h];
    ov[d] = cvt_pk(glu(ag[e], au[e], cg[h][e], cu[h][e], m), glu(ag[e + 1], au[e + 1], cg[h][e + 1], cu[h][e + 1], m));
    if constexpr (d == 3) st16(hb + (size_t)m * (144 * 1024) + (4 * (jt & 3) + Q) * 1024 + lane * 16, ov);
    else asm volatile("" : "+v"(ov));
  };
  for_seq<18>([&](auto GH) { for_seq<18>([&](auto GL) {
    constexpr int G = GH * 18 + GL, sl = k4_slab(G), f = k4_frag(k4_pos(G)), rb = G % 6, nb = (G + 5) % 6, NG = G + 5;
    const bf16x8 b = cat8(Bl[rb], Bh[rb]);
    constexpr int need = k4_need(G);
    if constexpr (need != 0) {
      asm volatile("s_waitcnt vmcnt(%2)\n\ts_barrier" : "+v"(A[need % 6][0]), "+v"(A[need % 6][1]) : "n"(k4_allow(G, need)) : "memory");
      SB();
    }
    if constexpr (sl == 0) mfma16_zero(acc[0][f], b, A[0][0]); else mfma16_acc(acc[0][f], b, A[sl % 6][0]);
    SB();
    if constexpr (NG < 324) { Bl[nb] = lds_tr(wbs[(k4_slab(NG) & 7) / 3] + ((k4_slab(NG) & 7) % 3) * K4_SLOT + k4_foff(k4_frag(k4_pos(NG)))); SB(); }
    if constexpr (sl == 0) mfma16_zero(acc[1][f], b, A[0][1]); else mfma16_acc(acc[1][f], b, A[sl % 6][1]);
    SB();
    if constexpr (NG < 324) { Bh[nb] = lds_tr(wbs[(k4_slab(NG) & 7) / 3] + ((k4_slab(NG) & 7) % 3) * K4_SLOT + k4_foff(k4_frag(k4_pos(NG))) + 2304); SB(); }
    constexpr int op = k4_op(G);
    if constexpr (op != 0) {
      constexpr int os = (op - 1) >> 3, kind = (op - 1) & 7;
      if constexpr (kind < 2) issue_a(os, A[os % 6][kind], kind);
      else issue_w(os, kind - 2);
      SB();
    }
    if constexpr (G == K4_T0 + 12) {
      touch_if0((int)(blockIdx.x / 216) + wid / 3, wd + (size_t)(blockIdx.x * 3 + wid) * 8192, avo * 8, lds0 + K4_PF + (wid & 3) * 256);
      SB();
    }
    if constexpr (G == K4_T0) {
      STAMP_MARK(3, 3);
      STAMP_CYC(3, 4);
#pragma unroll
      for (int m = 0; m < 2; ++m) { st[m] = *(LDS_AS f32x2*)(lds + K4_ST + (wid * 32 + m * 16 + r) * 8); c1[m] = -L2E * st[m][1]; }
      SB();
    }
    if constexpr (G >= K4_T0) {
      constexpr int t = G - K4_T0, u = t % 20;
      if constexpr (t < 80 && u == 18) {
#pragma unroll
        for (int h = 0; h < 2; ++h) {
          cg[h] = *(LDS_AS f32x4*)(csb + (2 * (t / 20) + h) * 64);
          cu[h] = *(LDS_AS f32x4*)(csb + 576 + (2 * (t / 20) + h) * 64);
        }
        SB();
      }
      if constexpr (t >= 20 && t < 80 && u >= 1 && u < 17 && (u & 1)) { slice(std::integral_constant<int, t / 20 - 1>{}, std::integral_constant<int, u / 2>{}); SB(); }
      if constexpr (t >= 81 && t < 89) { slice(std::integral_constant<int, 3>{}, std::integral_constant<int, t - 81>{}); SB(); }
    }
  }); });
  {
    asm volatile("s_nop 7\n\ts_nop 1" : "+v"(acc[0][8]), "+v"(acc[1][8]), "+v"(acc[0][17]), "+v"(acc[1][17]));
    const f32x4 cg8 = *(LDS_AS f32x4*)(csb + 512);
    const f32x4 cu8 = *(LDS_AS f32x4*)(csb + 576 + 512);
#pragma unroll
    for (int m = 0; m < 2; ++m) {
      const f32x4 ag = acc[m][8], au = acc[m][17];
      u32x2 o = {cvt_pk(glu(ag[0], au[0], cg8[0], cu8[0], m), glu(ag[1], au[1], cg8[1], cu8[1], m)),
                 cvt_pk(glu(ag[2], au[2], cg8[2], cu8[2], m), glu(ag[3], au[3], cg8[3], cu8[3], m))};
      st8(hb + (size_t)m * (144 * 1024) + (16 + ((jt >> 1) & 1)) * 1024 + (2 * (jt & 1) + (g >> 1)) * 256 + r * 16 + (g & 1) * 8, o);
    }
  }
  STAMP_MARK(3, 5);
  STAMP_END(3);
}

extern "C" void kernel_launch(void* const* d_in, const int* in_sizes, int n_in, void* d_out, int out_size, void* d_ws, size_t ws_size,
                              hipStream_t stream) {
  const char* x = (const char*)d_in[0];
  const char* wqkv = (const char*)d_in[1];
  const char* wo = (const char*)d_in[2];
  const char* wg = (const char*)d_in[4];
  const char* wu = (const char*)d_in[5];
  const char* wd = (const char*)d_in[6];
  char* ws = (char*)d_ws;
  (void)hipFuncSetAttribute((const void*)k1_ln_qkv, hipFuncAttributeMaxDynamicSharedMemorySize, K1_LDS);
  (void)hipFuncSetAttribute((const void*)k2_attn, hipFuncAttributeMaxDynamicSharedMemorySize, K2_LDS);
  (void)hipFuncSetAttribute((const void*)k_t80<0>, hipFuncAttributeMaxDynamicSharedMemorySize, T_LDS);
  (void)hipFuncSetAttribute((const void*)k_t80<1>, hipFuncAttributeMaxDynamicSharedMemorySize, T_LDS);
  (void)hipFuncSetAttribute((const void*)k4_gateup, hipFuncAttributeMaxDynamicSharedMemorySize, K4_LDS);
  k1_ln_qkv<<<234, 1024, K1_LDS, stream>>>(x, wqkv, ws);
  k2_attn<<<256, 768, K2_LDS, stream>>>(ws, wg, wu, wo);
  k_t80<0><<<234, 768, T_LDS, stream>>>(ws + WS_O, wo, x, ws, nullptr);
  k4_gateup<<<256, 512, K4_LDS, stream>>>(wg, wu, ws, wd);
  k_t80<1><<<234, 768, T_LDS, stream>>>(ws + WS_HID, wd, ws + WS_H, ws, (char*)d_out);
}
```

```cpp
#include <hip/hip_runtime.h>
#include <hip/hip_bf16.h>
#include <stdint.h>
#include <utility>
#include <type_traits>

typedef __attribute__((ext_vector_type(8))) short bf16x8;
typedef __attribute__((ext_vector_type(4))) short s16x4;
typedef __attribute__((ext_vector_type(4))) float f32x4;
typedef __attribute__((ext_vector_type(16))) float f32x16;
typedef __attribute__((ext_vector_type(4))) unsigned u32x4;
typedef __attribute__((ext_vector_type(2))) unsigned u32x2;
typedef __attribute__((ext_vector_type(2))) __bf16 bf16x2_t;
typedef __attribute__((ext_vector_type(2))) float f32x2;
#define LDS_AS __attribute__((address_space(3)))
typedef LDS_AS char* lds_ptr;
#define DI __device__ __forceinline__

constexpr int SEQ = 2048, DM = 576, NH = 32, FF = 4608;
constexpr float LN_EPS = 1e-5f;
constexpr float QSCALE = 0.23570226039551584f * 1.4426950408889634f;

constexpr size_t WS_QM = 0;
constexpr size_t WS_KM = WS_QM + (size_t)32 * 64 * 1024;
constexpr size_t WS_QX = WS_KM + (size_t)32 * 64 * 1024;
constexpr size_t WS_KX = WS_QX + (size_t)16 * 2048 * 8;
constexpr size_t WS_V = WS_KX + (size_t)16 * 2048 * 8;
constexpr size_t WS_O = WS_V + (size_t)32 * 128 * 592 + 1024;
constexpr size_t WS_H = WS_O + (size_t)128 * 18 * 1024;
constexpr size_t WS_ST = WS_H + (size_t)128 * 18 * 1024;
constexpr size_t WS_CSP = WS_ST + (size_t)2048 * 80;
constexpr size_t WS_CS = WS_CSP + (size_t)8 * 9216 * 4;
constexpr size_t WS_HID = WS_CS + (size_t)9216 * 4;
constexpr size_t WS_END = WS_HID + (size_t)128 * 144 * 1024;
static_assert(WS_O % 1024 == 0 && WS_H % 1024 == 0 && WS_ST % 256 == 0 && WS_CSP % 256 == 0 && WS_HID % 256 == 0, "align");

#define STAMP_BEGIN
#define STAMP_MARK(k, i)
#define STAMP_MARKW(k, i, T)
#define STAMP_ENDW(k, T)
#define STAMP_CYC0
#define STAMP_CYC(k, i)
#define STAMP_END(k)

DI unsigned cvt_pk(float lo, float hi) {
  f32x2 v = {lo, hi};
  bf16x2_t r = __builtin_convertvector(v, bf16x2_t);
  return __builtin_bit_cast(unsigned, r);
}
DI float bf_lo(unsigned u) { return __builtin_bit_cast(float, u << 16); }
DI float bf_hi(unsigned u) { return __builtin_bit_cast(float, u & 0xffff0000u); }
DI float dot2(unsigned a, unsigned b, float c) {
  return __builtin_amdgcn_fdot2_f32_bf16(__builtin_bit_cast(bf16x2_t, a), __builtin_bit_cast(bf16x2_t, b), c, false);
}
template <int CTRL> DI float dpp_add(float v) {
  int t = __builtin_amdgcn_update_dpp(0, __builtin_bit_cast(int, v), CTRL, 0xf, 0xf, true);
  return v + __builtin_bit_cast(float, t);
}
DI float sum8(float v) {
  v = dpp_add<0xB1>(v);
  v = dpp_add<0x4E>(v);
  v = dpp_add<0x141>(v);
  return v;
}
DI void glds16(const void* sbase, unsigned voff, unsigned lds_dst) {
  asm volatile("s_mov_b32 m0, %2\n\ts_nop 3\n\tglobal_load_lds_dwordx4 %0, %1" ::"v"(voff), "s"(sbase), "s"(lds_dst) : "memory");
}
#define ST_AUX " sc1"
DI void st16(void* p, u32x4 v) { asm volatile("global_store_dwordx4 %0, %1, off" ST_AUX "\n\ts_nop 1" ::"v"(p), "v"(v) : "memory"); }
DI void st8(void* p, u32x2 v) { *(u32x2*)p = v; }
DI void st4(void* p, unsigned v) { *(unsigned*)p = v; }
DI void glds16_if0(int c, const void* sbase, unsigned voff, unsigned lds_dst) {
  asm volatile("s_cmp_lg_u32 %3, 0\n\ts_cbranch_scc1 1f\n\ts_mov_b32 m0, %2\n\ts_nop 1\n\tglobal_load_lds_dwordx4 %0, %1\n1:"
               ::"v"(voff), "s"(sbase), "s"(lds_dst), "s"(c) : "memory", "scc");
}
DI void touch_if0(int c, const void* sbase, unsigned voff, unsigned lds_dst) {
  asm volatile("s_cmp_lg_u32 %3, 0\n\ts_cbranch_scc1 1f\n\ts_mov_b32 m0, %2\n\ts_nop 1\n\tglobal_load_lds_dword %0, %1\n1:"
               ::"v"(voff), "s"(sbase), "s"(lds_dst), "s"(c) : "memory", "scc");
}
DI void touch_lines(const char* base, unsigned voff, unsigned lds_dummy) {
  asm volatile("s_mov_b32 m0, %2\n\ts_nop 3\n\tglobal_load_lds_dword %0, %1" ::"v"(voff), "s"(base), "s"(lds_dummy) : "memory");
}
#define CODE_TOUCH(SYM, BYTES, LDS_DUMMY)                                                                          \
  do {                                                                                                             \
    const char* cp_;                                                                                               \
    asm volatile("s_getpc_b64 s[20:21]\n\ts_add_u32 s20, s20, " SYM "@rel32@lo+4\n\ts_addc_u32 s21, s21, " SYM     \
                 "@rel32@hi+12\n\ts_mov_b64 %0, s[20:21]" : "=s"(cp_)::"s20", "s21", "scc");                      \
    constexpr int nl_ = (BYTES) / 128, per_ = (nl_ + 28) / 29;                                                      \
    const int ln_ = (blockIdx.x >> 3) * per_ + lane;                                                               \
    if (lane < per_ && ln_ < nl_) touch_lines(cp_, ln_ * 128, LDS_DUMMY);                                          \
  } while (0)
#define SYM_K1 "_Z9k1_ln_qkvPKcS0_Pc"
#define SYM_K2 "_Z7k2_attnPcPKcS1_S1_"
#define SYM_K3 "_Z5k_t80ILi0EEvPKcS1_S1_PcS2_"
#define SYM_K4 "_Z9k4_gateupPKcS0_PcS0_"
#define SYM_K5 "_Z5k_t80ILi1EEvPKcS1_S1_PcS2_"
constexpr int LEN_K1 = 24000, LEN_K2 = 11200, LEN_K3 = 3700, LEN_K4 = 18200, LEN_K5 = 7600;
template <int N> DI void wait_bar() { asm volatile("s_waitcnt vmcnt(%0) lgkmcnt(0)\n\ts_barrier" ::"n"(N) : "memory"); }
template <int N> DI void wait_vm() { asm volatile("s_waitcnt vmcnt(%0)" ::"n"(N) : "memory"); }
DI void raw_bar() { asm volatile("s_barrier" ::: "memory"); }
DI void lds_bar() { asm volatile("s_waitcnt lgkmcnt(0)\n\ts_barrier" ::: "memory"); }
DI bf16x8 lds_r128(lds_ptr p) { return *(LDS_AS const bf16x8*)p; }
typedef short v4i16_t __attribute__((ext_vector_type(4)));
DI s16x4 lds_tr(lds_ptr p) { return __builtin_amdgcn_ds_read_tr16_b64_v4i16((LDS_AS v4i16_t*)p); }
DI bf16x8 cat8(s16x4 a, s16x4 b) { return __builtin_shufflevector(a, b, 0, 1, 2, 3, 4, 5, 6, 7); }
#define SB() __builtin_amdgcn_sched_barrier(0)
#define MFMA16(a, b, c) __builtin_amdgcn_mfma_f32_16x16x32_bf16(a, b, c, 0, 0, 0)
#define MFMA32(a, b, c) __builtin_amdgcn_mfma_f32_32x32x16_bf16(a, b, c, 0, 0, 0)
DI void mfma16_acc(f32x4& c, bf16x8 a, bf16x8 b) { asm volatile("v_mfma_f32_16x16x32_bf16 %0, %1, %2, %0" : "+v"(c) : "v"(a), "v"(b)); }
DI void mfma16_zero(f32x4& c, bf16x8 a, bf16x8 b) { asm volatile("v_mfma_f32_16x16x32_bf16 %0, %1, %2, 0" : "=&v"(c) : "v"(a), "v"(b)); }
template <class F, int... I> DI void for_seq_impl(F&& f, std::integer_sequence<int, I...>) { (f(std::integral_constant<int, I>{}), ...); }
template <int N, class F> DI void for_seq(F&& f) { for_seq_impl(f, std::make_integer_sequence<int, N>{}); }
DI int xcd_remap(int b, int nwg) {
  int q = nwg >> 3, r = nwg & 7, x = b & 7, o = b >> 3;
  return (x < r ? x * (q + 1) : r * (q + 1) + (x - r) * q) + o;
}

constexpr int K1_X = 0, K1_W = 40960, K1_SLOT = 12288, K1_ST = K1_W + K1_SLOT * 9, K1_CS = K1_W, K1_PF = K1_ST + 1024, K1_LDS = K1_PF + 1024;
constexpr int cmin(int a, int b) { return a < b ? a : b; }
constexpr int k1_nw(int kk) { return cmin(18, cmin(3 + 2 * kk, kk + 7)); }
constexpr int k1_nx(int kk) { return cmin(18, cmin(3 + 2 * kk, kk + 5)); }

template <bool ISV, bool HI>
DI void k1_consumer(lds_ptr lds, int cb, int lane, char* ws, int ct, int m0) {
  const int g = lane >> 4, q = (lane & 15) >> 2, p = lane & 3, r = lane & 15;
  constexpr int NM = HI ? 2 : 3, MB = HI ? 3 : 0;
  lds_ptr a0 = lds + K1_X + MB * 2048 + (r >> 3) * 1024 + (r & 7) * 128 + ((g ^ (r >> 1)) * 16);
  lds_ptr a1 = lds + K1_X + MB * 2048 + (r >> 3) * 1024 + (r & 7) * 128 + (((4 + g) ^ (r >> 1)) * 16);
  const int rho0 = 16 * (g >> 1) + 4 * (g & 1) + q;
  lds_ptr wb = lds + K1_W + rho0 * 128 + ((cb ^ ((rho0 >> 1) & 3)) * 32) + p * 8;
  lds_ptr wb5 = wb + 5 * K1_SLOT;
  asm volatile("" : "+v"(wb), "+v"(wb5));
  const u32x4 onesu = {0x3f803f80u, 0x3f803f80u, 0x3f803f80u, 0x3f803f80u};
  const bf16x8 ones = __builtin_bit_cast(bf16x8, onesu);
  f32x4 acc[3][3];
#pragma unroll
  for (int m = 0; m < 3; ++m)
#pragma unroll
    for (int j = 0; j < 3; ++j) acc[m][j] = f32x4{0.f, 0.f, 0.f, 0.f};
  bf16x8 fA[2][3];
  s16x4 wl[2][3], wh[2][3];
  auto rd1 = [&](auto KK, auto II) {
    constexpr int kk = KK, i = II, bf = kk & 1, sl = kk % 9;
    if constexpr (i >= 2 && i <= 4) {
      if constexpr (i - 2 < NM) fA[bf][i - 2] = lds_r128(((kk & 1) ? a1 : a0) + ((kk >> 1) & 3) * 10240 + (i - 2) * 2048);
    } else {
      constexpr int j = i < 2 ? 0 : (i - 3) / 2, hh = i < 2 ? i : (i - 3) % 2;
      lds_ptr bp = (sl >= 5 ? wb5 + (sl - 5) * K1_SLOT : wb + sl * K1_SLOT) + j * 4096 + hh * 1024;
      if constexpr (hh == 0) wl[bf][j] = lds_tr(bp); else wh[bf][j] = lds_tr(bp);
    }
  };
  lds_bar();
  STAMP_MARKW(0, 2, 512);
  for_seq<9>([&](auto I) { rd1(std::integral_constant<int, 0>{}, I); });
  SB();
  for_seq<18>([&](auto KK) {
    constexpr int kk = KK, bf = kk & 1;
    if constexpr (kk < 17) { raw_bar(); SB(); }
    for_seq<9>([&](auto II) {
      constexpr int i = II, j = i / 3, m = i % 3;
      const bf16x8 wv = cat8(wl[bf][j], wh[bf][j]);
      const bf16x8 av = (m < NM) ? fA[bf][m] : ones;
      acc[m][j] = ISV ? MFMA16(av, wv, acc[m][j]) : MFMA16(wv, av, acc[m][j]);
      SB();
      if constexpr (kk < 17) { rd1(std::integral_constant<int, kk + 1>{}, II); SB(); }
    });
  });
  STAMP_MARKW(0, 4, 512);
  lds_ptr cx = lds + K1_CS + cb * 3072 + lane * 16;
  if (HI) {
#pragma unroll
    for (int j = 0; j < 3; ++j) *(LDS_AS f32x4*)(cx + j * 1024) = acc[2][j];
  }
  lds_bar();
  STAMP_MARKW(0, 5, 512);
  f32x4 cs[3];
#pragma unroll
  for (int j = 0; j < 3; ++j) cs[j] = HI ? acc[2][j] : *(LDS_AS f32x4*)(cx + j * 1024);
  if (ISV) {
    f32x4 st[NM][2];
#pragma unroll
    for (int m = 0; m < NM; ++m) {
      st[m][0] = *(LDS_AS f32x4*)(lds + K1_ST + ((MB + m) * 16 + 4 * g) * 8);
      st[m][1] = *(LDS_AS f32x4*)(lds + K1_ST + ((MB + m) * 16 + 4 * g) * 8 + 16);
    }
#pragma unroll
    for (int j = 0; j < 3; ++j) {
      const int c = (ct - 6) * 192 + j * 64 + cb * 16 + r;
      const int head = c / 18, d = c - head * 18;
      char* vb = ws + WS_V + (size_t)(head * 128 + (m0 >> 4) + MB) * 592;
      char* vp = vb + (g & 1) * 288 + d * 16 + (g >> 1) * 8;
      const float c0 = cs[j][0];
#pragma unroll
      for (int m = 0; m < NM; ++m) {
        u32x2 o = {cvt_pk(fmaf(st[m][0][0], c0, acc[m][j][0]) * st[m][0][1], fmaf(st[m][0][2], c0, acc[m][j][1]) * st[m][0][3]),
                   cvt_pk(fmaf(st[m][1][0], c0, acc[m][j][2]) * st[m][1][1], fmaf(st[m][1][2], c0, acc[m][j][3]) * st[m][1][3])};
        st8(vp + m * 592, o);
      }
      if (d == 17 && g < NM) *(u32x4*)(vb + g * 592 + 576) = onesu;
    }
  } else {
    const bool isq = ct < 3;
    const float sc = isq ? QSCALE : 1.f;
    f32x2 st[NM];
#pragma unroll
    for (int m = 0; m < NM; ++m) { st[m] = *(LDS_AS f32x2*)(lds + K1_ST + ((MB + m) * 16 + r) * 8); st[m][1] *= sc; }
#pragma unroll
    for (int j = 0; j < 3; ++j) {
      const int c = (isq ? ct : ct - 3) * 192 + j * 64 + cb * 16 + 4 * g;
      const int pair = c / 36, qi = (c - pair * 36) >> 2;
      const int head = 2 * pair + (qi > 4 ? 1 : 0), a = qi > 4 ? qi - 5 : qi;
      char* mainp = ws + (isq ? WS_QM : WS_KM) + (size_t)head * 65536 + (a >> 1) * 512 + (a & 1) * 8;
      char* xp = ws + (isq ? WS_QX : WS_KX) + (size_t)pair * 16384;
#pragma unroll
      for (int m = 0; m < NM; ++m) {
        const int rw = m0 + (MB + m) * 16 + r;
        u32x2 o = {cvt_pk(fmaf(st[m][0], cs[j][0], acc[m][j][0]) * st[m][1], fmaf(st[m][0], cs[j][1], acc[m][j][1]) * st[m][1]),
                   cvt_pk(fmaf(st[m][0], cs[j][2], acc[m][j][2]) * st[m][1], fmaf(st[m][0], cs[j][3], acc[m][j][3]) * st[m][1])};
        char* dst = (qi == 4) ? xp + rw * 8 : mainp + (rw >> 5) * 1024 + (rw & 31) * 16;
        st8(dst, o);
      }
    }
  }
}

__global__ __launch_bounds__(1024) void k1_ln_qkv(const char* __restrict__ x, const char* __restrict__ wqkv, char* __restrict__ ws) {
  extern __shared__ __attribute__((aligned(16))) char smem[];
  STAMP_BEGIN
  lds_ptr lds = (lds_ptr)smem;
  const unsigned lds0 = (unsigned)(uintptr_t)lds;
  const int tid = threadIdx.x, lane = tid & 63, wid = __builtin_amdgcn_readfirstlane(tid >> 6);
  const int tile = xcd_remap(blockIdx.x, 234);
  const int cgp = tile / 78, rem = tile - cgp * 78;
  const int mt = rem / 3, ct = cgp * 3 + (rem - mt * 3);
  const int m0 = min(mt * 80, SEQ - 80);
  if (wid < 3) {
    unsigned wv[4];
#pragma unroll
    for (int i = 0; i < 4; ++i) {
      const int rho = 8 * i + (lane >> 3);
      const int gk = ((rho >> 4) & 1) * 2 + ((rho >> 2) & 1), tk = (rho >> 3) & 1, qk = rho & 3;
      wv[i] = (8 * gk + 4 * tk + qk) * 3456 + (((lane & 7) ^ (((rho >> 1) & 3) << 1)) * 16);
    }
    const char* wsrc = wqkv + ct * 384 + wid * 128;
    const unsigned wdst = lds0 + K1_W + wid * 4096;
    for_seq<18>([&](auto KK) {
      constexpr int kk = KK;
#pragma unroll
      for (int n = (kk == 0 ? 0 : k1_nw(kk - 1)); n < k1_nw(kk); ++n)
#pragma unroll
        for (int i = 0; i < 4; ++i) glds16(wsrc + (size_t)n * 32 * 3456, wv[i], wdst + (n % 9) * K1_SLOT + i * 1024);
      wait_bar<(k1_nw(kk) - kk - 1) * 4>();
    });
    return;
  }
  if (wid == 3) {
    const unsigned v0 = (lane >> 3) * 1152 + (((lane & 7) ^ (lane >> 4)) * 16);
    const unsigned v1 = (lane >> 3) * 1152 + (((lane & 7) ^ (4 + (lane >> 4))) * 16);
    const char* xs = x + (size_t)m0 * 1152;
    for_seq<18>([&](auto KK) {
      constexpr int kk = KK;
#pragma unroll
      for (int h = (kk == 0 ? 0 : k1_nx(kk - 1)); h < k1_nx(kk); ++h)
#pragma unroll
        for (int i = 0; i < 5; ++i) {
          const int pc = (h & 1) * 5 + i;
          glds16(xs + pc * 9216 + (h >> 1) * 128, (pc & 1) ? v1 : v0, lds0 + K1_X + ((h >> 1) & 3) * 10240 + pc * 1024);
        }
      wait_bar<(k1_nx(kk) - 2 * (kk >> 1) - 2) * 5>();
    });
    return;
  }
  if (wid < 8) {
    const int j = wid - 4;
    if (j == 0) CODE_TOUCH(SYM_K1, LEN_K1, lds0 + K1_PF);
    if (j == 1) CODE_TOUCH(SYM_K2, LEN_K2, lds0 + K1_PF + 256);
    if (j == 2) CODE_TOUCH(SYM_K3, LEN_K3, lds0 + K1_PF + 512);
    {
      const int ln = blockIdx.x * 148 + j * 37 + lane;
      if (lane < 37) {
        if (ln < 15552) touch_lines(wqkv, ln * 128, lds0 + K1_PF + j * 256);
        else if (ln < 33984) touch_lines(x, (ln - 15552) * 128, lds0 + K1_PF + j * 256);
      }
    }
    lds_ptr p0 = lds + K1_X + j * 1024 + lane * 16, p2 = lds + K1_X + min(j + 8, 9) * 1024 + lane * 16;
    float s1[3] = {0.f, 0.f, 0.f}, s2[3] = {0.f, 0.f, 0.f};
#pragma unroll 1
    for (int s = 0; s < 9; ++s) {
      raw_bar();
      const int so = (s & 3) * 10240;
      u32x4 v[3] = {*(LDS_AS u32x4*)(p0 + so), *(LDS_AS u32x4*)(p0 + so + 4096), *(LDS_AS u32x4*)(p2 + so)};
      SB();
      raw_bar();
      SB();
#pragma unroll
      for (int i = 0; i < 3; ++i)
#pragma unroll
        for (int e = 0; e < 4; ++e) { s1[i] = dot2(v[i][e], 0x3f803f80u, s1[i]); s2[i] = dot2(v[i][e], v[i][e], s2[i]); }
      asm volatile("" : "+v"(s1[0]), "+v"(s1[1]), "+v"(s1[2]), "+v"(s2[0]), "+v"(s2[1]), "+v"(s2[2]));
    }
#pragma unroll
    for (int i = 0; i < 3; ++i) {
      const float a = sum8(s1[i]), b = sum8(s2[i]);
      const float mean = a * (1.f / 576.f);
      const float var = fmaxf(b * (1.f / 576.f) - mean * mean, 0.f);
      const int pc = i < 2 ? j + 4 * i : min(j + 8, 9);
      if ((lane & 7) == 0) *(LDS_AS f32x2*)(lds + K1_ST + (pc * 8 + (lane >> 3)) * 8) = f32x2{-mean, __builtin_amdgcn_rsqf(var + LN_EPS)};
    }
    lds_bar();
    return;
  }
  if (ct >= 6) {
    if (wid < 12) k1_consumer<true, false>(lds, wid - 8, lane, ws, ct, m0);
    else k1_consumer<true, true>(lds, wid - 12, lane, ws, ct, m0);
  } else {
    if (wid < 12) k1_consumer<false, false>(lds, wid - 8, lane, ws, ct, m0);
    else k1_consumer<false, true>(lds, wid - 12, lane, ws, ct, m0);
  }
  STAMP_ENDW(0, 512);
}

constexpr int K2_KM = 0, K2_KX = 16384, K2_V = K2_KX + 4096, K2_CS = K2_V + 19 * 1024, K2_LDS = K2_CS + 4 * 2048 + 1024;

template <bool FIRST>
DI void k2_core(lds_ptr lds, char* ws, int hd, int t, int wid, int lane, bf16x8 q0, unsigned qx) {
  const int hi = lane >> 5, l31 = lane & 31;
  u32x4 q1u = {hi ? 0u : qx, 0u, 0u, 0u};
  bf16x8 q1 = __builtin_bit_cast(bf16x8, q1u);
  f32x16 S[9];
  lds_ptr kmp = lds + K2_KM + wid * 1024 + lane * 16;
  lds_ptr kxp = lds + K2_KX + (wid * 32 + l31) * 8 + (hd & 1) * 4;
  const float NEG = -1e30f;
  const int dq = l31 - 4 * hi;
  float mx = NEG;
#pragma unroll
  for (int ii = 0; ii < 9; ++ii) {
    const int i = ii == 0 ? 8 : ii - 1;
    bf16x8 k0 = lds_r128(kmp + i * 1024);
    u32x4 k1u = {*(LDS_AS unsigned*)(kxp + i * 256), 0u, 0u, 0u};
    f32x16 z = {};
    S[i] = MFMA32(k0, q0, z);
    S[i] = MFMA32(__builtin_bit_cast(bf16x8, k1u), q1, S[i]);
    if (FIRST && i < 8 && wid + i < 8) {
#pragma unroll
      for (int r = 0; r < 16; ++r) S[i][r] = NEG;
    }
    if (i == 0) {
#pragma unroll
      for (int r = 0; r < 16; ++r) S[0][r] = (((r & 3) + 8 * (r >> 2)) <= dq) ? S[8][r] : S[0][r];
    }
    if (i < 8) {
#pragma unroll
      for (int r = 0; r < 16; r += 2) mx = fmaxf(fmaxf(mx, S[i][r]), S[i][r + 1]);
    }
  }
  {
    auto rr = __builtin_amdgcn_permlane32_swap(__float_as_uint(mx), __float_as_uint(mx), false, false);
    mx = fmaxf(__uint_as_float(rr[0]), __uint_as_float(rr[1]));
  }
  wait_bar<0>();
  lds_ptr vp = lds + K2_V + wid * 2 * 592 + (l31 < 18 ? hi * 288 + l31 * 16 : 576);
  f32x16 O = {};
#pragma unroll
  for (int i = 0; i < 8; ++i) {
    u32x4 P[2];
#pragma unroll
    for (int s = 0; s < 2; ++s)
#pragma unroll
      for (int j = 0; j < 4; ++j)
        P[s][j] = cvt_pk(__builtin_amdgcn_exp2f(S[i][8 * s + 2 * j] - mx), __builtin_amdgcn_exp2f(S[i][8 * s + 2 * j + 1] - mx));
    if (i == 0) {
#pragma unroll
      for (int s = 0; s < 2; ++s) {
        u32x4 P8, P0;
#pragma unroll
        for (int j = 0; j < 4; ++j) {
          const int r0 = 8 * s + 2 * j, r1 = r0 + 1;
          const unsigned mk = ((((r0 & 3) + 8 * (r0 >> 2)) <= dq) ? 0xffffu : 0u) | ((((r1 & 3) + 8 * (r1 >> 2)) <= dq) ? 0xffff0000u : 0u);
          P8[j] = P[s][j] & mk;
          P0[j] = P[s][j] & ~mk;
        }
        O = MFMA32(lds_r128(vp + s * 592), __builtin_bit_cast(bf16x8, P0), O);
        O = MFMA32(lds_r128(vp + (16 + s) * 592), __builtin_bit_cast(bf16x8, P8), O);
      }
    } else {
#pragma unroll
      for (int s = 0; s < 2; ++s) O = MFMA32(lds_r128(vp + (2 * i + s) * 592), __builtin_bit_cast(bf16x8, P[s]), O);
    }
  }
  const float inv = __builtin_amdgcn_rcpf(O[10]);
  const int row = t * 32 + l31;
  char* ob = ws + WS_O + (size_t)(row >> 4) * 18 * 1024 + (row & 15) * 16;
  u32x4 o = {cvt_pk(O[0] * inv, O[1] * inv), cvt_pk(O[2] * inv, O[3] * inv), cvt_pk(O[4] * inv, O[5] * inv), cvt_pk(O[6] * inv, O[7] * inv)};
  st16(ob + (hd >> 1) * 1024 + (2 * (hd & 1) + hi) * 256, o);
  if (hi == 0) st4(ob + (16 + (hd >> 4)) * 1024 + ((hd >> 2) & 3) * 256 + (hd & 3) * 4, cvt_pk(O[8] * inv, O[9] * inv));
}

__global__ __launch_bounds__(768) void k2_attn(char* __restrict__ ws, const char* __restrict__ wg, const char* __restrict__ wu,
                                               const char* __restrict__ wd) {
  extern __shared__ __attribute__((aligned(16))) char smem[];
  STAMP_BEGIN
  lds_ptr lds = (lds_ptr)smem;
  const unsigned lds0 = (unsigned)(uintptr_t)lds;
  const int tid = threadIdx.x, lane = tid & 63, wid = __builtin_amdgcn_readfirstlane(tid >> 6);
  const int hd = (blockIdx.x & 7) * 4 + ((blockIdx.x >> 3) & 3), qb = blockIdx.x >> 5;
  if (wid >= 8) {
    asm volatile("s_barrier" ::: "memory");
    const int sw = wid - 8, crg = lane / 9, cu = lane - crg * 9;
    if (sw == 1) CODE_TOUCH(SYM_K4, LEN_K4, lds0 + K2_CS + 8192 + 256);
    if (sw == 2) CODE_TOUCH(SYM_K5, LEN_K5, lds0 + K2_CS + 8192 + 512);
    if (sw == 0 && lane < 21 && blockIdx.x * 21 + lane < 5184) touch_lines(wd, (blockIdx.x * 21 + lane) * 128, lds0 + K2_CS + 8192);
    float cs[8];
#pragma unroll
    for (int j = 0; j < 8; ++j) cs[j] = 0.f;
    const char* wsrc = (hd < 16 ? wg : wu) + (size_t)(hd & 15) * 576 + sw * 144 + cu * 16;
    u32x4 v[11];
#pragma unroll
    for (int i = 0; i < 11; ++i) {
      const int rr = crg + 7 * i;
      v[i] = u32x4{0, 0, 0, 0};
      if (lane < 63 && rr < 72) v[i] = *(const u32x4*)(wsrc + (size_t)(qb * 72 + rr) * 9216);
    }
    SB();
    asm volatile("s_barrier" ::: "memory");
    SB();
#pragma unroll
    for (int i = 0; i < 11; ++i)
#pragma unroll
      for (int j = 0; j < 4; ++j) { cs[2 * j] += bf_lo(v[i][j]); cs[2 * j + 1] += bf_hi(v[i][j]); }
    lds_ptr cp = lds + K2_CS + sw * 2048;
    *(LDS_AS f32x4*)(cp + lane * 32) = f32x4{cs[0], cs[1], cs[2], cs[3]};
    *(LDS_AS f32x4*)(cp + lane * 32 + 16) = f32x4{cs[4], cs[5], cs[6], cs[7]};
    asm volatile("s_waitcnt lgkmcnt(0)" ::: "memory");
    if (lane < 36) {
      f32x2 s = {0.f, 0.f};
#pragma unroll
      for (int i = 0; i < 7; ++i) s += *(LDS_AS f32x2*)(cp + i * 288 + lane * 8);
      *(f32x2*)(ws + WS_CSP + ((size_t)qb * 9216 + hd * 288 + sw * 72 + lane * 2) * 4) = s;
    }
    return;
  }
  const int t = qb * 8 + wid;
  bf16x8 q0;
  unsigned qx;
  {
    const unsigned vo = lane * 16;
    const int kt0 = qb * 8 - 8;
    asm volatile("global_load_dwordx4 %0, %2, %3\n\tglobal_load_dword %1, %4, %5"
                 : "=&v"(q0), "=&v"(qx)
                 : "v"(vo), "s"(ws + WS_QM + (size_t)hd * 65536 + (size_t)t * 1024), "v"((lane & 31) * 8),
                   "s"(ws + WS_QX + (size_t)(hd >> 1) * 16384 + (size_t)t * 256 + (hd & 1) * 4)
                 : "memory");
#pragma unroll
    for (int i = 0; i < 2; ++i) {
      const int lt = wid * 2 + i;
      const int kt = max(kt0 + lt, 0);
      glds16(ws + WS_KM + (size_t)hd * 65536 + (size_t)kt * 1024, vo, lds0 + K2_KM + lt * 1024);
    }
    if (wid >= 4) {
      const int k0 = max(qb * 256 - 256 + (wid - 4) * 128, 0);
      glds16(ws + WS_KX + (size_t)(hd >> 1) * 16384 + (size_t)k0 * 8, vo, lds0 + K2_KX + (wid - 4) * 1024);
    }
    const long vb0 = (long)(hd * 128 + qb * 16 - 16) * 592;
#pragma unroll
    for (int i = 0; i < 3; ++i) {
      const int pc = wid + 8 * i;
      if (pc < 19) {
        long off = vb0 + pc * 1024;
        unsigned v2 = vo;
        if (qb == 0) { off = (long)(hd * 128) * 592 + pc * 1024; v2 = (pc * 1024 + lane * 16 >= 9472) ? vo - 9472 + 65536 : vo + 65536; off -= 65536; }
        glds16(ws + WS_V + off, v2, lds0 + K2_V + pc * 1024);
      }
    }
  }
  STAMP_MARK(1, 2);
  asm volatile("s_waitcnt vmcnt(2)\n\ts_barrier" : "+v"(q0), "+v"(qx)::"memory");
  STAMP_MARK(1, 3);
  if (qb == 0) k2_core<true>(lds, ws, hd, t, wid, lane, q0, qx);
  else k2_core<false>(lds, ws, hd, t, wid, lane, q0, qx);
  STAMP_MARK(1, 4);
  STAMP_END(1);
}

constexpr int T_SLOT = 24576, T_NSLOT = 6, T_LDS = T_SLOT * T_NSLOT;

template <int MODE>
__global__ __launch_bounds__(768) void k_t80(const char* __restrict__ aimg, const char* __restrict__ w, const char* __restrict__ resid,
                                             char* __restrict__ ws, char* __restrict__ out) {
  extern __shared__ __attribute__((aligned(16))) char smem[];
  STAMP_BEGIN
  lds_ptr lds = (lds_ptr)smem;
  const unsigned lds0 = (unsigned)(uintptr_t)lds;
  constexpr int NSB = MODE == 0 ? 1 : 8, KSTEPS = NSB * 18;
  const int tid = threadIdx.x, lane = tid & 63, wid = __builtin_amdgcn_readfirstlane(tid >> 6);
  const int tile = xcd_remap(blockIdx.x, 234);
  const int mt = tile / 9, nt = tile - mt * 9;
  const int m0 = min(mt * 80, SEQ - 80), rb0 = m0 >> 4;
  if (wid >= 10) {
    __builtin_amdgcn_s_setprio(2);
    const int par = wid - 10;
    unsigned va[4], vb[4];
#pragma unroll
    for (int i = 0; i < 4; ++i) {
      const int rho = 8 * i + (lane >> 3);
      const int g_ = ((rho >> 4) & 1) * 2 + ((rho >> 2) & 1), t_ = (rho >> 3) & 1, q_ = rho & 3;
      int La, Lb;
      if (MODE == 0) { La = 36 * par + 18 * (g_ >> 1) + 8 * t_ + 4 * (g_ & 1) + q_; Lb = 288 * par + 18 * (4 * g_ + 2 * t_ + (q_ >> 1)) + 16 + (q_ & 1); }
      else { La = 32 * par + 16 * t_ + 4 * g_ + q_; Lb = 288 * par + 144 * (g_ >> 1) + 128 + 8 * (g_ & 1) + 4 * t_ + q_; }
      const unsigned csw = ((lane & 7) ^ (((rho >> 1) & 3) << 1)) * 16;
      va[i] = La * 1152 + csw; vb[i] = Lb * 1152 + csw;
    }
    const char* wsrc = w + nt * 128;
    const unsigned dst0 = lds0 + par * 4096;
    auto issue = [&](int sb, int st, int slot) {
#pragma unroll
      for (int j = 0; j < 3; ++j) {
        const int kp = st * 3 + j;
        const int base = kp == 8 ? 0 : (MODE == 0 ? 72 * kp : 144 * (kp >> 1) + 64 * (kp & 1));
        const char* src = wsrc + (size_t)(sb * 576 + base) * 1152;
#pragma unroll
        for (int i = 0; i < 4; ++i) glds16(src, kp == 8 ? vb[i] : va[i], dst0 + slot * T_SLOT + j * 8192 + i * 1024);
      }
    };
    if (MODE == 0) {
      issue(0, 0, 0); issue(0, 1, 1);
      wait_bar<12>(); issue(0, 2, 2); wait_bar<12>(); wait_bar<0>();
    } else {
      issue(0, 0, 0); issue(0, 1, 1);
      wait_bar<12>();
      issue(0, 2, 2); issue(1, 0, 3); issue(1, 1, 4);
      wait_bar<36>(); issue(1, 2, 5);
      wait_bar<36>(); issue(2, 0, 0); issue(2, 1, 1);
#pragma unroll 1
      for (int sb = 1; sb < 6; ++sb) {
        const int o = (sb & 1) * 3;
        wait_bar<48>(); issue(sb + 1, 2, 5 - o);
        wait_bar<48>(); issue(sb + 2, 0, o);
        wait_bar<48>(); issue(sb + 2, 1, o + 1);
      }
      wait_bar<48>(); issue(7, 2, 5);
      wait_bar<48>(); wait_bar<36>();
      wait_bar<24>(); wait_bar<12>(); wait_bar<0>();
    }
    return;
  }
  const int mi = wid >> 1, kg = wid & 1;
  const int g = lane >> 4, q = (lane & 15) >> 2, p = lane & 3;
  const char* ap = aimg + (size_t)(rb0 + mi) * KSTEPS * 1024 + kg * 1024 + lane * 16;
  bf16x8 a[9];
#pragma unroll
  for (int i = 0; i < 9; ++i) a[i] = *(const bf16x8*)(ap + i * 2048);
  const int er = tid >> 3, ec = tid & 7;
  u32x4 res;
  {
    const int col = nt * 64 + ec * 8;
    if (MODE == 0) res = *(const u32x4*)(resid + (size_t)(m0 + er) * 1152 + col * 2);
    else res = *(const u32x4*)(resid + (size_t)(rb0 + (er >> 4)) * 18432 + (col >> 5) * 1024 + ((col >> 3) & 3) * 256 + (er & 15) * 16);
  }
  float csp[8];
  const bool do_cs = MODE == 0 && blockIdx.x < 72 && wid < 2;
  if (do_cs) {
#pragma unroll
    for (int i = 0; i < 8; ++i) csp[i] = *(const float*)(ws + WS_CSP + ((size_t)i * 9216 + blockIdx.x * 128 + tid) * 4);
  }
  const int rho0 = 16 * (g >> 1) + 4 * (g & 1) + q;
  lds_ptr wb[4];
#pragma unroll
  for (int n = 0; n < 4; ++n) wb[n] = lds + kg * 4096 + rho0 * 128 + ((n ^ ((rho0 >> 1) & 3)) * 32) + p * 8;
  f32x4 acc[4];
#pragma unroll
  for (int n = 0; n < 4; ++n) acc[n] = f32x4{0.f, 0.f, 0.f, 0.f};
  auto sblock = [&](int sb, bool more) {
    const int o = (sb & 1) * 3 * T_SLOT;
#pragma unroll
    for (int st = 0; st < 3; ++st) {
      lds_bar();
#pragma unroll
      for (int j = 0; j < 3; ++j) {
        bf16x8 wf[4];
#pragma unroll
        for (int n = 0; n < 4; ++n) wf[n] = cat8(lds_tr(wb[n] + o + st * T_SLOT + j * 8192), lds_tr(wb[n] + o + st * T_SLOT + j * 8192 + 1024));
#pragma unroll
        for (int n = 0; n < 4; ++n) acc[n] = MFMA16(wf[n], a[st * 3 + j], acc[n]);
        if (more) a[st * 3 + j] = *(const bf16x8*)(ap + (size_t)(sb + 1) * 18432 + (st * 3 + j) * 2048);
      }
      SB();
    }
  };
#pragma unroll 1
  for (int sb = 0; sb < NSB - 1; ++sb) sblock(sb, true);
  sblock(NSB - 1, false);
  STAMP_MARK(MODE == 0 ? 2 : 4, 3);
  if (do_cs) *(float*)(ws + WS_CS + (size_t)(blockIdx.x * 128 + tid) * 4) = ((csp[0] + csp[1]) + (csp[2] + csp[3])) + ((csp[4] + csp[5]) + (csp[6] + csp[7]));
  lds_bar();
  {
    lds_ptr cw = lds + kg * (80 * 272) + (mi * 16 + (lane & 15)) * 272 + 16 * g;
#pragma unroll
    for (int n = 0; n < 4; ++n) *(LDS_AS f32x4*)(cw + n * 64) = acc[n];
  }
  lds_bar();
  {
    lds_ptr cr = lds + er * 272 + ec * 32;
    f32x4 a0 = *(LDS_AS f32x4*)(cr), a1 = *(LDS_AS f32x4*)(cr + 16);
    f32x4 b0 = *(LDS_AS f32x4*)(cr + 80 * 272), b1 = *(LDS_AS f32x4*)(cr + 80 * 272 + 16);
    a0 += b0; a1 += b1;
    u32x4 o;
    o[0] = cvt_pk(a0[0] + bf_lo(res[0]), a0[1] + bf_hi(res[0]));
    o[1] = cvt_pk(a0[2] + bf_lo(res[1]), a0[3] + bf_hi(res[1]));
    o[2] = cvt_pk(a1[0] + bf_lo(res[2]), a1[1] + bf_hi(res[2]));
    o[3] = cvt_pk(a1[2] + bf_lo(res[3]), a1[3] + bf_hi(res[3]));
    const int row = m0 + er, col = nt * 64 + ec * 8;
    if (MODE == 0) {
      st16(ws + WS_H + (size_t)(row >> 4) * 18432 + (col >> 5) * 1024 + ((col >> 3) & 3) * 256 + (row & 15) * 16, o);
      float s1 = 0.f, s2 = 0.f;
#pragma unroll
      for (int j = 0; j < 4; ++j) { s1 = dot2(o[j], 0x3f803f80u, s1); s2 = dot2(o[j], o[j], s2); }
      s1 = sum8(s1); s2 = sum8(s2);
      if (ec == 0) *(f32x2*)(ws + WS_ST + (size_t)row * 80 + nt * 8) = f32x2{s1, s2};
    } else {
      *(u32x4*)(out + (size_t)row * 1152 + col * 2) = o;
    }
  }
  STAMP_END(MODE == 0 ? 2 : 4);
}

constexpr int K4_SLOT = 18432, K4_ST = K4_SLOT * 8, K4_CS = K4_ST + 2048, K4_PF = K4_CS + 2048, K4_LDS = K4_PF + 1024;

constexpr int k4_frag(int i) { return i < 16 ? ((i >> 2) * 2 + (i & 1) + ((i & 2) ? 9 : 0)) : (i == 16 ? 8 : 17); }
constexpr int k4_foff(int f) { return (f >= 9 ? 9216 + (f - 9) * 32 : f * 32); }
constexpr int K4_T0 = 13 * 18;
constexpr int k4_slab(int G) { return G < K4_T0 ? G / 18 : (G - K4_T0 < 80 ? 13 + ((G - K4_T0) % 20) / 4 : 13 + (G - K4_T0 - 80) / 2); }
constexpr int k4_pos(int G) { return G < K4_T0 ? G % 18 : (G - K4_T0 < 80 ? ((G - K4_T0) / 20) * 4 + ((G - K4_T0) & 3) : 16 + ((G - K4_T0) & 1)); }
constexpr int k4_op(int G) {
  const int s = G / 18, i = G % 18;
  if (s > 12) return 0;
  if (s < 3) {
    const int pos[10] = {0, 2, 4, 6, 7, 9, 11, 13, 15, 16};
    for (int k = 0; k < 10; ++k) if (pos[k] == i) return 1 + (2 * s + 2 + k / 5) * 8 + k % 5;
    return 0;
  }
  if (s == 3) {
    const int pos[8] = {0, 2, 4, 6, 8, 11, 14, 16};
    for (int k = 0; k < 8; ++k) if (pos[k] == i) return k < 5 ? 1 + 8 * 8 + k : 1 + 9 * 8 + (k - 3);
    return 0;
  }
  if (i == 1) return 1 + (s + 5) * 8;
  if (i == 4) return 1 + (s + 5) * 8 + 1;
  if (s + 6 > 17) return 0;
  if (i == 8) return 1 + (s + 6) * 8 + 2;
  if (i == 13) return 1 + (s + 6) * 8 + 3;
  if (i == 16) return 1 + (s + 6) * 8 + 4;
  return 0;
}
constexpr int k4_allow(int G, int need) {
  int n = (need == 0) ? 4 : 0;
  for (int g = 0; g < G; ++g) {
    const int op = k4_op(g);
    if (op == 0) continue;
    const int sl = (op - 1) >> 3, kind = (op - 1) & 7;
    if (sl <= need) n = 0;
    else if (kind != 4) ++n;
  }
  return n;
}
constexpr int k4_need(int G) {
  if (G + 5 >= 324 || G == 0) return 0;
  int mx = 0;
  for (int g = 0; g < G + 5; ++g) mx = k4_slab(g) > mx ? k4_slab(g) : mx;
  return k4_slab(G + 5) > mx ? k4_slab(G + 5) : 0;
}

__global__ __launch_bounds__(512, 2) void k4_gateup(const char* __restrict__ wgate, const char* __restrict__ wup, char* __restrict__ ws,
                                                    const char* __restrict__ wd) {
  extern __shared__ __attribute__((aligned(16))) char smem[];
  STAMP_BEGIN
  lds_ptr lds = (lds_ptr)smem;
  const unsigned lds0 = (unsigned)(uintptr_t)lds;
  const int tid = threadIdx.x, lane = tid & 63, wid = __builtin_amdgcn_readfirstlane(tid >> 6);
  const int xcd = blockIdx.x & 7, idx = blockIdx.x >> 3;
  const int pn = 4 * (xcd >> 2) + (idx & 3), jt = 8 * (xcd & 3) + (idx >> 2);
  const int wm = wid & 3, wn = wid >> 2;
  const char* asrc = ws + WS_H + (size_t)(pn * 16 + 2 * wid) * 18432;
  const unsigned avo = lane * 16, avo2 = lane * 16 + 18432;
  const char* wsrc = (wn ? wup : wgate) + jt * 288;
  unsigned wvo[3];
#pragma unroll
  for (int i = 0; i < 3; ++i) {
    const int pc = i < 2 ? 2 * wm + i : 8;
    const int o = pc * 1024 + lane * 16;
    const int rho = o / 288, u = (o - rho * 288) >> 4;
    const int gk = ((rho >> 4) & 1) * 2 + ((rho >> 2) & 1), tk = (rho >> 3) & 1, qk = rho & 3;
    wvo[i] = (8 * gk + 4 * tk + qk) * 9216 + u * 16;
  }
  const unsigned wdst = lds0 + wn * 9216;
  auto issue_w = [&](int s, int i) {
    const unsigned so = (s & 7) * K4_SLOT;
    const char* wp = wsrc + (size_t)s * 32 * 9216;
    if (i < 2) glds16(wp, wvo[i], wdst + so + wm * 2048 + i * 1024);
    else glds16_if0(wm, wp, wvo[2], wdst + so + 8192);
  };
  bf16x8 A[6][2];
  auto issue_a = [&](int s, bf16x8& dst, int m) {
    asm volatile("global_load_dwordx4 %0, %1, %2" : "=v"(dst) : "v"(m ? avo2 : avo), "s"(asrc + s * 1024) : "memory");
  };
  f32x4 sp0, sp1, sp2, sp3;
  f32x2 sp4;
  asm volatile("global_load_dwordx4 %0, %5, %6\n\tglobal_load_dwordx4 %1, %5, %6 offset:16\n\tglobal_load_dwordx4 %2, %5, %6 offset:32\n\t"
               "global_load_dwordx4 %3, %5, %6 offset:48\n\tglobal_load_dwordx2 %4, %5, %6 offset:64"
               : "=&v"(sp0), "=&v"(sp1), "=&v"(sp2), "=&v"(sp3), "=&v"(sp4)
               : "v"((lane & 31) * 80), "s"(ws + WS_ST + (size_t)(pn * 256 + wid * 32) * 80) : "memory");
  if (wm == 1 && lane < 36) glds16(ws + WS_CS + (size_t)(wn * FF + jt * 144) * 4, lane * 16, lds0 + K4_CS + wn * 576);
  for_seq<2>([&](auto S) { issue_w(S, 0); issue_w(S, 1); issue_w(S, 2); issue_a(S, A[S][0], 0); issue_a(S, A[S][1], 1); });
  {
    asm volatile("s_waitcnt vmcnt(8)" : "+v"(sp0), "+v"(sp1), "+v"(sp2), "+v"(sp3), "+v"(sp4)::"memory");
    const float s1 = sp0[0] + sp0[2] + sp1[0] + sp1[2] + sp2[0] + sp2[2] + sp3[0] + sp3[2] + sp4[0];
    const float s2 = sp0[1] + sp0[3] + sp1[1] + sp1[3] + sp2[1] + sp2[3] + sp3[1] + sp3[3] + sp4[1];
    const float mean = s1 * (1.f / 576.f);
    const float var = fmaxf(s2 * (1.f / 576.f) - mean * mean, 0.f);
    *(LDS_AS f32x2*)(lds + K4_ST + (wid * 32 + (lane & 31)) * 8) = f32x2{-mean, __builtin_amdgcn_rsqf(var + LN_EPS)};
    SB();
  }
  const int g = lane >> 4, q = (lane & 15) >> 2, p = lane & 3, r = lane & 15;
  lds_ptr wb = lds + (16 * (g >> 1) + 4 * (g & 1) + q) * 288 + p * 8;
  lds_ptr wbs[3] = {wb, wb + 3 * K4_SLOT, wb + 6 * K4_SLOT};
  asm volatile("" : "+v"(wbs[1]), "+v"(wbs[2]));
  lds_ptr csb = lds + K4_CS + 16 * g;
  asm volatile("" : "+v"(csb));
  f32x4 acc[2][18];
  asm volatile("s_waitcnt vmcnt(%2)\n\ts_barrier" : "+v"(A[0][0]), "+v"(A[0][1]) : "n"(k4_allow(0, 0)) : "memory");
  STAMP_MARK(3, 2);
  STAMP_CYC0
  s16x4 Bl[6], Bh[6];
  for_seq<5>([&](auto I) { Bl[I] = lds_tr(wb + k4_foff(k4_frag(I))); Bh[I] = lds_tr(wb + k4_foff(k4_frag(I)) + 2304); });
  SB();
  const float L2E = 1.4426950408889634f;
  char* hb = ws + WS_HID + (size_t)(pn * 16 + wid * 2) * (144 * 1024) + (size_t)(jt >> 2) * 18432;
  f32x2 st[2];
  float c1[2];
  auto glu = [&](float ag, float au, float cg, float cu, int m) -> float {
    const float tg = fmaf(st[m][0], cg, ag), tu = fmaf(st[m][0], cu, au);
    const float e = __builtin_amdgcn_exp2f(tg * c1[m]);
    return tu * st[m][1] * __builtin_amdgcn_rcpf(1.f + e);
  };
  f32x4 cg[2], cu[2];
  u32x4 ov;
  auto slice = [&](auto QQ, auto KK) {
    constexpr int Q = QQ, k = KK, m = k >> 2, d = k & 3, h = d >> 1, e = (d & 1) * 2;
    const f32x4 ag = acc[m][2 * Q + h], au = acc[m][9 + 2 * Q + h];
    ov[d] = cvt_pk(glu(ag[e], au[e], cg[h][e], cu[h][e], m), glu(ag[e + 1], au[e + 1], cg[h][e + 1], cu[h][e + 1], m));
    if constexpr (d == 3) st16(hb + (size_t)m * (144 * 1024) + (4 * (jt & 3) + Q) * 1024 + lane * 16, ov);
    else asm volatile("" : "+v"(ov));
  };
  for_seq<18>([&](auto GH) { for_seq<18>([&](auto GL) {
    constexpr int G = GH * 18 + GL, sl = k4_slab(G), f = k4_frag(k4_pos(G)), rb = G % 6, nb = (G + 5) % 6, NG = G + 5;
    const bf16x8 b = cat8(Bl[rb], Bh[rb]);
    constexpr int need = k4_need(G);
    if constexpr (need != 0) {
      asm volatile("s_waitcnt vmcnt(%2)\n\ts_barrier" : "+v"(A[need % 6][0]), "+v"(A[need % 6][1]) : "n"(k4_allow(G, need)) : "memory");
      SB();
    }
    if constexpr (sl == 0) mfma16_zero(acc[0][f], b, A[0][0]); else mfma16_acc(acc[0][f], b, A[sl % 6][0]);
    SB();
    if constexpr (NG < 324) { Bl[nb] = lds_tr(wbs[(k4_slab(NG) & 7) / 3] + ((k4_slab(NG) & 7) % 3) * K4_SLOT + k4_foff(k4_frag(k4_pos(NG)))); SB(); }
    if constexpr (sl == 0) mfma16_zero(acc[1][f], b, A[0][1]); else mfma16_acc(acc[1][f], b, A[sl % 6][1]);
    SB();
    if constexpr (NG < 324) { Bh[nb] = lds_tr(wbs[(k4_slab(NG) & 7) / 3] + ((k4_slab(NG) & 7) % 3) * K4_SLOT + k4_foff(k4_frag(k4_pos(NG))) + 2304); SB(); }
    constexpr int op = k4_op(G);
    if constexpr (op != 0) {
      constexpr int os = (op - 1) >> 3, kind = (op - 1) & 7;
      if constexpr (kind < 2) issue_a(os, A[os % 6][kind], kind);
      else issue_w(os, kind - 2);
      SB();
    }
    if constexpr (G == K4_T0 + 12) {
      touch_if0((int)(blockIdx.x / 216) + wid / 3, wd + (size_t)(blockIdx.x * 3 + wid) * 8192, avo * 8, lds0 + K4_PF + (wid & 3) * 256);
      SB();
    }
    if constexpr (G == K4_T0) {
      STAMP_MARK(3, 3);
      STAMP_CYC(3, 4);
#pragma unroll
      for (int m = 0; m < 2; ++m) { st[m] = *(LDS_AS f32x2*)(lds + K4_ST + (wid * 32 + m * 16 + r) * 8); c1[m] = -L2E * st[m][1]; }
      SB();
    }
    if constexpr (G >= K4_T0) {
      constexpr int t = G - K4_T0, u = t % 20;
      if constexpr (t < 80 && u == 18) {
#pragma unroll
        for (int h = 0; h < 2; ++h) {
          cg[h] = *(LDS_AS f32x4*)(csb + (2 * (t / 20) + h) * 64);
          cu[h] = *(LDS_AS f32x4*)(csb + 576 + (2 * (t / 20) + h) * 64);
        }
        SB();
      }
      if constexpr (t >= 20 && t < 80 && u >= 1 && u < 17 && (u & 1)) { slice(std::integral_constant<int, t / 20 - 1>{}, std::integral_constant<int, u / 2>{}); SB(); }
      if constexpr (t >= 81 && t < 89) { slice(std::integral_constant<int, 3>{}, std::integral_constant<int, t - 81>{}); SB(); }
    }
  }); });
  {
    asm volatile("s_nop 7\n\ts_nop 1" : "+v"(acc[0][8]), "+v"(acc[1][8]), "+v"(acc[0][17]), "+v"(acc[1][17]));
    const f32x4 cg8 = *(LDS_AS f32x4*)(csb + 512);
    const f32x4 cu8 = *(LDS_AS f32x4*)(csb + 576 + 512);
#pragma unroll
    for (int m = 0; m < 2; ++m) {
      const f32x4 ag = acc[m][8], au = acc[m][17];
      u32x2 o = {cvt_pk(glu(ag[0], au[0], cg8[0], cu8[0], m), glu(ag[1], au[1], cg8[1], cu8[1], m)),
                 cvt_pk(glu(ag[2], au[2], cg8[2], cu8[2], m), glu(ag[3], au[3], cg8[3], cu8[3], m))};
      st8(hb + (size_t)m * (144 * 1024) + (16 + ((jt >> 1) & 1)) * 1024 + (2 * (jt & 1) + (g >> 1)) * 256 + r * 16 + (g & 1) * 8, o);
    }
  }
  STAMP_MARK(3, 5);
  STAMP_END(3);
}

extern "C" void kernel_launch(void* const* d_in, const int* in_sizes, int n_in, void* d_out, int out_size, void* d_ws, size_t ws_size,
                              hipStream_t stream) {
  const char* x = (const char*)d_in[0];
  const char* wqkv = (const char*)d_in[1];
  const char* wo = (const char*)d_in[2];
  const char* wg = (const char*)d_in[4];
  const char* wu = (const char*)d_in[5];
  const char* wd = (const char*)d_in[6];
  char* ws = (char*)d_ws;
  (void)hipFuncSetAttribute((const void*)k1_ln_qkv, hipFuncAttributeMaxDynamicSharedMemorySize, K1_LDS);
  (void)hipFuncSetAttribute((const void*)k2_attn, hipFuncAttributeMaxDynamicSharedMemorySize, K2_LDS);
  (void)hipFuncSetAttribute((const void*)k_t80<0>, hipFuncAttributeMaxDynamicSharedMemorySize, T_LDS);
  (void)hipFuncSetAttribute((const void*)k_t80<1>, hipFuncAttributeMaxDynamicSharedMemorySize, T_LDS);
  (void)hipFuncSetAttribute((const void*)k4_gateup, hipFuncAttributeMaxDynamicSharedMemorySize, K4_LDS);
  k1_ln_qkv<<<234, 1024, K1_LDS, stream>>>(x, wqkv, ws);
  k2_attn<<<256, 768, K2_LDS, stream>>>(ws, wg, wu, wo);
  k_t80<0><<<234, 768, T_LDS, stream>>>(ws + WS_O, wo, x, ws, nullptr);
  k4_gateup<<<256, 512, K4_LDS, stream>>>(wg, wu, ws, wd);
  k_t80<1><<<234, 768, T_LDS, stream>>>(ws + WS_HID, wd, ws + WS_H, ws, (char*)d_out);
}
```

```cpp
#include <hip/hip_runtime.h>
#include <hip/hip_bf16.h>
#include <stdint.h>
#include <utility>
#include <type_traits>

typedef __attribute__((ext_vector_type(8))) short bf16x8;
typedef __attribute__((ext_vector_type(4))) short s16x4;
typedef __attribute__((ext_vector_type(4))) float f32x4;
typedef __attribute__((ext_vector_type(16))) float f32x16;
typedef __attribute__((ext_vector_type(4))) unsigned u32x4;
typedef __attribute__((ext_vector_type(2))) unsigned u32x2;
typedef __attribute__((ext_vector_type(2))) __bf16 bf16x2_t;
typedef __attribute__((ext_vector_type(2))) float f32x2;
#define LDS_AS __attribute__((address_space(3)))
typedef LDS_AS char* lds_ptr;
#define DI __device__ __forceinline__

constexpr int SEQ = 2048, DM = 576, NH = 32, FF = 4608;
constexpr float LN_EPS = 1e-5f;
constexpr float QSCALE = 0.23570226039551584f * 1.4426950408889634f;

constexpr size_t WS_QM = 0;
constexpr size_t WS_KM = WS_QM + (size_t)32 * 64 * 1024;
constexpr size_t WS_QX = WS_KM + (size_t)32 * 64 * 1024;
constexpr size_t WS_KX = WS_QX + (size_t)16 * 2048 * 8;
constexpr size_t WS_V = WS_KX + (size_t)16 * 2048 * 8;
constexpr size_t WS_O = WS_V + (size_t)32 * 128 * 592 + 1024;
constexpr size_t WS_H = WS_O + (size_t)128 * 18 * 1024;
constexpr size_t WS_ST = WS_H + (size_t)128 * 18 * 1024;
constexpr size_t WS_CSP = WS_ST + (size_t)2048 * 80;
constexpr size_t WS_CS = WS_CSP + (size_t)8 * 9216 * 4;
constexpr size_t WS_HID = WS_CS + (size_t)9216 * 4;
constexpr size_t WS_CNT = WS_HID + (size_t)128 * 144 * 1024;
constexpr size_t WS_END = WS_CNT + 2048;
static_assert(WS_O % 1024 == 0 && WS_H % 1024 == 0 && WS_ST % 256 == 0 && WS_CSP % 256 == 0 && WS_HID % 256 == 0, "align");

#define STAMP_BEGIN
#define STAMP_MARK(k, i)
#define STAMP_MARKW(k, i, T)
#define STAMP_ENDW(k, T)
#define STAMP_CYC0
#define STAMP_CYC(k, i)
#define STAMP_END(k)

DI unsigned cvt_pk(float lo, float hi) {
  f32x2 v = {lo, hi};
  bf16x2_t r = __builtin_convertvector(v, bf16x2_t);
  return __builtin_bit_cast(unsigned, r);
}
DI float bf_lo(unsigned u) { return __builtin_bit_cast(float, u << 16); }
DI float bf_hi(unsigned u) { return __builtin_bit_cast(float, u & 0xffff0000u); }
DI float dot2(unsigned a, unsigned b, float c) {
  return __builtin_amdgcn_fdot2_f32_bf16(__builtin_bit_cast(bf16x2_t, a), __builtin_bit_cast(bf16x2_t, b), c, false);
}
template <int CTRL> DI float dpp_add(float v) {
  int t = __builtin_amdgcn_update_dpp(0, __builtin_bit_cast(int, v), CTRL, 0xf, 0xf, true);
  return v + __builtin_bit_cast(float, t);
}
DI float sum8(float v) {
  v = dpp_add<0xB1>(v);
  v = dpp_add<0x4E>(v);
  v = dpp_add<0x141>(v);
  return v;
}
DI void glds16(const void* sbase, unsigned voff, unsigned lds_dst) {
  asm volatile("s_mov_b32 m0, %2\n\ts_nop 3\n\tglobal_load_lds_dwordx4 %0, %1" ::"v"(voff), "s"(sbase), "s"(lds_dst) : "memory");
}
#define ST_AUX " sc1"
DI void st16(void* p, u32x4 v) { asm volatile("global_store_dwordx4 %0, %1, off" ST_AUX "\n\ts_nop 1" ::"v"(p), "v"(v) : "memory"); }
DI void st8(void* p, u32x2 v) { *(u32x2*)p = v; }
DI void st8wt(void* p, u32x2 v) { asm volatile("global_store_dwordx2 %0, %1, off sc1" ::"v"(p), "v"(v) : "memory"); }
DI void st4(void* p, unsigned v) { asm volatile("global_store_dword %0, %1, off sc1" ::"v"(p), "v"(v) : "memory"); }
DI void glds16_if0(int c, const void* sbase, unsigned voff, unsigned lds_dst) {
  asm volatile("s_cmp_lg_u32 %3, 0\n\ts_cbranch_scc1 1f\n\ts_mov_b32 m0, %2\n\ts_nop 1\n\tglobal_load_lds_dwordx4 %0, %1\n1:"
               ::"v"(voff), "s"(sbase), "s"(lds_dst), "s"(c) : "memory", "scc");
}
DI void touch_if0(int c, const void* sbase, unsigned voff, unsigned lds_dst) {
  asm volatile("s_cmp_lg_u32 %3, 0\n\ts_cbranch_scc1 1f\n\ts_mov_b32 m0, %2\n\ts_nop 1\n\tglobal_load_lds_dword %0, %1\n1:"
               ::"v"(voff), "s"(sbase), "s"(lds_dst), "s"(c) : "memory", "scc");
}
DI void touch_lines(const char* base, unsigned voff, unsigned lds_dummy) {
  asm volatile("s_mov_b32 m0, %2\n\ts_nop 3\n\tglobal_load_lds_dword %0, %1" ::"v"(voff), "s"(base), "s"(lds_dummy) : "memory");
}
#define CODE_TOUCH(SYM, BYTES, LDS_DUMMY)                                                                          \
  do {                                                                                                             \
    const char* cp_;                                                                                               \
    asm volatile("s_getpc_b64 s[20:21]\n\ts_add_u32 s20, s20, " SYM "@rel32@lo+4\n\ts_addc_u32 s21, s21, " SYM     \
                 "@rel32@hi+12\n\ts_mov_b64 %0, s[20:21]" : "=s"(cp_)::"s20", "s21", "scc");                      \
    constexpr int nl_ = (BYTES) / 128, per_ = (nl_ + 28) / 29;                                                      \
    const int ln_ = (blockIdx.x >> 3) * per_ + lane;                                                               \
    if (lane < per_ && ln_ < nl_) touch_lines(cp_, ln_ * 128, LDS_DUMMY);                                          \
  } while (0)
#define SYM_K1 "_Z9k1_ln_qkvPKcS0_PcS0_"
#define SYM_K2 "_Z7k2_attnPcPKcS1_S1_S1_"
#define SYM_K4 "_Z9k4_gateupPKcS0_PcS0_S1_"
constexpr int LEN_K1 = 24000, LEN_K2 = 16200, LEN_K4 = 33000;
template <int N> DI void wait_bar() { asm volatile("s_waitcnt vmcnt(%0) lgkmcnt(0)\n\ts_barrier" ::"n"(N) : "memory"); }
template <int N> DI void wait_vm() { asm volatile("s_waitcnt vmcnt(%0)" ::"n"(N) : "memory"); }
DI void raw_bar() { asm volatile("s_barrier" ::: "memory"); }
DI void lds_bar() { asm volatile("s_waitcnt lgkmcnt(0)\n\ts_barrier" ::: "memory"); }
DI bf16x8 lds_r128(lds_ptr p) { return *(LDS_AS const bf16x8*)p; }
typedef short v4i16_t __attribute__((ext_vector_type(4)));
DI s16x4 lds_tr(lds_ptr p) { return __builtin_amdgcn_ds_read_tr16_b64_v4i16((LDS_AS v4i16_t*)p); }
DI bf16x8 cat8(s16x4 a, s16x4 b) { return __builtin_shufflevector(a, b, 0, 1, 2, 3, 4, 5, 6, 7); }
#define SB() __builtin_amdgcn_sched_barrier(0)
#define MFMA16(a, b, c) __builtin_amdgcn_mfma_f32_16x16x32_bf16(a, b, c, 0, 0, 0)
#define MFMA32(a, b, c) __builtin_amdgcn_mfma_f32_32x32x16_bf16(a, b, c, 0, 0, 0)
DI void mfma16_acc(f32x4& c, bf16x8 a, bf16x8 b) { asm volatile("v_mfma_f32_16x16x32_bf16 %0, %1, %2, %0" : "+v"(c) : "v"(a), "v"(b)); }
DI void mfma16_zero(f32x4& c, bf16x8 a, bf16x8 b) { asm volatile("v_mfma_f32_16x16x32_bf16 %0, %1, %2, 0" : "=&v"(c) : "v"(a), "v"(b)); }
template <class F, int... I> DI void for_seq_impl(F&& f, std::integer_sequence<int, I...>) { (f(std::integral_constant<int, I>{}), ...); }
template <int N, class F> DI void for_seq(F&& f) { for_seq_impl(f, std::make_integer_sequence<int, N>{}); }
DI int xcd_remap(int b, int nwg) {
  int q = nwg >> 3, r = nwg & 7, x = b & 7, o = b >> 3;
  return (x < r ? x * (q + 1) : r * (q + 1) + (x - r) * q) + o;
}

constexpr int K1_X = 0, K1_W = 40960, K1_SLOT = 12288, K1_ST = K1_W + K1_SLOT * 9, K1_CS = K1_W, K1_PF = K1_ST + 1024, K1_LDS = K1_PF + 1024;
constexpr int cmin(int a, int b) { return a < b ? a : b; }
constexpr int k1_nw(int kk) { return cmin(18, cmin(3 + 2 * kk, kk + 7)); }
constexpr int k1_nx(int kk) { return cmin(18, cmin(3 + 2 * kk, kk + 5)); }

template <bool ISV, bool HI>
DI void k1_consumer(lds_ptr lds, int cb, int lane, char* ws, int ct, int m0) {
  const int g = lane >> 4, q = (lane & 15) >> 2, p = lane & 3, r = lane & 15;
  constexpr int NM = HI ? 2 : 3, MB = HI ? 3 : 0;
  lds_ptr a0 = lds + K1_X + MB * 2048 + (r >> 3) * 1024 + (r & 7) * 128 + ((g ^ (r >> 1)) * 16);
  lds_ptr a1 = lds + K1_X + MB * 2048 + (r >> 3) * 1024 + (r & 7) * 128 + (((4 + g) ^ (r >> 1)) * 16);
  const int rho0 = 16 * (g >> 1) + 4 * (g & 1) + q;
  lds_ptr wb = lds + K1_W + rho0 * 128 + ((cb ^ ((rho0 >> 1) & 3)) * 32) + p * 8;
  lds_ptr wb5 = wb + 5 * K1_SLOT;
  asm volatile("" : "+v"(wb), "+v"(wb5));
  const u32x4 onesu = {0x3f803f80u, 0x3f803f80u, 0x3f803f80u, 0x3f803f80u};
  const bf16x8 ones = __builtin_bit_cast(bf16x8, onesu);
  f32x4 acc[3][3];
#pragma unroll
  for (int m = 0; m < 3; ++m)
#pragma unroll
    for (int j = 0; j < 3; ++j) acc[m][j] = f32x4{0.f, 0.f, 0.f, 0.f};
  bf16x8 fA[2][3];
  s16x4 wl[2][3], wh[2][3];
  auto rd1 = [&](auto KK, auto II) {
    constexpr int kk = KK, i = II, bf = kk & 1, sl = kk % 9;
    if constexpr (i >= 2 && i <= 4) {
      if constexpr (i - 2 < NM) fA[bf][i - 2] = lds_r128(((kk & 1) ? a1 : a0) + ((kk >> 1) & 3) * 10240 + (i - 2) * 2048);
    } else {
      constexpr int j = i < 2 ? 0 : (i - 3) / 2, hh = i < 2 ? i : (i - 3) % 2;
      lds_ptr bp = (sl >= 5 ? wb5 + (sl - 5) * K1_SLOT : wb + sl * K1_SLOT) + j * 4096 + hh * 1024;
      if constexpr (hh == 0) wl[bf][j] = lds_tr(bp); else wh[bf][j] = lds_tr(bp);
    }
  };
  lds_bar();
  STAMP_MARKW(0, 2, 512);
  for_seq<9>([&](auto I) { rd1(std::integral_constant<int, 0>{}, I); });
  SB();
  for_seq<18>([&](auto KK) {
    constexpr int kk = KK, bf = kk & 1;
    if constexpr (kk < 17) { raw_bar(); SB(); }
    for_seq<9>([&](auto II) {
      constexpr int i = II, j = i / 3, m = i % 3;
      const bf16x8 wv = cat8(wl[bf][j], wh[bf][j]);
      const bf16x8 av = (m < NM) ? fA[bf][m] : ones;
      acc[m][j] = ISV ? MFMA16(av, wv, acc[m][j]) : MFMA16(wv, av, acc[m][j]);
      SB();
      if constexpr (kk < 17) { rd1(std::integral_constant<int, kk + 1>{}, II); SB(); }
    });
  });
  STAMP_MARKW(0, 4, 512);
  lds_ptr cx = lds + K1_CS + cb * 3072 + lane * 16;
  if (HI) {
#pragma unroll
    for (int j = 0; j < 3; ++j) *(LDS_AS f32x4*)(cx + j * 1024) = acc[2][j];
  }
  lds_bar();
  STAMP_MARKW(0, 5, 512);
  f32x4 cs[3];
#pragma unroll
  for (int j = 0; j < 3; ++j) cs[j] = HI ? acc[2][j] : *(LDS_AS f32x4*)(cx + j * 1024);
  if (ISV) {
    f32x4 st[NM][2];
#pragma unroll
    for (int m = 0; m < NM; ++m) {
      st[m][0] = *(LDS_AS f32x4*)(lds + K1_ST + ((MB + m) * 16 + 4 * g) * 8);
      st[m][1] = *(LDS_AS f32x4*)(lds + K1_ST + ((MB + m) * 16 + 4 * g) * 8 + 16);
    }
#pragma unroll
    for (int j = 0; j < 3; ++j) {
      const int c = (ct - 6) * 192 + j * 64 + cb * 16 + r;
      const int head = c / 18, d = c - head * 18;
      char* vb = ws + WS_V + (size_t)(head * 128 + (m0 >> 4) + MB) * 592;
      char* vp = vb + (g & 1) * 288 + d * 16 + (g >> 1) * 8;
      const float c0 = cs[j][0];
#pragma unroll
      for (int m = 0; m < NM; ++m) {
        u32x2 o = {cvt_pk(fmaf(st[m][0][0], c0, acc[m][j][0]) * st[m][0][1], fmaf(st[m][0][2], c0, acc[m][j][1]) * st[m][0][3]),
                   cvt_pk(fmaf(st[m][1][0], c0, acc[m][j][2]) * st[m][1][1], fmaf(st[m][1][2], c0, acc[m][j][3]) * st[m][1][3])};
        st8(vp + m * 592, o);
      }
      if (d == 17 && g < NM) *(u32x4*)(vb + g * 592 + 576) = onesu;
    }
  } else {
    const bool isq = ct < 3;
    const float sc = isq ? QSCALE : 1.f;
    f32x2 st[NM];
#pragma unroll
    for (int m = 0; m < NM; ++m) { st[m] = *(LDS_AS f32x2*)(lds + K1_ST + ((MB + m) * 16 + r) * 8); st[m][1] *= sc; }
#pragma unroll
    for (int j = 0; j < 3; ++j) {
      const int c = (isq ? ct : ct - 3) * 192 + j * 64 + cb * 16 + 4 * g;
      const int pair = c / 36, qi = (c - pair * 36) >> 2;
      const int head = 2 * pair + (qi > 4 ? 1 : 0), a = qi > 4 ? qi - 5 : qi;
      char* mainp = ws + (isq ? WS_QM : WS_KM) + (size_t)head * 65536 + (a >> 1) * 512 + (a & 1) * 8;
      char* xp = ws + (isq ? WS_QX : WS_KX) + (size_t)pair * 16384;
#pragma unroll
      for (int m = 0; m < NM; ++m) {
        const int rw = m0 + (MB + m) * 16 + r;
        u32x2 o = {cvt_pk(fmaf(st[m][0], cs[j][0], acc[m][j][0]) * st[m][1], fmaf(st[m][0], cs[j][1], acc[m][j][1]) * st[m][1]),
                   cvt_pk(fmaf(st[m][0], cs[j][2], acc[m][j][2]) * st[m][1], fmaf(st[m][0], cs[j][3], acc[m][j][3]) * st[m][1])};
        char* dst = (qi == 4) ? xp + rw * 8 : mainp + (rw >> 5) * 1024 + (rw & 31) * 16;
        st8(dst, o);
      }
    }
  }
}

__global__ __launch_bounds__(1024) void k1_ln_qkv(const char* __restrict__ x, const char* __restrict__ wqkv, char* __restrict__ ws, const char* __restrict__ wo) {
  extern __shared__ __attribute__((aligned(16))) char smem[];
  STAMP_BEGIN
  lds_ptr lds = (lds_ptr)smem;
  const unsigned lds0 = (unsigned)(uintptr_t)lds;
  const int tid = threadIdx.x, lane = tid & 63, wid = __builtin_amdgcn_readfirstlane(tid >> 6);
  const int tile = xcd_remap(blockIdx.x, 234);
  const int cgp = tile / 78, rem = tile - cgp * 78;
  const int mt = rem / 3, ct = cgp * 3 + (rem - mt * 3);
  const int m0 = min(mt * 80, SEQ - 80);
  if (wid < 3) {
    unsigned wv[4];
#pragma unroll
    for (int i = 0; i < 4; ++i) {
      const int rho = 8 * i + (lane >> 3);
      const int gk = ((rho >> 4) & 1) * 2 + ((rho >> 2) & 1), tk = (rho >> 3) & 1, qk = rho & 3;
      wv[i] = (8 * gk + 4 * tk + qk) * 3456 + (((lane & 7) ^ (((rho >> 1) & 3) << 1)) * 16);
    }
    const char* wsrc = wqkv + ct * 384 + wid * 128;
    const unsigned wdst = lds0 + K1_W + wid * 4096;
    for_seq<18>([&](auto KK) {
      constexpr int kk = KK;
#pragma unroll
      for (int n = (kk == 0 ? 0 : k1_nw(kk - 1)); n < k1_nw(kk); ++n)
#pragma unroll
        for (int i = 0; i < 4; ++i) glds16(wsrc + (size_t)n * 32 * 3456, wv[i], wdst + (n % 9) * K1_SLOT + i * 1024);
      wait_bar<(k1_nw(kk) - kk - 1) * 4>();
    });
    return;
  }
  if (wid == 3) {
    const unsigned v0 = (lane >> 3) * 1152 + (((lane & 7) ^ (lane >> 4)) * 16);
    const unsigned v1 = (lane >> 3) * 1152 + (((lane & 7) ^ (4 + (lane >> 4))) * 16);
    const char* xs = x + (size_t)m0 * 1152;
    for_seq<18>([&](auto KK) {
      constexpr int kk = KK;
#pragma unroll
      for (int h = (kk == 0 ? 0 : k1_nx(kk - 1)); h < k1_nx(kk); ++h)
#pragma unroll
        for (int i = 0; i < 5; ++i) {
          const int pc = (h & 1) * 5 + i;
          glds16(xs + pc * 9216 + (h >> 1) * 128, (pc & 1) ? v1 : v0, lds0 + K1_X + ((h >> 1) & 3) * 10240 + pc * 1024);
        }
      wait_bar<(k1_nx(kk) - 2 * (kk >> 1) - 2) * 5>();
    });
    return;
  }
  if (wid < 8) {
    const int j = wid - 4;
    if (j == 0) CODE_TOUCH(SYM_K1, LEN_K1, lds0 + K1_PF);
    if (j == 1) CODE_TOUCH(SYM_K2, LEN_K2, lds0 + K1_PF + 256);
    if (j == 2 && blockIdx.x == 0 && lane < 16) *(unsigned*)(ws + WS_CNT + lane * 128) = 0u;
    {
      const int ln = blockIdx.x * 168 + j * 42 + lane;
      if (lane < 42) {
        if (ln < 15552) touch_lines(wqkv, ln * 128, lds0 + K1_PF + j * 256);
        else if (ln < 33984) touch_lines(x, (ln - 15552) * 128, lds0 + K1_PF + j * 256);
        else if (ln < 39168) touch_lines(wo, (ln - 33984) * 128, lds0 + K1_PF + j * 256);
      }
    }
    lds_ptr p0 = lds + K1_X + j * 1024 + lane * 16, p2 = lds + K1_X + min(j + 8, 9) * 1024 + lane * 16;
    float s1[3] = {0.f, 0.f, 0.f}, s2[3] = {0.f, 0.f, 0.f};
#pragma unroll 1
    for (int s = 0; s < 9; ++s) {
      raw_bar();
      const int so = (s & 3) * 10240;
      u32x4 v[3] = {*(LDS_AS u32x4*)(p0 + so), *(LDS_AS u32x4*)(p0 + so + 4096), *(LDS_AS u32x4*)(p2 + so)};
      SB();
      raw_bar();
      SB();
#pragma unroll
      for (int i = 0; i < 3; ++i)
#pragma unroll
        for (int e = 0; e < 4; ++e) { s1[i] = dot2(v[i][e], 0x3f803f80u, s1[i]); s2[i] = dot2(v[i][e], v[i][e], s2[i]); }
      asm volatile("" : "+v"(s1[0]), "+v"(s1[1]), "+v"(s1[2]), "+v"(s2[0]), "+v"(s2[1]), "+v"(s2[2]));
    }
#pragma unroll
    for (int i = 0; i < 3; ++i) {
      const float a = sum8(s1[i]), b = sum8(s2[i]);
      const float mean = a * (1.f / 576.f);
      const float var = fmaxf(b * (1.f / 576.f) - mean * mean, 0.f);
      const int pc = i < 2 ? j + 4 * i : min(j + 8, 9);
      if ((lane & 7) == 0) *(LDS_AS f32x2*)(lds + K1_ST + (pc * 8 + (lane >> 3)) * 8) = f32x2{-mean, __builtin_amdgcn_rsqf(var + LN_EPS)};
    }
    lds_bar();
    return;
  }
  if (ct >= 6) {
    if (wid < 12) k1_consumer<true, false>(lds, wid - 8, lane, ws, ct, m0);
    else k1_consumer<true, true>(lds, wid - 12, lane, ws, ct, m0);
  } else {
    if (wid < 12) k1_consumer<false, false>(lds, wid - 8, lane, ws, ct, m0);
    else k1_consumer<false, true>(lds, wid - 12, lane, ws, ct, m0);
  }
  STAMP_ENDW(0, 512);
}

constexpr int K2_KM = 0, K2_KX = 16384, K2_V = K2_KX + 4096, K2_CS = 44032, K2_W = K2_CS + 5120, K2_LDS = K2_W + 73728;

template <bool FIRST>
DI void k2_core(lds_ptr lds, char* ws, int hd, int t, int wid, int lane, bf16x8 q0, unsigned qx) {
  const int hi = lane >> 5, l31 = lane & 31;
  u32x4 q1u = {hi ? 0u : qx, 0u, 0u, 0u};
  bf16x8 q1 = __builtin_bit_cast(bf16x8, q1u);
  f32x16 S[9];
  lds_ptr kmp = lds + K2_KM + wid * 1024 + lane * 16;
  lds_ptr kxp = lds + K2_KX + (wid * 32 + l31) * 8 + (hd & 1) * 4;
  const float NEG = -1e30f;
  const int dq = l31 - 4 * hi;
  float mx = NEG;
#pragma unroll
  for (int ii = 0; ii < 9; ++ii) {
    const int i = ii == 0 ? 8 : ii - 1;
    bf16x8 k0 = lds_r128(kmp + i * 1024);
    u32x4 k1u = {*(LDS_AS unsigned*)(kxp + i * 256), 0u, 0u, 0u};
    f32x16 z = {};
    S[i] = MFMA32(k0, q0, z);
    S[i] = MFMA32(__builtin_bit_cast(bf16x8, k1u), q1, S[i]);
    if (FIRST && i < 8 && wid + i < 8) {
#pragma unroll
      for (int r = 0; r < 16; ++r) S[i][r] = NEG;
    }
    if (i == 0) {
#pragma unroll
      for (int r = 0; r < 16; ++r) S[0][r] = (((r & 3) + 8 * (r >> 2)) <= dq) ? S[8][r] : S[0][r];
    }
    if (i < 8) {
#pragma unroll
      for (int r = 0; r < 16; r += 2) mx = fmaxf(fmaxf(mx, S[i][r]), S[i][r + 1]);
    }
  }
  {
    auto rr = __builtin_amdgcn_permlane32_swap(__float_as_uint(mx), __float_as_uint(mx), false, false);
    mx = fmaxf(__uint_as_float(rr[0]), __uint_as_float(rr[1]));
  }
  wait_bar<0>();
  lds_ptr vp = lds + K2_V + wid * 2 * 592 + (l31 < 18 ? hi * 288 + l31 * 16 : 576);
  f32x16 O = {};
#pragma unroll
  for (int i = 0; i < 8; ++i) {
    u32x4 P[2];
#pragma unroll
    for (int s = 0; s < 2; ++s)
#pragma unroll
      for (int j = 0; j < 4; ++j)
        P[s][j] = cvt_pk(__builtin_amdgcn_exp2f(S[i][8 * s + 2 * j] - mx), __builtin_amdgcn_exp2f(S[i][8 * s + 2 * j + 1] - mx));
    if (i == 0) {
#pragma unroll
      for (int s = 0; s < 2; ++s) {
        u32x4 P8, P0;
#pragma unroll
        for (int j = 0; j < 4; ++j) {
          const int r0 = 8 * s + 2 * j, r1 = r0 + 1;
          const unsigned mk = ((((r0 & 3) + 8 * (r0 >> 2)) <= dq) ? 0xffffu : 0u) | ((((r1 & 3) + 8 * (r1 >> 2)) <= dq) ? 0xffff0000u : 0u);
          P8[j] = P[s][j] & mk;
          P0[j] = P[s][j] & ~mk;
        }
        O = MFMA32(lds_r128(vp + s * 592), __builtin_bit_cast(bf16x8, P0), O);
        O = MFMA32(lds_r128(vp + (16 + s) * 592), __builtin_bit_cast(bf16x8, P8), O);
      }
    } else {
#pragma unroll
      for (int s = 0; s < 2; ++s) O = MFMA32(lds_r128(vp + (2 * i + s) * 592), __builtin_bit_cast(bf16x8, P[s]), O);
    }
  }
  const float inv = __builtin_amdgcn_rcpf(O[10]);
  const int row = t * 32 + l31;
  char* ob = ws + WS_O + (size_t)(row >> 4) * 18 * 1024 + (row & 15) * 16;
  u32x4 o = {cvt_pk(O[0] * inv, O[1] * inv), cvt_pk(O[2] * inv, O[3] * inv), cvt_pk(O[4] * inv, O[5] * inv), cvt_pk(O[6] * inv, O[7] * inv)};
  st16(ob + (hd >> 1) * 1024 + (2 * (hd & 1) + hi) * 256, o);
  if (hi == 0) st4(ob + (16 + (hd >> 4)) * 1024 + ((hd >> 2) & 3) * 256 + (hd & 3) * 4, cvt_pk(O[8] * inv, O[9] * inv));
}

__global__ __launch_bounds__(768) void k2_attn(char* __restrict__ ws, const char* __restrict__ wg, const char* __restrict__ wu,
                                               const char* __restrict__ wo, const char* __restrict__ x) {
  extern __shared__ __attribute__((aligned(16))) char smem[];
  STAMP_BEGIN
  lds_ptr lds = (lds_ptr)smem;
  const unsigned lds0 = (unsigned)(uintptr_t)lds;
  const int tid = threadIdx.x, lane = tid & 63, wid = __builtin_amdgcn_readfirstlane(tid >> 6);
  const int hd = (blockIdx.x & 7) * 4 + ((blockIdx.x >> 3) & 3), qb = blockIdx.x >> 5;
  const bool has_tile = blockIdx.x < 234;
  const int tile = has_tile ? xcd_remap(blockIdx.x, 234) : 0;
  const int mt = tile / 9, nt = tile - mt * 9;
  const int m0 = min(mt * 80, SEQ - 80), rb0 = m0 >> 4;
  if (wid >= 10) {
    raw_bar();
    const int sw = wid - 10;
    if (sw == 0) CODE_TOUCH(SYM_K4, LEN_K4, lds0 + K2_CS + 4096);
    const bool act = blockIdx.x < 216;
    const int unit = act ? (int)blockIdx.x * 2 + sw : 0, part = unit / 144, c64 = unit - part * 144;
    const char* wsrc = (c64 < 72 ? wg + c64 * 128 : wu + (c64 - 72) * 128) + (lane & 7) * 16 + (size_t)(part * 192 + (lane >> 3)) * 9216;
    u32x4 v[24];
    if (act) {
#pragma unroll
      for (int i = 0; i < 24; ++i) v[i] = *(const u32x4*)(wsrc + (size_t)i * 8 * 9216);
    }
    SB();
    raw_bar();
    raw_bar();
    SB();
    if (act) {
      float cs[8];
#pragma unroll
      for (int j = 0; j < 8; ++j) cs[j] = 0.f;
#pragma unroll
      for (int i = 0; i < 24; ++i)
#pragma unroll
        for (int j = 0; j < 4; ++j) { cs[2 * j] += bf_lo(v[i][j]); cs[2 * j + 1] += bf_hi(v[i][j]); }
      lds_ptr cp = lds + K2_CS + sw * 2048;
      *(LDS_AS f32x4*)(cp + lane * 32) = f32x4{cs[0], cs[1], cs[2], cs[3]};
      *(LDS_AS f32x4*)(cp + lane * 32 + 16) = f32x4{cs[4], cs[5], cs[6], cs[7]};
      asm volatile("s_waitcnt lgkmcnt(0)" ::: "memory");
      float t = 0.f;
#pragma unroll
      for (int i = 0; i < 8; ++i) t += *(LDS_AS float*)(cp + i * 256 + lane * 4);
      *(float*)(ws + WS_CSP + ((size_t)part * 9216 + c64 * 64 + lane) * 4) = t;
    }
    if (has_tile) { raw_bar(); raw_bar(); }
    return;
  }
  if (wid >= 8) {
    raw_bar();
    if (has_tile) {
      const int par = wid - 8;
      unsigned va[4], vb[4];
#pragma unroll
      for (int i = 0; i < 4; ++i) {
        const int rho = 8 * i + (lane >> 3);
        const int g_ = ((rho >> 4) & 1) * 2 + ((rho >> 2) & 1), t_ = (rho >> 3) & 1, q_ = rho & 3;
        const int La = 36 * par + 18 * (g_ >> 1) + 8 * t_ + 4 * (g_ & 1) + q_, Lb = 288 * par + 18 * (4 * g_ + 2 * t_ + (q_ >> 1)) + 16 + (q_ & 1);
        const unsigned csw = ((lane & 7) ^ (((rho >> 1) & 3) << 1)) * 16;
        va[i] = La * 1152 + csw; vb[i] = Lb * 1152 + csw;
      }
#pragma unroll
      for (int kp = 0; kp < 9; ++kp)
#pragma unroll
        for (int i = 0; i < 4; ++i)
          glds16(wo + nt * 128 + (size_t)(kp == 8 ? 0 : 72 * kp) * 1152, kp == 8 ? vb[i] : va[i], lds0 + K2_W + par * 4096 + kp * 8192 + i * 1024);
    }
    raw_bar();
  } else {
    const int t = qb * 8 + wid;
    bf16x8 q0;
    unsigned qx;
    const unsigned vo = lane * 16;
    const int kt0 = qb * 8 - 8;
    asm volatile("global_load_dwordx4 %0, %2, %3\n\tglobal_load_dword %1, %4, %5"
                 : "=&v"(q0), "=&v"(qx)
                 : "v"(vo), "s"(ws + WS_QM + (size_t)hd * 65536 + (size_t)t * 1024), "v"((lane & 31) * 8),
                   "s"(ws + WS_QX + (size_t)(hd >> 1) * 16384 + (size_t)t * 256 + (hd & 1) * 4)
                 : "memory");
#pragma unroll
    for (int i = 0; i < 2; ++i) {
      const int lt = wid * 2 + i;
      const int kt = max(kt0 + lt, 0);
      glds16(ws + WS_KM + (size_t)hd * 65536 + (size_t)kt * 1024, vo, lds0 + K2_KM + lt * 1024);
    }
    if (wid >= 4) {
      const int k0 = max(qb * 256 - 256 + (wid - 4) * 128, 0);
      glds16(ws + WS_KX + (size_t)(hd >> 1) * 16384 + (size_t)k0 * 8, vo, lds0 + K2_KX + (wid - 4) * 1024);
    }
    const long vb0 = (long)(hd * 128 + qb * 16 - 16) * 592;
#pragma unroll
    for (int i = 0; i < 3; ++i) {
      const int pc = wid + 8 * i;
      if (pc < 19) {
        long off = vb0 + pc * 1024;
        unsigned v2 = vo;
        if (qb == 0) { off = (long)(hd * 128) * 592 + pc * 1024; v2 = (pc * 1024 + lane * 16 >= 9472) ? vo - 9472 + 65536 : vo + 65536; off -= 65536; }
        glds16(ws + WS_V + off, v2, lds0 + K2_V + pc * 1024);
      }
    }
    asm volatile("s_waitcnt vmcnt(2)\n\ts_barrier" : "+v"(q0), "+v"(qx)::"memory");
    STAMP_MARK(1, 2);
    if (qb == 0) k2_core<true>(lds, ws, hd, t, wid, lane, q0, qx);
    else k2_core<false>(lds, ws, hd, t, wid, lane, q0, qx);
    STAMP_MARK(1, 3);
  }
  wait_bar<0>();
  STAMP_MARK(1, 4);
  unsigned* cnt = (unsigned*)(ws + WS_CNT);
  if (tid == 0) __hip_atomic_fetch_add(cnt + qb * 32, 1u, __ATOMIC_RELAXED, __HIP_MEMORY_SCOPE_AGENT);
  if (!has_tile) { STAMP_END(1); return; }
  const int mi = wid >> 1, kg = wid & 1;
  const int g = lane >> 4, q = (lane & 15) >> 2, p = lane & 3;
  const int er = tid >> 3, ec = tid & 7;
  const u32x4 res = *(const u32x4*)(x + (size_t)(m0 + er) * 1152 + (nt * 64 + ec * 8) * 2);
  const int rho0 = 16 * (g >> 1) + 4 * (g & 1) + q;
  lds_ptr wb[4];
#pragma unroll
  for (int n = 0; n < 4; ++n) { wb[n] = lds + K2_W + kg * 4096 + rho0 * 128 + ((n ^ ((rho0 >> 1) & 3)) * 32) + p * 8; asm volatile("" : "+v"(wb[n])); }
  bf16x8 wf[5][4];
#pragma unroll
  for (int j = 0; j < 5; ++j)
#pragma unroll
    for (int n = 0; n < 4; ++n) wf[j][n] = cat8(lds_tr(wb[n] + j * 8192), lds_tr(wb[n] + j * 8192 + 1024));
  if (tid == 576) {
    const unsigned* c0 = cnt + (m0 >> 8) * 32;
    const unsigned* c1 = cnt + ((m0 + 79) >> 8) * 32;
    for (int spin = 0; spin < (1 << 18); ++spin) {
      const unsigned a = __hip_atomic_load(c0, __ATOMIC_RELAXED, __HIP_MEMORY_SCOPE_AGENT);
      const unsigned b = __hip_atomic_load(c1, __ATOMIC_RELAXED, __HIP_MEMORY_SCOPE_AGENT);
      if (a >= 32u && b >= 32u) break;
      __builtin_amdgcn_s_sleep(1);
    }
  }
  SB();
  raw_bar();
  SB();
  STAMP_MARK(1, 5);
  const __amdgpu_buffer_rsrc_t ors = __builtin_amdgcn_make_buffer_rsrc((void*)(ws + WS_O), 0, 128 * 18432, 0x00020000);
  const int aoff = (rb0 + mi) * 18432 + kg * 1024 + lane * 16;
  bf16x8 a[9];
#pragma unroll
  for (int i = 0; i < 9; ++i) a[i] = __builtin_bit_cast(bf16x8, __builtin_amdgcn_raw_buffer_load_b128(ors, aoff + i * 2048, 0, 16));
  SB();
  f32x4 acc[4];
#pragma unroll
  for (int n = 0; n < 4; ++n) acc[n] = f32x4{0.f, 0.f, 0.f, 0.f};
#pragma unroll
  for (int j = 0; j < 9; ++j) {
#pragma unroll
    for (int n = 0; n < 4; ++n) {
      acc[n] = MFMA16(wf[j % 5][n], a[j], acc[n]);
      if (j + 5 < 9) wf[j % 5][n] = cat8(lds_tr(wb[n] + (j + 5) * 8192),
                                         lds_tr(wb[n] + (j + 5) * 8192 + 1024));
    }
  }
  STAMP_MARK(1, 6);
  {
    lds_ptr cw = lds + kg * (80 * 272) + (mi * 16 + (lane & 15)) * 272 + 16 * g;
#pragma unroll
    for (int n = 0; n < 4; ++n) *(LDS_AS f32x4*)(cw + n * 64) = acc[n];
  }
  lds_bar();
  {
    lds_ptr cr = lds + er * 272 + ec * 32;
    f32x4 a0 = *(LDS_AS f32x4*)(cr), a1 = *(LDS_AS f32x4*)(cr + 16);
    f32x4 b0 = *(LDS_AS f32x4*)(cr + 80 * 272), b1 = *(LDS_AS f32x4*)(cr + 80 * 272 + 16);
    a0 += b0; a1 += b1;
    u32x4 o;
    o[0] = cvt_pk(a0[0] + bf_lo(res[0]), a0[1] + bf_hi(res[0]));
    o[1] = cvt_pk(a0[2] + bf_lo(res[1]), a0[3] + bf_hi(res[1]));
    o[2] = cvt_pk(a1[0] + bf_lo(res[2]), a1[1] + bf_hi(res[2]));
    o[3] = cvt_pk(a1[2] + bf_lo(res[3]), a1[3] + bf_hi(res[3]));
    const int row = m0 + er, col = nt * 64 + ec * 8;
    st16(ws + WS_H + (size_t)(row >> 4) * 18432 + (col >> 5) * 1024 + ((col >> 3) & 3) * 256 + (row & 15) * 16, o);
    float s1 = 0.f, s2 = 0.f;
#pragma unroll
    for (int j = 0; j < 4; ++j) { s1 = dot2(o[j], 0x3f803f80u, s1); s2 = dot2(o[j], o[j], s2); }
    s1 = sum8(s1); s2 = sum8(s2);
    if (ec == 0) *(f32x2*)(ws + WS_ST + (size_t)row * 80 + nt * 8) = f32x2{s1, s2};
  }
  STAMP_END(1);
}

#define P5MAP 0
constexpr int T_SLOT = 24576, T_LDS = T_SLOT * 6;

DI void down_phase(lds_ptr lds, unsigned lds0, const char* __restrict__ w, char* __restrict__ ws, char* __restrict__ out, int pn) {
  int tid = threadIdx.x;
  asm volatile("" : "+v"(tid));
  const int lane = tid & 63, wid = __builtin_amdgcn_readfirstlane(tid >> 6);
  const bool has_tile = blockIdx.x < 234;
  const int tile = has_tile ? xcd_remap(blockIdx.x, 234) : 0;
  const int mt = tile / 9, nt = tile - mt * 9;
  const int m0 = min(mt * 80, SEQ - 80), rb0 = m0 >> 4;
  unsigned* cnt = (unsigned*)(ws + WS_CNT + 1024);
  wait_bar<0>();
  STAMP_MARK(2, 6);
  if (tid == 0) __hip_atomic_fetch_add(cnt + pn * 32, 1u, __ATOMIC_RELAXED, __HIP_MEMORY_SCOPE_AGENT);
  if (!has_tile) return;
  u32x4 res[2];
  const int ec = tid & 7, col = nt * 64 + ec * 8;
#pragma unroll
  for (int i = 0; i < 2; ++i) {
    const int er = min((tid >> 3) + 64 * i, 79);
    res[i] = *(const u32x4*)(ws + WS_H + (size_t)(rb0 + (er >> 4)) * 18432 + (col >> 5) * 1024 + ((col >> 3) & 3) * 256 + (er & 15) * 16);
  }
  if (wid >= 6) {
    const int par = wid - 6;
    unsigned va[4], vb[4];
#pragma unroll
    for (int i = 0; i < 4; ++i) {
      const int rho = 8 * i + (lane >> 3);
      const int g_ = ((rho >> 4) & 1) * 2 + ((rho >> 2) & 1), t_ = (rho >> 3) & 1, q_ = rho & 3;
      const int La = 32 * par + 16 * t_ + 4 * g_ + q_, Lb = 288 * par + 144 * (g_ >> 1) + 128 + 8 * (g_ & 1) + 4 * t_ + q_;
      const unsigned csw = ((lane & 7) ^ (((rho >> 1) & 3) << 1)) * 16;
      va[i] = La * 1152 + csw; vb[i] = Lb * 1152 + csw;
    }
    const char* wsrc = w + nt * 128;
    const unsigned dst0 = lds0 + par * 4096;
    auto issue = [&](int sb, int st, int slot) {
#pragma unroll
      for (int j = 0; j < 3; ++j) {
        const int kp = st * 3 + j;
        const int base = kp == 8 ? 0 : 144 * (kp >> 1) + 64 * (kp & 1);
        const char* src = wsrc + (size_t)(sb * 576 + base) * 1152;
#pragma unroll
        for (int i = 0; i < 4; ++i) glds16(src, kp == 8 ? vb[i] : va[i], dst0 + slot * T_SLOT + j * 8192 + i * 1024);
      }
    };
    issue(0, 0, 0); issue(0, 1, 1); issue(0, 2, 2); issue(1, 0, 3); issue(1, 1, 4);
    __builtin_amdgcn_s_setprio(2);
    wait_bar<48>(); issue(1, 2, 5);
    wait_bar<48>(); issue(2, 0, 0);
    wait_bar<48>(); issue(2, 1, 1);
#pragma unroll 1
    for (int sb = 1; sb < 6; ++sb) {
      const int o = (sb & 1) * 3;
      wait_bar<48>(); issue(sb + 1, 2, 5 - o);
      wait_bar<48>(); issue(sb + 2, 0, o);
      wait_bar<48>(); issue(sb + 2, 1, o + 1);
    }
    wait_bar<48>(); issue(7, 2, 5);
    wait_bar<48>(); wait_bar<36>();
    wait_bar<24>(); wait_bar<12>(); wait_bar<0>();
  } else {
    if (wid == 5) {
      if (lane == 0) {
        const unsigned* c0 = cnt + (m0 >> 8) * 32;
        const unsigned* c1 = cnt + ((m0 + 79) >> 8) * 32;
        for (int spin = 0; spin < (1 << 18); ++spin) {
          const unsigned a = __hip_atomic_load(c0, __ATOMIC_RELAXED, __HIP_MEMORY_SCOPE_AGENT);
          const unsigned b = __hip_atomic_load(c1, __ATOMIC_RELAXED, __HIP_MEMORY_SCOPE_AGENT);
          if (a >= 32u && b >= 32u) break;
          __builtin_amdgcn_s_sleep(1);
        }
      }
      SB();
#pragma unroll 1
      for (int i = 0; i < 24; ++i) raw_bar();
    } else {
      const int g = lane >> 4, q = (lane & 15) >> 2, p = lane & 3;
      const int rho0 = 16 * (g >> 1) + 4 * (g & 1) + q;
      lds_ptr wb[4], wp[4], wq[4], wr[4];
#pragma unroll
      for (int n = 0; n < 4; ++n) {
        wb[n] = lds + rho0 * 128 + ((n ^ ((rho0 >> 1) & 3)) * 32) + p * 8;
        wp[n] = wb[n] + 2 * T_SLOT; wq[n] = wb[n] + 3 * T_SLOT; wr[n] = wb[n] + 5 * T_SLOT;
        asm volatile("" : "+v"(wp[n]), "+v"(wq[n]), "+v"(wr[n]));
      }
      f32x4 acc[4];
#pragma unroll
      for (int n = 0; n < 4; ++n) acc[n] = f32x4{0.f, 0.f, 0.f, 0.f};
      const __amdgpu_buffer_rsrc_t hrs = __builtin_amdgcn_make_buffer_rsrc((void*)(ws + WS_HID), 0, 128 * 144 * 1024, 0x00020000);
      const int aoff = (rb0 + wid) * (144 * 1024) + lane * 16;
      bf16x8 a[2][18];
      raw_bar();
      SB();
      STAMP_MARK(2, 7);
#pragma unroll
      for (int i = 0; i < 36; ++i) { a[i / 18][i % 18] = __builtin_bit_cast(bf16x8, __builtin_amdgcn_raw_buffer_load_b128(hrs, aoff + (i & 3) * 1024, (i >> 2) * 4096, 16)); SB(); }
      s16x4 wl[8], wh[8];
#pragma unroll
      for (int i = 0; i < 7; ++i) { wl[i] = lds_tr(wb[i & 3] + (i >> 2) * 4096); wh[i] = lds_tr(wb[i & 3] + (i >> 2) * 4096 + 1024); }
      SB();
      auto sblock = [&](int sb, auto PAR, auto MORE, auto LOAD) {
        constexpr bool more = MORE, load = LOAD;
        constexpr int par = PAR, o = par * 3 * T_SLOT, on = 3 * T_SLOT - o;
        lds_ptr bc[4], bc2[4], bn[4];
#pragma unroll
        for (int n = 0; n < 4; ++n) { bc[n] = par ? wq[n] : wb[n]; bc2[n] = par ? wr[n] : wp[n]; bn[n] = par ? wb[n] : wq[n]; }
        for_seq<72>([&](auto II) {
          constexpr int i = II, kk = i >> 2, n = i & 3, ni = i + 7, nk = ni >> 2, nn = ni & 3;
          acc[n] = MFMA16(cat8(wl[i & 7], wh[i & 7]), a[par][kk], acc[n]);
          SB();
          if constexpr (ni % 24 == 0 && (more || ni < 72)) { lds_bar(); SB(); }
          if constexpr (more || ni < 72) {
            lds_ptr np = nk >= 18 ? bn[nn] + (nk - 18) * 4096 : (nk >= 12 ? bc2[nn] + (nk - 12) * 4096 : bc[nn] + nk * 4096);
            wl[ni & 7] = lds_tr(np); wh[ni & 7] = lds_tr(np + 1024);
            SB();
          }
          if constexpr (load && n == 3) {
            a[par][kk] = __builtin_bit_cast(bf16x8, __builtin_amdgcn_raw_buffer_load_b128(hrs, aoff + (kk & 3) * 1024, (sb + 2) * 18432 + (kk >> 2) * 4096, 16));
            SB();
          }
        });
      };
      using I0 = std::integral_constant<int, 0>; using I1 = std::integral_constant<int, 1>;
#pragma unroll 1
      for (int sb = 0; sb < 6; sb += 2) { sblock(sb, I0{}, std::true_type{}, std::true_type{}); sblock(sb + 1, I1{}, std::true_type{}, std::true_type{}); }
      sblock(6, I0{}, std::true_type{}, std::false_type{});
      sblock(7, I1{}, std::false_type{}, std::false_type{});
      lds_ptr cw = lds + (wid * 16 + (lane & 15)) * 272 + 16 * g;
#pragma unroll
      for (int n = 0; n < 4; ++n) *(LDS_AS f32x4*)(cw + n * 64) = acc[n];
    }
  }
  lds_bar();
#pragma unroll
  for (int i = 0; i < 2; ++i) {
    const int er = (tid >> 3) + 64 * i;
    if (er < 80) {
      lds_ptr cr = lds + er * 272 + ec * 32;
      const f32x4 a0 = *(LDS_AS f32x4*)(cr), a1 = *(LDS_AS f32x4*)(cr + 16);
      u32x4 o;
      o[0] = cvt_pk(a0[0] + bf_lo(res[i][0]), a0[1] + bf_hi(res[i][0]));
      o[1] = cvt_pk(a0[2] + bf_lo(res[i][1]), a0[3] + bf_hi(res[i][1]));
      o[2] = cvt_pk(a1[0] + bf_lo(res[i][2]), a1[1] + bf_hi(res[i][2]));
      o[3] = cvt_pk(a1[2] + bf_lo(res[i][3]), a1[3] + bf_hi(res[i][3]));
      *(u32x4*)(out + (size_t)(m0 + er) * 1152 + col * 2) = o;
    }
  }
}

constexpr int K4_SLOT = 18432, K4_ST = K4_SLOT * 8, K4_CS = K4_ST + 2048, K4_PF = K4_CS + 2048, K4_CP = K4_PF + 1024, K4_LDS = K4_CP + 3 * 1152 + 256;

constexpr int k4_frag(int i) { return i < 16 ? ((i >> 2) * 2 + (i & 1) + ((i & 2) ? 9 : 0)) : (i == 16 ? 8 : 17); }
constexpr int k4_foff(int f) { return (f >= 9 ? 9216 + (f - 9) * 32 : f * 32); }
constexpr int K4_T0 = 13 * 18;
constexpr int k4_slab(int G) { return G < K4_T0 ? G / 18 : (G - K4_T0 < 80 ? 13 + ((G - K4_T0) % 20) / 4 : 13 + (G - K4_T0 - 80) / 2); }
constexpr int k4_pos(int G) { return G < K4_T0 ? G % 18 : (G - K4_T0 < 80 ? ((G - K4_T0) / 20) * 4 + ((G - K4_T0) & 3) : 16 + ((G - K4_T0) & 1)); }
constexpr int k4_op(int G) {
  const int s = G / 18, i = G % 18;
  if (s > 12) return 0;
  if (s < 3) {
    const int pos[10] = {0, 2, 4, 6, 7, 9, 11, 13, 15, 16};
    for (int k = 0; k < 10; ++k) if (pos[k] == i) return 1 + (2 * s + 2 + k / 5) * 8 + k % 5;
    return 0;
  }
  if (s == 3) {
    const int pos[8] = {0, 2, 4, 6, 8, 11, 14, 16};
    for (int k = 0; k < 8; ++k) if (pos[k] == i) return k < 5 ? 1 + 8 * 8 + k : 1 + 9 * 8 + (k - 3);
    return 0;
  }
  if (i == 1) return 1 + (s + 5) * 8;
  if (i == 4) return 1 + (s + 5) * 8 + 1;
  if (s + 6 > 17) return 0;
  if (i == 8) return 1 + (s + 6) * 8 + 2;
  if (i == 13) return 1 + (s + 6) * 8 + 3;
  if (i == 16) return 1 + (s + 6) * 8 + 4;
  return 0;
}
constexpr int k4_allow(int G, int need) {
  int n = (need == 0) ? 4 : 0;
  for (int g = 0; g < G; ++g) {
    const int op = k4_op(g);
    if (op == 0) continue;
    const int sl = (op - 1) >> 3, kind = (op - 1) & 7;
    if (sl <= need) n = 0;
    else if (kind != 4) ++n;
  }
  return n;
}
constexpr int k4_need(int G) {
  if (G + 5 >= 324 || G == 0) return 0;
  int mx = 0;
  for (int g = 0; g < G + 5; ++g) mx = k4_slab(g) > mx ? k4_slab(g) : mx;
  return k4_slab(G + 5) > mx ? k4_slab(G + 5) : 0;
}

__global__ __launch_bounds__(512, 2) void k4_gateup(const char* __restrict__ wgate, const char* __restrict__ wup, char* __restrict__ ws,
                                                    const char* __restrict__ wd, char* __restrict__ out) {
  extern __shared__ __attribute__((aligned(16))) char smem[];
  STAMP_BEGIN
  lds_ptr lds = (lds_ptr)smem;
  const unsigned lds0 = (unsigned)(uintptr_t)lds;
  const int tid = threadIdx.x, lane = tid & 63, wid = __builtin_amdgcn_readfirstlane(tid >> 6);
  const int xcd = blockIdx.x & 7, idx = blockIdx.x >> 3;
  const int pn = 4 * (xcd >> 2) + (idx & 3), jt = 8 * (xcd & 3) + (idx >> 2);
  const int wm = wid & 3, wn = wid >> 2;
  const char* asrc = ws + WS_H + (size_t)(pn * 16 + 2 * wid) * 18432;
  const unsigned avo = lane * 16, avo2 = lane * 16 + 18432;
  const char* wsrc = (wn ? wup : wgate) + jt * 288;
  unsigned wvo[3];
#pragma unroll
  for (int i = 0; i < 3; ++i) {
    const int pc = i < 2 ? 2 * wm + i : 8;
    const int o = pc * 1024 + lane * 16;
    const int rho = o / 288, u = (o - rho * 288) >> 4;
    const int gk = ((rho >> 4) & 1) * 2 + ((rho >> 2) & 1), tk = (rho >> 3) & 1, qk = rho & 3;
    wvo[i] = (8 * gk + 4 * tk + qk) * 9216 + u * 16;
  }
  const unsigned wdst = lds0 + wn * 9216;
  auto issue_w = [&](int s, int i) {
    const unsigned so = (s & 7) * K4_SLOT;
    const char* wp = wsrc + (size_t)s * 32 * 9216;
    if (i < 2) glds16(wp, wvo[i], wdst + so + wm * 2048 + i * 1024);
    else glds16_if0(wm, wp, wvo[2], wdst + so + 8192);
  };
  bf16x8 A[6][2];
  auto issue_a = [&](int s, bf16x8& dst, int m) {
    asm volatile("global_load_dwordx4 %0, %1, %2" : "=v"(dst) : "v"(m ? avo2 : avo), "s"(asrc + s * 1024) : "memory");
  };
  f32x4 sp0, sp1, sp2, sp3;
  f32x2 sp4;
  asm volatile("global_load_dwordx4 %0, %5, %6\n\tglobal_load_dwordx4 %1, %5, %6 offset:16\n\tglobal_load_dwordx4 %2, %5, %6 offset:32\n\t"
               "global_load_dwordx4 %3, %5, %6 offset:48\n\tglobal_load_dwordx2 %4, %5, %6 offset:64"
               : "=&v"(sp0), "=&v"(sp1), "=&v"(sp2), "=&v"(sp3), "=&v"(sp4)
               : "v"((lane & 31) * 80), "s"(ws + WS_ST + (size_t)(pn * 256 + wid * 32) * 80) : "memory");
  if (wm < 3 && lane < 36) glds16(ws + WS_CSP + (size_t)(wm * 9216 + wn * FF + jt * 144) * 4, lane * 16, lds0 + K4_CP + wm * 1152 + wn * 576);
  for_seq<2>([&](auto S) { issue_w(S, 0); issue_w(S, 1); issue_w(S, 2); issue_a(S, A[S][0], 0); issue_a(S, A[S][1], 1); });
  {
    asm volatile("s_waitcnt vmcnt(8)" : "+v"(sp0), "+v"(sp1), "+v"(sp2), "+v"(sp3), "+v"(sp4)::"memory");
    const float s1 = sp0[0] + sp0[2] + sp1[0] + sp1[2] + sp2[0] + sp2[2] + sp3[0] + sp3[2] + sp4[0];
    const float s2 = sp0[1] + sp0[3] + sp1[1] + sp1[3] + sp2[1] + sp2[3] + sp3[1] + sp3[3] + sp4[1];
    const float mean = s1 * (1.f / 576.f);
    const float var = fmaxf(s2 * (1.f / 576.f) - mean * mean, 0.f);
    *(LDS_AS f32x2*)(lds + K4_ST + (wid * 32 + (lane & 31)) * 8) = f32x2{-mean, __builtin_amdgcn_rsqf(var + LN_EPS)};
    SB();
  }
  const int g = lane >> 4, q = (lane & 15) >> 2, p = lane & 3, r = lane & 15;
  lds_ptr wb = lds + (16 * (g >> 1) + 4 * (g & 1) + q) * 288 + p * 8;
  lds_ptr wbs[3] = {wb, wb + 3 * K4_SLOT, wb + 6 * K4_SLOT};
  asm volatile("" : "+v"(wbs[1]), "+v"(wbs[2]));
  lds_ptr csb = lds + K4_CS + 16 * g;
  asm volatile("" : "+v"(csb));
  f32x4 acc[2][18];
  asm volatile("s_waitcnt vmcnt(%2)\n\ts_barrier" : "+v"(A[0][0]), "+v"(A[0][1]) : "n"(k4_allow(0, 0)) : "memory");
  STAMP_MARK(2, 2);
  STAMP_CYC0
  s16x4 Bl[6], Bh[6];
  for_seq<5>([&](auto I) { Bl[I] = lds_tr(wb + k4_foff(k4_frag(I))); Bh[I] = lds_tr(wb + k4_foff(k4_frag(I)) + 2304); });
  SB();
  const float L2E = 1.4426950408889634f;
  char* hb = ws + WS_HID + (size_t)(pn * 16 + wid * 2) * (144 * 1024) + (size_t)(jt >> 2) * 18432;
  f32x2 st[2];
  float c1[2];
  auto glu = [&](float ag, float au, float cg, float cu, int m) -> float {
    const float tg = fmaf(st[m][0], cg, ag), tu = fmaf(st[m][0], cu, au);
    const float e = __builtin_amdgcn_exp2f(tg * c1[m]);
    return tu * st[m][1] * __builtin_amdgcn_rcpf(1.f + e);
  };
  f32x4 cg[2], cu[2];
  u32x4 ov;
  auto slice = [&](auto QQ, auto KK) {
    constexpr int Q = QQ, k = KK, m = k >> 2, d = k & 3, h = d >> 1, e = (d & 1) * 2;
    const f32x4 ag = acc[m][2 * Q + h], au = acc[m][9 + 2 * Q + h];
    ov[d] = cvt_pk(glu(ag[e], au[e], cg[h][e], cu[h][e], m), glu(ag[e + 1], au[e + 1], cg[h][e + 1], cu[h][e + 1], m));
    if constexpr (d == 3) st16(hb + (size_t)m * (144 * 1024) + (4 * (jt & 3) + Q) * 1024 + lane * 16, ov);
    else asm volatile("" : "+v"(ov));
  };
  for_seq<18>([&](auto GH) { for_seq<18>([&](auto GL) {
    constexpr int G = GH * 18 + GL, sl = k4_slab(G), f = k4_frag(k4_pos(G)), rb = G % 6, nb = (G + 5) % 6, NG = G + 5;
    const bf16x8 b = cat8(Bl[rb], Bh[rb]);
    constexpr int need = k4_need(G);
    if constexpr (need != 0) {
      asm volatile("s_waitcnt vmcnt(%2)\n\ts_barrier" : "+v"(A[need % 6][0]), "+v"(A[need % 6][1]) : "n"(k4_allow(G, need)) : "memory");
      SB();
    }
    if constexpr (sl == 0) mfma16_zero(acc[0][f], b, A[0][0]); else mfma16_acc(acc[0][f], b, A[sl % 6][0]);
    SB();
    if constexpr (NG < 324) { Bl[nb] = lds_tr(wbs[(k4_slab(NG) & 7) / 3] + ((k4_slab(NG) & 7) % 3) * K4_SLOT + k4_foff(k4_frag(k4_pos(NG)))); SB(); }
    if constexpr (sl == 0) mfma16_zero(acc[1][f], b, A[0][1]); else mfma16_acc(acc[1][f], b, A[sl % 6][1]);
    SB();
    if constexpr (NG < 324) { Bh[nb] = lds_tr(wbs[(k4_slab(NG) & 7) / 3] + ((k4_slab(NG) & 7) % 3) * K4_SLOT + k4_foff(k4_frag(k4_pos(NG))) + 2304); SB(); }
    constexpr int op = k4_op(G);
    if constexpr (op != 0) {
      constexpr int os = (op - 1) >> 3, kind = (op - 1) & 7;
      if constexpr (kind < 2) issue_a(os, A[os % 6][kind], kind);
      else issue_w(os, kind - 2);
      SB();
    }
    if constexpr (G % 18 == 10 && (G / 18 == 5 || G / 18 == 7 || G / 18 == 9)) {
      constexpr int k = (G / 18 - 5) / 2;
      touch_if0((int)(blockIdx.x / 216) + (wid ^ (k + 1)), wd + (size_t)(blockIdx.x * 3 + k) * 8192, avo * 8, lds0 + K4_PF + k * 256);
      SB();
    }
    if constexpr (G == 60) {
      lds_ptr cpp = lds + K4_CP + wid * 144 + lane * 4;
      const float t = *(LDS_AS float*)(cpp) + *(LDS_AS float*)(cpp + 1152) + *(LDS_AS float*)(cpp + 2304);
      *(LDS_AS float*)(lds + K4_CS + wid * 144 + min(lane, 36) * 4 + (lane >= 36 ? 1152 : 0)) = t;
      asm volatile("s_waitcnt lgkmcnt(0)" ::: "memory");
      SB();
    }
    if constexpr (G == K4_T0) {
      STAMP_MARK(2, 3);
      STAMP_CYC(2, 4);
#pragma unroll
      for (int m = 0; m < 2; ++m) { st[m] = *(LDS_AS f32x2*)(lds + K4_ST + (wid * 32 + m * 16 + r) * 8); c1[m] = -L2E * st[m][1]; }
      SB();
    }
    if constexpr (G >= K4_T0) {
      constexpr int t = G - K4_T0, u = t % 20;
      if constexpr (t < 80 && u == 18) {
#pragma unroll
        for (int h = 0; h < 2; ++h) {
          cg[h] = *(LDS_AS f32x4*)(csb + (2 * (t / 20) + h) * 64);
          cu[h] = *(LDS_AS f32x4*)(csb + 576 + (2 * (t / 20) + h) * 64);
        }
        SB();
      }
      if constexpr (t >= 20 && t < 80 && u >= 1 && u < 17 && (u & 1)) { slice(std::integral_constant<int, t / 20 - 1>{}, std::integral_constant<int, u / 2>{}); SB(); }
      if constexpr (t >= 81 && t < 89) { slice(std::integral_constant<int, 3>{}, std::integral_constant<int, t - 81>{}); SB(); }
    }
  }); });
  {
    asm volatile("s_nop 7\n\ts_nop 1" : "+v"(acc[0][8]), "+v"(acc[1][8]), "+v"(acc[0][17]), "+v"(acc[1][17]));
    const f32x4 cg8 = *(LDS_AS f32x4*)(csb + 512);
    const f32x4 cu8 = *(LDS_AS f32x4*)(csb + 576 + 512);
#pragma unroll
    for (int m = 0; m < 2; ++m) {
      const f32x4 ag = acc[m][8], au = acc[m][17];
      u32x2 o = {cvt_pk(glu(ag[0], au[0], cg8[0], cu8[0], m), glu(ag[1], au[1], cg8[1], cu8[1], m)),
                 cvt_pk(glu(ag[2], au[2], cg8[2], cu8[2], m), glu(ag[3], au[3], cg8[3], cu8[3], m))};
      st8wt(hb + (size_t)m * (144 * 1024) + (16 + ((jt >> 1) & 1)) * 1024 + (2 * (jt & 1) + (g >> 1)) * 256 + r * 16 + (g & 1) * 8, o);
    }
  }
  STAMP_MARK(2, 5);
  down_phase(lds, lds0, wd, ws, out, pn);
  STAMP_END(2);
}

extern "C" void kernel_launch(void* const* d_in, const int* in_sizes, int n_in, void* d_out, int out_size, void* d_ws, size_t ws_size,
                              hipStream_t stream) {
  const char* x = (const char*)d_in[0];
  const char* wqkv = (const char*)d_in[1];
  const char* wo = (const char*)d_in[2];
  const char* wg = (const char*)d_in[4];
  const char* wu = (const char*)d_in[5];
  const char* wd = (const char*)d_in[6];
  char* ws = (char*)d_ws;
  (void)hipFuncSetAttribute((const void*)k1_ln_qkv, hipFuncAttributeMaxDynamicSharedMemorySize, K1_LDS);
  (void)hipFuncSetAttribute((const void*)k2_attn, hipFuncAttributeMaxDynamicSharedMemorySize, K2_LDS);
  (void)hipFuncSetAttribute((const void*)k4_gateup, hipFuncAttributeMaxDynamicSharedMemorySize, K4_LDS);
  k1_ln_qkv<<<234, 1024, K1_LDS, stream>>>(x, wqkv, ws, wo);
  k2_attn<<<256, 768, K2_LDS, stream>>>(ws, wg, wu, wo, x);
  k4_gateup<<<256, 512, K4_LDS, stream>>>(wg, wu, ws, wd, (char*)d_out);
}
```

```cpp
#include <hip/hip_runtime.h>
#include <hip/hip_bf16.h>
#include <stdint.h>
#include <utility>
#include <type_traits>

typedef __attribute__((ext_vector_type(8))) short bf16x8;
typedef __attribute__((ext_vector_type(4))) short s16x4;
typedef __attribute__((ext_vector_type(4))) float f32x4;
typedef __attribute__((ext_vector_type(16))) float f32x16;
typedef __attribute__((ext_vector_type(4))) unsigned u32x4;
typedef __attribute__((ext_vector_type(2))) unsigned u32x2;
typedef __attribute__((ext_vector_type(2))) __bf16 bf16x2_t;
typedef __attribute__((ext_vector_type(2))) float f32x2;
#define LDS_AS __attribute__((address_space(3)))
typedef LDS_AS char* lds_ptr;
#define DI __device__ __forceinline__

constexpr int SEQ = 2048, DM = 576, NH = 32, FF = 4608;
constexpr float LN_EPS = 1e-5f;
constexpr float QSCALE = 0.23570226039551584f * 1.4426950408889634f;

constexpr size_t WS_QM = 0;
constexpr size_t WS_KM = WS_QM + (size_t)32 * 64 * 1024;
constexpr size_t WS_QX = WS_KM + (size_t)32 * 64 * 1024;
constexpr size_t WS_KX = WS_QX + (size_t)16 * 2048 * 8;
constexpr size_t WS_V = WS_KX + (size_t)16 * 2048 * 8;
constexpr size_t WS_O = WS_V + (size_t)32 * 128 * 592 + 1024;
constexpr size_t WS_H = WS_O + (size_t)128 * 18 * 1024;
constexpr size_t WS_ST = WS_H + (size_t)128 * 18 * 1024;
constexpr size_t WS_CSP = WS_ST + (size_t)2048 * 80;
constexpr size_t WS_CS = WS_CSP + (size_t)8 * 9216 * 4;
constexpr size_t WS_HID = WS_CS + (size_t)9216 * 4;
constexpr size_t WS_CNT = WS_HID + (size_t)128 * 144 * 1024;
constexpr size_t WS_END = WS_CNT + 2048;
static_assert(WS_O % 1024 == 0 && WS_H % 1024 == 0 && WS_ST % 256 == 0 && WS_CSP % 256 == 0 && WS_HID % 256 == 0, "align");

#define STAMP_BEGIN
#define STAMP_MARK(k, i)
#define STAMP_MARKW(k, i, T)
#define STAMP_ENDW(k, T)
#define STAMP_CYC0
#define STAMP_CYC(k, i)
#define STAMP_END(k)

DI unsigned cvt_pk(float lo, float hi) {
  f32x2 v = {lo, hi};
  bf16x2_t r = __builtin_convertvector(v, bf16x2_t);
  return __builtin_bit_cast(unsigned, r);
}
DI float bf_lo(unsigned u) { return __builtin_bit_cast(float, u << 16); }
DI float bf_hi(unsigned u) { return __builtin_bit_cast(float, u & 0xffff0000u); }
DI float dot2(unsigned a, unsigned b, float c) {
  return __builtin_amdgcn_fdot2_f32_bf16(__builtin_bit_cast(bf16x2_t, a), __builtin_bit_cast(bf16x2_t, b), c, false);
}
template <int CTRL> DI float dpp_add(float v) {
  int t = __builtin_amdgcn_update_dpp(0, __builtin_bit_cast(int, v), CTRL, 0xf, 0xf, true);
  return v + __builtin_bit_cast(float, t);
}
DI float sum8(float v) {
  v = dpp_add<0xB1>(v);
  v = dpp_add<0x4E>(v);
  v = dpp_add<0x141>(v);
  return v;
}
DI void glds16(const void* sbase, unsigned voff, unsigned lds_dst) {
  asm volatile("s_mov_b32 m0, %2\n\ts_nop 3\n\tglobal_load_lds_dwordx4 %0, %1" ::"v"(voff), "s"(sbase), "s"(lds_dst) : "memory");
}
#define ST_AUX " sc1"
DI void st16(void* p, u32x4 v) { asm volatile("global_store_dwordx4 %0, %1, off" ST_AUX "\n\ts_nop 1" ::"v"(p), "v"(v) : "memory"); }
DI void st8(void* p, u32x2 v) { *(u32x2*)p = v; }
DI void st8wt(void* p, u32x2 v) { asm volatile("global_store_dwordx2 %0, %1, off sc1" ::"v"(p), "v"(v) : "memory"); }
DI void st4(void* p, unsigned v) { asm volatile("global_store_dword %0, %1, off sc1" ::"v"(p), "v"(v) : "memory"); }
DI void glds16_if0(int c, const void* sbase, unsigned voff, unsigned lds_dst) {
  asm volatile("s_cmp_lg_u32 %3, 0\n\ts_cbranch_scc1 1f\n\ts_mov_b32 m0, %2\n\ts_nop 1\n\tglobal_load_lds_dwordx4 %0, %1\n1:"
               ::"v"(voff), "s"(sbase), "s"(lds_dst), "s"(c) : "memory", "scc");
}
DI void touch_if0(int c, const void* sbase, unsigned voff, unsigned lds_dst) {
  asm volatile("s_cmp_lg_u32 %3, 0\n\ts_cbranch_scc1 1f\n\ts_mov_b32 m0, %2\n\ts_nop 1\n\tglobal_load_lds_dword %0, %1\n1:"
               ::"v"(voff), "s"(sbase), "s"(lds_dst), "s"(c) : "memory", "scc");
}
DI void touch_lines(const char* base, unsigned voff, unsigned lds_dummy) {
  asm volatile("s_mov_b32 m0, %2\n\ts_nop 3\n\tglobal_load_lds_dword %0, %1" ::"v"(voff), "s"(base), "s"(lds_dummy) : "memory");
}
#define CODE_TOUCH(SYM, BYTES, LDS_DUMMY)                                                                          \
  do {                                                                                                             \
    const char* cp_;                                                                                               \
    asm volatile("s_getpc_b64 s[20:21]\n\ts_add_u32 s20, s20, " SYM "@rel32@lo+4\n\ts_addc_u32 s21, s21, " SYM     \
                 "@rel32@hi+12\n\ts_mov_b64 %0, s[20:21]" : "=s"(cp_)::"s20", "s21", "scc");                      \
    constexpr int nl_ = (BYTES) / 128, per_ = (nl_ + 28) / 29;                                                      \
    const int ln_ = (blockIdx.x >> 3) * per_ + lane;                                                               \
    if (lane < per_ && ln_ < nl_) touch_lines(cp_, ln_ * 128, LDS_DUMMY);                                          \
  } while (0)
#define SYM_K1 "_Z9k1_ln_qkvPKcS0_PcS0_S0_S0_S0_S0_"
#define SYM_K2 "_Z7k2_attnPcPKcS1_S1_S1_"
#define SYM_K4 "_Z9k4_gateupPKcS0_PcS0_S1_"
constexpr int LEN_K1 = 24000, LEN_K2 = 16200, LEN_K4 = 33000;
template <int N> DI void wait_bar() { asm volatile("s_waitcnt vmcnt(%0) lgkmcnt(0)\n\ts_barrier" ::"n"(N) : "memory"); }
template <int N> DI void wait_vm() { asm volatile("s_waitcnt vmcnt(%0)" ::"n"(N) : "memory"); }
DI void raw_bar() { asm volatile("s_barrier" ::: "memory"); }
DI void lds_bar() { asm volatile("s_waitcnt lgkmcnt(0)\n\ts_barrier" ::: "memory"); }
DI bf16x8 lds_r128(lds_ptr p) { return *(LDS_AS const bf16x8*)p; }
typedef short v4i16_t __attribute__((ext_vector_type(4)));
DI s16x4 lds_tr(lds_ptr p) { return __builtin_amdgcn_ds_read_tr16_b64_v4i16((LDS_AS v4i16_t*)p); }
DI bf16x8 cat8(s16x4 a, s16x4 b) { return __builtin_shufflevector(a, b, 0, 1, 2, 3, 4, 5, 6, 7); }
#define SB() __builtin_amdgcn_sched_barrier(0)
#define MFMA16(a, b, c) __builtin_amdgcn_mfma_f32_16x16x32_bf16(a, b, c, 0, 0, 0)
#define MFMA32(a, b, c) __builtin_amdgcn_mfma_f32_32x32x16_bf16(a, b, c, 0, 0, 0)
DI void mfma16_acc(f32x4& c, bf16x8 a, bf16x8 b) { asm volatile("v_mfma_f32_16x16x32_bf16 %0, %1, %2, %0" : "+v"(c) : "v"(a), "v"(b)); }
DI void mfma16_zero(f32x4& c, bf16x8 a, bf16x8 b) { asm volatile("v_mfma_f32_16x16x32_bf16 %0, %1, %2, 0" : "=&v"(c) : "v"(a), "v"(b)); }
template <class F, int... I> DI void for_seq_impl(F&& f, std::integer_sequence<int, I...>) { (f(std::integral_constant<int, I>{}), ...); }
template <int N, class F> DI void for_seq(F&& f) { for_seq_impl(f, std::make_integer_sequence<int, N>{}); }
DI int xcd_remap(int b, int nwg) {
  int q = nwg >> 3, r = nwg & 7, x = b & 7, o = b >> 3;
  return (x < r ? x * (q + 1) : r * (q + 1) + (x - r) * q) + o;
}

constexpr int K1_X = 0, K1_W = 40960, K1_SLOT = 12288, K1_ST = K1_W + K1_SLOT * 9, K1_CS = K1_W, K1_PF = K1_ST + 1024, K1_LDS = K1_PF + 1024;
constexpr int cmin(int a, int b) { return a < b ? a : b; }
constexpr int k1_nw(int kk) { return cmin(18, cmin(3 + 2 * kk, kk + 7)); }
constexpr int k1_nx(int kk) { return cmin(18, cmin(3 + 2 * kk, kk + 5)); }

template <bool ISV, bool HI>
DI void k1_consumer(lds_ptr lds, int cb, int lane, char* ws, int ct, int m0) {
  const int g = lane >> 4, q = (lane & 15) >> 2, p = lane & 3, r = lane & 15;
  constexpr int NM = HI ? 2 : 3, MB = HI ? 3 : 0;
  lds_ptr a0 = lds + K1_X + MB * 2048 + (r >> 3) * 1024 + (r & 7) * 128 + ((g ^ (r >> 1)) * 16);
  lds_ptr a1 = lds + K1_X + MB * 2048 + (r >> 3) * 1024 + (r & 7) * 128 + (((4 + g) ^ (r >> 1)) * 16);
  const int rho0 = 16 * (g >> 1) + 4 * (g & 1) + q;
  lds_ptr wb = lds + K1_W + rho0 * 128 + ((cb ^ ((rho0 >> 1) & 3)) * 32) + p * 8;
  lds_ptr wb5 = wb + 5 * K1_SLOT;
  asm volatile("" : "+v"(wb), "+v"(wb5));
  const u32x4 onesu = {0x3f803f80u, 0x3f803f80u, 0x3f803f80u, 0x3f803f80u};
  const bf16x8 ones = __builtin_bit_cast(bf16x8, onesu);
  f32x4 acc[3][3];
#pragma unroll
  for (int m = 0; m < 3; ++m)
#pragma unroll
    for (int j = 0; j < 3; ++j) acc[m][j] = f32x4{0.f, 0.f, 0.f, 0.f};
  bf16x8 fA[2][3];
  s16x4 wl[2][3], wh[2][3];
  auto rd1 = [&](auto KK, auto II) {
    constexpr int kk = KK, i = II, bf = kk & 1, sl = kk % 9;
    if constexpr (i >= 2 && i <= 4) {
      if constexpr (i - 2 < NM) fA[bf][i - 2] = lds_r128(((kk & 1) ? a1 : a0) + ((kk >> 1) & 3) * 10240 + (i - 2) * 2048);
    } else {
      constexpr int j = i < 2 ? 0 : (i - 3) / 2, hh = i < 2 ? i : (i - 3) % 2;
      lds_ptr bp = (sl >= 5 ? wb5 + (sl - 5) * K1_SLOT : wb + sl * K1_SLOT) + j * 4096 + hh * 1024;
      if constexpr (hh == 0) wl[bf][j] = lds_tr(bp); else wh[bf][j] = lds_tr(bp);
    }
  };
  lds_bar();
  STAMP_MARKW(0, 2, 512);
  for_seq<9>([&](auto I) { rd1(std::integral_constant<int, 0>{}, I); });
  SB();
  for_seq<18>([&](auto KK) {
    constexpr int kk = KK, bf = kk & 1;
    if constexpr (kk < 17) { raw_bar(); SB(); }
    for_seq<9>([&](auto II) {
      constexpr int i = II, j = i / 3, m = i % 3;
      const bf16x8 wv = cat8(wl[bf][j], wh[bf][j]);
      const bf16x8 av = (m < NM) ? fA[bf][m] : ones;
      acc[m][j] = ISV ? MFMA16(av, wv, acc[m][j]) : MFMA16(wv, av, acc[m][j]);
      SB();
      if constexpr (kk < 17) { rd1(std::integral_constant<int, kk + 1>{}, II); SB(); }
    });
  });
  STAMP_MARKW(0, 4, 512);
  lds_ptr cx = lds + K1_CS + cb * 3072 + lane * 16;
  if (HI) {
#pragma unroll
    for (int j = 0; j < 3; ++j) *(LDS_AS f32x4*)(cx + j * 1024) = acc[2][j];
  }
  lds_bar();
  STAMP_MARKW(0, 5, 512);
  f32x4 cs[3];
#pragma unroll
  for (int j = 0; j < 3; ++j) cs[j] = HI ? acc[2][j] : *(LDS_AS f32x4*)(cx + j * 1024);
  if (ISV) {
    f32x4 st[NM][2];
#pragma unroll
    for (int m = 0; m < NM; ++m) {
      st[m][0] = *(LDS_AS f32x4*)(lds + K1_ST + ((MB + m) * 16 + 4 * g) * 8);
      st[m][1] = *(LDS_AS f32x4*)(lds + K1_ST + ((MB + m) * 16 + 4 * g) * 8 + 16);
    }
#pragma unroll
    for (int j = 0; j < 3; ++j) {
      const int c = (ct - 6) * 192 + j * 64 + cb * 16 + r;
      const int head = c / 18, d = c - head * 18;
      char* vb = ws + WS_V + (size_t)(head * 128 + (m0 >> 4) + MB) * 592;
      char* vp = vb + (g & 1) * 288 + d * 16 + (g >> 1) * 8;
      const float c0 = cs[j][0];
#pragma unroll
      for (int m = 0; m < NM; ++m) {
        u32x2 o = {cvt_pk(fmaf(st[m][0][0], c0, acc[m][j][0]) * st[m][0][1], fmaf(st[m][0][2], c0, acc[m][j][1]) * st[m][0][3]),
                   cvt_pk(fmaf(st[m][1][0], c0, acc[m][j][2]) * st[m][1][1], fmaf(st[m][1][2], c0, acc[m][j][3]) * st[m][1][3])};
        st8(vp + m * 592, o);
      }
      if (d == 17 && g < NM) *(u32x4*)(vb + g * 592 + 576) = onesu;
    }
  } else {
    const bool isq = ct < 3;
    const float sc = isq ? QSCALE : 1.f;
    f32x2 st[NM];
#pragma unroll
    for (int m = 0; m < NM; ++m) { st[m] = *(LDS_AS f32x2*)(lds + K1_ST + ((MB + m) * 16 + r) * 8); st[m][1] *= sc; }
#pragma unroll
    for (int j = 0; j < 3; ++j) {
      const int c = (isq ? ct : ct - 3) * 192 + j * 64 + cb * 16 + 4 * g;
      const int pair = c / 36, qi = (c - pair * 36) >> 2;
      const int head = 2 * pair + (qi > 4 ? 1 : 0), a = qi > 4 ? qi - 5 : qi;
      char* mainp = ws + (isq ? WS_QM : WS_KM) + (size_t)head * 65536 + (a >> 1) * 512 + (a & 1) * 8;
      char* xp = ws + (isq ? WS_QX : WS_KX) + (size_t)pair * 16384;
#pragma unroll
      for (int m = 0; m < NM; ++m) {
        const int rw = m0 + (MB + m) * 16 + r;
        u32x2 o = {cvt_pk(fmaf(st[m][0], cs[j][0], acc[m][j][0]) * st[m][1], fmaf(st[m][0], cs[j][1], acc[m][j][1]) * st[m][1]),
                   cvt_pk(fmaf(st[m][0], cs[j][2], acc[m][j][2]) * st[m][1], fmaf(st[m][0], cs[j][3], acc[m][j][3]) * st[m][1])};
        char* dst = (qi == 4) ? xp + rw * 8 : mainp + (rw >> 5) * 1024 + (rw & 31) * 16;
        st8(dst, o);
      }
    }
  }
}

__global__ __launch_bounds__(1024) void k1_ln_qkv(const char* __restrict__ x, const char* __restrict__ wqkv, char* __restrict__ ws, const char* __restrict__ wo,
    const char* __restrict__ wg, const char* __restrict__ wu, const char* __restrict__ wd, const char* __restrict__ out) {
  extern __shared__ __attribute__((aligned(16))) char smem[];
  STAMP_BEGIN
  lds_ptr lds = (lds_ptr)smem;
  const unsigned lds0 = (unsigned)(uintptr_t)lds;
  const int tid = threadIdx.x, lane = tid & 63, wid = __builtin_amdgcn_readfirstlane(tid >> 6);
  const int tile = xcd_remap(blockIdx.x, 234);
  const int cgp = tile / 78, rem = tile - cgp * 78;
  const int mt = rem / 3, ct = cgp * 3 + (rem - mt * 3);
  const int m0 = min(mt * 80, SEQ - 80);
  if (wid < 3) {
    unsigned wv[4];
#pragma unroll
    for (int i = 0; i < 4; ++i) {
      const int rho = 8 * i + (lane >> 3);
      const int gk = ((rho >> 4) & 1) * 2 + ((rho >> 2) & 1), tk = (rho >> 3) & 1, qk = rho & 3;
      wv[i] = (8 * gk + 4 * tk + qk) * 3456 + (((lane & 7) ^ (((rho >> 1) & 3) << 1)) * 16);
    }
    const char* wsrc = wqkv + ct * 384 + wid * 128;
    const unsigned wdst = lds0 + K1_W + wid * 4096;
    for_seq<18>([&](auto KK) {
      constexpr int kk = KK;
#pragma unroll
      for (int n = (kk == 0 ? 0 : k1_nw(kk - 1)); n < k1_nw(kk); ++n)
#pragma unroll
        for (int i = 0; i < 4; ++i) glds16(wsrc + (size_t)n * 32 * 3456, wv[i], wdst + (n % 9) * K1_SLOT + i * 1024);
      wait_bar<(k1_nw(kk) - kk - 1) * 4>();
    });
    return;
  }
  if (wid == 3) {
    const unsigned v0 = (lane >> 3) * 1152 + (((lane & 7) ^ (lane >> 4)) * 16);
    const unsigned v1 = (lane >> 3) * 1152 + (((lane & 7) ^ (4 + (lane >> 4))) * 16);
    const char* xs = x + (size_t)m0 * 1152;
    for_seq<18>([&](auto KK) {
      constexpr int kk = KK;
#pragma unroll
      for (int h = (kk == 0 ? 0 : k1_nx(kk - 1)); h < k1_nx(kk); ++h)
#pragma unroll
        for (int i = 0; i < 5; ++i) {
          const int pc = (h & 1) * 5 + i;
          glds16(xs + pc * 9216 + (h >> 1) * 128, (pc & 1) ? v1 : v0, lds0 + K1_X + ((h >> 1) & 3) * 10240 + pc * 1024);
        }
      wait_bar<(k1_nx(kk) - 2 * (kk >> 1) - 2) * 5>();
    });
    return;
  }
  if (wid < 8) {
    const int j = wid - 4;
    if (j == 0) CODE_TOUCH(SYM_K1, LEN_K1, lds0 + K1_PF);
    if (j == 1) CODE_TOUCH(SYM_K2, LEN_K2, lds0 + K1_PF + 256);
    if (j == 2 && blockIdx.x == 0 && lane < 16) *(unsigned*)(ws + WS_CNT + lane * 128) = 0u;
    if (j == 3) {
      const int gi = blockIdx.x >> 3;
      const unsigned off = (gi & 7) * (64u * 65536u) + lane * 65536u;
      if (gi < 8) { if (off < (unsigned)WS_END) touch_lines(ws, off, lds0 + K1_PF + 768); }
      else if (gi < 10) { if (off < 5308416u) touch_lines(wg, off, lds0 + K1_PF + 768); }
      else if (gi >= 16 && gi < 18) { if (off < 5308416u) touch_lines(wu, off, lds0 + K1_PF + 768); }
      else if (gi >= 24 && gi < 26) { if (off < 5308416u) touch_lines(wd, off, lds0 + K1_PF + 768); }
      else if (gi == 26) { if (lane * 65536u < 2359296u) touch_lines(out, lane * 65536u, lds0 + K1_PF + 768); }
    }
    {
      const int ln = blockIdx.x * 168 + j * 42 + lane;
      if (lane < 42) {
        if (ln < 15552) touch_lines(wqkv, ln * 128, lds0 + K1_PF + j * 256);
        else if (ln < 33984) touch_lines(x, (ln - 15552) * 128, lds0 + K1_PF + j * 256);
        else if (ln < 39168) touch_lines(wo, (ln - 33984) * 128, lds0 + K1_PF + j * 256);
      }
    }
    lds_ptr p0 = lds + K1_X + j * 1024 + lane * 16, p2 = lds + K1_X + min(j + 8, 9) * 1024 + lane * 16;
    float s1[3] = {0.f, 0.f, 0.f}, s2[3] = {0.f, 0.f, 0.f};
#pragma unroll 1
    for (int s = 0; s < 9; ++s) {
      raw_bar();
      const int so = (s & 3) * 10240;
      u32x4 v[3] = {*(LDS_AS u32x4*)(p0 + so), *(LDS_AS u32x4*)(p0 + so + 4096), *(LDS_AS u32x4*)(p2 + so)};
      SB();
      raw_bar();
      SB();
#pragma unroll
      for (int i = 0; i < 3; ++i)
#pragma unroll
        for (int e = 0; e < 4; ++e) { s1[i] = dot2(v[i][e], 0x3f803f80u, s1[i]); s2[i] = dot2(v[i][e], v[i][e], s2[i]); }
      asm volatile("" : "+v"(s1[0]), "+v"(s1[1]), "+v"(s1[2]), "+v"(s2[0]), "+v"(s2[1]), "+v"(s2[2]));
    }
#pragma unroll
    for (int i = 0; i < 3; ++i) {
      const float a = sum8(s1[i]), b = sum8(s2[i]);
      const float mean = a * (1.f / 576.f);
      const float var = fmaxf(b * (1.f / 576.f) - mean * mean, 0.f);
      const int pc = i < 2 ? j + 4 * i : min(j + 8, 9);
      if ((lane & 7) == 0) *(LDS_AS f32x2*)(lds + K1_ST + (pc * 8 + (lane >> 3)) * 8) = f32x2{-mean, __builtin_amdgcn_rsqf(var + LN_EPS)};
    }
    lds_bar();
    return;
  }
  if (ct >= 6) {
    if (wid < 12) k1_consumer<true, false>(lds, wid - 8, lane, ws, ct, m0);
    else k1_consumer<true, true>(lds, wid - 12, lane, ws, ct, m0);
  } else {
    if (wid < 12) k1_consumer<false, false>(lds, wid - 8, lane, ws, ct, m0);
    else k1_consumer<false, true>(lds, wid - 12, lane, ws, ct, m0);
  }
  STAMP_ENDW(0, 512);
}

constexpr int K2_KM = 0, K2_KX = 16384, K2_V = K2_KX + 4096, K2_CS = 44032, K2_W = K2_CS + 5120, K2_LDS = K2_W + 73728;

template <bool FIRST>
DI void k2_core(lds_ptr lds, char* ws, int hd, int t, int wid, int lane, bf16x8 q0, unsigned qx) {
  const int hi = lane >> 5, l31 = lane & 31;
  u32x4 q1u = {hi ? 0u : qx, 0u, 0u, 0u};
  bf16x8 q1 = __builtin_bit_cast(bf16x8, q1u);
  f32x16 S[9];
  lds_ptr kmp = lds + K2_KM + wid * 1024 + lane * 16;
  lds_ptr kxp = lds + K2_KX + (wid * 32 + l31) * 8 + (hd & 1) * 4;
  const float NEG = -1e30f;
  const int dq = l31 - 4 * hi;
  float mx = NEG;
#pragma unroll
  for (int ii = 0; ii < 9; ++ii) {
    const int i = ii == 0 ? 8 : ii - 1;
    bf16x8 k0 = lds_r128(kmp + i * 1024);
    u32x4 k1u = {*(LDS_AS unsigned*)(kxp + i * 256), 0u, 0u, 0u};
    f32x16 z = {};
    S[i] = MFMA32(k0, q0, z);
    S[i] = MFMA32(__builtin_bit_cast(bf16x8, k1u), q1, S[i]);
    if (FIRST && i < 8 && wid + i < 8) {
#pragma unroll
      for (int r = 0; r < 16; ++r) S[i][r] = NEG;
    }
    if (i == 0) {
#pragma unroll
      for (int r = 0; r < 16; ++r) S[0][r] = (((r & 3) + 8 * (r >> 2)) <= dq) ? S[8][r] : S[0][r];
    }
    if (i < 8) {
#pragma unroll
      for (int r = 0; r < 16; r += 2) mx = fmaxf(fmaxf(mx, S[i][r]), S[i][r + 1]);
    }
  }
  {
    auto rr = __builtin_amdgcn_permlane32_swap(__float_as_uint(mx), __float_as_uint(mx), false, false);
    mx = fmaxf(__uint_as_float(rr[0]), __uint_as_float(rr[1]));
  }
  wait_bar<0>();
  lds_ptr vp = lds + K2_V + wid * 2 * 592 + (l31 < 18 ? hi * 288 + l31 * 16 : 576);
  f32x16 O = {};
#pragma unroll
  for (int i = 0; i < 8; ++i) {
    u32x4 P[2];
#pragma unroll
    for (int s = 0; s < 2; ++s)
#pragma unroll
      for (int j = 0; j < 4; ++j)
        P[s][j] = cvt_pk(__builtin_amdgcn_exp2f(S[i][8 * s + 2 * j] - mx), __builtin_amdgcn_exp2f(S[i][8 * s + 2 * j + 1] - mx));
    if (i == 0) {
#pragma unroll
      for (int s = 0; s < 2; ++s) {
        u32x4 P8, P0;
#pragma unroll
        for (int j = 0; j < 4; ++j) {
          const int r0 = 8 * s + 2 * j, r1 = r0 + 1;
          const unsigned mk = ((((r0 & 3) + 8 * (r0 >> 2)) <= dq) ? 0xffffu : 0u) | ((((r1 & 3) + 8 * (r1 >> 2)) <= dq) ? 0xffff0000u : 0u);
          P8[j] = P[s][j] & mk;
          P0[j] = P[s][j] & ~mk;
        }
        O = MFMA32(lds_r128(vp + s * 592), __builtin_bit_cast(bf16x8, P0), O);
        O = MFMA32(lds_r128(vp + (16 + s) * 592), __builtin_bit_cast(bf16x8, P8), O);
      }
    } else {
#pragma unroll
      for (int s = 0; s < 2; ++s) O = MFMA32(lds_r128(vp + (2 * i + s) * 592), __builtin_bit_cast(bf16x8, P[s]), O);
    }
  }
  const float inv = __builtin_amdgcn_rcpf(O[10]);
  const int row = t * 32 + l31;
  char* ob = ws + WS_O + (size_t)(row >> 4) * 18 * 1024 + (row & 15) * 16;
  u32x4 o = {cvt_pk(O[0] * inv, O[1] * inv), cvt_pk(O[2] * inv, O[3] * inv), cvt_pk(O[4] * inv, O[5] * inv), cvt_pk(O[6] * inv, O[7] * inv)};
  st16(ob + (hd >> 1) * 1024 + (2 * (hd & 1) + hi) * 256, o);
  if (hi == 0) st4(ob + (16 + (hd >> 4)) * 1024 + ((hd >> 2) & 3) * 256 + (hd & 3) * 4, cvt_pk(O[8] * inv, O[9] * inv));
}

__global__ __launch_bounds__(768) void k2_attn(char* __restrict__ ws, const char* __restrict__ wg, const char* __restrict__ wu,
                                               const char* __restrict__ wo, const char* __restrict__ x) {
  extern __shared__ __attribute__((aligned(16))) char smem[];
  STAMP_BEGIN
  lds_ptr lds = (lds_ptr)smem;
  const unsigned lds0 = (unsigned)(uintptr_t)lds;
  const int tid = threadIdx.x, lane = tid & 63, wid = __builtin_amdgcn_readfirstlane(tid >> 6);
  const int hd = (blockIdx.x & 7) * 4 + ((blockIdx.x >> 3) & 3), qb = blockIdx.x >> 5;
  const bool has_tile = blockIdx.x < 234;
  const int tile = has_tile ? xcd_remap(blockIdx.x, 234) : 0;
  const int mt = tile / 9, nt = tile - mt * 9;
  const int m0 = min(mt * 80, SEQ - 80), rb0 = m0 >> 4;
  if (wid >= 10) {
    raw_bar();
    const int sw = wid - 10;
    if (sw == 0) CODE_TOUCH(SYM_K4, LEN_K4, lds0 + K2_CS + 4096);
    const bool act = blockIdx.x < 216;
    const int unit = act ? (int)blockIdx.x * 2 + sw : 0, part = unit / 144, c64 = unit - part * 144;
    const char* wsrc = (c64 < 72 ? wg + c64 * 128 : wu + (c64 - 72) * 128) + (lane & 7) * 16 + (size_t)(part * 192 + (lane >> 3)) * 9216;
    u32x4 v[24];
    if (act) {
#pragma unroll
      for (int i = 0; i < 24; ++i) v[i] = *(const u32x4*)(wsrc + (size_t)i * 8 * 9216);
    }
    SB();
    raw_bar();
    raw_bar();
    SB();
    if (act) {
      float cs[8];
#pragma unroll
      for (int j = 0; j < 8; ++j) cs[j] = 0.f;
#pragma unroll
      for (int i = 0; i < 24; ++i)
#pragma unroll
        for (int j = 0; j < 4; ++j) { cs[2 * j] += bf_lo(v[i][j]); cs[2 * j + 1] += bf_hi(v[i][j]); }
      lds_ptr cp = lds + K2_CS + sw * 2048;
      *(LDS_AS f32x4*)(cp + lane * 32) = f32x4{cs[0], cs[1], cs[2], cs[3]};
      *(LDS_AS f32x4*)(cp + lane * 32 + 16) = f32x4{cs[4], cs[5], cs[6], cs[7]};
      asm volatile("s_waitcnt lgkmcnt(0)" ::: "memory");
      float t = 0.f;
#pragma unroll
      for (int i = 0; i < 8; ++i) t += *(LDS_AS float*)(cp + i * 256 + lane * 4);
      *(float*)(ws + WS_CSP + ((size_t)part * 9216 + c64 * 64 + lane) * 4) = t;
    }
    if (has_tile) { raw_bar(); raw_bar(); }
    return;
  }
  if (wid >= 8) {
    raw_bar();
    if (has_tile) {
      const int par = wid - 8;
      unsigned va[4], vb[4];
#pragma unroll
      for (int i = 0; i < 4; ++i) {
        const int rho = 8 * i + (lane >> 3);
        const int g_ = ((rho >> 4) & 1) * 2 + ((rho >> 2) & 1), t_ = (rho >> 3) & 1, q_ = rho & 3;
        const int La = 36 * par + 18 * (g_ >> 1) + 8 * t_ + 4 * (g_ & 1) + q_, Lb = 288 * par + 18 * (4 * g_ + 2 * t_ + (q_ >> 1)) + 16 + (q_ & 1);
        const unsigned csw = ((lane & 7) ^ (((rho >> 1) & 3) << 1)) * 16;
        va[i] = La * 1152 + csw; vb[i] = Lb * 1152 + csw;
      }
#pragma unroll
      for (int kp = 0; kp < 9; ++kp)
#pragma unroll
        for (int i = 0; i < 4; ++i)
          glds16(wo + nt * 128 + (size_t)(kp == 8 ? 0 : 72 * kp) * 1152, kp == 8 ? vb[i] : va[i], lds0 + K2_W + par * 4096 + kp * 8192 + i * 1024);
    }
    raw_bar();
  } else {
    const int t = qb * 8 + wid;
    bf16x8 q0;
    unsigned qx;
    const unsigned vo = lane * 16;
    const int kt0 = qb * 8 - 8;
    asm volatile("global_load_dwordx4 %0, %2, %3\n\tglobal_load_dword %1, %4, %5"
                 : "=&v"(q0), "=&v"(qx)
                 : "v"(vo), "s"(ws + WS_QM + (size_t)hd * 65536 + (size_t)t * 1024), "v"((lane & 31) * 8),
                   "s"(ws + WS_QX + (size_t)(hd >> 1) * 16384 + (size_t)t * 256 + (hd & 1) * 4)
                 : "memory");
#pragma unroll
    for (int i = 0; i < 2; ++i) {
      const int lt = wid * 2 + i;
      const int kt = max(kt0 + lt, 0);
      glds16(ws + WS_KM + (size_t)hd * 65536 + (size_t)kt * 1024, vo, lds0 + K2_KM + lt * 1024);
    }
    if (wid >= 4) {
      const int k0 = max(qb * 256 - 256 + (wid - 4) * 128, 0);
      glds16(ws + WS_KX + (size_t)(hd >> 1) * 16384 + (size_t)k0 * 8, vo, lds0 + K2_KX + (wid - 4) * 1024);
    }
    const long vb0 = (long)(hd * 128 + qb * 16 - 16) * 592;
#pragma unroll
    for (int i = 0; i < 3; ++i) {
      const int pc = wid + 8 * i;
      if (pc < 19) {
        long off = vb0 + pc * 1024;
        unsigned v2 = vo;
        if (qb == 0) { off = (long)(hd * 128) * 592 + pc * 1024; v2 = (pc * 1024 + lane * 16 >= 9472) ? vo - 9472 + 65536 : vo + 65536; off -= 65536; }
        glds16(ws + WS_V + off, v2, lds0 + K2_V + pc * 1024);
      }
    }
    asm volatile("s_waitcnt vmcnt(2)\n\ts_barrier" : "+v"(q0), "+v"(qx)::"memory");
    STAMP_MARK(1, 2);
    if (qb == 0) k2_core<true>(lds, ws, hd, t, wid, lane, q0, qx);
    else k2_core<false>(lds, ws, hd, t, wid, lane, q0, qx);
    STAMP_MARK(1, 3);
  }
  wait_bar<0>();
  STAMP_MARK(1, 4);
  unsigned* cnt = (unsigned*)(ws + WS_CNT);
  if (tid == 0) __hip_atomic_fetch_add(cnt + qb * 32, 1u, __ATOMIC_RELAXED, __HIP_MEMORY_SCOPE_AGENT);
  if (!has_tile) { STAMP_END(1); return; }
  const int mi = wid >> 1, kg = wid & 1;
  const int g = lane >> 4, q = (lane & 15) >> 2, p = lane & 3;
  const int er = tid >> 3, ec = tid & 7;
  const u32x4 res = *(const u32x4*)(x + (size_t)(m0 + er) * 1152 + (nt * 64 + ec * 8) * 2);
  const int rho0 = 16 * (g >> 1) + 4 * (g & 1) + q;
  lds_ptr wb[4];
#pragma unroll
  for (int n = 0; n < 4; ++n) { wb[n] = lds + K2_W + kg * 4096 + rho0 * 128 + ((n ^ ((rho0 >> 1) & 3)) * 32) + p * 8; asm volatile("" : "+v"(wb[n])); }
  bf16x8 wf[5][4];
#pragma unroll
  for (int j = 0; j < 5; ++j)
#pragma unroll
    for (int n = 0; n < 4; ++n) wf[j][n] = cat8(lds_tr(wb[n] + j * 8192), lds_tr(wb[n] + j * 8192 + 1024));
  if (tid == 576) {
    const unsigned* c0 = cnt + (m0 >> 8) * 32;
    const unsigned* c1 = cnt + ((m0 + 79) >> 8) * 32;
    for (int spin = 0; spin < (1 << 18); ++spin) {
      const unsigned a = __hip_atomic_load(c0, __ATOMIC_RELAXED, __HIP_MEMORY_SCOPE_AGENT);
      const unsigned b = __hip_atomic_load(c1, __ATOMIC_RELAXED, __HIP_MEMORY_SCOPE_AGENT);
      if (a >= 32u && b >= 32u) break;
      __builtin_amdgcn_s_sleep(1);
    }
  }
  SB();
  raw_bar();
  SB();
  STAMP_MARK(1, 5);
  const __amdgpu_buffer_rsrc_t ors = __builtin_amdgcn_make_buffer_rsrc((void*)(ws + WS_O), 0, 128 * 18432, 0x00020000);
  const int aoff = (rb0 + mi) * 18432 + kg * 1024 + lane * 16;
  bf16x8 a[9];
#pragma unroll
  for (int i = 0; i < 9; ++i) a[i] = __builtin_bit_cast(bf16x8, __builtin_amdgcn_raw_buffer_load_b128(ors, aoff + i * 2048, 0, 16));
  SB();
  f32x4 acc[4];
#pragma unroll
  for (int n = 0; n < 4; ++n) acc[n] = f32x4{0.f, 0.f, 0.f, 0.f};
#pragma unroll
  for (int j = 0; j < 9; ++j) {
#pragma unroll
    for (int n = 0; n < 4; ++n) {
      acc[n] = MFMA16(wf[j % 5][n], a[j], acc[n]);
      if (j + 5 < 9) wf[j % 5][n] = cat8(lds_tr(wb[n] + (j + 5) * 8192),
                                         lds_tr(wb[n] + (j + 5) * 8192 + 1024));
    }
  }
  STAMP_MARK(1, 6);
  {
    lds_ptr cw = lds + kg * (80 * 272) + (mi * 16 + (lane & 15)) * 272 + 16 * g;
#pragma unroll
    for (int n = 0; n < 4; ++n) *(LDS_AS f32x4*)(cw + n * 64) = acc[n];
  }
  lds_bar();
  {
    lds_ptr cr = lds + er * 272 + ec * 32;
    f32x4 a0 = *(LDS_AS f32x4*)(cr), a1 = *(LDS_AS f32x4*)(cr + 16);
    f32x4 b0 = *(LDS_AS f32x4*)(cr + 80 * 272), b1 = *(LDS_AS f32x4*)(cr + 80 * 272 + 16);
    a0 += b0; a1 += b1;
    u32x4 o;
    o[0] = cvt_pk(a0[0] + bf_lo(res[0]), a0[1] + bf_hi(res[0]));
    o[1] = cvt_pk(a0[2] + bf_lo(res[1]), a0[3] + bf_hi(res[1]));
    o[2] = cvt_pk(a1[0] + bf_lo(res[2]), a1[1] + bf_hi(res[2]));
    o[3] = cvt_pk(a1[2] + bf_lo(res[3]), a1[3] + bf_hi(res[3]));
    const int row = m0 + er, col = nt * 64 + ec * 8;
    st16(ws + WS_H + (size_t)(row >> 4) * 18432 + (col >> 5) * 1024 + ((col >> 3) & 3) * 256 + (row & 15) * 16, o);
    float s1 = 0.f, s2 = 0.f;
#pragma unroll
    for (int j = 0; j < 4; ++j) { s1 = dot2(o[j], 0x3f803f80u, s1); s2 = dot2(o[j], o[j], s2); }
    s1 = sum8(s1); s2 = sum8(s2);
    if (ec == 0) *(f32x2*)(ws + WS_ST + (size_t)row * 80 + nt * 8) = f32x2{s1, s2};
  }
  STAMP_END(1);
}

#define P5MAP 0
constexpr int T_SLOT = 24576, T_LDS = T_SLOT * 6;

DI void down_phase(lds_ptr lds, unsigned lds0, const char* __restrict__ w, char* __restrict__ ws, char* __restrict__ out, int pn) {
  int tid = threadIdx.x;
  asm volatile("" : "+v"(tid));
  const int lane = tid & 63, wid = __builtin_amdgcn_readfirstlane(tid >> 6);
  const bool has_tile = blockIdx.x < 234;
  const int tile = has_tile ? xcd_remap(blockIdx.x, 234) : 0;
  const int mt = tile / 9, nt = tile - mt * 9;
  const int m0 = min(mt * 80, SEQ - 80), rb0 = m0 >> 4;
  unsigned* cnt = (unsigned*)(ws + WS_CNT + 1024);
  wait_bar<0>();
  STAMP_MARK(2, 6);
  if (tid == 0) __hip_atomic_fetch_add(cnt + pn * 32, 1u, __ATOMIC_RELAXED, __HIP_MEMORY_SCOPE_AGENT);
  if (!has_tile) return;
  u32x4 res[2];
  const int ec = tid & 7, col = nt * 64 + ec * 8;
#pragma unroll
  for (int i = 0; i < 2; ++i) {
    const int er = min((tid >> 3) + 64 * i, 79);
    res[i] = *(const u32x4*)(ws + WS_H + (size_t)(rb0 + (er >> 4)) * 18432 + (col >> 5) * 1024 + ((col >> 3) & 3) * 256 + (er & 15) * 16);
  }
  if (wid >= 6) {
    const int par = wid - 6;
    unsigned va[4], vb[4];
#pragma unroll
    for (int i = 0; i < 4; ++i) {
      const int rho = 8 * i + (lane >> 3);
      const int g_ = ((rho >> 4) & 1) * 2 + ((rho >> 2) & 1), t_ = (rho >> 3) & 1, q_ = rho & 3;
      const int La = 32 * par + 16 * t_ + 4 * g_ + q_, Lb = 288 * par + 144 * (g_ >> 1) + 128 + 8 * (g_ & 1) + 4 * t_ + q_;
      const unsigned csw = ((lane & 7) ^ (((rho >> 1) & 3) << 1)) * 16;
      va[i] = La * 1152 + csw; vb[i] = Lb * 1152 + csw;
    }
    const char* wsrc = w + nt * 128;
    const unsigned dst0 = lds0 + par * 4096;
    auto issue = [&](int sb, int st, int slot) {
#pragma unroll
      for (int j = 0; j < 3; ++j) {
        const int kp = st * 3 + j;
        const int base = kp == 8 ? 0 : 144 * (kp >> 1) + 64 * (kp & 1);
        const char* src = wsrc + (size_t)(sb * 576 + base) * 1152;
#pragma unroll
        for (int i = 0; i < 4; ++i) glds16(src, kp == 8 ? vb[i] : va[i], dst0 + slot * T_SLOT + j * 8192 + i * 1024);
      }
    };
    issue(0, 0, 0); issue(0, 1, 1); issue(0, 2, 2); issue(1, 0, 3); issue(1, 1, 4);
    __builtin_amdgcn_s_setprio(2);
    wait_bar<48>(); issue(1, 2, 5);
    wait_bar<48>(); issue(2, 0, 0);
    wait_bar<48>(); issue(2, 1, 1);
#pragma unroll 1
    for (int sb = 1; sb < 6; ++sb) {
      const int o = (sb & 1) * 3;
      wait_bar<48>(); issue(sb + 1, 2, 5 - o);
      wait_bar<48>(); issue(sb + 2, 0, o);
      wait_bar<48>(); issue(sb + 2, 1, o + 1);
    }
    wait_bar<48>(); issue(7, 2, 5);
    wait_bar<48>(); wait_bar<36>();
    wait_bar<24>(); wait_bar<12>(); wait_bar<0>();
  } else {
    if (wid == 5) {
      if (lane == 0) {
        const unsigned* c0 = cnt + (m0 >> 8) * 32;
        const unsigned* c1 = cnt + ((m0 + 79) >> 8) * 32;
        for (int spin = 0; spin < (1 << 18); ++spin) {
          const unsigned a = __hip_atomic_load(c0, __ATOMIC_RELAXED, __HIP_MEMORY_SCOPE_AGENT);
          const unsigned b = __hip_atomic_load(c1, __ATOMIC_RELAXED, __HIP_MEMORY_SCOPE_AGENT);
          if (a >= 32u && b >= 32u) break;
          __builtin_amdgcn_s_sleep(1);
        }
      }
      SB();
#pragma unroll 1
      for (int i = 0; i < 24; ++i) raw_bar();
    } else {
      const int g = lane >> 4, q = (lane & 15) >> 2, p = lane & 3;
      const int rho0 = 16 * (g >> 1) + 4 * (g & 1) + q;
      lds_ptr wb[4], wp[4], wq[4], wr[4];
#pragma unroll
      for (int n = 0; n < 4; ++n) {
        wb[n] = lds + rho0 * 128 + ((n ^ ((rho0 >> 1) & 3)) * 32) + p * 8;
        wp[n] = wb[n] + 2 * T_SLOT; wq[n] = wb[n] + 3 * T_SLOT; wr[n] = wb[n] + 5 * T_SLOT;
        asm volatile("" : "+v"(wp[n]), "+v"(wq[n]), "+v"(wr[n]));
      }
      f32x4 acc[4];
#pragma unroll
      for (int n = 0; n < 4; ++n) acc[n] = f32x4{0.f, 0.f, 0.f, 0.f};
      const __amdgpu_buffer_rsrc_t hrs = __builtin_amdgcn_make_buffer_rsrc((void*)(ws + WS_HID), 0, 128 * 144 * 1024, 0x00020000);
      const int aoff = (rb0 + wid) * (144 * 1024) + lane * 16;
      bf16x8 a[2][18];
      raw_bar();
      SB();
      STAMP_MARK(2, 7);
#pragma unroll
      for (int i = 0; i < 36; ++i) { a[i / 18][i % 18] = __builtin_bit_cast(bf16x8, __builtin_amdgcn_raw_buffer_load_b128(hrs, aoff + (i & 3) * 1024, (i >> 2) * 4096, 16)); SB(); }
      s16x4 wl[8], wh[8];
#pragma unroll
      for (int i = 0; i < 7; ++i) { wl[i] = lds_tr(wb[i & 3] + (i >> 2) * 4096); wh[i] = lds_tr(wb[i & 3] + (i >> 2) * 4096 + 1024); }
      SB();
      auto sblock = [&](int sb, auto PAR, auto MORE, auto LOAD) {
        constexpr bool more = MORE, load = LOAD;
        constexpr int par = PAR, o = par * 3 * T_SLOT, on = 3 * T_SLOT - o;
        lds_ptr bc[4], bc2[4], bn[4];
#pragma unroll
        for (int n = 0; n < 4; ++n) { bc[n] = par ? wq[n] : wb[n]; bc2[n] = par ? wr[n] : wp[n]; bn[n] = par ? wb[n] : wq[n]; }
        for_seq<72>([&](auto II) {
          constexpr int i = II, kk = i >> 2, n = i & 3, ni = i + 7, nk = ni >> 2, nn = ni & 3;
          acc[n] = MFMA16(cat8(wl[i & 7], wh[i & 7]), a[par][kk], acc[n]);
          SB();
          if constexpr (ni % 24 == 0 && (more || ni < 72)) { lds_bar(); SB(); }
          if constexpr (more || ni < 72) {
            lds_ptr np = nk >= 18 ? bn[nn] + (nk - 18) * 4096 : (nk >= 12 ? bc2[nn] + (nk - 12) * 4096 : bc[nn] + nk * 4096);
            wl[ni & 7] = lds_tr(np); wh[ni & 7] = lds_tr(np + 1024);
            SB();
          }
          if constexpr (load && n == 3) {
            a[par][kk] = __builtin_bit_cast(bf16x8, __builtin_amdgcn_raw_buffer_load_b128(hrs, aoff + (kk & 3) * 1024, (sb + 2) * 18432 + (kk >> 2) * 4096, 16));
            SB();
          }
        });
      };
      using I0 = std::integral_constant<int, 0>; using I1 = std::integral_constant<int, 1>;
#pragma unroll 1
      for (int sb = 0; sb < 6; sb += 2) { sblock(sb, I0{}, std::true_type{}, std::true_type{}); sblock(sb + 1, I1{}, std::true_type{}, std::true_type{}); }
      sblock(6, I0{}, std::true_type{}, std::false_type{});
      sblock(7, I1{}, std::false_type{}, std::false_type{});
      lds_ptr cw = lds + (wid * 16 + (lane & 15)) * 272 + 16 * g;
#pragma unroll
      for (int n = 0; n < 4; ++n) *(LDS_AS f32x4*)(cw + n * 64) = acc[n];
    }
  }
  lds_bar();
#pragma unroll
  for (int i = 0; i < 2; ++i) {
    const int er = (tid >> 3) + 64 * i;
    if (er < 80) {
      lds_ptr cr = lds + er * 272 + ec * 32;
      const f32x4 a0 = *(LDS_AS f32x4*)(cr), a1 = *(LDS_AS f32x4*)(cr + 16);
      u32x4 o;
      o[0] = cvt_pk(a0[0] + bf_lo(res[i][0]), a0[1] + bf_hi(res[i][0]));
      o[1] = cvt_pk(a0[2] + bf_lo(res[i][1]), a0[3] + bf_hi(res[i][1]));
      o[2] = cvt_pk(a1[0] + bf_lo(res[i][2]), a1[1] + bf_hi(res[i][2]));
      o[3] = cvt_pk(a1[2] + bf_lo(res[i][3]), a1[3] + bf_hi(res[i][3]));
      *(u32x4*)(out + (size_t)(m0 + er) * 1152 + col * 2) = o;
    }
  }
}

constexpr int K4_SLOT = 18432, K4_ST = K4_SLOT * 8, K4_CS = K4_ST + 2048, K4_PF = K4_CS + 2048, K4_CP = K4_PF + 1024, K4_LDS = K4_CP + 3 * 1152 + 256;

constexpr int k4_frag(int i) { return i < 16 ? ((i >> 2) * 2 + (i & 1) + ((i & 2) ? 9 : 0)) : (i == 16 ? 8 : 17); }
constexpr int k4_foff(int f) { return (f >= 9 ? 9216 + (f - 9) * 32 : f * 32); }
constexpr int K4_T0 = 13 * 18;
constexpr int k4_slab(int G) { return G < K4_T0 ? G / 18 : (G - K4_T0 < 80 ? 13 + ((G - K4_T0) % 20) / 4 : 13 + (G - K4_T0 - 80) / 2); }
constexpr int k4_pos(int G) { return G < K4_T0 ? G % 18 : (G - K4_T0 < 80 ? ((G - K4_T0) / 20) * 4 + ((G - K4_T0) & 3) : 16 + ((G - K4_T0) & 1)); }
constexpr int k4_op(int G) {
  const int s = G / 18, i = G % 18;
  if (s > 12) return 0;
  if (s < 3) {
    const int pos[10] = {0, 2, 4, 6, 7, 9, 11, 13, 15, 16};
    for (int k = 0; k < 10; ++k) if (pos[k] == i) return 1 + (2 * s + 2 + k / 5) * 8 + k % 5;
    return 0;
  }
  if (s == 3) {
    const int pos[8] = {0, 2, 4, 6, 8, 11, 14, 16};
    for (int k = 0; k < 8; ++k) if (pos[k] == i) return k < 5 ? 1 + 8 * 8 + k : 1 + 9 * 8 + (k - 3);
    return 0;
  }
  if (i == 1) return 1 + (s + 5) * 8;
  if (i == 4) return 1 + (s + 5) * 8 + 1;
  if (s + 6 > 17) return 0;
  if (i == 8) return 1 + (s + 6) * 8 + 2;
  if (i == 13) return 1 + (s + 6) * 8 + 3;
  if (i == 16) return 1 + (s + 6) * 8 + 4;
  return 0;
}
constexpr int k4_allow(int G, int need) {
  int n = (need == 0) ? 4 : 0;
  for (int g = 0; g < G; ++g) {
    const int op = k4_op(g);
    if (op == 0) continue;
    const int sl = (op - 1) >> 3, kind = (op - 1) & 7;
    if (sl <= need) n = 0;
    else if (kind != 4) ++n;
  }
  return n;
}
constexpr int k4_need(int G) {
  if (G + 5 >= 324 || G == 0) return 0;
  int mx = 0;
  for (int g = 0; g < G + 5; ++g) mx = k4_slab(g) > mx ? k4_slab(g) : mx;
  return k4_slab(G + 5) > mx ? k4_slab(G + 5) : 0;
}

__global__ __launch_bounds__(512, 2) void k4_gateup(const char* __restrict__ wgate, const char* __restrict__ wup, char* __restrict__ ws,
                                                    const char* __restrict__ wd, char* __restrict__ out) {
  extern __shared__ __attribute__((aligned(16))) char smem[];
  STAMP_BEGIN
  lds_ptr lds = (lds_ptr)smem;
  const unsigned lds0 = (unsigned)(uintptr_t)lds;
  const int tid = threadIdx.x, lane = tid & 63, wid = __builtin_amdgcn_readfirstlane(tid >> 6);
  const int xcd = blockIdx.x & 7, idx = blockIdx.x >> 3;
  const int pn = 4 * (xcd >> 2) + (idx & 3), jt = 8 * (xcd & 3) + (idx >> 2);
  const int wm = wid & 3, wn = wid >> 2;
  const char* asrc = ws + WS_H + (size_t)(pn * 16 + 2 * wid) * 18432;
  const unsigned avo = lane * 16, avo2 = lane * 16 + 18432;
  const char* wsrc = (wn ? wup : wgate) + jt * 288;
  unsigned wvo[3];
#pragma unroll
  for (int i = 0; i < 3; ++i) {
    const int pc = i < 2 ? 2 * wm + i : 8;
    const int o = pc * 1024 + lane * 16;
    const int rho = o / 288, u = (o - rho * 288) >> 4;
    const int gk = ((rho >> 4) & 1) * 2 + ((rho >> 2) & 1), tk = (rho >> 3) & 1, qk = rho & 3;
    wvo[i] = (8 * gk + 4 * tk + qk) * 9216 + u * 16;
  }
  const unsigned wdst = lds0 + wn * 9216;
  auto issue_w = [&](int s, int i) {
    const unsigned so = (s & 7) * K4_SLOT;
    const char* wp = wsrc + (size_t)s * 32 * 9216;
    if (i < 2) glds16(wp, wvo[i], wdst + so + wm * 2048 + i * 1024);
    else glds16_if0(wm, wp, wvo[2], wdst + so + 8192);
  };
  bf16x8 A[6][2];
  auto issue_a = [&](int s, bf16x8& dst, int m) {
    asm volatile("global_load_dwordx4 %0, %1, %2" : "=v"(dst) : "v"(m ? avo2 : avo), "s"(asrc + s * 1024) : "memory");
  };
  f32x4 sp0, sp1, sp2, sp3;
  f32x2 sp4;
  asm volatile("global_load_dwordx4 %0, %5, %6\n\tglobal_load_dwordx4 %1, %5, %6 offset:16\n\tglobal_load_dwordx4 %2, %5, %6 offset:32\n\t"
               "global_load_dwordx4 %3, %5, %6 offset:48\n\tglobal_load_dwordx2 %4, %5, %6 offset:64"
               : "=&v"(sp0), "=&v"(sp1), "=&v"(sp2), "=&v"(sp3), "=&v"(sp4)
               : "v"((lane & 31) * 80), "s"(ws + WS_ST + (size_t)(pn * 256 + wid * 32) * 80) : "memory");
  if (wm < 3 && lane < 36) glds16(ws + WS_CSP + (size_t)(wm * 9216 + wn * FF + jt * 144) * 4, lane * 16, lds0 + K4_CP + wm * 1152 + wn * 576);
  for_seq<2>([&](auto S) { issue_w(S, 0); issue_w(S, 1); issue_w(S, 2); issue_a(S, A[S][0], 0); issue_a(S, A[S][1], 1); });
  {
    asm volatile("s_waitcnt vmcnt(8)" : "+v"(sp0), "+v"(sp1), "+v"(sp2), "+v"(sp3), "+v"(sp4)::"memory");
    const float s1 = sp0[0] + sp0[2] + sp1[0] + sp1[2] + sp2[0] + sp2[2] + sp3[0] + sp3[2] + sp4[0];
    const float s2 = sp0[1] + sp0[3] + sp1[1] + sp1[3] + sp2[1] + sp2[3] + sp3[1] + sp3[3] + sp4[1];
    const float mean = s1 * (1.f / 576.f);
    const float var = fmaxf(s2 * (1.f / 576.f) - mean * mean, 0.f);
    *(LDS_AS f32x2*)(lds + K4_ST + (wid * 32 + (lane & 31)) * 8) = f32x2{-mean, __builtin_amdgcn_rsqf(var + LN_EPS)};
    SB();
  }
  const int g = lane >> 4, q = (lane & 15) >> 2, p = lane & 3, r = lane & 15;
  lds_ptr wb = lds + (16 * (g >> 1) + 4 * (g & 1) + q) * 288 + p * 8;
  lds_ptr wbs[3] = {wb, wb + 3 * K4_SLOT, wb + 6 * K4_SLOT};
  asm volatile("" : "+v"(wbs[1]), "+v"(wbs[2]));
  lds_ptr csb = lds + K4_CS + 16 * g;
  asm volatile("" : "+v"(csb));
  f32x4 acc[2][18];
  asm volatile("s_waitcnt vmcnt(%2)\n\ts_barrier" : "+v"(A[0][0]), "+v"(A[0][1]) : "n"(k4_allow(0, 0)) : "memory");
  STAMP_MARK(2, 2);
  STAMP_CYC0
  s16x4 Bl[6], Bh[6];
  for_seq<5>([&](auto I) { Bl[I] = lds_tr(wb + k4_foff(k4_frag(I))); Bh[I] = lds_tr(wb + k4_foff(k4_frag(I)) + 2304); });
  SB();
  const float L2E = 1.4426950408889634f;
  char* hb = ws + WS_HID + (size_t)(pn * 16 + wid * 2) * (144 * 1024) + (size_t)(jt >> 2) * 18432;
  f32x2 st[2];
  float c1[2];
  auto glu = [&](float ag, float au, float cg, float cu, int m) -> float {
    const float tg = fmaf(st[m][0], cg, ag), tu = fmaf(st[m][0], cu, au);
    const float e = __builtin_amdgcn_exp2f(tg * c1[m]);
    return tu * st[m][1] * __builtin_amdgcn_rcpf(1.f + e);
  };
  f32x4 cg[2], cu[2];
  u32x4 ov;
  auto slice = [&](auto QQ, auto KK) {
    constexpr int Q = QQ, k = KK, m = k >> 2, d = k & 3, h = d >> 1, e = (d & 1) * 2;
    const f32x4 ag = acc[m][2 * Q + h], au = acc[m][9 + 2 * Q + h];
    ov[d] = cvt_pk(glu(ag[e], au[e], cg[h][e], cu[h][e], m), glu(ag[e + 1], au[e + 1], cg[h][e + 1], cu[h][e + 1], m));
    if constexpr (d == 3) st16(hb + (size_t)m * (144 * 1024) + (4 * (jt & 3) + Q) * 1024 + lane * 16, ov);
    else asm volatile("" : "+v"(ov));
  };
  for_seq<18>([&](auto GH) { for_seq<18>([&](auto GL) {
    constexpr int G = GH * 18 + GL, sl = k4_slab(G), f = k4_frag(k4_pos(G)), rb = G % 6, nb = (G + 5) % 6, NG = G + 5;
    const bf16x8 b = cat8(Bl[rb], Bh[rb]);
    constexpr int need = k4_need(G);
    if constexpr (need != 0) {
      asm volatile("s_waitcnt vmcnt(%2)\n\ts_barrier" : "+v"(A[need % 6][0]), "+v"(A[need % 6][1]) : "n"(k4_allow(G, need)) : "memory");
      SB();
    }
    if constexpr (sl == 0) mfma16_zero(acc[0][f], b, A[0][0]); else mfma16_acc(acc[0][f], b, A[sl % 6][0]);
    SB();
    if constexpr (NG < 324) { Bl[nb] = lds_tr(wbs[(k4_slab(NG) & 7) / 3] + ((k4_slab(NG) & 7) % 3) * K4_SLOT + k4_foff(k4_frag(k4_pos(NG)))); SB(); }
    if constexpr (sl == 0) mfma16_zero(acc[1][f], b, A[0][1]); else mfma16_acc(acc[1][f], b, A[sl % 6][1]);
    SB();
    if constexpr (NG < 324) { Bh[nb] = lds_tr(wbs[(k4_slab(NG) & 7) / 3] + ((k4_slab(NG) & 7) % 3) * K4_SLOT + k4_foff(k4_frag(k4_pos(NG))) + 2304); SB(); }
    constexpr int op = k4_op(G);
    if constexpr (op != 0) {
      constexpr int os = (op - 1) >> 3, kind = (op - 1) & 7;
      if constexpr (kind < 2) issue_a(os, A[os % 6][kind], kind);
      else issue_w(os, kind - 2);
      SB();
    }
    if constexpr (G % 18 == 10 && (G / 18 == 5 || G / 18 == 7 || G / 18 == 9)) {
      constexpr int k = (G / 18 - 5) / 2;
      touch_if0((int)(blockIdx.x / 216) + (wid ^ (k + 1)), wd + (size_t)(blockIdx.x * 3 + k) * 8192, avo * 8, lds0 + K4_PF + k * 256);
      SB();
    }
    if constexpr (G == 60) {
      lds_ptr cpp = lds + K4_CP + wid * 144 + lane * 4;
      const float t = *(LDS_AS float*)(cpp) + *(LDS_AS float*)(cpp + 1152) + *(LDS_AS float*)(cpp + 2304);
      *(LDS_AS float*)(lds + K4_CS + wid * 144 + min(lane, 36) * 4 + (lane >= 36 ? 1152 : 0)) = t;
      asm volatile("s_waitcnt lgkmcnt(0)" ::: "memory");
      SB();
    }
    if constexpr (G == K4_T0) {
      STAMP_MARK(2, 3);
      STAMP_CYC(2, 4);
#pragma unroll
      for (int m = 0; m < 2; ++m) { st[m] = *(LDS_AS f32x2*)(lds + K4_ST + (wid * 32 + m * 16 + r) * 8); c1[m] = -L2E * st[m][1]; }
      SB();
    }
    if constexpr (G >= K4_T0) {
      constexpr int t = G - K4_T0, u = t % 20;
      if constexpr (t < 80 && u == 18) {
#pragma unroll
        for (int h = 0; h < 2; ++h) {
          cg[h] = *(LDS_AS f32x4*)(csb + (2 * (t / 20) + h) * 64);
          cu[h] = *(LDS_AS f32x4*)(csb + 576 + (2 * (t / 20) + h) * 64);
        }
        SB();
      }
      if constexpr (t >= 20 && t < 80 && u >= 1 && u < 17 && (u & 1)) { slice(std::integral_constant<int, t / 20 - 1>{}, std::integral_constant<int, u / 2>{}); SB(); }
      if constexpr (t >= 81 && t < 89) { slice(std::integral_constant<int, 3>{}, std::integral_constant<int, t - 81>{}); SB(); }
    }
  }); });
  {
    asm volatile("s_nop 7\n\ts_nop 1" : "+v"(acc[0][8]), "+v"(acc[1][8]), "+v"(acc[0][17]), "+v"(acc[1][17]));
    const f32x4 cg8 = *(LDS_AS f32x4*)(csb + 512);
    const f32x4 cu8 = *(LDS_AS f32x4*)(csb + 576 + 512);
#pragma unroll
    for (int m = 0; m < 2; ++m) {
      const f32x4 ag = acc[m][8], au = acc[m][17];
      u32x2 o = {cvt_pk(glu(ag[0], au[0], cg8[0], cu8[0], m), glu(ag[1], au[1], cg8[1], cu8[1], m)),
                 cvt_pk(glu(ag[2], au[2], cg8[2], cu8[2], m), glu(ag[3], au[3], cg8[3], cu8[3], m))};
      st8wt(hb + (size_t)m * (144 * 1024) + (16 + ((jt >> 1) & 1)) * 1024 + (2 * (jt & 1) + (g >> 1)) * 256 + r * 16 + (g & 1) * 8, o);
    }
  }
  STAMP_MARK(2, 5);
  down_phase(lds, lds0, wd, ws, out, pn);
  STAMP_END(2);
}

extern "C" void kernel_launch(void* const* d_in, const int* in_sizes, int n_in, void* d_out, int out_size, void* d_ws, size_t ws_size,
                              hipStream_t stream) {
  const char* x = (const char*)d_in[0];
  const char* wqkv = (const char*)d_in[1];
  const char* wo = (const char*)d_in[2];
  const char* wg = (const char*)d_in[4];
  const char* wu = (const char*)d_in[5];
  const char* wd = (const char*)d_in[6];
  char* ws = (char*)d_ws;
  (void)hipFuncSetAttribute((const void*)k1_ln_qkv, hipFuncAttributeMaxDynamicSharedMemorySize, K1_LDS);
  (void)hipFuncSetAttribute((const void*)k2_attn, hipFuncAttributeMaxDynamicSharedMemorySize, K2_LDS);
  (void)hipFuncSetAttribute((const void*)k4_gateup, hipFuncAttributeMaxDynamicSharedMemorySize, K4_LDS);
  k1_ln_qkv<<<234, 1024, K1_LDS, stream>>>(x, wqkv, ws, wo, wg, wu, wd, (const char*)d_out);
  k2_attn<<<256, 768, K2_LDS, stream>>>(ws, wg, wu, wo, x);
  k4_gateup<<<256, 512, K4_LDS, stream>>>(wg, wu, ws, wd, (char*)d_out);
}
```

```cpp
#include <hip/hip_runtime.h>
#include <hip/hip_bf16.h>
#include <stdint.h>
#include <utility>
#include <type_traits>

typedef __attribute__((ext_vector_type(8))) short bf16x8;
typedef __attribute__((ext_vector_type(4))) short s16x4;
typedef __attribute__((ext_vector_type(4))) float f32x4;
typedef __attribute__((ext_vector_type(16))) float f32x16;
typedef __attribute__((ext_vector_type(4))) unsigned u32x4;
typedef __attribute__((ext_vector_type(2))) unsigned u32x2;
typedef __attribute__((ext_vector_type(2))) __bf16 bf16x2_t;
typedef __attribute__((ext_vector_type(2))) float f32x2;
#define LDS_AS __attribute__((address_space(3)))
typedef LDS_AS char* lds_ptr;
#define DI __device__ __forceinline__

constexpr int SEQ = 2048, DM = 576, NH = 32, FF = 4608;
constexpr float LN_EPS = 1e-5f;
constexpr float QSCALE = 0.23570226039551584f * 1.4426950408889634f;

constexpr size_t WS_QM = 0;
constexpr size_t WS_KM = WS_QM + (size_t)32 * 64 * 1024;
constexpr size_t WS_QX = WS_KM + (size_t)32 * 64 * 1024;
constexpr size_t WS_KX = WS_QX + (size_t)16 * 2048 * 8;
constexpr size_t WS_V = WS_KX + (size_t)16 * 2048 * 8;
constexpr size_t WS_O = WS_V + (size_t)32 * 128 * 592 + 1024;
constexpr size_t WS_H = WS_O + (size_t)128 * 18 * 1024;
constexpr size_t WS_ST = WS_H + (size_t)128 * 18 * 1024;
constexpr size_t WS_CSP = WS_ST + (size_t)2048 * 80;
constexpr size_t WS_CS = WS_CSP + (size_t)8 * 9216 * 4;
constexpr size_t WS_HID = WS_CS + (size_t)9216 * 4;
constexpr size_t WS_CNT = WS_HID + (size_t)128 * 144 * 1024;
constexpr size_t WS_PART = WS_CNT + 16384;
constexpr size_t WS_END = WS_PART + (size_t)2048 * 9216;
static_assert(WS_O % 1024 == 0 && WS_H % 1024 == 0 && WS_ST % 256 == 0 && WS_CSP % 256 == 0 && WS_HID % 256 == 0, "align");

#define STAMP_BEGIN
#define STAMP_MARK(k, i)
#define STAMP_MARKW(k, i, T)
#define STAMP_ENDW(k, T)
#define STAMP_CYC0
#define STAMP_CYC(k, i)
#define STAMP_END(k)

DI unsigned cvt_pk(float lo, float hi) {
  f32x2 v = {lo, hi};
  bf16x2_t r = __builtin_convertvector(v, bf16x2_t);
  return __builtin_bit_cast(unsigned, r);
}
DI float bf_lo(unsigned u) { return __builtin_bit_cast(float, u << 16); }
DI float bf_hi(unsigned u) { return __builtin_bit_cast(float, u & 0xffff0000u); }
DI float dot2(unsigned a, unsigned b, float c) {
  return __builtin_amdgcn_fdot2_f32_bf16(__builtin_bit_cast(bf16x2_t, a), __builtin_bit_cast(bf16x2_t, b), c, false);
}
template <int CTRL> DI float dpp_add(float v) {
  int t = __builtin_amdgcn_update_dpp(0, __builtin_bit_cast(int, v), CTRL, 0xf, 0xf, true);
  return v + __builtin_bit_cast(float, t);
}
DI float sum8(float v) {
  v = dpp_add<0xB1>(v);
  v = dpp_add<0x4E>(v);
  v = dpp_add<0x141>(v);
  return v;
}
DI void glds16(const void* sbase, unsigned voff, unsigned lds_dst) {
  asm volatile("s_mov_b32 m0, %2\n\ts_nop 3\n\tglobal_load_lds_dwordx4 %0, %1" ::"v"(voff), "s"(sbase), "s"(lds_dst) : "memory");
}
#define ST_AUX " sc1"
DI void st16(void* p, u32x4 v) { asm volatile("global_store_dwordx4 %0, %1, off" ST_AUX "\n\ts_nop 1" ::"v"(p), "v"(v) : "memory"); }
DI void st8(void* p, u32x2 v) { *(u32x2*)p = v; }
DI void st8wt(void* p, u32x2 v) { asm volatile("global_store_dwordx2 %0, %1, off sc1" ::"v"(p), "v"(v) : "memory"); }
DI void st4(void* p, unsigned v) { asm volatile("global_store_dword %0, %1, off sc1" ::"v"(p), "v"(v) : "memory"); }
DI void glds16_if0(int c, const void* sbase, unsigned voff, unsigned lds_dst) {
  asm volatile("s_cmp_lg_u32 %3, 0\n\ts_cbranch_scc1 1f\n\ts_mov_b32 m0, %2\n\ts_nop 1\n\tglobal_load_lds_dwordx4 %0, %1\n1:"
               ::"v"(voff), "s"(sbase), "s"(lds_dst), "s"(c) : "memory", "scc");
}
DI void touch_if0(int c, const void* sbase, unsigned voff, unsigned lds_dst) {
  asm volatile("s_cmp_lg_u32 %3, 0\n\ts_cbranch_scc1 1f\n\ts_mov_b32 m0, %2\n\ts_nop 1\n\tglobal_load_lds_dword %0, %1\n1:"
               ::"v"(voff), "s"(sbase), "s"(lds_dst), "s"(c) : "memory", "scc");
}
DI void touch_lines(const char* base, unsigned voff, unsigned lds_dummy) {
  asm volatile("s_mov_b32 m0, %2\n\ts_nop 3\n\tglobal_load_lds_dword %0, %1" ::"v"(voff), "s"(base), "s"(lds_dummy) : "memory");
}
#define CODE_TOUCH(SYM, BYTES, LDS_DUMMY)                                                                          \
  do {                                                                                                             \
    const char* cp_;                                                                                               \
    asm volatile("s_getpc_b64 s[20:21]\n\ts_add_u32 s20, s20, " SYM "@rel32@lo+4\n\ts_addc_u32 s21, s21, " SYM     \
                 "@rel32@hi+12\n\ts_mov_b64 %0, s[20:21]" : "=s"(cp_)::"s20", "s21", "scc");                      \
    constexpr int nl_ = (BYTES) / 128, per_ = (nl_ + 28) / 29;                                                      \
    const int ln_ = (blockIdx.x >> 3) * per_ + lane;                                                               \
    if (lane < per_ && ln_ < nl_) touch_lines(cp_, ln_ * 128, LDS_DUMMY);                                          \
  } while (0)
#define SYM_K1 "_Z9k1_ln_qkvPKcS0_PcS0_"
#define SYM_K2 "_Z7k2_attnPcPKcS1_S1_S1_"
#define SYM_K4 "_Z9k4_gateupPKcS0_PcS0_S1_"
constexpr int LEN_K1 = 24000, LEN_K2 = 11200, LEN_K4 = 30000;
template <int N> DI void wait_bar() { asm volatile("s_waitcnt vmcnt(%0) lgkmcnt(0)\n\ts_barrier" ::"n"(N) : "memory"); }
template <int N> DI void wait_vm() { asm volatile("s_waitcnt vmcnt(%0)" ::"n"(N) : "memory"); }
DI unsigned lds_arrive(lds_ptr c, int lane) {
  unsigned old = 0;
  if (lane == 0) old = __hip_atomic_fetch_add((LDS_AS unsigned*)c, 1u, __ATOMIC_RELAXED, __HIP_MEMORY_SCOPE_WORKGROUP);
  return __builtin_amdgcn_readfirstlane(old);
}
DI void lds_sync(lds_ptr c, unsigned n, int lane) {
  asm volatile("" ::: "memory");
  if (lds_arrive(c, lane) + 1 != n) {
    for (int spin = 0; spin < (1 << 20); ++spin) {
      const unsigned v = __builtin_amdgcn_readfirstlane(__hip_atomic_load((LDS_AS unsigned*)c, __ATOMIC_RELAXED, __HIP_MEMORY_SCOPE_WORKGROUP));
      if (v >= n) break;
      __builtin_amdgcn_s_sleep(1);
    }
  }
  asm volatile("" ::: "memory");
}
DI void lds_post(lds_ptr c, int lane) {
  asm volatile("" ::: "memory");
  if (lane == 0) (void)__hip_atomic_fetch_add((LDS_AS unsigned*)c, 1u, __ATOMIC_RELAXED, __HIP_MEMORY_SCOPE_WORKGROUP);
}
DI void lds_wait(lds_ptr c, unsigned n) {
  for (int spin = 0; spin < (1 << 20); ++spin) {
    const unsigned v = __builtin_amdgcn_readfirstlane(__hip_atomic_load((LDS_AS unsigned*)c, __ATOMIC_RELAXED, __HIP_MEMORY_SCOPE_WORKGROUP));
    if (v >= n) break;
    __builtin_amdgcn_s_sleep(1);
  }
  asm volatile("" ::: "memory");
}
DI void raw_bar() { asm volatile("s_barrier" ::: "memory"); }
DI void lds_bar() { asm volatile("s_waitcnt lgkmcnt(0)\n\ts_barrier" ::: "memory"); }
DI bf16x8 lds_r128(lds_ptr p) { return *(LDS_AS const bf16x8*)p; }
typedef short v4i16_t __attribute__((ext_vector_type(4)));
DI s16x4 lds_tr(lds_ptr p) { return __builtin_amdgcn_ds_read_tr16_b64_v4i16((LDS_AS v4i16_t*)p); }
DI bf16x8 cat8(s16x4 a, s16x4 b) { return __builtin_shufflevector(a, b, 0, 1, 2, 3, 4, 5, 6, 7); }
#define SB() __builtin_amdgcn_sched_barrier(0)
#define MFMA16(a, b, c) __builtin_amdgcn_mfma_f32_16x16x32_bf16(a, b, c, 0, 0, 0)
#define MFMA32(a, b, c) __builtin_amdgcn_mfma_f32_32x32x16_bf16(a, b, c, 0, 0, 0)
DI void mfma16_acc(f32x4& c, bf16x8 a, bf16x8 b) { asm volatile("v_mfma_f32_16x16x32_bf16 %0, %1, %2, %0" : "+v"(c) : "v"(a), "v"(b)); }
DI void mfma16_zero(f32x4& c, bf16x8 a, bf16x8 b) { asm volatile("v_mfma_f32_16x16x32_bf16 %0, %1, %2, 0" : "=&v"(c) : "v"(a), "v"(b)); }
template <class F, int... I> DI void for_seq_impl(F&& f, std::integer_sequence<int, I...>) { (f(std::integral_constant<int, I>{}), ...); }
template <int N, class F> DI void for_seq(F&& f) { for_seq_impl(f, std::make_integer_sequence<int, N>{}); }
DI int xcd_remap(int b, int nwg) {
  int q = nwg >> 3, r = nwg & 7, x = b & 7, o = b >> 3;
  return (x < r ? x * (q + 1) : r * (q + 1) + (x - r) * q) + o;
}

constexpr int K1_X = 0, K1_W = 40960, K1_SLOT = 12288, K1_ST = K1_W + K1_SLOT * 9, K1_CS = K1_W, K1_PF = K1_ST + 1024, K1_LDS = K1_PF + 1024;
constexpr int cmin(int a, int b) { return a < b ? a : b; }
constexpr int k1_nw(int kk) { return cmin(18, cmin(3 + 2 * kk, kk + 7)); }
constexpr int k1_nx(int kk) { return cmin(18, cmin(3 + 2 * kk, kk + 5)); }

template <bool ISV, bool HI>
DI void k1_consumer(lds_ptr lds, int cb, int lane, char* ws, int ct, int m0) {
  const int g = lane >> 4, q = (lane & 15) >> 2, p = lane & 3, r = lane & 15;
  constexpr int NM = HI ? 2 : 3, MB = HI ? 3 : 0;
  lds_ptr a0 = lds + K1_X + MB * 2048 + (r >> 3) * 1024 + (r & 7) * 128 + ((g ^ (r >> 1)) * 16);
  lds_ptr a1 = lds + K1_X + MB * 2048 + (r >> 3) * 1024 + (r & 7) * 128 + (((4 + g) ^ (r >> 1)) * 16);
  const int rho0 = 16 * (g >> 1) + 4 * (g & 1) + q;
  lds_ptr wb = lds + K1_W + rho0 * 128 + ((cb ^ ((rho0 >> 1) & 3)) * 32) + p * 8;
  lds_ptr wb5 = wb + 5 * K1_SLOT;
  asm volatile("" : "+v"(wb), "+v"(wb5));
  const u32x4 onesu = {0x3f803f80u, 0x3f803f80u, 0x3f803f80u, 0x3f803f80u};
  const bf16x8 ones = __builtin_bit_cast(bf16x8, onesu);
  f32x4 acc[3][3];
#pragma unroll
  for (int m = 0; m < 3; ++m)
#pragma unroll
    for (int j = 0; j < 3; ++j) acc[m][j] = f32x4{0.f, 0.f, 0.f, 0.f};
  bf16x8 fA[2][3];
  s16x4 wl[2][3], wh[2][3];
  auto rd1 = [&](auto KK, auto II) {
    constexpr int kk = KK, i = II, bf = kk & 1, sl = kk % 9;
    if constexpr (i >= 2 && i <= 4) {
      if constexpr (i - 2 < NM) fA[bf][i - 2] = lds_r128(((kk & 1) ? a1 : a0) + ((kk >> 1) & 3) * 10240 + (i - 2) * 2048);
    } else {
      constexpr int j = i < 2 ? 0 : (i - 3) / 2, hh = i < 2 ? i : (i - 3) % 2;
      lds_ptr bp = (sl >= 5 ? wb5 + (sl - 5) * K1_SLOT : wb + sl * K1_SLOT) + j * 4096 + hh * 1024;
      if constexpr (hh == 0) wl[bf][j] = lds_tr(bp); else wh[bf][j] = lds_tr(bp);
    }
  };
  lds_bar();
  STAMP_MARKW(0, 2, 512);
  for_seq<9>([&](auto I) { rd1(std::integral_constant<int, 0>{}, I); });
  SB();
  for_seq<18>([&](auto KK) {
    constexpr int kk = KK, bf = kk & 1;
    if constexpr (kk < 17) { raw_bar(); SB(); }
    for_seq<9>([&](auto II) {
      constexpr int i = II, j = i / 3, m = i % 3;
      const bf16x8 wv = cat8(wl[bf][j], wh[bf][j]);
      const bf16x8 av = (m < NM) ? fA[bf][m] : ones;
      acc[m][j] = ISV ? MFMA16(av, wv, acc[m][j]) : MFMA16(wv, av, acc[m][j]);
      SB();
      if constexpr (kk < 17) { rd1(std::integral_constant<int, kk + 1>{}, II); SB(); }
    });
  });
  STAMP_MARKW(0, 4, 512);
  lds_ptr cx = lds + K1_CS + cb * 3072 + lane * 16;
  if (HI) {
#pragma unroll
    for (int j = 0; j < 3; ++j) *(LDS_AS f32x4*)(cx + j * 1024) = acc[2][j];
  }
  lds_bar();
  STAMP_MARKW(0, 5, 512);
  f32x4 cs[3];
#pragma unroll
  for (int j = 0; j < 3; ++j) cs[j] = HI ? acc[2][j] : *(LDS_AS f32x4*)(cx + j * 1024);
  if (ISV) {
    f32x4 st[NM][2];
#pragma unroll
    for (int m = 0; m < NM; ++m) {
      st[m][0] = *(LDS_AS f32x4*)(lds + K1_ST + ((MB + m) * 16 + 4 * g) * 8);
      st[m][1] = *(LDS_AS f32x4*)(lds + K1_ST + ((MB + m) * 16 + 4 * g) * 8 + 16);
    }
#pragma unroll
    for (int j = 0; j < 3; ++j) {
      const int c = (ct - 6) * 192 + j * 64 + cb * 16 + r;
      const int head = c / 18, d = c - head * 18;
      char* vb = ws + WS_V + (size_t)(head * 128 + (m0 >> 4) + MB) * 592;
      char* vp = vb + (g & 1) * 288 + d * 16 + (g >> 1) * 8;
      const float c0 = cs[j][0];
#pragma unroll
      for (int m = 0; m < NM; ++m) {
        u32x2 o = {cvt_pk(fmaf(st[m][0][0], c0, acc[m][j][0]) * st[m][0][1], fmaf(st[m][0][2], c0, acc[m][j][1]) * st[m][0][3]),
                   cvt_pk(fmaf(st[m][1][0], c0, acc[m][j][2]) * st[m][1][1], fmaf(st[m][1][2], c0, acc[m][j][3]) * st[m][1][3])};
        st8(vp + m * 592, o);
      }
      if (d == 17 && g < NM) *(u32x4*)(vb + g * 592 + 576) = onesu;
    }
  } else {
    const bool isq = ct < 3;
    const float sc = isq ? QSCALE : 1.f;
    f32x2 st[NM];
#pragma unroll
    for (int m = 0; m < NM; ++m) { st[m] = *(LDS_AS f32x2*)(lds + K1_ST + ((MB + m) * 16 + r) * 8); st[m][1] *= sc; }
#pragma unroll
    for (int j = 0; j < 3; ++j) {
      const int c = (isq ? ct : ct - 3) * 192 + j * 64 + cb * 16 + 4 * g;
      const int pair = c / 36, qi = (c - pair * 36) >> 2;
      const int head = 2 * pair + (qi > 4 ? 1 : 0), a = qi > 4 ? qi - 5 : qi;
      char* mainp = ws + (isq ? WS_QM : WS_KM) + (size_t)head * 65536 + (a >> 1) * 512 + (a & 1) * 8;
      char* xp = ws + (isq ? WS_QX : WS_KX) + (size_t)pair * 16384;
#pragma unroll
      for (int m = 0; m < NM; ++m) {
        const int rw = m0 + (MB + m) * 16 + r;
        u32x2 o = {cvt_pk(fmaf(st[m][0], cs[j][0], acc[m][j][0]) * st[m][1], fmaf(st[m][0], cs[j][1], acc[m][j][1]) * st[m][1]),
                   cvt_pk(fmaf(st[m][0], cs[j][2], acc[m][j][2]) * st[m][1], fmaf(st[m][0], cs[j][3], acc[m][j][3]) * st[m][1])};
        char* dst = (qi == 4) ? xp + rw * 8 : mainp + (rw >> 5) * 1024 + (rw & 31) * 16;
        st8(dst, o);
      }
    }
  }
}

__global__ __launch_bounds__(1024) void k1_ln_qkv(const char* __restrict__ x, const char* __restrict__ wqkv, char* __restrict__ ws, const char* __restrict__ wo) {
  extern __shared__ __attribute__((aligned(16))) char smem[];
  STAMP_BEGIN
  lds_ptr lds = (lds_ptr)smem;
  const unsigned lds0 = (unsigned)(uintptr_t)lds;
  const int tid = threadIdx.x, lane = tid & 63, wid = __builtin_amdgcn_readfirstlane(tid >> 6);
  const int tile = xcd_remap(blockIdx.x, 234);
  const int cgp = tile / 78, rem = tile - cgp * 78;
  const int mt = rem / 3, ct = cgp * 3 + (rem - mt * 3);
  const int m0 = min(mt * 80, SEQ - 80);
  if (wid < 3) {
    unsigned wv[4];
#pragma unroll
    for (int i = 0; i < 4; ++i) {
      const int rho = 8 * i + (lane >> 3);
      const int gk = ((rho >> 4) & 1) * 2 + ((rho >> 2) & 1), tk = (rho >> 3) & 1, qk = rho & 3;
      wv[i] = (8 * gk + 4 * tk + qk) * 3456 + (((lane & 7) ^ (((rho >> 1) & 3) << 1)) * 16);
    }
    const char* wsrc = wqkv + ct * 384 + wid * 128;
    const unsigned wdst = lds0 + K1_W + wid * 4096;
    for_seq<18>([&](auto KK) {
      constexpr int kk = KK;
#pragma unroll
      for (int n = (kk == 0 ? 0 : k1_nw(kk - 1)); n < k1_nw(kk); ++n)
#pragma unroll
        for (int i = 0; i < 4; ++i) glds16(wsrc + (size_t)n * 32 * 3456, wv[i], wdst + (n % 9) * K1_SLOT + i * 1024);
      wait_bar<(k1_nw(kk) - kk - 1) * 4>();
    });
    return;
  }
  if (wid == 3) {
    const unsigned v0 = (lane >> 3) * 1152 + (((lane & 7) ^ (lane >> 4)) * 16);
    const unsigned v1 = (lane >> 3) * 1152 + (((lane & 7) ^ (4 + (lane >> 4))) * 16);
    const char* xs = x + (size_t)m0 * 1152;
    for_seq<18>([&](auto KK) {
      constexpr int kk = KK;
#pragma unroll
      for (int h = (kk == 0 ? 0 : k1_nx(kk - 1)); h < k1_nx(kk); ++h)
#pragma unroll
        for (int i = 0; i < 5; ++i) {
          const int pc = (h & 1) * 5 + i;
          glds16(xs + pc * 9216 + (h >> 1) * 128, (pc & 1) ? v1 : v0, lds0 + K1_X + ((h >> 1) & 3) * 10240 + pc * 1024);
        }
      wait_bar<(k1_nx(kk) - 2 * (kk >> 1) - 2) * 5>();
    });
    return;
  }
  if (wid < 8) {
    const int j = wid - 4;
    if (j == 0) CODE_TOUCH(SYM_K1, LEN_K1, lds0 + K1_PF);
    if (j == 1) CODE_TOUCH(SYM_K2, LEN_K2, lds0 + K1_PF + 256);
    if (j >= 2 && blockIdx.x == 0) *(unsigned*)(ws + WS_CNT + ((j - 2) * 64 + lane) * 128) = 0u;
    {
      const int ln = blockIdx.x * 168 + j * 42 + lane;
      if (lane < 42) {
        if (ln < 15552) touch_lines(wqkv, ln * 128, lds0 + K1_PF + j * 256);
        else if (ln < 33984) touch_lines(x, (ln - 15552) * 128, lds0 + K1_PF + j * 256);
        else if (ln < 39168) touch_lines(wo, (ln - 33984) * 128, lds0 + K1_PF + j * 256);
      }
    }
    lds_ptr p0 = lds + K1_X + j * 1024 + lane * 16, p2 = lds + K1_X + min(j + 8, 9) * 1024 + lane * 16;
    float s1[3] = {0.f, 0.f, 0.f}, s2[3] = {0.f, 0.f, 0.f};
#pragma unroll 1
    for (int s = 0; s < 9; ++s) {
      raw_bar();
      const int so = (s & 3) * 10240;
      u32x4 v[3] = {*(LDS_AS u32x4*)(p0 + so), *(LDS_AS u32x4*)(p0 + so + 4096), *(LDS_AS u32x4*)(p2 + so)};
      SB();
      raw_bar();
      SB();
#pragma unroll
      for (int i = 0; i < 3; ++i)
#pragma unroll
        for (int e = 0; e < 4; ++e) { s1[i] = dot2(v[i][e], 0x3f803f80u, s1[i]); s2[i] = dot2(v[i][e], v[i][e], s2[i]); }
      asm volatile("" : "+v"(s1[0]), "+v"(s1[1]), "+v"(s1[2]), "+v"(s2[0]), "+v"(s2[1]), "+v"(s2[2]));
    }
#pragma unroll
    for (int i = 0; i < 3; ++i) {
      const float a = sum8(s1[i]), b = sum8(s2[i]);
      const float mean = a * (1.f / 576.f);
      const float var = fmaxf(b * (1.f / 576.f) - mean * mean, 0.f);
      const int pc = i < 2 ? j + 4 * i : min(j + 8, 9);
      if ((lane & 7) == 0) *(LDS_AS f32x2*)(lds + K1_ST + (pc * 8 + (lane >> 3)) * 8) = f32x2{-mean, __builtin_amdgcn_rsqf(var + LN_EPS)};
    }
    lds_bar();
    return;
  }
  if (ct >= 6) {
    if (wid < 12) k1_consumer<true, false>(lds, wid - 8, lane, ws, ct, m0);
    else k1_consumer<true, true>(lds, wid - 12, lane, ws, ct, m0);
  } else {
    if (wid < 12) k1_consumer<false, false>(lds, wid - 8, lane, ws, ct, m0);
    else k1_consumer<false, true>(lds, wid - 12, lane, ws, ct, m0);
  }
  STAMP_ENDW(0, 512);
}

#define K2SYNC 0
#define K2_SL0 4
#define K2_SL1 4
constexpr int K2_KM = 0, K2_KX = 16384, K2_V = K2_KX + 4096, K2_SY = 44032, K2_BI = K2_V + 19 * 1024 + 128, K2_CS = 44288, K2_W = K2_CS + 4864, K2_LDS = K2_W + 73728;

#define MFMA32K8(a, b, c) __builtin_amdgcn_mfma_f32_32x32x8bf16_1k(a, b, c, 0, 0, 0)
DI void k2_core(lds_ptr lds, char* ws, int hd, int t, int wid, int lane, bf16x8 q0, unsigned qx) {
  const int hi = lane >> 5, l31 = lane & 31;
  const u32x2 q1u = {hi ? 0x3f80u : ((hd & 1) ? 0u : qx), (!hi && (hd & 1)) ? qx : 0u};
  const s16x4 q1 = __builtin_bit_cast(s16x4, q1u);
  f32x16 S[9];
  lds_ptr kmp = lds + K2_KM + wid * 1024 + lane * 16;
  lds_ptr kxp = hi ? lds + K2_BI + wid * 256 : lds + K2_KX + (wid * 32 + l31) * 8;
  const float NEG = -1e30f;
  const int dq = l31 - 4 * hi;
  float mx0 = NEG, mx1 = NEG;
  bf16x8 kf[3];
  s16x4 kx[3];
  auto rdk = [&](auto NN) {
    constexpr int n = NN, i = n == 0 ? 8 : n - 1;
    kf[n % 3] = lds_r128(kmp + i * 1024);
    kx[n % 3] = *(LDS_AS const s16x4*)(kxp + i * 256);
  };
  auto post = [&](auto NN) {
    constexpr int n = NN, i = n == 0 ? 8 : n - 1;
    if constexpr (i < 8) {
      if constexpr (i == 0) {
#pragma unroll
        for (int r = 0; r < 16; ++r) S[0][r] = (((r & 3) + 8 * (r >> 2)) <= dq) ? S[8][r] : S[0][r];
      }
#pragma unroll
      for (int r = 0; r < 16; r += 4) {
        mx0 = fmaxf(fmaxf(mx0, S[i][r]), S[i][r + 1]);
        mx1 = fmaxf(fmaxf(mx1, S[i][r + 2]), S[i][r + 3]);
      }
    }
  };
  rdk(std::integral_constant<int, 0>{});
  rdk(std::integral_constant<int, 1>{});
  SB();
  for_seq<9>([&](auto NN) {
    constexpr int n = NN, i = n == 0 ? 8 : n - 1;
    const f32x16 z = {};
    S[i] = MFMA32(kf[n % 3], q0, z);
    SB();
    if constexpr (n + 2 < 9) { rdk(std::integral_constant<int, n + 2>{}); SB(); }
    S[i] = MFMA32K8(kx[n % 3], q1, S[i]);
    SB();
    if constexpr (n >= 1) { post(std::integral_constant<int, n - 1>{}); SB(); }
  });
  post(std::integral_constant<int, 8>{});
  float mx = fmaxf(mx0, mx1);
  {
    auto rr = __builtin_amdgcn_permlane32_swap(__float_as_uint(mx), __float_as_uint(mx), false, false);
    mx = fmaxf(__uint_as_float(rr[0]), __uint_as_float(rr[1]));
  }
  s16x4 mq, mk1;
  {
    const float nm = -mx;
    const unsigned h1 = __float_as_uint(nm) & 0xffff0000u;
    const float r1 = nm - __uint_as_float(h1);
    const unsigned h2 = __float_as_uint(r1) & 0xffff0000u;
    const float r2 = r1 - __uint_as_float(h2);
    const u32x2 mu = {hi ? 0u : ((h1 >> 16) | h2), hi ? 0u : (__float_as_uint(r2) >> 16)};
    mq = __builtin_bit_cast(s16x4, mu);
    const u32x2 ou = {0x3f803f80u, 0x00003f80u};
    mk1 = __builtin_bit_cast(s16x4, ou);
  }
  S[1] = MFMA32K8(mk1, mq, S[1]);
  S[2] = MFMA32K8(mk1, mq, S[2]);
  SB();
  lds_ptr vp = lds + K2_V + wid * 2 * 592 + (l31 < 18 ? hi * 288 + l31 * 16 : 576);
  f32x16 O;
  bf16x8 vf[3];
  auto rdv = [&](auto EE) {
    constexpr int e = EE, blk = e < 14 ? e + 2 : ((e - 14) >> 1) + ((e & 1) ? 16 : 0);
    if constexpr (e < 18) vf[e % 3] = lds_r128(vp + blk * 592);
  };
  rdv(std::integral_constant<int, 0>{});
  rdv(std::integral_constant<int, 1>{});
  SB();
  for_seq<16>([&](auto HH) {
    constexpr int h = HH, i = ((h >> 1) + 1) & 7, s = h & 1;
    u32x4 P;
#pragma unroll
    for (int j = 0; j < 4; ++j) P[j] = cvt_pk(__builtin_amdgcn_exp2f(S[i][8 * s + 2 * j]), __builtin_amdgcn_exp2f(S[i][8 * s + 2 * j + 1]));
    if constexpr (i == 0) {
      constexpr int e = 14 + 2 * s;
      u32x4 P8, P0;
#pragma unroll
      for (int j = 0; j < 4; ++j) {
        const int r0 = 8 * s + 2 * j, r1 = r0 + 1;
        const unsigned mk = ((((r0 & 3) + 8 * (r0 >> 2)) <= dq) ? 0xffffu : 0u) | ((((r1 & 3) + 8 * (r1 >> 2)) <= dq) ? 0xffff0000u : 0u);
        P8[j] = P[j] & mk;
        P0[j] = P[j] & ~mk;
      }
      rdv(std::integral_constant<int, e + 2>{});
      SB();
      O = MFMA32(vf[e % 3], __builtin_bit_cast(bf16x8, P0), O);
      SB();
      rdv(std::integral_constant<int, e + 3>{});
      O = MFMA32(vf[(e + 1) % 3], __builtin_bit_cast(bf16x8, P8), O);
      SB();
    } else {
      rdv(std::integral_constant<int, h + 2>{});
      SB();
      if constexpr (h == 0) { const f32x16 z = {}; O = MFMA32(vf[0], __builtin_bit_cast(bf16x8, P), z); }
      else O = MFMA32(vf[h % 3], __builtin_bit_cast(bf16x8, P), O);
      if constexpr (s == 0 && i + 2 <= 8) S[(i + 2) & 7] = MFMA32K8(mk1, mq, S[(i + 2) & 7]);
      SB();
    }
  });
  const float inv = __builtin_amdgcn_rcpf(O[10]);
  const int row = t * 32 + l31;
  char* ob = ws + WS_O + (size_t)(row >> 4) * 18 * 1024 + (row & 15) * 16;
  u32x4 o = {cvt_pk(O[0] * inv, O[1] * inv), cvt_pk(O[2] * inv, O[3] * inv), cvt_pk(O[4] * inv, O[5] * inv), cvt_pk(O[6] * inv, O[7] * inv)};
  st16(ob + (hd >> 1) * 1024 + (2 * (hd & 1) + hi) * 256, o);
  if (hi == 0) st4(ob + (16 + (hd >> 4)) * 1024 + ((hd >> 2) & 3) * 256 + (hd & 3) * 4, cvt_pk(O[8] * inv, O[9] * inv));
}

__global__ __launch_bounds__(768) void k2_attn(char* __restrict__ ws, const char* __restrict__ wg, const char* __restrict__ wu,
                                               const char* __restrict__ wo, const char* __restrict__ x) {
  extern __shared__ __attribute__((aligned(16))) char smem[];
  STAMP_BEGIN
  lds_ptr lds = (lds_ptr)smem;
  const unsigned lds0 = (unsigned)(uintptr_t)lds;
  const int tid = threadIdx.x, lane = tid & 63, wid = __builtin_amdgcn_readfirstlane(tid >> 6);
  const int hd = (blockIdx.x & 7) * 4 + ((blockIdx.x >> 3) & 3), qb = blockIdx.x >> 5;
  const bool has_tile = blockIdx.x < 234;
  const int tile = has_tile ? xcd_remap(blockIdx.x, 234) : 0;
  const int mt = tile / 9, nt = tile - mt * 9;
  const int m0 = min(mt * 80, SEQ - 80), rb0 = m0 >> 4;
  if (wid == 11) {
    if (lane < 16) *(LDS_AS u32x2*)(lds + K2_BI + lane * 256) = u32x2{(qb == 0 && lane < 8) ? 0xf149u : 0u, 0u};
    if (lane < 4) *(LDS_AS unsigned*)(lds + K2_SY + lane * 4) = 0u;
  }
  if (wid >= 10) {
    lds_bar();
    __builtin_amdgcn_s_sleep(K2_SL1);
    const int sw = wid - 10;
    if (sw == 0) CODE_TOUCH(SYM_K4, LEN_K4, lds0 + K2_CS + 4096);
    const bool act = blockIdx.x < 216;
    const int unit = act ? (int)blockIdx.x * 2 + sw : 0, part = unit / 144, c64 = unit - part * 144;
    const char* wsrc = (c64 < 72 ? wg + c64 * 128 : wu + (c64 - 72) * 128) + (lane & 7) * 16 + (size_t)(part * 192 + (lane >> 3)) * 9216;
    u32x4 v[24];
    if (act) {
#pragma unroll
      for (int i = 0; i < 24; ++i) v[i] = *(const u32x4*)(wsrc + (size_t)i * 8 * 9216);
    }
    if (act) {
      float cs[8];
#pragma unroll
      for (int j = 0; j < 8; ++j) cs[j] = 0.f;
#pragma unroll
      for (int i = 0; i < 24; ++i)
#pragma unroll
        for (int j = 0; j < 4; ++j) { cs[2 * j] += bf_lo(v[i][j]); cs[2 * j + 1] += bf_hi(v[i][j]); }
      lds_ptr cp = lds + K2_CS + sw * 2048;
      *(LDS_AS f32x4*)(cp + lane * 32) = f32x4{cs[0], cs[1], cs[2], cs[3]};
      *(LDS_AS f32x4*)(cp + lane * 32 + 16) = f32x4{cs[4], cs[5], cs[6], cs[7]};
      asm volatile("s_waitcnt lgkmcnt(0)" ::: "memory");
      float t = 0.f;
#pragma unroll
      for (int i = 0; i < 8; ++i) t += *(LDS_AS float*)(cp + i * 256 + lane * 4);
      *(float*)(ws + WS_CSP + ((size_t)part * 9216 + c64 * 64 + lane) * 4) = t;
    }
    if (has_tile) { raw_bar(); raw_bar(); }
    return;
  }
  if (wid >= 8) {
    raw_bar();
    __builtin_amdgcn_s_sleep(K2_SL0);
    if (has_tile) {
      const int par = wid - 8;
      unsigned va[4], vb[4];
#pragma unroll
      for (int i = 0; i < 4; ++i) {
        const int rho = 8 * i + (lane >> 3);
        const int g_ = ((rho >> 4) & 1) * 2 + ((rho >> 2) & 1), t_ = (rho >> 3) & 1, q_ = rho & 3;
        const int La = 36 * par + 18 * (g_ >> 1) + 8 * t_ + 4 * (g_ & 1) + q_, Lb = 288 * par + 18 * (4 * g_ + 2 * t_ + (q_ >> 1)) + 16 + (q_ & 1);
        const unsigned csw = ((lane & 7) ^ (((rho >> 1) & 3) << 1)) * 16;
        va[i] = La * 1152 + csw; vb[i] = Lb * 1152 + csw;
      }
#pragma unroll
      for (int kp = 0; kp < 9; ++kp)
#pragma unroll
        for (int i = 0; i < 4; ++i)
          glds16(wo + nt * 128 + (size_t)(kp == 8 ? 0 : 72 * kp) * 1152, kp == 8 ? vb[i] : va[i], lds0 + K2_W + par * 4096 + kp * 8192 + i * 1024);
    }
  } else {
    const int t = qb * 8 + wid;
    bf16x8 q0;
    unsigned qx;
    const unsigned vo = lane * 16;
    const int kt0 = qb * 8 - 8;
    asm volatile("global_load_dwordx4 %0, %2, %3\n\tglobal_load_dword %1, %4, %5"
                 : "=&v"(q0), "=&v"(qx)
                 : "v"(vo), "s"(ws + WS_QM + (size_t)hd * 65536 + (size_t)t * 1024), "v"((lane & 31) * 8),
                   "s"(ws + WS_QX + (size_t)(hd >> 1) * 16384 + (size_t)t * 256 + (hd & 1) * 4)
                 : "memory");
#pragma unroll
    for (int i = 0; i < 2; ++i) {
      const int lt = wid * 2 + i;
      const int kt = max(kt0 + lt, 0);
      glds16(ws + WS_KM + (size_t)hd * 65536 + (size_t)kt * 1024, vo, lds0 + K2_KM + lt * 1024);
    }
    if (wid >= 4) {
      const int k0 = max(qb * 256 - 256 + (wid - 4) * 128, 0);
      glds16(ws + WS_KX + (size_t)(hd >> 1) * 16384 + (size_t)k0 * 8, vo, lds0 + K2_KX + (wid - 4) * 1024);
    }
    const long vb0 = (long)(hd * 128 + qb * 16 - 16) * 592;
#pragma unroll
    for (int i = 0; i < 3; ++i) {
      const int pc = wid + 8 * i;
      if (pc < 19) {
        long off = vb0 + pc * 1024;
        unsigned v2 = vo;
        if (qb == 0) { off = (long)(hd * 128) * 592 + pc * 1024; v2 = (pc * 1024 + lane * 16 >= 9472) ? vo - 9472 + 65536 : vo + 65536; off -= 65536; }
        glds16(ws + WS_V + off, v2, lds0 + K2_V + pc * 1024);
      }
    }
    raw_bar();
    asm volatile("s_waitcnt vmcnt(0)" : "+v"(q0), "+v"(qx)::"memory");
    lds_sync(lds + K2_SY, 8, lane);
    k2_core(lds, ws, hd, t, wid, lane, q0, qx);
    STAMP_MARK(1, 3);
    STAMP_MARKW(1, 7, 448);
  }
  wait_vm<0>();
  STAMP_MARK(1, 4);
  unsigned* cnt = (unsigned*)(ws + WS_CNT);
  if (wid < 8) {
    if (lds_arrive(lds + K2_SY + 8, lane) == 7 && lane == 0) __hip_atomic_fetch_add(cnt + qb * 32, 1u, __ATOMIC_RELAXED, __HIP_MEMORY_SCOPE_AGENT);
  }
  if (!has_tile) { STAMP_END(1); return; }
  lds_sync(lds + K2_SY + 12, 10, lane);
  const int mi = wid >> 1, kg = wid & 1;
  const int g = lane >> 4, q = (lane & 15) >> 2, p = lane & 3;
  const int er = tid >> 3, ec = tid & 7;
  const u32x4 res = *(const u32x4*)(x + (size_t)(m0 + er) * 1152 + (nt * 64 + ec * 8) * 2);
  const int rho0 = 16 * (g >> 1) + 4 * (g & 1) + q;
  lds_ptr wb[4];
#pragma unroll
  for (int n = 0; n < 4; ++n) { wb[n] = lds + K2_W + kg * 4096 + rho0 * 128 + ((n ^ ((rho0 >> 1) & 3)) * 32) + p * 8; asm volatile("" : "+v"(wb[n])); }
  bf16x8 wf[5][4];
#pragma unroll
  for (int j = 0; j < 5; ++j)
#pragma unroll
    for (int n = 0; n < 4; ++n) wf[j][n] = cat8(lds_tr(wb[n] + j * 8192), lds_tr(wb[n] + j * 8192 + 1024));
  if (tid == 576) {
    const unsigned* c0 = cnt + (m0 >> 8) * 32;
    const unsigned* c1 = cnt + ((m0 + 79) >> 8) * 32;
    for (int spin = 0; spin < (1 << 18); ++spin) {
      const unsigned a = __hip_atomic_load(c0, __ATOMIC_RELAXED, __HIP_MEMORY_SCOPE_AGENT);
      const unsigned b = __hip_atomic_load(c1, __ATOMIC_RELAXED, __HIP_MEMORY_SCOPE_AGENT);
      if (a >= 32u && b >= 32u) break;
      __builtin_amdgcn_s_sleep(1);
    }
  }
  SB();
  raw_bar();
  SB();
  STAMP_MARK(1, 5);
  const __amdgpu_buffer_rsrc_t ors = __builtin_amdgcn_make_buffer_rsrc((void*)(ws + WS_O), 0, 128 * 18432, 0x00020000);
  const int aoff = (rb0 + mi) * 18432 + kg * 1024 + lane * 16;
  bf16x8 a[9];
#pragma unroll
  for (int i = 0; i < 9; ++i) a[i] = __builtin_bit_cast(bf16x8, __builtin_amdgcn_raw_buffer_load_b128(ors, aoff + i * 2048, 0, 16));
  SB();
  f32x4 acc[4];
#pragma unroll
  for (int n = 0; n < 4; ++n) acc[n] = f32x4{0.f, 0.f, 0.f, 0.f};
#pragma unroll
  for (int j = 0; j < 9; ++j) {
#pragma unroll
    for (int n = 0; n < 4; ++n) {
      acc[n] = MFMA16(wf[j % 5][n], a[j], acc[n]);
      if (j + 5 < 9) wf[j % 5][n] = cat8(lds_tr(wb[n] + (j + 5) * 8192),
                                         lds_tr(wb[n] + (j + 5) * 8192 + 1024));
    }
  }
  STAMP_MARK(1, 6);
  {
    lds_ptr cw = lds + kg * (80 * 272) + (mi * 16 + (lane & 15)) * 272 + 16 * g;
#pragma unroll
    for (int n = 0; n < 4; ++n) *(LDS_AS f32x4*)(cw + n * 64) = acc[n];
  }
  lds_bar();
  {
    lds_ptr cr = lds + er * 272 + ec * 32;
    f32x4 a0 = *(LDS_AS f32x4*)(cr), a1 = *(LDS_AS f32x4*)(cr + 16);
    f32x4 b0 = *(LDS_AS f32x4*)(cr + 80 * 272), b1 = *(LDS_AS f32x4*)(cr + 80 * 272 + 16);
    a0 += b0; a1 += b1;
    u32x4 o;
    o[0] = cvt_pk(a0[0] + bf_lo(res[0]), a0[1] + bf_hi(res[0]));
    o[1] = cvt_pk(a0[2] + bf_lo(res[1]), a0[3] + bf_hi(res[1]));
    o[2] = cvt_pk(a1[0] + bf_lo(res[2]), a1[1] + bf_hi(res[2]));
    o[3] = cvt_pk(a1[2] + bf_lo(res[3]), a1[3] + bf_hi(res[3]));
    const int row = m0 + er, col = nt * 64 + ec * 8;
    st16(ws + WS_H + (size_t)(row >> 4) * 18432 + (col >> 5) * 1024 + ((col >> 3) & 3) * 256 + (row & 15) * 16, o);
    float s1 = 0.f, s2 = 0.f;
#pragma unroll
    for (int j = 0; j < 4; ++j) { s1 = dot2(o[j], 0x3f803f80u, s1); s2 = dot2(o[j], o[j], s2); }
    s1 = sum8(s1); s2 = sum8(s2);
    if (ec == 0) *(f32x2*)(ws + WS_ST + (size_t)row * 80 + nt * 8) = f32x2{s1, s2};
  }
  STAMP_END(1);
}

constexpr int K4_SLOT = 18432, K4_ST = K4_SLOT * 8, K4_CS = K4_ST + 2048, K4_PF = K4_CS + 2048, K4_CP = K4_PF + 1024, K4_SY = 160000, K4_LDS = 160 * 1024;

constexpr int k4_frag(int i) { return i < 16 ? ((i >> 2) * 2 + (i & 1) + ((i & 2) ? 9 : 0)) : (i == 16 ? 8 : 17); }
constexpr int k4_foff(int f) { return (f >= 9 ? 9216 + (f - 9) * 32 : f * 32); }
constexpr int K4_T0 = 13 * 18;
constexpr int k4_slab(int G) { return G < K4_T0 ? G / 18 : (G - K4_T0 < 80 ? 13 + ((G - K4_T0) % 20) / 4 : 13 + (G - K4_T0 - 80) / 2); }
constexpr int k4_pos(int G) { return G < K4_T0 ? G % 18 : (G - K4_T0 < 80 ? ((G - K4_T0) / 20) * 4 + ((G - K4_T0) & 3) : 16 + ((G - K4_T0) & 1)); }
constexpr int k4_op(int G) {
  const int s = G / 18, i = G % 18;
  if (s > 12) return 0;
  if (s < 3) {
    const int pos[10] = {0, 2, 4, 6, 7, 9, 11, 13, 15, 16};
    for (int k = 0; k < 10; ++k) if (pos[k] == i) return 1 + (2 * s + 2 + k / 5) * 8 + k % 5;
    return 0;
  }
  if (s == 3) {
    const int pos[8] = {0, 2, 4, 6, 8, 11, 14, 16};
    for (int k = 0; k < 8; ++k) if (pos[k] == i) return k < 5 ? 1 + 8 * 8 + k : 1 + 9 * 8 + (k - 3);
    return 0;
  }
  if (i == 1) return 1 + (s + 5) * 8;
  if (i == 4) return 1 + (s + 5) * 8 + 1;
  if (s + 6 > 17) return 0;
  if (i == 8) return 1 + (s + 6) * 8 + 2;
  if (i == 13) return 1 + (s + 6) * 8 + 3;
  if (i == 16) return 1 + (s + 6) * 8 + 4;
  return 0;
}
constexpr int k4_allow(int G, int need) {
  int n = (need == 0) ? 4 : 0;
  for (int g = 0; g < G; ++g) {
    const int op = k4_op(g);
    if (op == 0) continue;
    const int sl = (op - 1) >> 3, kind = (op - 1) & 7;
    if (sl <= need) n = 0;
    else if (kind != 4) ++n;
  }
  return n;
}
constexpr int k4_need(int G) {
  if (G + 5 >= 324 || G == 0) return 0;
  int mx = 0;
  for (int g = 0; g < G + 5; ++g) mx = k4_slab(g) > mx ? k4_slab(g) : mx;
  return k4_slab(G + 5) > mx ? k4_slab(G + 5) : 0;
}

constexpr int d_off(int i) { return i < 6 ? 36864 + i * 9216 : (i < 10 ? (i - 6) * 9216 : (i < 17 ? 92160 + (i - 10) * 9216 : 36864)); }
constexpr int d_i(int H) { return H < 90 ? H / 9 : (H - 90 < 64 ? 10 + ((H - 90) % 16) / 2 : 10 + (H - 154)); }
constexpr int d_f(int H) { return H < 90 ? H % 9 : (H - 90 < 64 ? ((H - 90) / 16) * 2 + ((H - 90) & 1) : 8); }
constexpr int d_first(int H) {
  for (int h = 0; h < H; ++h) if (d_i(h) == d_i(H)) return 0;
  return 1;
}
DI void ld2_sc1(bf16x8& d0, bf16x8& d1, unsigned o0, unsigned o1, const char* base) {
  asm volatile("global_load_dwordx4 %0, %2, %4 sc1\n\tglobal_load_dwordx4 %1, %3, %4 sc1" : "=&v"(d0), "=&v"(d1) : "v"(o0), "v"(o1), "s"(base) : "memory");
}
template <int N> DI void wait_vm2(bf16x8& d0, bf16x8& d1) { asm volatile("s_waitcnt vmcnt(%2)" : "+v"(d0), "+v"(d1) : "n"(N) : "memory"); }
DI void poll_count(const unsigned* c, unsigned n) {
  for (int spin = 0; spin < (1 << 18); ++spin) {
    if (__hip_atomic_load(c, __ATOMIC_RELAXED, __HIP_MEMORY_SCOPE_AGENT) >= n) break;
    __builtin_amdgcn_s_sleep(1);
  }
}

__global__ __launch_bounds__(512, 2) void k4_gateup(const char* __restrict__ wgate, const char* __restrict__ wup, char* __restrict__ ws,
                                                    const char* __restrict__ wd, char* __restrict__ out) {
  extern __shared__ __attribute__((aligned(16))) char smem[];
  STAMP_BEGIN
  lds_ptr lds = (lds_ptr)smem;
  const unsigned lds0 = (unsigned)(uintptr_t)lds;
  const int tid = threadIdx.x, lane = tid & 63, wid = __builtin_amdgcn_readfirstlane(tid >> 6);
  const int xcd = blockIdx.x & 7, idx = blockIdx.x >> 3;
  const int pn = 4 * (xcd >> 2) + (idx & 3), jt = 8 * (xcd & 3) + (idx >> 2);
  const int wm = wid & 3, wn = wid >> 2;
  const char* asrc = ws + WS_H + (size_t)(pn * 16 + 2 * wid) * 18432;
  const unsigned avo = lane * 16, avo2 = lane * 16 + 18432;
  const char* wsrc = (wn ? wup : wgate) + jt * 288;
  unsigned wvo[3];
#pragma unroll
  for (int i = 0; i < 3; ++i) {
    const int pc = i < 2 ? 2 * wm + i : 8;
    const int o = pc * 1024 + lane * 16;
    const int rho = o / 288, u = (o - rho * 288) >> 4;
    const int gk = ((rho >> 4) & 1) * 2 + ((rho >> 2) & 1), tk = (rho >> 3) & 1, qk = rho & 3;
    wvo[i] = (8 * gk + 4 * tk + qk) * 9216 + u * 16;
  }
  const unsigned wdst = lds0 + wn * 9216;
  auto issue_w = [&](int s, int i) {
    const unsigned so = (s & 7) * K4_SLOT;
    const char* wp = wsrc + (size_t)s * 32 * 9216;
    if (i < 2) glds16(wp, wvo[i], wdst + so + wm * 2048 + i * 1024);
    else glds16_if0(wm, wp, wvo[2], wdst + so + 8192);
  };
  bf16x8 A[6][2];
  auto issue_a = [&](int s, bf16x8& dst, int m) {
    asm volatile("global_load_dwordx4 %0, %1, %2" : "=v"(dst) : "v"(m ? avo2 : avo), "s"(asrc + s * 1024) : "memory");
  };
  f32x4 sp0, sp1, sp2, sp3;
  f32x2 sp4;
  asm volatile("global_load_dwordx4 %0, %5, %6\n\tglobal_load_dwordx4 %1, %5, %6 offset:16\n\tglobal_load_dwordx4 %2, %5, %6 offset:32\n\t"
               "global_load_dwordx4 %3, %5, %6 offset:48\n\tglobal_load_dwordx2 %4, %5, %6 offset:64"
               : "=&v"(sp0), "=&v"(sp1), "=&v"(sp2), "=&v"(sp3), "=&v"(sp4)
               : "v"((lane & 31) * 80), "s"(ws + WS_ST + (size_t)(pn * 256 + wid * 32) * 80) : "memory");
  if (wm < 3 && lane < 36) glds16(ws + WS_CSP + (size_t)(wm * 9216 + wn * FF + jt * 144) * 4, lane * 16, lds0 + K4_CP + wm * 1152 + wn * 576);
  for_seq<2>([&](auto S) { issue_w(S, 0); issue_w(S, 1); issue_w(S, 2); issue_a(S, A[S][0], 0); issue_a(S, A[S][1], 1); });
  {
    asm volatile("s_waitcnt vmcnt(8)" : "+v"(sp0), "+v"(sp1), "+v"(sp2), "+v"(sp3), "+v"(sp4)::"memory");
    const float s1 = sp0[0] + sp0[2] + sp1[0] + sp1[2] + sp2[0] + sp2[2] + sp3[0] + sp3[2] + sp4[0];
    const float s2 = sp0[1] + sp0[3] + sp1[1] + sp1[3] + sp2[1] + sp2[3] + sp3[1] + sp3[3] + sp4[1];
    const float mean = s1 * (1.f / 576.f);
    const float var = fmaxf(s2 * (1.f / 576.f) - mean * mean, 0.f);
    *(LDS_AS f32x2*)(lds + K4_ST + (wid * 32 + (lane & 31)) * 8) = f32x2{-mean, __builtin_amdgcn_rsqf(var + LN_EPS)};
    *(LDS_AS unsigned*)(lds + K4_SY) = 0u;
    SB();
  }
  const int g = lane >> 4, q = (lane & 15) >> 2, p = lane & 3, r = lane & 15;
  lds_ptr wb = lds + (16 * (g >> 1) + 4 * (g & 1) + q) * 288 + p * 8;
  lds_ptr wbs[3] = {wb, wb + 3 * K4_SLOT, wb + 6 * K4_SLOT};
  asm volatile("" : "+v"(wbs[1]), "+v"(wbs[2]));
  lds_ptr csb = lds + K4_CS + 16 * g;
  asm volatile("" : "+v"(csb));
  f32x4 acc[2][18];
  asm volatile("s_waitcnt vmcnt(%2)\n\ts_barrier" : "+v"(A[0][0]), "+v"(A[0][1]) : "n"(k4_allow(0, 0)) : "memory");
  STAMP_MARK(2, 2);
  STAMP_CYC0
  s16x4 Bl[6], Bh[6];
  for_seq<5>([&](auto I) { Bl[I] = lds_tr(wb + k4_foff(k4_frag(I))); Bh[I] = lds_tr(wb + k4_foff(k4_frag(I)) + 2304); });
  SB();
  const float L2E = 1.4426950408889634f;
  char* hb = ws + WS_HID + (size_t)(pn * 16 + wid * 2) * (144 * 1024) + (size_t)(jt >> 2) * 18432;
  f32x2 st[2];
  float c1[2];
  auto glu = [&](float ag, float au, float cg, float cu, int m) -> float {
    const float tg = fmaf(st[m][0], cg, ag), tu = fmaf(st[m][0], cu, au);
    const float e = __builtin_amdgcn_exp2f(tg * c1[m]);
    return tu * st[m][1] * __builtin_amdgcn_rcpf(1.f + e);
  };
  f32x4 cg[2], cu[2];
  u32x4 own[4][2];
  auto slice = [&](auto QQ, auto KK) {
    constexpr int Q = QQ, k = KK, m = k >> 2, d = k & 3, h = d >> 1, e = (d & 1) * 2;
    const f32x4 ag = acc[m][2 * Q + h], au = acc[m][9 + 2 * Q + h];
    own[Q][m][d] = cvt_pk(glu(ag[e], au[e], cg[h][e], cu[h][e], m), glu(ag[e + 1], au[e + 1], cg[h][e + 1], cu[h][e + 1], m));
    if constexpr (d == 3) st16(hb + (size_t)m * (144 * 1024) + (4 * (jt & 3) + Q) * 1024 + lane * 16, own[Q][m]);
    else asm volatile("" : "+v"(own[Q][m]));
  };
  unsigned dvo[2], dvl[2];
  const char* dsrc = wd + (size_t)(jt >> 2) * 576 * 1152 + (jt & 3) * 288;
  auto dvoff = [&]() {
    int ln = tid;
    asm volatile("" : "+v"(ln));
    ln &= 63;
#pragma unroll
    for (int i = 0; i < 2; ++i) {
      const int o = (i ? 8 : wid) * 1024 + ln * 16;
      const int rho = o / 288, u = (o - rho * 288) >> 4;
      const int gk = ((rho >> 4) & 1) * 2 + ((rho >> 2) & 1), tk = (rho >> 3) & 1, qk = rho & 3;
      dvo[i] = (16 * tk + 4 * gk + qk) * 1152 + u * 16;
      dvl[i] = (144 * (gk >> 1) + 128 + 8 * (gk & 1) + 4 * tk + qk) * 1152 + u * 16;
    }
  };
  auto issue_d = [&](auto II, int pc) {
    constexpr int i = II;
    const int k = (4 * (jt & 3) + i) & 15;
    const char* src = dsrc + (size_t)(i < 16 ? 144 * (k >> 2) + 32 * (k & 3) : 288 * (i - 16)) * 1152;
    if (pc == 0) glds16(src, i < 16 ? dvo[0] : dvl[0], lds0 + d_off(i) + wid * 1024);
    else glds16_if0(wid ^ (i & 7), src, i < 16 ? dvo[1] : dvl[1], lds0 + d_off(i) + 8192);
  };
  for_seq<18>([&](auto GH) { for_seq<18>([&](auto GL) {
    constexpr int G = GH * 18 + GL, sl = k4_slab(G), f = k4_frag(k4_pos(G)), rb = G % 6, nb = (G + 5) % 6, NG = G + 5;
    const bf16x8 b = cat8(Bl[rb], Bh[rb]);
    constexpr int need = k4_need(G);
    if constexpr (need != 0) {
      asm volatile("s_waitcnt vmcnt(%2)\n\ts_barrier" : "+v"(A[need % 6][0]), "+v"(A[need % 6][1]) : "n"(k4_allow(G, need)) : "memory");
      SB();
    }
    if constexpr (sl == 0) mfma16_zero(acc[0][f], b, A[0][0]); else mfma16_acc(acc[0][f], b, A[sl % 6][0]);
    SB();
    if constexpr (NG < 324) { Bl[nb] = lds_tr(wbs[(k4_slab(NG) & 7) / 3] + ((k4_slab(NG) & 7) % 3) * K4_SLOT + k4_foff(k4_frag(k4_pos(NG)))); SB(); }
    if constexpr (sl == 0) mfma16_zero(acc[1][f], b, A[0][1]); else mfma16_acc(acc[1][f], b, A[sl % 6][1]);
    SB();
    if constexpr (NG < 324) { Bh[nb] = lds_tr(wbs[(k4_slab(NG) & 7) / 3] + ((k4_slab(NG) & 7) % 3) * K4_SLOT + k4_foff(k4_frag(k4_pos(NG))) + 2304); SB(); }
    constexpr int op = k4_op(G);
    if constexpr (op != 0) {
      constexpr int os = (op - 1) >> 3, kind = (op - 1) & 7;
      if constexpr (kind < 2) issue_a(os, A[os % 6][kind], kind);
      else issue_w(os, kind - 2);
      SB();
    }
    if constexpr (G % 18 == 10 && (G / 18 == 5 || G / 18 == 7 || G / 18 == 9)) {
      constexpr int k = (G / 18 - 5) / 2;
      touch_if0((int)(blockIdx.x / 216) + (wid ^ (k + 1)), wd + (size_t)(blockIdx.x * 3 + k) * 8192, avo * 8, lds0 + K4_PF + k * 256);
      SB();
    }
    if constexpr (G == 60) {
      lds_ptr cpp = lds + K4_CP + wid * 144 + lane * 4;
      const float t = *(LDS_AS float*)(cpp) + *(LDS_AS float*)(cpp + 1152) + *(LDS_AS float*)(cpp + 2304);
      *(LDS_AS float*)(lds + K4_CS + wid * 144 + min(lane, 36) * 4 + (lane >= 36 ? 1152 : 0)) = t;
      asm volatile("s_waitcnt lgkmcnt(0)" ::: "memory");
      SB();
    }
    if constexpr (G == K4_T0) {
      STAMP_MARK(2, 3);
      STAMP_CYC(2, 4);
#pragma unroll
      for (int m = 0; m < 2; ++m) { st[m] = *(LDS_AS f32x2*)(lds + K4_ST + (wid * 32 + m * 16 + r) * 8); c1[m] = -L2E * st[m][1]; }
      SB();
    }
    if constexpr (G >= K4_T0) {
      constexpr int t = G - K4_T0, u = t % 20;
      if constexpr (t == 11) { dvoff(); SB(); }
      if constexpr (t >= 12 && t < 24) { issue_d(std::integral_constant<int, (t - 12) / 2>{}, t & 1); SB(); }
      if constexpr (t < 80 && u == 18) {
#pragma unroll
        for (int h = 0; h < 2; ++h) {
          cg[h] = *(LDS_AS f32x4*)(csb + (2 * (t / 20) + h) * 64);
          cu[h] = *(LDS_AS f32x4*)(csb + 576 + (2 * (t / 20) + h) * 64);
        }
        SB();
      }
      if constexpr (t >= 20 && t < 80 && u >= 1 && u < 17 && (u & 1)) { slice(std::integral_constant<int, t / 20 - 1>{}, std::integral_constant<int, u / 2>{}); SB(); }
      if constexpr (t >= 81 && t < 89) { slice(std::integral_constant<int, 3>{}, std::integral_constant<int, t - 81>{}); SB(); }
    }
  }); });
  {
    asm volatile("s_nop 7\n\ts_nop 1" : "+v"(acc[0][8]), "+v"(acc[1][8]), "+v"(acc[0][17]), "+v"(acc[1][17]));
    const f32x4 cg8 = *(LDS_AS f32x4*)(csb + 512);
    const f32x4 cu8 = *(LDS_AS f32x4*)(csb + 576 + 512);
#pragma unroll
    for (int m = 0; m < 2; ++m) {
      const f32x4 ag = acc[m][8], au = acc[m][17];
      u32x2 o = {cvt_pk(glu(ag[0], au[0], cg8[0], cu8[0], m), glu(ag[1], au[1], cg8[1], cu8[1], m)),
                 cvt_pk(glu(ag[2], au[2], cg8[2], cu8[2], m), glu(ag[3], au[3], cg8[3], cu8[3], m))};
      st8wt(hb + (size_t)m * (144 * 1024) + (16 + ((jt >> 1) & 1)) * 1024 + (2 * (jt & 1) + (g >> 1)) * 256 + r * 16 + (g & 1) * 8, o);
    }
  }
  STAMP_MARK(2, 5);
  const int sb = jt >> 2, nq = jt & 3;
  unsigned* cntA = (unsigned*)(ws + WS_CNT + 1024) + (pn * 8 + sb) * 32;
  unsigned* cntB = (unsigned*)(ws + WS_CNT + 1024 + 8192) + (pn * 4 + nq) * 32;
  wait_bar<10>();
  auto dptr = [&](auto HH) -> lds_ptr {
    constexpr int H = HH, o = d_off(d_i(H)) + d_f(H) * 32;
    return wbs[o / (3 * K4_SLOT)] + o % (3 * K4_SLOT);
  };
  for_seq<5>([&](auto I) { Bl[I] = lds_tr(dptr(I)); Bh[I] = lds_tr(dptr(I) + 2304); });
  SB();
  f32x4 dacc[2][9];
  bf16x8 DA[14][2];
  char* pb = ws + WS_PART + (size_t)(((pn * 4 + nq) * 8 + wid) * 8 + sb) * 9216 + lane * 16;
  for_seq<18>([&](auto HH) { for_seq<9>([&](auto HL) {
    constexpr int H = HH * 9 + HL, i = d_i(H), f = d_f(H), rb = H % 6, nb = (H + 5) % 6, NH = H + 5;
    if constexpr (H == 36) {
      if (tid == 0) poll_count(cntA, 4u);
      SB();
      wait_bar<0>();
      STAMP_MARK(2, 6);
      issue_d(std::integral_constant<int, 17>{}, 0); issue_d(std::integral_constant<int, 17>{}, 1);
      const unsigned ho = lane * 16, ho2 = lane * 16 + 144 * 1024;
      for_seq<14>([&](auto JJ) {
        constexpr int j = JJ;
        const int k = j < 12 ? ((4 * nq + 4 + j) & 15) : 4 + j;
        ld2_sc1(DA[j][0], DA[j][1], ho, ho2, hb + k * 1024);
      });
      SB();
    }
    if constexpr (i >= 4 && d_first(H)) {
      wait_vm2<2 * (17 - i)>(DA[i - 4][0], DA[i - 4][1]);
      SB();
    }
    if constexpr (H == 90) { raw_bar(); SB(); }
    const bf16x8 b = cat8(Bl[rb], Bh[rb]);
    bf16x8 a0, a1;
    if constexpr (i < 4) { a0 = __builtin_bit_cast(bf16x8, own[i][0]); a1 = __builtin_bit_cast(bf16x8, own[i][1]); }
    else { a0 = DA[i - 4][0]; a1 = DA[i - 4][1]; }
    if constexpr (i == 0) mfma16_zero(dacc[0][f], b, a0); else mfma16_acc(dacc[0][f], b, a0);
    SB();
    if constexpr (NH < 162) { Bl[nb] = lds_tr(dptr(std::integral_constant<int, NH>{})); SB(); }
    if constexpr (i == 0) mfma16_zero(dacc[1][f], b, a1); else mfma16_acc(dacc[1][f], b, a1);
    SB();
    if constexpr (NH < 162) { Bh[nb] = lds_tr(dptr(std::integral_constant<int, NH>{}) + 2304); SB(); }
    if constexpr (H < 22) { issue_d(std::integral_constant<int, 6 + H / 2>{}, H & 1); SB(); }
    if constexpr (H == 24) {
      wait_vm<11>();
      if (lds_arrive(lds + K4_SY, lane) == 7 && lane == 0) __hip_atomic_fetch_add(cntA, 1u, __ATOMIC_RELAXED, __HIP_MEMORY_SCOPE_AGENT);
      SB();
    }
    if constexpr (H >= 106 && (((H - 90) % 16 == 3 || (H - 90) % 16 == 5))) {
      constexpr int pr = (H - 90) / 16 - 1, m = ((H - 90) % 16 == 5);
      const f32x4 x0 = dacc[m][2 * pr], x1 = dacc[m][2 * pr + 1];
      st16(pb + (m * 4 + pr) * 1024, u32x4{cvt_pk(x0[0], x0[1]), cvt_pk(x0[2], x0[3]), cvt_pk(x1[0], x1[1]), cvt_pk(x1[2], x1[3])});
      SB();
    }
  }); });
  {
    asm volatile("s_nop 7\n\ts_nop 1" : "+v"(dacc[0][8]), "+v"(dacc[1][8]));
    const f32x4 x0 = dacc[0][8], x1 = dacc[1][8];
    st16(pb + 8192, u32x4{cvt_pk(x0[0], x0[1]), cvt_pk(x0[2], x0[3]), cvt_pk(x1[0], x1[1]), cvt_pk(x1[2], x1[3])});
  }
  wait_bar<0>();
  STAMP_MARK(2, 7);
  if (tid == 0) __hip_atomic_fetch_add(cntB, 1u, __ATOMIC_RELAXED, __HIP_MEMORY_SCOPE_AGENT);
  {
    const int nc = wid == 1 ? 2 : 1;
    const int rbase = pn * 256 + sb * 32;
    int row[2][2], col[2][2];
    row[0][0] = row[0][1] = rbase + (wid >> 2) * 16 + r; col[0][0] = nq * 144 + (wid & 3) * 32 + 4 * g; col[0][1] = col[0][0] + 16;
    row[1][0] = rbase + r; row[1][1] = rbase + 16 + r; col[1][0] = col[1][1] = nq * 144 + 128 + 4 * g;
    u32x2 res[2][2];
#pragma unroll
    for (int c = 0; c < 2; ++c)
#pragma unroll
      for (int e = 0; e < 2; ++e)
        res[c][e] = *(const u32x2*)(ws + WS_H + (size_t)(row[c][e] >> 4) * 18432 + (col[c][e] >> 5) * 1024 + ((col[c][e] >> 3) & 3) * 256 + (row[c][e] & 15) * 16 + ((col[c][e] >> 2) & 1) * 8);
    if (tid == 0) poll_count(cntB, 8u);
    SB();
    raw_bar();
    SB();
    const __amdgpu_buffer_rsrc_t prs = __builtin_amdgcn_make_buffer_rsrc((void*)(ws + WS_PART + (size_t)((pn * 4 + nq) * 8 + sb) * 8 * 9216), 0, 8 * 9216, 0x00020000);
#pragma unroll
    for (int c = 0; c < 2; ++c) {
      if (c < nc) {
        u32x4 pv[8];
#pragma unroll
        for (int k = 0; k < 8; ++k) pv[k] = __builtin_amdgcn_raw_buffer_load_b128(prs, lane * 16 + (c ? 8192 : wid * 1024), k * 9216, 16);
        float sum[8];
#pragma unroll
        for (int e = 0; e < 4; ++e) { sum[2 * e] = bf_lo(res[c][e >> 1][e & 1]); sum[2 * e + 1] = bf_hi(res[c][e >> 1][e & 1]); }
        float acc2[8];
#pragma unroll
        for (int e = 0; e < 8; ++e) acc2[e] = 0.f;
#pragma unroll
        for (int k = 0; k < 8; ++k)
#pragma unroll
          for (int e = 0; e < 4; ++e) { acc2[2 * e] += bf_lo(pv[k][e]); acc2[2 * e + 1] += bf_hi(pv[k][e]); }
#pragma unroll
        for (int e = 0; e < 2; ++e)
          *(u32x2*)(out + (size_t)row[c][e] * 1152 + col[c][e] * 2) =
              u32x2{cvt_pk(sum[4 * e] + acc2[4 * e], sum[4 * e + 1] + acc2[4 * e + 1]), cvt_pk(sum[4 * e + 2] + acc2[4 * e + 2], sum[4 * e + 3] + acc2[4 * e + 3])};
      }
    }
  }
  STAMP_END(2);
}

extern "C" void kernel_launch(void* const* d_in, const int* in_sizes, int n_in, void* d_out, int out_size, void* d_ws, size_t ws_size,
                              hipStream_t stream) {
  const char* x = (const char*)d_in[0];
  const char* wqkv = (const char*)d_in[1];
  const char* wo = (const char*)d_in[2];
  const char* wg = (const char*)d_in[4];
  const char* wu = (const char*)d_in[5];
  const char* wd = (const char*)d_in[6];
  char* ws = (char*)d_ws + ((ws_size - WS_END) & ~(size_t)0x1fffff);
  (void)hipFuncSetAttribute((const void*)k1_ln_qkv, hipFuncAttributeMaxDynamicSharedMemorySize, K1_LDS);
  (void)hipFuncSetAttribute((const void*)k2_attn, hipFuncAttributeMaxDynamicSharedMemorySize, K2_LDS);
  (void)hipFuncSetAttribute((const void*)k4_gateup, hipFuncAttributeMaxDynamicSharedMemorySize, K4_LDS);
  k1_ln_qkv<<<234, 1024, K1_LDS, stream>>>(x, wqkv, ws, wo);
  k2_attn<<<256, 768, K2_LDS, stream>>>(ws, wg, wu, wo, x);
  k4_gateup<<<256, 512, K4_LDS, stream>>>(wg, wu, ws, wd, (char*)d_out);
}
```

```cpp
#define STAMPS 1
#include <hip/hip_runtime.h>
#include <cstdio>
#include <hip/hip_bf16.h>
#include <stdint.h>
#include <utility>
#include <type_traits>

typedef __attribute__((ext_vector_type(8))) short bf16x8;
typedef __attribute__((ext_vector_type(4))) short s16x4;
typedef __attribute__((ext_vector_type(4))) float f32x4;
typedef __attribute__((ext_vector_type(16))) float f32x16;
typedef __attribute__((ext_vector_type(4))) unsigned u32x4;
typedef __attribute__((ext_vector_type(2))) unsigned u32x2;
typedef __attribute__((ext_vector_type(2))) __bf16 bf16x2_t;
typedef __attribute__((ext_vector_type(2))) float f32x2;
#define LDS_AS __attribute__((address_space(3)))
typedef LDS_AS char* lds_ptr;
#define DI __device__ __forceinline__

constexpr int SEQ = 2048, DM = 576, NH = 32, FF = 4608;
constexpr float LN_EPS = 1e-5f;
constexpr float QSCALE = 0.23570226039551584f * 1.4426950408889634f;

constexpr size_t WS_QM = 0;
constexpr size_t WS_KM = WS_QM + (size_t)32 * 64 * 1024;
constexpr size_t WS_QX = WS_KM + (size_t)32 * 64 * 1024;
constexpr size_t WS_KX = WS_QX + (size_t)16 * 2048 * 8;
constexpr size_t WS_V = WS_KX + (size_t)16 * 2048 * 8;
constexpr size_t WS_O = WS_V + (size_t)32 * 128 * 592 + 1024;
constexpr size_t WS_H = WS_O + (size_t)128 * 18 * 1024;
constexpr size_t WS_ST = WS_H + (size_t)128 * 18 * 1024;
constexpr size_t WS_CSP = WS_ST + (size_t)2048 * 80;
constexpr size_t WS_CS = WS_CSP + (size_t)8 * 9216 * 4;
constexpr size_t WS_HID = WS_CS + (size_t)9216 * 4;
constexpr size_t WS_CNT = WS_HID + (size_t)128 * 144 * 1024;
constexpr size_t WS_PART = WS_CNT + 16384;
constexpr size_t WS_END = WS_PART + (size_t)2048 * 9216;
static_assert(WS_O % 1024 == 0 && WS_H % 1024 == 0 && WS_ST % 256 == 0 && WS_CSP % 256 == 0 && WS_HID % 256 == 0, "align");

constexpr size_t WS_DBG = (size_t)64 << 20;
#define STAMP_BEGIN const unsigned long long stamp0_ = __builtin_amdgcn_s_memrealtime();
#define STAMP_MARK(k, i) do { if (threadIdx.x == 0) ((unsigned long long*)(ws + WS_DBG))[((k) * 256 + blockIdx.x) * 8 + (i)] = __builtin_amdgcn_s_memrealtime(); } while (0)
#define STAMP_MARKW(k, i, T) do { if (threadIdx.x == T) ((unsigned long long*)(ws + WS_DBG))[((k) * 256 + blockIdx.x) * 8 + (i)] = __builtin_amdgcn_s_memrealtime(); } while (0)
#define STAMP_ENDW(k, T) do { asm volatile("s_waitcnt vmcnt(0)" ::: "memory"); __syncthreads(); if (threadIdx.x == T) { unsigned long long* d_ = (unsigned long long*)(ws + WS_DBG) + ((k) * 256 + blockIdx.x) * 8; d_[0] = stamp0_; d_[1] = __builtin_amdgcn_s_memrealtime(); } } while (0)
#define STAMP_CYC0 const unsigned long long cyc0_ = __builtin_readcyclecounter();
#define STAMP_CYC(k, i) do { if (threadIdx.x == 0) ((unsigned long long*)(ws + WS_DBG))[((k) * 256 + blockIdx.x) * 8 + (i)] = stamp0_ + (__builtin_readcyclecounter() - cyc0_) / 10; } while (0)
#define STAMP_END(k) do { asm volatile("s_waitcnt vmcnt(0)" ::: "memory"); __syncthreads(); if (threadIdx.x == 0) { unsigned long long* d_ = (unsigned long long*)(ws + WS_DBG) + ((k) * 256 + blockIdx.x) * 8; d_[0] = stamp0_; d_[1] = __builtin_amdgcn_s_memrealtime(); } } while (0)

DI unsigned cvt_pk(float lo, float hi) {
  f32x2 v = {lo, hi};
  bf16x2_t r = __builtin_convertvector(v, bf16x2_t);
  return __builtin_bit_cast(unsigned, r);
}
DI float bf_lo(unsigned u) { return __builtin_bit_cast(float, u << 16); }
DI float bf_hi(unsigned u) { return __builtin_bit_cast(float, u & 0xffff0000u); }
DI float dot2(unsigned a, unsigned b, float c) {
  return __builtin_amdgcn_fdot2_f32_bf16(__builtin_bit_cast(bf16x2_t, a), __builtin_bit_cast(bf16x2_t, b), c, false);
}
template <int CTRL> DI float dpp_add(float v) {
  int t = __builtin_amdgcn_update_dpp(0, __builtin_bit_cast(int, v), CTRL, 0xf, 0xf, true);
  return v + __builtin_bit_cast(float, t);
}
DI float sum8(float v) {
  v = dpp_add<0xB1>(v);
  v = dpp_add<0x4E>(v);
  v = dpp_add<0x141>(v);
  return v;
}
DI void glds16(const void* sbase, unsigned voff, unsigned lds_dst) {
  asm volatile("s_mov_b32 m0, %2\n\ts_nop 3\n\tglobal_load_lds_dwordx4 %0, %1" ::"v"(voff), "s"(sbase), "s"(lds_dst) : "memory");
}
#define ST_AUX " sc1"
DI void st16(void* p, u32x4 v) { asm volatile("global_store_dwordx4 %0, %1, off" ST_AUX "\n\ts_nop 1" ::"v"(p), "v"(v) : "memory"); }
DI void st8(void* p, u32x2 v) { *(u32x2*)p = v; }
DI void st8wt(void* p, u32x2 v) { asm volatile("global_store_dwordx2 %0, %1, off sc1" ::"v"(p), "v"(v) : "memory"); }
DI void st4(void* p, unsigned v) { asm volatile("global_store_dword %0, %1, off sc1" ::"v"(p), "v"(v) : "memory"); }
DI void glds16_if0(int c, const void* sbase, unsigned voff, unsigned lds_dst) {
  asm volatile("s_cmp_lg_u32 %3, 0\n\ts_cbranch_scc1 1f\n\ts_mov_b32 m0, %2\n\ts_nop 1\n\tglobal_load_lds_dwordx4 %0, %1\n1:"
               ::"v"(voff), "s"(sbase), "s"(lds_dst), "s"(c) : "memory", "scc");
}
DI void touch_if0(int c, const void* sbase, unsigned voff, unsigned lds_dst) {
  asm volatile("s_cmp_lg_u32 %3, 0\n\ts_cbranch_scc1 1f\n\ts_mov_b32 m0, %2\n\ts_nop 1\n\tglobal_load_lds_dword %0, %1\n1:"
               ::"v"(voff), "s"(sbase), "s"(lds_dst), "s"(c) : "memory", "scc");
}
DI void touch_lines(const char* base, unsigned voff, unsigned lds_dummy) {
  asm volatile("s_mov_b32 m0, %2\n\ts_nop 3\n\tglobal_load_lds_dword %0, %1" ::"v"(voff), "s"(base), "s"(lds_dummy) : "memory");
}
#define CODE_TOUCH(SYM, BYTES, LDS_DUMMY)                                                                          \
  do {                                                                                                             \
    const char* cp_;                                                                                               \
    asm volatile("s_getpc_b64 s[20:21]\n\ts_add_u32 s20, s20, " SYM "@rel32@lo+4\n\ts_addc_u32 s21, s21, " SYM     \
                 "@rel32@hi+12\n\ts_mov_b64 %0, s[20:21]" : "=s"(cp_)::"s20", "s21", "scc");                      \
    constexpr int nl_ = (BYTES) / 128, per_ = (nl_ + 28) / 29;                                                      \
    const int ln_ = (blockIdx.x >> 3) * per_ + lane;                                                               \
    if (lane < per_ && ln_ < nl_) touch_lines(cp_, ln_ * 128, LDS_DUMMY);                                          \
  } while (0)
#define SYM_K1 "_Z9k1_ln_qkvPKcS0_PcS0_"
#define SYM_K2 "_Z7k2_attnPcPKcS1_S1_S1_"
#define SYM_K4 "_Z9k4_gateupPKcS0_PcS0_S1_"
constexpr int LEN_K1 = 24000, LEN_K2 = 11200, LEN_K4 = 30000;
template <int N> DI void wait_bar() { asm volatile("s_waitcnt vmcnt(%0) lgkmcnt(0)\n\ts_barrier" ::"n"(N) : "memory"); }
template <int N> DI void wait_vm() { asm volatile("s_waitcnt vmcnt(%0)" ::"n"(N) : "memory"); }
DI unsigned lds_arrive(lds_ptr c, int lane) {
  unsigned old = 0;
  if (lane == 0) old = __hip_atomic_fetch_add((LDS_AS unsigned*)c, 1u, __ATOMIC_RELAXED, __HIP_MEMORY_SCOPE_WORKGROUP);
  return __builtin_amdgcn_readfirstlane(old);
}
DI void lds_sync(lds_ptr c, unsigned n, int lane) {
  asm volatile("" ::: "memory");
  if (lds_arrive(c, lane) + 1 != n) {
    for (int spin = 0; spin < (1 << 20); ++spin) {
      const unsigned v = __builtin_amdgcn_readfirstlane(__hip_atomic_load((LDS_AS unsigned*)c, __ATOMIC_RELAXED, __HIP_MEMORY_SCOPE_WORKGROUP));
      if (v >= n) break;
      __builtin_amdgcn_s_sleep(1);
    }
  }
  asm volatile("" ::: "memory");
}
DI void lds_post(lds_ptr c, int lane) {
  asm volatile("" ::: "memory");
  if (lane == 0) (void)__hip_atomic_fetch_add((LDS_AS unsigned*)c, 1u, __ATOMIC_RELAXED, __HIP_MEMORY_SCOPE_WORKGROUP);
}
DI void lds_wait(lds_ptr c, unsigned n) {
  for (int spin = 0; spin < (1 << 20); ++spin) {
    const unsigned v = __builtin_amdgcn_readfirstlane(__hip_atomic_load((LDS_AS unsigned*)c, __ATOMIC_RELAXED, __HIP_MEMORY_SCOPE_WORKGROUP));
    if (v >= n) break;
    __builtin_amdgcn_s_sleep(1);
  }
  asm volatile("" ::: "memory");
}
DI void raw_bar() { asm volatile("s_barrier" ::: "memory"); }
DI void lds_bar() { asm volatile("s_waitcnt lgkmcnt(0)\n\ts_barrier" ::: "memory"); }
DI bf16x8 lds_r128(lds_ptr p) { return *(LDS_AS const bf16x8*)p; }
typedef short v4i16_t __attribute__((ext_vector_type(4)));
DI s16x4 lds_tr(lds_ptr p) { return __builtin_amdgcn_ds_read_tr16_b64_v4i16((LDS_AS v4i16_t*)p); }
DI bf16x8 cat8(s16x4 a, s16x4 b) { return __builtin_shufflevector(a, b, 0, 1, 2, 3, 4, 5, 6, 7); }
#define SB() __builtin_amdgcn_sched_barrier(0)
#define MFMA16(a, b, c) __builtin_amdgcn_mfma_f32_16x16x32_bf16(a, b, c, 0, 0, 0)
#define MFMA32(a, b, c) __builtin_amdgcn_mfma_f32_32x32x16_bf16(a, b, c, 0, 0, 0)
DI void mfma16_acc(f32x4& c, bf16x8 a, bf16x8 b) { asm volatile("v_mfma_f32_16x16x32_bf16 %0, %1, %2, %0" : "+v"(c) : "v"(a), "v"(b)); }
DI void mfma16_zero(f32x4& c, bf16x8 a, bf16x8 b) { asm volatile("v_mfma_f32_16x16x32_bf16 %0, %1, %2, 0" : "=&v"(c) : "v"(a), "v"(b)); }
template <class F, int... I> DI void for_seq_impl(F&& f, std::integer_sequence<int, I...>) { (f(std::integral_constant<int, I>{}), ...); }
template <int N, class F> DI void for_seq(F&& f) { for_seq_impl(f, std::make_integer_sequence<int, N>{}); }
DI int xcd_remap(int b, int nwg) {
  int q = nwg >> 3, r = nwg & 7, x = b & 7, o = b >> 3;
  return (x < r ? x * (q + 1) : r * (q + 1) + (x - r) * q) + o;
}

constexpr int K1_X = 0, K1_W = 40960, K1_SLOT = 12288, K1_ST = K1_W + K1_SLOT * 9, K1_CS = K1_W, K1_PF = K1_ST + 1024, K1_LDS = K1_PF + 1024;
constexpr int cmin(int a, int b) { return a < b ? a : b; }
constexpr int k1_nw(int kk) { return cmin(18, cmin(3 + 2 * kk, kk + 7)); }
constexpr int k1_nx(int kk) { return cmin(18, cmin(3 + 2 * kk, kk + 5)); }

template <bool ISV, bool HI>
DI void k1_consumer(lds_ptr lds, int cb, int lane, char* ws, int ct, int m0) {
  const int g = lane >> 4, q = (lane & 15) >> 2, p = lane & 3, r = lane & 15;
  constexpr int NM = HI ? 2 : 3, MB = HI ? 3 : 0;
  lds_ptr a0 = lds + K1_X + MB * 2048 + (r >> 3) * 1024 + (r & 7) * 128 + ((g ^ (r >> 1)) * 16);
  lds_ptr a1 = lds + K1_X + MB * 2048 + (r >> 3) * 1024 + (r & 7) * 128 + (((4 + g) ^ (r >> 1)) * 16);
  const int rho0 = 16 * (g >> 1) + 4 * (g & 1) + q;
  lds_ptr wb = lds + K1_W + rho0 * 128 + ((cb ^ ((rho0 >> 1) & 3)) * 32) + p * 8;
  lds_ptr wb5 = wb + 5 * K1_SLOT;
  asm volatile("" : "+v"(wb), "+v"(wb5));
  const u32x4 onesu = {0x3f803f80u, 0x3f803f80u, 0x3f803f80u, 0x3f803f80u};
  const bf16x8 ones = __builtin_bit_cast(bf16x8, onesu);
  f32x4 acc[3][3];
#pragma unroll
  for (int m = 0; m < 3; ++m)
#pragma unroll
    for (int j = 0; j < 3; ++j) acc[m][j] = f32x4{0.f, 0.f, 0.f, 0.f};
  bf16x8 fA[2][3];
  s16x4 wl[2][3], wh[2][3];
  auto rd1 = [&](auto KK, auto II) {
    constexpr int kk = KK, i = II, bf = kk & 1, sl = kk % 9;
    if constexpr (i >= 2 && i <= 4) {
      if constexpr (i - 2 < NM) fA[bf][i - 2] = lds_r128(((kk & 1) ? a1 : a0) + ((kk >> 1) & 3) * 10240 + (i - 2) * 2048);
    } else {
      constexpr int j = i < 2 ? 0 : (i - 3) / 2, hh = i < 2 ? i : (i - 3) % 2;
      lds_ptr bp = (sl >= 5 ? wb5 + (sl - 5) * K1_SLOT : wb + sl * K1_SLOT) + j * 4096 + hh * 1024;
      if constexpr (hh == 0) wl[bf][j] = lds_tr(bp); else wh[bf][j] = lds_tr(bp);
    }
  };
  lds_bar();
  STAMP_MARKW(0, 2, 512);
  const unsigned long long cy0_ = __builtin_readcyclecounter();
  for_seq<9>([&](auto I) { rd1(std::integral_constant<int, 0>{}, I); });
  SB();
  for_seq<18>([&](auto KK) {
    constexpr int kk = KK, bf = kk & 1;
    if constexpr (kk < 17) { raw_bar(); SB(); }
    for_seq<9>([&](auto II) {
      constexpr int i = II, j = i / 3, m = i % 3;
      const bf16x8 wv = cat8(wl[bf][j], wh[bf][j]);
      const bf16x8 av = (m < NM) ? fA[bf][m] : ones;
      acc[m][j] = ISV ? MFMA16(av, wv, acc[m][j]) : MFMA16(wv, av, acc[m][j]);
      SB();
      if constexpr (kk < 17) { rd1(std::integral_constant<int, kk + 1>{}, II); SB(); }
    });
  });
  STAMP_MARKW(0, 4, 512);
  if (threadIdx.x == 512) ((unsigned long long*)(ws + WS_DBG))[(256 * 6 + blockIdx.x) * 8] = __builtin_readcyclecounter() - cy0_;
  lds_ptr cx = lds + K1_CS + cb * 3072 + lane * 16;
  if (HI) {
#pragma unroll
    for (int j = 0; j < 3; ++j) *(LDS_AS f32x4*)(cx + j * 1024) = acc[2][j];
  }
  lds_bar();
  STAMP_MARKW(0, 5, 512);
  f32x4 cs[3];
#pragma unroll
  for (int j = 0; j < 3; ++j) cs[j] = HI ? acc[2][j] : *(LDS_AS f32x4*)(cx + j * 1024);
  if (ISV) {
    f32x4 st[NM][2];
#pragma unroll
    for (int m = 0; m < NM; ++m) {
      st[m][0] = *(LDS_AS f32x4*)(lds + K1_ST + ((MB + m) * 16 + 4 * g) * 8);
      st[m][1] = *(LDS_AS f32x4*)(lds + K1_ST + ((MB + m) * 16 + 4 * g) * 8 + 16);
    }
#pragma unroll
    for (int j = 0; j < 3; ++j) {
      const int c = (ct - 6) * 192 + j * 64 + cb * 16 + r;
      const int head = c / 18, d = c - head * 18;
      char* vb = ws + WS_V + (size_t)(head * 128 + (m0 >> 4) + MB) * 592;
      char* vp = vb + (g & 1) * 288 + d * 16 + (g >> 1) * 8;
      const float c0 = cs[j][0];
#pragma unroll
      for (int m = 0; m < NM; ++m) {
        u32x2 o = {cvt_pk(fmaf(st[m][0][0], c0, acc[m][j][0]) * st[m][0][1], fmaf(st[m][0][2], c0, acc[m][j][1]) * st[m][0][3]),
                   cvt_pk(fmaf(st[m][1][0], c0, acc[m][j][2]) * st[m][1][1], fmaf(st[m][1][2], c0, acc[m][j][3]) * st[m][1][3])};
        st8(vp + m * 592, o);
      }
      if (d == 17 && g < NM) *(u32x4*)(vb + g * 592 + 576) = onesu;
    }
  } else {
    const bool isq = ct < 3;
    const float sc = isq ? QSCALE : 1.f;
    f32x2 st[NM];
#pragma unroll
    for (int m = 0; m < NM; ++m) { st[m] = *(LDS_AS f32x2*)(lds + K1_ST + ((MB + m) * 16 + r) * 8); st[m][1] *= sc; }
#pragma unroll
    for (int j = 0; j < 3; ++j) {
      const int c = (isq ? ct : ct - 3) * 192 + j * 64 + cb * 16 + 4 * g;
      const int pair = c / 36, qi = (c - pair * 36) >> 2;
      const int head = 2 * pair + (qi > 4 ? 1 : 0), a = qi > 4 ? qi - 5 : qi;
      char* mainp = ws + (isq ? WS_QM : WS_KM) + (size_t)head * 65536 + (a >> 1) * 512 + (a & 1) * 8;
      char* xp = ws + (isq ? WS_QX : WS_KX) + (size_t)pair * 16384;
#pragma unroll
      for (int m = 0; m < NM; ++m) {
        const int rw = m0 + (MB + m) * 16 + r;
        u32x2 o = {cvt_pk(fmaf(st[m][0], cs[j][0], acc[m][j][0]) * st[m][1], fmaf(st[m][0], cs[j][1], acc[m][j][1]) * st[m][1]),
                   cvt_pk(fmaf(st[m][0], cs[j][2], acc[m][j][2]) * st[m][1], fmaf(st[m][0], cs[j][3], acc[m][j][3]) * st[m][1])};
        char* dst = (qi == 4) ? xp + rw * 8 : mainp + (rw >> 5) * 1024 + (rw & 31) * 16;
        st8(dst, o);
      }
    }
  }
}

__global__ __launch_bounds__(1024) void k1_ln_qkv(const char* __restrict__ x, const char* __restrict__ wqkv, char* __restrict__ ws, const char* __restrict__ wo) {
  extern __shared__ __attribute__((aligned(16))) char smem[];
  STAMP_BEGIN
  lds_ptr lds = (lds_ptr)smem;
  const unsigned lds0 = (unsigned)(uintptr_t)lds;
  const int tid = threadIdx.x, lane = tid & 63, wid = __builtin_amdgcn_readfirstlane(tid >> 6);
  const int tile = xcd_remap(blockIdx.x, 234);
  const int cgp = tile / 78, rem = tile - cgp * 78;
  const int mt = rem / 3, ct = cgp * 3 + (rem - mt * 3);
  const int m0 = min(mt * 80, SEQ - 80);
  if (wid < 3) {
    unsigned wv[4];
#pragma unroll
    for (int i = 0; i < 4; ++i) {
      const int rho = 8 * i + (lane >> 3);
      const int gk = ((rho >> 4) & 1) * 2 + ((rho >> 2) & 1), tk = (rho >> 3) & 1, qk = rho & 3;
      wv[i] = (8 * gk + 4 * tk + qk) * 3456 + (((lane & 7) ^ (((rho >> 1) & 3) << 1)) * 16);
    }
    const char* wsrc = wqkv + ct * 384 + wid * 128;
    const unsigned wdst = lds0 + K1_W + wid * 4096;
    for_seq<18>([&](auto KK) {
      constexpr int kk = KK;
#pragma unroll
      for (int n = (kk == 0 ? 0 : k1_nw(kk - 1)); n < k1_nw(kk); ++n)
#pragma unroll
        for (int i = 0; i < 4; ++i) glds16(wsrc + (size_t)n * 32 * 3456, wv[i], wdst + (n % 9) * K1_SLOT + i * 1024);
      wait_bar<(k1_nw(kk) - kk - 1) * 4>();
    });
    return;
  }
  if (wid == 3) {
    const unsigned v0 = (lane >> 3) * 1152 + (((lane & 7) ^ (lane >> 4)) * 16);
    const unsigned v1 = (lane >> 3) * 1152 + (((lane & 7) ^ (4 + (lane >> 4))) * 16);
    const char* xs = x + (size_t)m0 * 1152;
    for_seq<18>([&](auto KK) {
      constexpr int kk = KK;
#pragma unroll
      for (int h = (kk == 0 ? 0 : k1_nx(kk - 1)); h < k1_nx(kk); ++h)
#pragma unroll
        for (int i = 0; i < 5; ++i) {
          const int pc = (h & 1) * 5 + i;
          glds16(xs + pc * 9216 + (h >> 1) * 128, (pc & 1) ? v1 : v0, lds0 + K1_X + ((h >> 1) & 3) * 10240 + pc * 1024);
        }
      wait_bar<(k1_nx(kk) - 2 * (kk >> 1) - 2) * 5>();
    });
    return;
  }
  if (wid < 8) {
    const int j = wid - 4;
    if (j == 0) CODE_TOUCH(SYM_K1, LEN_K1, lds0 + K1_PF);
    if (j == 1) CODE_TOUCH(SYM_K2, LEN_K2, lds0 + K1_PF + 256);
    if (j >= 2 && blockIdx.x == 0) *(unsigned*)(ws + WS_CNT + ((j - 2) * 64 + lane) * 128) = 0u;
    {
      const int ln = blockIdx.x * 168 + j * 42 + lane;
      if (lane < 42) {
        if (ln < 15552) touch_lines(wqkv, ln * 128, lds0 + K1_PF + j * 256);
        else if (ln < 33984) touch_lines(x, (ln - 15552) * 128, lds0 + K1_PF + j * 256);
        else if (ln < 39168) touch_lines(wo, (ln - 33984) * 128, lds0 + K1_PF + j * 256);
      }
    }
    lds_ptr p0 = lds + K1_X + j * 1024 + lane * 16, p2 = lds + K1_X + min(j + 8, 9) * 1024 + lane * 16;
    float s1[3] = {0.f, 0.f, 0.f}, s2[3] = {0.f, 0.f, 0.f};
#pragma unroll 1
    for (int s = 0; s < 9; ++s) {
      raw_bar();
      const int so = (s & 3) * 10240;
      u32x4 v[3] = {*(LDS_AS u32x4*)(p0 + so), *(LDS_AS u32x4*)(p0 + so + 4096), *(LDS_AS u32x4*)(p2 + so)};
      SB();
      raw_bar();
      SB();
#pragma unroll
      for (int i = 0; i < 3; ++i)
#pragma unroll
        for (int e = 0; e < 4; ++e) { s1[i] = dot2(v[i][e], 0x3f803f80u, s1[i]); s2[i] = dot2(v[i][e], v[i][e], s2[i]); }
      asm volatile("" : "+v"(s1[0]), "+v"(s1[1]), "+v"(s1[2]), "+v"(s2[0]), "+v"(s2[1]), "+v"(s2[2]));
    }
#pragma unroll
    for (int i = 0; i < 3; ++i) {
      const float a = sum8(s1[i]), b = sum8(s2[i]);
      const float mean = a * (1.f / 576.f);
      const float var = fmaxf(b * (1.f / 576.f) - mean * mean, 0.f);
      const int pc = i < 2 ? j + 4 * i : min(j + 8, 9);
      if ((lane & 7) == 0) *(LDS_AS f32x2*)(lds + K1_ST + (pc * 8 + (lane >> 3)) * 8) = f32x2{-mean, __builtin_amdgcn_rsqf(var + LN_EPS)};
    }
    lds_bar();
    return;
  }
  if (ct >= 6) {
    if (wid < 12) k1_consumer<true, false>(lds, wid - 8, lane, ws, ct, m0);
    else k1_consumer<true, true>(lds, wid - 12, lane, ws, ct, m0);
  } else {
    if (wid < 12) k1_consumer<false, false>(lds, wid - 8, lane, ws, ct, m0);
    else k1_consumer<false, true>(lds, wid - 12, lane, ws, ct, m0);
  }
  STAMP_ENDW(0, 512);
}

#define K2SYNC 0
#define K2_SL0 4
#define K2_SL1 4
constexpr int K2_KM = 0, K2_KX = 16384, K2_V = K2_KX + 4096, K2_SY = 44032, K2_BI = K2_V + 19 * 1024 + 128, K2_CS = 44288, K2_W = K2_CS + 4864, K2_LDS = K2_W + 73728;

#define MFMA32K8(a, b, c) __builtin_amdgcn_mfma_f32_32x32x8bf16_1k(a, b, c, 0, 0, 0)
DI void k2_core(lds_ptr lds, char* ws, int hd, int t, int wid, int lane, bf16x8 q0, unsigned qx) {
  const int hi = lane >> 5, l31 = lane & 31;
  const u32x2 q1u = {hi ? 0x3f80u : ((hd & 1) ? 0u : qx), (!hi && (hd & 1)) ? qx : 0u};
  const s16x4 q1 = __builtin_bit_cast(s16x4, q1u);
  f32x16 S[9];
  lds_ptr kmp = lds + K2_KM + wid * 1024 + lane * 16;
  lds_ptr kxp = hi ? lds + K2_BI + wid * 256 : lds + K2_KX + (wid * 32 + l31) * 8;
  const float NEG = -1e30f;
  const int dq = l31 - 4 * hi;
  float mx0 = NEG, mx1 = NEG;
  bf16x8 kf[3];
  s16x4 kx[3];
  auto rdk = [&](auto NN) {
    constexpr int n = NN, i = n == 0 ? 8 : n - 1;
    kf[n % 3] = lds_r128(kmp + i * 1024);
    kx[n % 3] = *(LDS_AS const s16x4*)(kxp + i * 256);
  };
  auto post = [&](auto NN) {
    constexpr int n = NN, i = n == 0 ? 8 : n - 1;
    if constexpr (i < 8) {
      if constexpr (i == 0) {
#pragma unroll
        for (int r = 0; r < 16; ++r) S[0][r] = (((r & 3) + 8 * (r >> 2)) <= dq) ? S[8][r] : S[0][r];
      }
#pragma unroll
      for (int r = 0; r < 16; r += 4) {
        mx0 = fmaxf(fmaxf(mx0, S[i][r]), S[i][r + 1]);
        mx1 = fmaxf(fmaxf(mx1, S[i][r + 2]), S[i][r + 3]);
      }
    }
  };
  rdk(std::integral_constant<int, 0>{});
  rdk(std::integral_constant<int, 1>{});
  SB();
  for_seq<9>([&](auto NN) {
    constexpr int n = NN, i = n == 0 ? 8 : n - 1;
    const f32x16 z = {};
    S[i] = MFMA32(kf[n % 3], q0, z);
    SB();
    if constexpr (n + 2 < 9) { rdk(std::integral_constant<int, n + 2>{}); SB(); }
    S[i] = MFMA32K8(kx[n % 3], q1, S[i]);
    SB();
    if constexpr (n >= 1) { post(std::integral_constant<int, n - 1>{}); SB(); }
  });
  post(std::integral_constant<int, 8>{});
  float mx = fmaxf(mx0, mx1);
  {
    auto rr = __builtin_amdgcn_permlane32_swap(__float_as_uint(mx), __float_as_uint(mx), false, false);
    mx = fmaxf(__uint_as_float(rr[0]), __uint_as_float(rr[1]));
  }
  s16x4 mq, mk1;
  {
    const float nm = -mx;
    const unsigned h1 = __float_as_uint(nm) & 0xffff0000u;
    const float r1 = nm - __uint_as_float(h1);
    const unsigned h2 = __float_as_uint(r1) & 0xffff0000u;
    const float r2 = r1 - __uint_as_float(h2);
    const u32x2 mu = {hi ? 0u : ((h1 >> 16) | h2), hi ? 0u : (__float_as_uint(r2) >> 16)};
    mq = __builtin_bit_cast(s16x4, mu);
    const u32x2 ou = {0x3f803f80u, 0x00003f80u};
    mk1 = __builtin_bit_cast(s16x4, ou);
  }
  S[1] = MFMA32K8(mk1, mq, S[1]);
  S[2] = MFMA32K8(mk1, mq, S[2]);
  SB();
  lds_ptr vp = lds + K2_V + wid * 2 * 592 + (l31 < 18 ? hi * 288 + l31 * 16 : 576);
  f32x16 O;
  bf16x8 vf[3];
  auto rdv = [&](auto EE) {
    constexpr int e = EE, blk = e < 14 ? e + 2 : ((e - 14) >> 1) + ((e & 1) ? 16 : 0);
    if constexpr (e < 18) vf[e % 3] = lds_r128(vp + blk * 592);
  };
  rdv(std::integral_constant<int, 0>{});
  rdv(std::integral_constant<int, 1>{});
  SB();
  for_seq<16>([&](auto HH) {
    constexpr int h = HH, i = ((h >> 1) + 1) & 7, s = h & 1;
    u32x4 P;
#pragma unroll
    for (int j = 0; j < 4; ++j) P[j] = cvt_pk(__builtin_amdgcn_exp2f(S[i][8 * s + 2 * j]), __builtin_amdgcn_exp2f(S[i][8 * s + 2 * j + 1]));
    if constexpr (i == 0) {
      constexpr int e = 14 + 2 * s;
      u32x4 P8, P0;
#pragma unroll
      for (int j = 0; j < 4; ++j) {
        const int r0 = 8 * s + 2 * j, r1 = r0 + 1;
        const unsigned mk = ((((r0 & 3) + 8 * (r0 >> 2)) <= dq) ? 0xffffu : 0u) | ((((r1 & 3) + 8 * (r1 >> 2)) <= dq) ? 0xffff0000u : 0u);
        P8[j] = P[j] & mk;
        P0[j] = P[j] & ~mk;
      }
      rdv(std::integral_constant<int, e + 2>{});
      SB();
      O = MFMA32(vf[e % 3], __builtin_bit_cast(bf16x8, P0), O);
      SB();
      rdv(std::integral_constant<int, e + 3>{});
      O = MFMA32(vf[(e + 1) % 3], __builtin_bit_cast(bf16x8, P8), O);
      SB();
    } else {
      rdv(std::integral_constant<int, h + 2>{});
      SB();
      if constexpr (h == 0) { const f32x16 z = {}; O = MFMA32(vf[0], __builtin_bit_cast(bf16x8, P), z); }
      else O = MFMA32(vf[h % 3], __builtin_bit_cast(bf16x8, P), O);
      if constexpr (s == 0 && i + 2 <= 8) S[(i + 2) & 7] = MFMA32K8(mk1, mq, S[(i + 2) & 7]);
      SB();
    }
  });
  const float inv = __builtin_amdgcn_rcpf(O[10]);
  const int row = t * 32 + l31;
  char* ob = ws + WS_O + (size_t)(row >> 4) * 18 * 1024 + (row & 15) * 16;
  u32x4 o = {cvt_pk(O[0] * inv, O[1] * inv), cvt_pk(O[2] * inv, O[3] * inv), cvt_pk(O[4] * inv, O[5] * inv), cvt_pk(O[6] * inv, O[7] * inv)};
  st16(ob + (hd >> 1) * 1024 + (2 * (hd & 1) + hi) * 256, o);
  if (hi == 0) st4(ob + (16 + (hd >> 4)) * 1024 + ((hd >> 2) & 3) * 256 + (hd & 3) * 4, cvt_pk(O[8] * inv, O[9] * inv));
}

__global__ __launch_bounds__(768) void k2_attn(char* __restrict__ ws, const char* __restrict__ wg, const char* __restrict__ wu,
                                               const char* __restrict__ wo, const char* __restrict__ x) {
  extern __shared__ __attribute__((aligned(16))) char smem[];
  STAMP_BEGIN
  lds_ptr lds = (lds_ptr)smem;
  const unsigned lds0 = (unsigned)(uintptr_t)lds;
  const int tid = threadIdx.x, lane = tid & 63, wid = __builtin_amdgcn_readfirstlane(tid >> 6);
  const int hd = (blockIdx.x & 7) * 4 + ((blockIdx.x >> 3) & 3), qb = blockIdx.x >> 5;
  const bool has_tile = blockIdx.x < 234;
  const int tile = has_tile ? xcd_remap(blockIdx.x, 234) : 0;
  const int mt = tile / 9, nt = tile - mt * 9;
  const int m0 = min(mt * 80, SEQ - 80), rb0 = m0 >> 4;
  if (wid == 11) {
    if (lane < 16) *(LDS_AS u32x2*)(lds + K2_BI + lane * 256) = u32x2{(qb == 0 && lane < 8) ? 0xf149u : 0u, 0u};
    if (lane < 4) *(LDS_AS unsigned*)(lds + K2_SY + lane * 4) = 0u;
  }
  if (wid >= 10) {
    lds_bar();
    __builtin_amdgcn_s_sleep(K2_SL1);
    const int sw = wid - 10;
    if (sw == 0) CODE_TOUCH(SYM_K4, LEN_K4, lds0 + K2_CS + 4096);
    const bool act = blockIdx.x < 216;
    const int unit = act ? (int)blockIdx.x * 2 + sw : 0, part = unit / 144, c64 = unit - part * 144;
    const char* wsrc = (c64 < 72 ? wg + c64 * 128 : wu + (c64 - 72) * 128) + (lane & 7) * 16 + (size_t)(part * 192 + (lane >> 3)) * 9216;
    u32x4 v[24];
    if (act) {
#pragma unroll
      for (int i = 0; i < 24; ++i) v[i] = *(const u32x4*)(wsrc + (size_t)i * 8 * 9216);
    }
    if (act) {
      float cs[8];
#pragma unroll
      for (int j = 0; j < 8; ++j) cs[j] = 0.f;
#pragma unroll
      for (int i = 0; i < 24; ++i)
#pragma unroll
        for (int j = 0; j < 4; ++j) { cs[2 * j] += bf_lo(v[i][j]); cs[2 * j + 1] += bf_hi(v[i][j]); }
      lds_ptr cp = lds + K2_CS + sw * 2048;
      *(LDS_AS f32x4*)(cp + lane * 32) = f32x4{cs[0], cs[1], cs[2], cs[3]};
      *(LDS_AS f32x4*)(cp + lane * 32 + 16) = f32x4{cs[4], cs[5], cs[6], cs[7]};
      asm volatile("s_waitcnt lgkmcnt(0)" ::: "memory");
      float t = 0.f;
#pragma unroll
      for (int i = 0; i < 8; ++i) t += *(LDS_AS float*)(cp + i * 256 + lane * 4);
      *(float*)(ws + WS_CSP + ((size_t)part * 9216 + c64 * 64 + lane) * 4) = t;
    }
    if (has_tile) { raw_bar(); raw_bar(); }
    return;
  }
  if (wid >= 8) {
    raw_bar();
    __builtin_amdgcn_s_sleep(K2_SL0);
    if (has_tile) {
      const int par = wid - 8;
      unsigned va[4], vb[4];
#pragma unroll
      for (int i = 0; i < 4; ++i) {
        const int rho = 8 * i + (lane >> 3);
        const int g_ = ((rho >> 4) & 1) * 2 + ((rho >> 2) & 1), t_ = (rho >> 3) & 1, q_ = rho & 3;
        const int La = 36 * par + 18 * (g_ >> 1) + 8 * t_ + 4 * (g_ & 1) + q_, Lb = 288 * par + 18 * (4 * g_ + 2 * t_ + (q_ >> 1)) + 16 + (q_ & 1);
        const unsigned csw = ((lane & 7) ^ (((rho >> 1) & 3) << 1)) * 16;
        va[i] = La * 1152 + csw; vb[i] = Lb * 1152 + csw;
      }
#pragma unroll
      for (int kp = 0; kp < 9; ++kp)
#pragma unroll
        for (int i = 0; i < 4; ++i)
          glds16(wo + nt * 128 + (size_t)(kp == 8 ? 0 : 72 * kp) * 1152, kp == 8 ? vb[i] : va[i], lds0 + K2_W + par * 4096 + kp * 8192 + i * 1024);
    }
  } else {
    const int t = qb * 8 + wid;
    bf16x8 q0;
    unsigned qx;
    const unsigned vo = lane * 16;
    const int kt0 = qb * 8 - 8;
    asm volatile("global_load_dwordx4 %0, %2, %3\n\tglobal_load_dword %1, %4, %5"
                 : "=&v"(q0), "=&v"(qx)
                 : "v"(vo), "s"(ws + WS_QM + (size_t)hd * 65536 + (size_t)t * 1024), "v"((lane & 31) * 8),
                   "s"(ws + WS_QX + (size_t)(hd >> 1) * 16384 + (size_t)t * 256 + (hd & 1) * 4)
                 : "memory");
#pragma unroll
    for (int i = 0; i < 2; ++i) {
      const int lt = wid * 2 + i;
      const int kt = max(kt0 + lt, 0);
      glds16(ws + WS_KM + (size_t)hd * 65536 + (size_t)kt * 1024, vo, lds0 + K2_KM + lt * 1024);
    }
    if (wid >= 4) {
      const int k0 = max(qb * 256 - 256 + (wid - 4) * 128, 0);
      glds16(ws + WS_KX + (size_t)(hd >> 1) * 16384 + (size_t)k0 * 8, vo, lds0 + K2_KX + (wid - 4) * 1024);
    }
    const long vb0 = (long)(hd * 128 + qb * 16 - 16) * 592;
#pragma unroll
    for (int i = 0; i < 3; ++i) {
      const int pc = wid + 8 * i;
      if (pc < 19) {
        long off = vb0 + pc * 1024;
        unsigned v2 = vo;
        if (qb == 0) { off = (long)(hd * 128) * 592 + pc * 1024; v2 = (pc * 1024 + lane * 16 >= 9472) ? vo - 9472 + 65536 : vo + 65536; off -= 65536; }
        glds16(ws + WS_V + off, v2, lds0 + K2_V + pc * 1024);
      }
    }
    raw_bar();
    asm volatile("s_waitcnt vmcnt(0)" : "+v"(q0), "+v"(qx)::"memory");
    lds_sync(lds + K2_SY, 8, lane);
    k2_core(lds, ws, hd, t, wid, lane, q0, qx);
    STAMP_MARK(1, 3);
    STAMP_MARKW(1, 7, 448);
  }
  wait_vm<0>();
  STAMP_MARK(1, 4);
  unsigned* cnt = (unsigned*)(ws + WS_CNT);
  if (wid < 8) {
    if (lds_arrive(lds + K2_SY + 8, lane) == 7 && lane == 0) __hip_atomic_fetch_add(cnt + qb * 32, 1u, __ATOMIC_RELAXED, __HIP_MEMORY_SCOPE_AGENT);
  }
  if (!has_tile) { STAMP_END(1); return; }
  lds_sync(lds + K2_SY + 12, 10, lane);
  const int mi = wid >> 1, kg = wid & 1;
  const int g = lane >> 4, q = (lane & 15) >> 2, p = lane & 3;
  const int er = tid >> 3, ec = tid & 7;
  const u32x4 res = *(const u32x4*)(x + (size_t)(m0 + er) * 1152 + (nt * 64 + ec * 8) * 2);
  const int rho0 = 16 * (g >> 1) + 4 * (g & 1) + q;
  lds_ptr wb[4];
#pragma unroll
  for (int n = 0; n < 4; ++n) { wb[n] = lds + K2_W + kg * 4096 + rho0 * 128 + ((n ^ ((rho0 >> 1) & 3)) * 32) + p * 8; asm volatile("" : "+v"(wb[n])); }
  bf16x8 wf[5][4];
#pragma unroll
  for (int j = 0; j < 5; ++j)
#pragma unroll
    for (int n = 0; n < 4; ++n) wf[j][n] = cat8(lds_tr(wb[n] + j * 8192), lds_tr(wb[n] + j * 8192 + 1024));
  if (tid == 576) {
    const unsigned* c0 = cnt + (m0 >> 8) * 32;
    const unsigned* c1 = cnt + ((m0 + 79) >> 8) * 32;
    for (int spin = 0; spin < (1 << 18); ++spin) {
      const unsigned a = __hip_atomic_load(c0, __ATOMIC_RELAXED, __HIP_MEMORY_SCOPE_AGENT);
      const unsigned b = __hip_atomic_load(c1, __ATOMIC_RELAXED, __HIP_MEMORY_SCOPE_AGENT);
      if (a >= 32u && b >= 32u) break;
      __builtin_amdgcn_s_sleep(1);
    }
  }
  SB();
  raw_bar();
  SB();
  STAMP_MARK(1, 5);
  const __amdgpu_buffer_rsrc_t ors = __builtin_amdgcn_make_buffer_rsrc((void*)(ws + WS_O), 0, 128 * 18432, 0x00020000);
  const int aoff = (rb0 + mi) * 18432 + kg * 1024 + lane * 16;
  bf16x8 a[9];
#pragma unroll
  for (int i = 0; i < 9; ++i) a[i] = __builtin_bit_cast(bf16x8, __builtin_amdgcn_raw_buffer_load_b128(ors, aoff + i * 2048, 0, 16));
  SB();
  f32x4 acc[4];
#pragma unroll
  for (int n = 0; n < 4; ++n) acc[n] = f32x4{0.f, 0.f, 0.f, 0.f};
#pragma unroll
  for (int j = 0; j < 9; ++j) {
#pragma unroll
    for (int n = 0; n < 4; ++n) {
      acc[n] = MFMA16(wf[j % 5][n], a[j], acc[n]);
      if (j + 5 < 9) wf[j % 5][n] = cat8(lds_tr(wb[n] + (j + 5) * 8192),
                                         lds_tr(wb[n] + (j + 5) * 8192 + 1024));
    }
  }
  STAMP_MARK(1, 6);
  {
    lds_ptr cw = lds + kg * (80 * 272) + (mi * 16 + (lane & 15)) * 272 + 16 * g;
#pragma unroll
    for (int n = 0; n < 4; ++n) *(LDS_AS f32x4*)(cw + n * 64) = acc[n];
  }
  lds_bar();
  {
    lds_ptr cr = lds + er * 272 + ec * 32;
    f32x4 a0 = *(LDS_AS f32x4*)(cr), a1 = *(LDS_AS f32x4*)(cr + 16);
    f32x4 b0 = *(LDS_AS f32x4*)(cr + 80 * 272), b1 = *(LDS_AS f32x4*)(cr + 80 * 272 + 16);
    a0 += b0; a1 += b1;
    u32x4 o;
    o[0] = cvt_pk(a0[0] + bf_lo(res[0]), a0[1] + bf_hi(res[0]));
    o[1] = cvt_pk(a0[2] + bf_lo(res[1]), a0[3] + bf_hi(res[1]));
    o[2] = cvt_pk(a1[0] + bf_lo(res[2]), a1[1] + bf_hi(res[2]));
    o[3] = cvt_pk(a1[2] + bf_lo(res[3]), a1[3] + bf_hi(res[3]));
    const int row = m0 + er, col = nt * 64 + ec * 8;
    st16(ws + WS_H + (size_t)(row >> 4) * 18432 + (col >> 5) * 1024 + ((col >> 3) & 3) * 256 + (row & 15) * 16, o);
    float s1 = 0.f, s2 = 0.f;
#pragma unroll
    for (int j = 0; j < 4; ++j) { s1 = dot2(o[j], 0x3f803f80u, s1); s2 = dot2(o[j], o[j], s2); }
    s1 = sum8(s1); s2 = sum8(s2);
    if (ec == 0) *(f32x2*)(ws + WS_ST + (size_t)row * 80 + nt * 8) = f32x2{s1, s2};
  }
  STAMP_END(1);
}

constexpr int K4_SLOT = 18432, K4_ST = K4_SLOT * 8, K4_CS = K4_ST + 2048, K4_PF = K4_CS + 2048, K4_CP = K4_PF + 1024, K4_SY = 160000, K4_LDS = 160 * 1024;

constexpr int k4_frag(int i) { return i < 16 ? ((i >> 2) * 2 + (i & 1) + ((i & 2) ? 9 : 0)) : (i == 16 ? 8 : 17); }
constexpr int k4_foff(int f) { return (f >= 9 ? 9216 + (f - 9) * 32 : f * 32); }
constexpr int K4_T0 = 13 * 18;
constexpr int k4_slab(int G) { return G < K4_T0 ? G / 18 : (G - K4_T0 < 80 ? 13 + ((G - K4_T0) % 20) / 4 : 13 + (G - K4_T0 - 80) / 2); }
constexpr int k4_pos(int G) { return G < K4_T0 ? G % 18 : (G - K4_T0 < 80 ? ((G - K4_T0) / 20) * 4 + ((G - K4_T0) & 3) : 16 + ((G - K4_T0) & 1)); }
constexpr int k4_op(int G) {
  const int s = G / 18, i = G % 18;
  if (s > 12) return 0;
  if (s < 3) {
    const int pos[10] = {0, 2, 4, 6, 7, 9, 11, 13, 15, 16};
    for (int k = 0; k < 10; ++k) if (pos[k] == i) return 1 + (2 * s + 2 + k / 5) * 8 + k % 5;
    return 0;
  }
  if (s == 3) {
    const int pos[8] = {0, 2, 4, 6, 8, 11, 14, 16};
    for (int k = 0; k < 8; ++k) if (pos[k] == i) return k < 5 ? 1 + 8 * 8 + k : 1 + 9 * 8 + (k - 3);
    return 0;
  }
  if (i == 1) return 1 + (s + 5) * 8;
  if (i == 4) return 1 + (s + 5) * 8 + 1;
  if (s + 6 > 17) return 0;
  if (i == 8) return 1 + (s + 6) * 8 + 2;
  if (i == 13) return 1 + (s + 6) * 8 + 3;
  if (i == 16) return 1 + (s + 6) * 8 + 4;
  return 0;
}
constexpr int k4_allow(int G, int need) {
  int n = (need == 0) ? 4 : 0;
  for (int g = 0; g < G; ++g) {
    const int op = k4_op(g);
    if (op == 0) continue;
    const int sl = (op - 1) >> 3, kind = (op - 1) & 7;
    if (sl <= need) n = 0;
    else if (kind != 4) ++n;
  }
  return n;
}
constexpr int k4_need(int G) {
  if (G + 5 >= 324 || G == 0) return 0;
  int mx = 0;
  for (int g = 0; g < G + 5; ++g) mx = k4_slab(g) > mx ? k4_slab(g) : mx;
  return k4_slab(G + 5) > mx ? k4_slab(G + 5) : 0;
}

constexpr int d_off(int i) { return i < 6 ? 36864 + i * 9216 : (i < 10 ? (i - 6) * 9216 : (i < 17 ? 92160 + (i - 10) * 9216 : 36864)); }
constexpr int d_i(int H) { return H < 90 ? H / 9 : (H - 90 < 64 ? 10 + ((H - 90) % 16) / 2 : 10 + (H - 154)); }
constexpr int d_f(int H) { return H < 90 ? H % 9 : (H - 90 < 64 ? ((H - 90) / 16) * 2 + ((H - 90) & 1) : 8); }
constexpr int d_first(int H) {
  for (int h = 0; h < H; ++h) if (d_i(h) == d_i(H)) return 0;
  return 1;
}
DI void ld2_sc1(bf16x8& d0, bf16x8& d1, unsigned o0, unsigned o1, const char* base) {
  asm volatile("global_load_dwordx4 %0, %2, %4 sc1\n\tglobal_load_dwordx4 %1, %3, %4 sc1" : "=&v"(d0), "=&v"(d1) : "v"(o0), "v"(o1), "s"(base) : "memory");
}
template <int N> DI void wait_vm2(bf16x8& d0, bf16x8& d1) { asm volatile("s_waitcnt vmcnt(%2)" : "+v"(d0), "+v"(d1) : "n"(N) : "memory"); }
DI void poll_count(const unsigned* c, unsigned n) {
  for (int spin = 0; spin < (1 << 18); ++spin) {
    if (__hip_atomic_load(c, __ATOMIC_RELAXED, __HIP_MEMORY_SCOPE_AGENT) >= n) break;
    __builtin_amdgcn_s_sleep(1);
  }
}

__global__ __launch_bounds__(512, 2) void k4_gateup(const char* __restrict__ wgate, const char* __restrict__ wup, char* __restrict__ ws,
                                                    const char* __restrict__ wd, char* __restrict__ out) {
  extern __shared__ __attribute__((aligned(16))) char smem[];
  STAMP_BEGIN
  lds_ptr lds = (lds_ptr)smem;
  const unsigned lds0 = (unsigned)(uintptr_t)lds;
  const int tid = threadIdx.x, lane = tid & 63, wid = __builtin_amdgcn_readfirstlane(tid >> 6);
  const int xcd = blockIdx.x & 7, idx = blockIdx.x >> 3;
  const int pn = 4 * (xcd >> 2) + (idx & 3), jt = 8 * (xcd & 3) + (idx >> 2);
  const int wm = wid & 3, wn = wid >> 2;
  const char* asrc = ws + WS_H + (size_t)(pn * 16 + 2 * wid) * 18432;
  const unsigned avo = lane * 16, avo2 = lane * 16 + 18432;
  const char* wsrc = (wn ? wup : wgate) + jt * 288;
  unsigned wvo[3];
#pragma unroll
  for (int i = 0; i < 3; ++i) {
    const int pc = i < 2 ? 2 * wm + i : 8;
    const int o = pc * 1024 + lane * 16;
    const int rho = o / 288, u = (o - rho * 288) >> 4;
    const int gk = ((rho >> 4) & 1) * 2 + ((rho >> 2) & 1), tk = (rho >> 3) & 1, qk = rho & 3;
    wvo[i] = (8 * gk + 4 * tk + qk) * 9216 + u * 16;
  }
  const unsigned wdst = lds0 + wn * 9216;
  auto issue_w = [&](int s, int i) {
    const unsigned so = (s & 7) * K4_SLOT;
    const char* wp = wsrc + (size_t)s * 32 * 9216;
    if (i < 2) glds16(wp, wvo[i], wdst + so + wm * 2048 + i * 1024);
    else glds16_if0(wm, wp, wvo[2], wdst + so + 8192);
  };
  bf16x8 A[6][2];
  auto issue_a = [&](int s, bf16x8& dst, int m) {
    asm volatile("global_load_dwordx4 %0, %1, %2" : "=v"(dst) : "v"(m ? avo2 : avo), "s"(asrc + s * 1024) : "memory");
  };
  f32x4 sp0, sp1, sp2, sp3;
  f32x2 sp4;
  asm volatile("global_load_dwordx4 %0, %5, %6\n\tglobal_load_dwordx4 %1, %5, %6 offset:16\n\tglobal_load_dwordx4 %2, %5, %6 offset:32\n\t"
               "global_load_dwordx4 %3, %5, %6 offset:48\n\tglobal_load_dwordx2 %4, %5, %6 offset:64"
               : "=&v"(sp0), "=&v"(sp1), "=&v"(sp2), "=&v"(sp3), "=&v"(sp4)
               : "v"((lane & 31) * 80), "s"(ws + WS_ST + (size_t)(pn * 256 + wid * 32) * 80) : "memory");
  if (wm < 3 && lane < 36) glds16(ws + WS_CSP + (size_t)(wm * 9216 + wn * FF + jt * 144) * 4, lane * 16, lds0 + K4_CP + wm * 1152 + wn * 576);
  for_seq<2>([&](auto S) { issue_w(S, 0); issue_w(S, 1); issue_w(S, 2); issue_a(S, A[S][0], 0); issue_a(S, A[S][1], 1); });
  {
    asm volatile("s_waitcnt vmcnt(8)" : "+v"(sp0), "+v"(sp1), "+v"(sp2), "+v"(sp3), "+v"(sp4)::"memory");
    const float s1 = sp0[0] + sp0[2] + sp1[0] + sp1[2] + sp2[0] + sp2[2] + sp3[0] + sp3[2] + sp4[0];
    const float s2 = sp0[1] + sp0[3] + sp1[1] + sp1[3] + sp2[1] + sp2[3] + sp3[1] + sp3[3] + sp4[1];
    const float mean = s1 * (1.f / 576.f);
    const float var = fmaxf(s2 * (1.f / 576.f) - mean * mean, 0.f);
    *(LDS_AS f32x2*)(lds + K4_ST + (wid * 32 + (lane & 31)) * 8) = f32x2{-mean, __builtin_amdgcn_rsqf(var + LN_EPS)};
    *(LDS_AS unsigned*)(lds + K4_SY) = 0u;
    SB();
  }
  const int g = lane >> 4, q = (lane & 15) >> 2, p = lane & 3, r = lane & 15;
  lds_ptr wb = lds + (16 * (g >> 1) + 4 * (g & 1) + q) * 288 + p * 8;
  lds_ptr wbs[3] = {wb, wb + 3 * K4_SLOT, wb + 6 * K4_SLOT};
  asm volatile("" : "+v"(wbs[1]), "+v"(wbs[2]));
  lds_ptr csb = lds + K4_CS + 16 * g;
  asm volatile("" : "+v"(csb));
  f32x4 acc[2][18];
  asm volatile("s_waitcnt vmcnt(%2)\n\ts_barrier" : "+v"(A[0][0]), "+v"(A[0][1]) : "n"(k4_allow(0, 0)) : "memory");
  STAMP_MARK(2, 2);
  STAMP_CYC0
  s16x4 Bl[6], Bh[6];
  for_seq<5>([&](auto I) { Bl[I] = lds_tr(wb + k4_foff(k4_frag(I))); Bh[I] = lds_tr(wb + k4_foff(k4_frag(I)) + 2304); });
  SB();
  const float L2E = 1.4426950408889634f;
  char* hb = ws + WS_HID + (size_t)(pn * 16 + wid * 2) * (144 * 1024) + (size_t)(jt >> 2) * 18432;
  f32x2 st[2];
  float c1[2];
  auto glu = [&](float ag, float au, float cg, float cu, int m) -> float {
    const float tg = fmaf(st[m][0], cg, ag), tu = fmaf(st[m][0], cu, au);
    const float e = __builtin_amdgcn_exp2f(tg * c1[m]);
    return tu * st[m][1] * __builtin_amdgcn_rcpf(1.f + e);
  };
  f32x4 cg[2], cu[2];
  u32x4 own[4][2];
  auto slice = [&](auto QQ, auto KK) {
    constexpr int Q = QQ, k = KK, m = k >> 2, d = k & 3, h = d >> 1, e = (d & 1) * 2;
    const f32x4 ag = acc[m][2 * Q + h], au = acc[m][9 + 2 * Q + h];
    own[Q][m][d] = cvt_pk(glu(ag[e], au[e], cg[h][e], cu[h][e], m), glu(ag[e + 1], au[e + 1], cg[h][e + 1], cu[h][e + 1], m));
    if constexpr (d == 3) st16(hb + (size_t)m * (144 * 1024) + (4 * (jt & 3) + Q) * 1024 + lane * 16, own[Q][m]);
    else asm volatile("" : "+v"(own[Q][m]));
  };
  unsigned dvo[2], dvl[2];
  const char* dsrc = wd + (size_t)(jt >> 2) * 576 * 1152 + (jt & 3) * 288;
  auto dvoff = [&]() {
    int ln = tid;
    asm volatile("" : "+v"(ln));
    ln &= 63;
#pragma unroll
    for (int i = 0; i < 2; ++i) {
      const int o = (i ? 8 : wid) * 1024 + ln * 16;
      const int rho = o / 288, u = (o - rho * 288) >> 4;
      const int gk = ((rho >> 4) & 1) * 2 + ((rho >> 2) & 1), tk = (rho >> 3) & 1, qk = rho & 3;
      dvo[i] = (16 * tk + 4 * gk + qk) * 1152 + u * 16;
      dvl[i] = (144 * (gk >> 1) + 128 + 8 * (gk & 1) + 4 * tk + qk) * 1152 + u * 16;
    }
  };
  auto issue_d = [&](auto II, int pc) {
    constexpr int i = II;
    const int k = (4 * (jt & 3) + i) & 15;
    const char* src = dsrc + (size_t)(i < 16 ? 144 * (k >> 2) + 32 * (k & 3) : 288 * (i - 16)) * 1152;
    if (pc == 0) glds16(src, i < 16 ? dvo[0] : dvl[0], lds0 + d_off(i) + wid * 1024);
    else glds16_if0(wid ^ (i & 7), src, i < 16 ? dvo[1] : dvl[1], lds0 + d_off(i) + 8192);
  };
  for_seq<18>([&](auto GH) { for_seq<18>([&](auto GL) {
    constexpr int G = GH * 18 + GL, sl = k4_slab(G), f = k4_frag(k4_pos(G)), rb = G % 6, nb = (G + 5) % 6, NG = G + 5;
    const bf16x8 b = cat8(Bl[rb], Bh[rb]);
    constexpr int need = k4_need(G);
    if constexpr (need != 0) {
      asm volatile("s_waitcnt vmcnt(%2)\n\ts_barrier" : "+v"(A[need % 6][0]), "+v"(A[need % 6][1]) : "n"(k4_allow(G, need)) : "memory");
      SB();
    }
    if constexpr (sl == 0) mfma16_zero(acc[0][f], b, A[0][0]); else mfma16_acc(acc[0][f], b, A[sl % 6][0]);
    SB();
    if constexpr (NG < 324) { Bl[nb] = lds_tr(wbs[(k4_slab(NG) & 7) / 3] + ((k4_slab(NG) & 7) % 3) * K4_SLOT + k4_foff(k4_frag(k4_pos(NG)))); SB(); }
    if constexpr (sl == 0) mfma16_zero(acc[1][f], b, A[0][1]); else mfma16_acc(acc[1][f], b, A[sl % 6][1]);
    SB();
    if constexpr (NG < 324) { Bh[nb] = lds_tr(wbs[(k4_slab(NG) & 7) / 3] + ((k4_slab(NG) & 7) % 3) * K4_SLOT + k4_foff(k4_frag(k4_pos(NG))) + 2304); SB(); }
    constexpr int op = k4_op(G);
    if constexpr (op != 0) {
      constexpr int os = (op - 1) >> 3, kind = (op - 1) & 7;
      if constexpr (kind < 2) issue_a(os, A[os % 6][kind], kind);
      else issue_w(os, kind - 2);
      SB();
    }
    if constexpr (G % 18 == 10 && (G / 18 == 5 || G / 18 == 7 || G / 18 == 9)) {
      constexpr int k = (G / 18 - 5) / 2;
      touch_if0((int)(blockIdx.x / 216) + (wid ^ (k + 1)), wd + (size_t)(blockIdx.x * 3 + k) * 8192, avo * 8, lds0 + K4_PF + k * 256);
      SB();
    }
    if constexpr (G == 60) {
      lds_ptr cpp = lds + K4_CP + wid * 144 + lane * 4;
      const float t = *(LDS_AS float*)(cpp) + *(LDS_AS float*)(cpp + 1152) + *(LDS_AS float*)(cpp + 2304);
      *(LDS_AS float*)(lds + K4_CS + wid * 144 + min(lane, 36) * 4 + (lane >= 36 ? 1152 : 0)) = t;
      asm volatile("s_waitcnt lgkmcnt(0)" ::: "memory");
      SB();
    }
    if constexpr (G == K4_T0) {
      STAMP_MARK(2, 3);
      STAMP_CYC(2, 4);
#pragma unroll
      for (int m = 0; m < 2; ++m) { st[m] = *(LDS_AS f32x2*)(lds + K4_ST + (wid * 32 + m * 16 + r) * 8); c1[m] = -L2E * st[m][1]; }
      SB();
    }
    if constexpr (G >= K4_T0) {
      constexpr int t = G - K4_T0, u = t % 20;
      if constexpr (t == 11) { dvoff(); SB(); }
      if constexpr (t >= 12 && t < 24) { issue_d(std::integral_constant<int, (t - 12) / 2>{}, t & 1); SB(); }
      if constexpr (t < 80 && u == 18) {
#pragma unroll
        for (int h = 0; h < 2; ++h) {
          cg[h] = *(LDS_AS f32x4*)(csb + (2 * (t / 20) + h) * 64);
          cu[h] = *(LDS_AS f32x4*)(csb + 576 + (2 * (t / 20) + h) * 64);
        }
        SB();
      }
      if constexpr (t >= 20 && t < 80 && u >= 1 && u < 17 && (u & 1)) { slice(std::integral_constant<int, t / 20 - 1>{}, std::integral_constant<int, u / 2>{}); SB(); }
      if constexpr (t >= 81 && t < 89) { slice(std::integral_constant<int, 3>{}, std::integral_constant<int, t - 81>{}); SB(); }
    }
  }); });
  {
    asm volatile("s_nop 7\n\ts_nop 1" : "+v"(acc[0][8]), "+v"(acc[1][8]), "+v"(acc[0][17]), "+v"(acc[1][17]));
    const f32x4 cg8 = *(LDS_AS f32x4*)(csb + 512);
    const f32x4 cu8 = *(LDS_AS f32x4*)(csb + 576 + 512);
#pragma unroll
    for (int m = 0; m < 2; ++m) {
      const f32x4 ag = acc[m][8], au = acc[m][17];
      u32x2 o = {cvt_pk(glu(ag[0], au[0], cg8[0], cu8[0], m), glu(ag[1], au[1], cg8[1], cu8[1], m)),
                 cvt_pk(glu(ag[2], au[2], cg8[2], cu8[2], m), glu(ag[3], au[3], cg8[3], cu8[3], m))};
      st8wt(hb + (size_t)m * (144 * 1024) + (16 + ((jt >> 1) & 1)) * 1024 + (2 * (jt & 1) + (g >> 1)) * 256 + r * 16 + (g & 1) * 8, o);
    }
  }
  STAMP_MARK(2, 5);
  const int sb = jt >> 2, nq = jt & 3;
  unsigned* cntA = (unsigned*)(ws + WS_CNT + 1024) + (pn * 8 + sb) * 32;
  unsigned* cntB = (unsigned*)(ws + WS_CNT + 1024 + 8192) + (pn * 4 + nq) * 32;
  wait_bar<10>();
  auto dptr = [&](auto HH) -> lds_ptr {
    constexpr int H = HH, o = d_off(d_i(H)) + d_f(H) * 32;
    return wbs[o / (3 * K4_SLOT)] + o % (3 * K4_SLOT);
  };
  for_seq<5>([&](auto I) { Bl[I] = lds_tr(dptr(I)); Bh[I] = lds_tr(dptr(I) + 2304); });
  SB();
  f32x4 dacc[2][9];
  bf16x8 DA[14][2];
  char* pb = ws + WS_PART + (size_t)(((pn * 4 + nq) * 8 + wid) * 8 + sb) * 9216 + lane * 16;
  for_seq<18>([&](auto HH) { for_seq<9>([&](auto HL) {
    constexpr int H = HH * 9 + HL, i = d_i(H), f = d_f(H), rb = H % 6, nb = (H + 5) % 6, NH = H + 5;
    if constexpr (H == 36) {
      if (tid == 0) poll_count(cntA, 4u);
      SB();
      wait_bar<0>();
      STAMP_MARK(2, 6);
      issue_d(std::integral_constant<int, 17>{}, 0); issue_d(std::integral_constant<int, 17>{}, 1);
      const unsigned ho = lane * 16, ho2 = lane * 16 + 144 * 1024;
      for_seq<14>([&](auto JJ) {
        constexpr int j = JJ;
        const int k = j < 12 ? ((4 * nq + 4 + j) & 15) : 4 + j;
        ld2_sc1(DA[j][0], DA[j][1], ho, ho2, hb + k * 1024);
      });
      SB();
    }
    if constexpr (i >= 4 && d_first(H)) {
      wait_vm2<2 * (17 - i)>(DA[i - 4][0], DA[i - 4][1]);
      SB();
    }
    if constexpr (H == 90) { raw_bar(); SB(); }
    const bf16x8 b = cat8(Bl[rb], Bh[rb]);
    bf16x8 a0, a1;
    if constexpr (i < 4) { a0 = __builtin_bit_cast(bf16x8, own[i][0]); a1 = __builtin_bit_cast(bf16x8, own[i][1]); }
    else { a0 = DA[i - 4][0]; a1 = DA[i - 4][1]; }
    if constexpr (i == 0) mfma16_zero(dacc[0][f], b, a0); else mfma16_acc(dacc[0][f], b, a0);
    SB();
    if constexpr (NH < 162) { Bl[nb] = lds_tr(dptr(std::integral_constant<int, NH>{})); SB(); }
    if constexpr (i == 0) mfma16_zero(dacc[1][f], b, a1); else mfma16_acc(dacc[1][f], b, a1);
    SB();
    if constexpr (NH < 162) { Bh[nb] = lds_tr(dptr(std::integral_constant<int, NH>{}) + 2304); SB(); }
    if constexpr (H < 22) { issue_d(std::integral_constant<int, 6 + H / 2>{}, H & 1); SB(); }
    if constexpr (H == 24) {
      wait_vm<11>();
      if (lds_arrive(lds + K4_SY, lane) == 7 && lane == 0) __hip_atomic_fetch_add(cntA, 1u, __ATOMIC_RELAXED, __HIP_MEMORY_SCOPE_AGENT);
      SB();
    }
    if constexpr (H >= 106 && (((H - 90) % 16 == 3 || (H - 90) % 16 == 5))) {
      constexpr int pr = (H - 90) / 16 - 1, m = ((H - 90) % 16 == 5);
      const f32x4 x0 = dacc[m][2 * pr], x1 = dacc[m][2 * pr + 1];
      st16(pb + (m * 4 + pr) * 1024, u32x4{cvt_pk(x0[0], x0[1]), cvt_pk(x0[2], x0[3]), cvt_pk(x1[0], x1[1]), cvt_pk(x1[2], x1[3])});
      SB();
    }
  }); });
  {
    asm volatile("s_nop 7\n\ts_nop 1" : "+v"(dacc[0][8]), "+v"(dacc[1][8]));
    const f32x4 x0 = dacc[0][8], x1 = dacc[1][8];
    st16(pb + 8192, u32x4{cvt_pk(x0[0], x0[1]), cvt_pk(x0[2], x0[3]), cvt_pk(x1[0], x1[1]), cvt_pk(x1[2], x1[3])});
  }
  wait_bar<0>();
  STAMP_MARK(2, 7);
  if (tid == 0) __hip_atomic_fetch_add(cntB, 1u, __ATOMIC_RELAXED, __HIP_MEMORY_SCOPE_AGENT);
  {
    const int nc = wid == 1 ? 2 : 1;
    const int rbase = pn * 256 + sb * 32;
    int row[2][2], col[2][2];
    row[0][0] = row[0][1] = rbase + (wid >> 2) * 16 + r; col[0][0] = nq * 144 + (wid & 3) * 32 + 4 * g; col[0][1] = col[0][0] + 16;
    row[1][0] = rbase + r; row[1][1] = rbase + 16 + r; col[1][0] = col[1][1] = nq * 144 + 128 + 4 * g;
    u32x2 res[2][2];
#pragma unroll
    for (int c = 0; c < 2; ++c)
#pragma unroll
      for (int e = 0; e < 2; ++e)
        res[c][e] = *(const u32x2*)(ws + WS_H + (size_t)(row[c][e] >> 4) * 18432 + (col[c][e] >> 5) * 1024 + ((col[c][e] >> 3) & 3) * 256 + (row[c][e] & 15) * 16 + ((col[c][e] >> 2) & 1) * 8);
    if (tid == 0) poll_count(cntB, 8u);
    SB();
    raw_bar();
    SB();
    const __amdgpu_buffer_rsrc_t prs = __builtin_amdgcn_make_buffer_rsrc((void*)(ws + WS_PART + (size_t)((pn * 4 + nq) * 8 + sb) * 8 * 9216), 0, 8 * 9216, 0x00020000);
#pragma unroll
    for (int c = 0; c < 2; ++c) {
      if (c < nc) {
        u32x4 pv[8];
#pragma unroll
        for (int k = 0; k < 8; ++k) pv[k] = __builtin_amdgcn_raw_buffer_load_b128(prs, lane * 16 + (c ? 8192 : wid * 1024), k * 9216, 16);
        float sum[8];
#pragma unroll
        for (int e = 0; e < 4; ++e) { sum[2 * e] = bf_lo(res[c][e >> 1][e & 1]); sum[2 * e + 1] = bf_hi(res[c][e >> 1][e & 1]); }
        float acc2[8];
#pragma unroll
        for (int e = 0; e < 8; ++e) acc2[e] = 0.f;
#pragma unroll
        for (int k = 0; k < 8; ++k)
#pragma unroll
          for (int e = 0; e < 4; ++e) { acc2[2 * e] += bf_lo(pv[k][e]); acc2[2 * e + 1] += bf_hi(pv[k][e]); }
#pragma unroll
        for (int e = 0; e < 2; ++e)
          *(u32x2*)(out + (size_t)row[c][e] * 1152 + col[c][e] * 2) =
              u32x2{cvt_pk(sum[4 * e] + acc2[4 * e], sum[4 * e + 1] + acc2[4 * e + 1]), cvt_pk(sum[4 * e + 2] + acc2[4 * e + 2], sum[4 * e + 3] + acc2[4 * e + 3])};
      }
    }
  }
  STAMP_END(2);
}

__global__ void k_print(char* ws) {
  const unsigned long long* d = (const unsigned long long*)(ws + WS_DBG);
  const int nb[3] = {234, 256, 256};
  unsigned long long s0 = 0, pe = 0;
  for (int k = 0; k < 3; ++k) {
    unsigned long long s = ~0ull, e = 0; double mk[8] = {0,0,0,0,0,0,0,0};
    for (int b = 0; b < nb[k]; ++b) { const unsigned long long* p = d + (k * 256 + b) * 8; s = p[0] < s ? p[0] : s; e = p[1] > e ? p[1] : e; }
    for (int b = 0; b < 200; ++b) { const unsigned long long* p = d + (k * 256 + b) * 8; for (int i = 1; i < 8; ++i) mk[i] += (double)(long long)(p[i] - p[0]) * 0.01 / 200; }
    if (k == 0) s0 = s;
    printf("k%d gap %.2f span %.2f | dur %.2f marks %.2f %.2f %.2f %.2f %.2f %.2f\n", k, k ? (double)(s - pe) * 0.01 : 0.0, (double)(e - s) * 0.01, mk[1], mk[2], mk[3], mk[4], mk[5], mk[6], mk[7]);
    pe = e;
  }
  printf("total %.2f\n", (double)(pe - s0) * 0.01);
}
extern "C" void kernel_launch(void* const* d_in, const int* in_sizes, int n_in, void* d_out, int out_size, void* d_ws, size_t ws_size,
                              hipStream_t stream) {
  const char* x = (const char*)d_in[0];
  const char* wqkv = (const char*)d_in[1];
  const char* wo = (const char*)d_in[2];
  const char* wg = (const char*)d_in[4];
  const char* wu = (const char*)d_in[5];
  const char* wd = (const char*)d_in[6];
  char* ws = (char*)d_ws;
  (void)hipFuncSetAttribute((const void*)k1_ln_qkv, hipFuncAttributeMaxDynamicSharedMemorySize, K1_LDS);
  (void)hipFuncSetAttribute((const void*)k2_attn, hipFuncAttributeMaxDynamicSharedMemorySize, K2_LDS);
  (void)hipFuncSetAttribute((const void*)k4_gateup, hipFuncAttributeMaxDynamicSharedMemorySize, K4_LDS);
  k1_ln_qkv<<<234, 1024, K1_LDS, stream>>>(x, wqkv, ws, wo);
  k2_attn<<<256, 768, K2_LDS, stream>>>(ws, wg, wu, wo, x);
  k4_gateup<<<256, 512, K4_LDS, stream>>>(wg, wu, ws, wd, (char*)d_out);
  k_print<<<1, 1, 0, stream>>>(ws);
}
```

```cpp
#define STAMPS 1
#include <hip/hip_runtime.h>
#include <hip/hip_bf16.h>
#include <stdint.h>
#include <utility>
#include <type_traits>

typedef __attribute__((ext_vector_type(8))) short bf16x8;
typedef __attribute__((ext_vector_type(4))) short s16x4;
typedef __attribute__((ext_vector_type(4))) float f32x4;
typedef __attribute__((ext_vector_type(16))) float f32x16;
typedef __attribute__((ext_vector_type(4))) unsigned u32x4;
typedef __attribute__((ext_vector_type(2))) unsigned u32x2;
typedef __attribute__((ext_vector_type(2))) __bf16 bf16x2_t;
typedef __attribute__((ext_vector_type(2))) float f32x2;
#define LDS_AS __attribute__((address_space(3)))
typedef LDS_AS char* lds_ptr;
#define DI __device__ __forceinline__

constexpr int SEQ = 2048, DM = 576, NH = 32, FF = 4608;
constexpr float LN_EPS = 1e-5f;
constexpr float QSCALE = 0.23570226039551584f * 1.4426950408889634f;

constexpr size_t WS_QM = 0;
constexpr size_t WS_KM = WS_QM + (size_t)32 * 64 * 1024;
constexpr size_t WS_QX = WS_KM + (size_t)32 * 64 * 1024;
constexpr size_t WS_KX = WS_QX + (size_t)16 * 2048 * 8;
constexpr size_t WS_V = WS_KX + (size_t)16 * 2048 * 8;
constexpr size_t WS_O = WS_V + (size_t)32 * 128 * 592 + 1024;
constexpr size_t WS_H = WS_O + (size_t)128 * 18 * 1024;
constexpr size_t WS_ST = WS_H + (size_t)128 * 18 * 1024;
constexpr size_t WS_CSP = WS_ST + (size_t)2048 * 80;
constexpr size_t WS_CS = WS_CSP + (size_t)8 * 9216 * 4;
constexpr size_t WS_HID = WS_CS + (size_t)9216 * 4;
constexpr size_t WS_CNT = WS_HID + (size_t)128 * 144 * 1024;
constexpr size_t WS_PART = WS_CNT + 16384;
constexpr size_t WS_END = WS_PART + (size_t)2048 * 9216;
static_assert(WS_O % 1024 == 0 && WS_H % 1024 == 0 && WS_ST % 256 == 0 && WS_CSP % 256 == 0 && WS_HID % 256 == 0, "align");

constexpr size_t WS_DBG = (size_t)64 << 20;
#define STAMP_BEGIN const unsigned long long stamp0_ = __builtin_amdgcn_s_memrealtime();
#define STAMP_MARK(k, i) do { if (threadIdx.x == 0) ((unsigned long long*)(ws + WS_DBG))[((k) * 256 + blockIdx.x) * 8 + (i)] = __builtin_amdgcn_s_memrealtime(); } while (0)
#define STAMP_MARKW(k, i, T) do { if (threadIdx.x == T) ((unsigned long long*)(ws + WS_DBG))[((k) * 256 + blockIdx.x) * 8 + (i)] = __builtin_amdgcn_s_memrealtime(); } while (0)
#define STAMP_ENDW(k, T) do { asm volatile("s_waitcnt vmcnt(0)" ::: "memory"); __syncthreads(); if (threadIdx.x == T) { unsigned long long* d_ = (unsigned long long*)(ws + WS_DBG) + ((k) * 256 + blockIdx.x) * 8; d_[0] = stamp0_; d_[1] = __builtin_amdgcn_s_memrealtime(); } } while (0)
#define STAMP_CYC0 const unsigned long long cyc0_ = __builtin_readcyclecounter();
#define STAMP_CYC(k, i) do { if (threadIdx.x == 0) ((unsigned long long*)(ws + WS_DBG))[((k) * 256 + blockIdx.x) * 8 + (i)] = stamp0_ + (__builtin_readcyclecounter() - cyc0_) / 10; } while (0)
#define STAMP_END(k) do { asm volatile("s_waitcnt vmcnt(0)" ::: "memory"); __syncthreads(); if (threadIdx.x == 0) { unsigned long long* d_ = (unsigned long long*)(ws + WS_DBG) + ((k) * 256 + blockIdx.x) * 8; d_[0] = stamp0_; d_[1] = __builtin_amdgcn_s_memrealtime(); } } while (0)

DI unsigned cvt_pk(float lo, float hi) {
  f32x2 v = {lo, hi};
  bf16x2_t r = __builtin_convertvector(v, bf16x2_t);
  return __builtin_bit_cast(unsigned, r);
}
DI float bf_lo(unsigned u) { return __builtin_bit_cast(float, u << 16); }
DI float bf_hi(unsigned u) { return __builtin_bit_cast(float, u & 0xffff0000u); }
DI float dot2(unsigned a, unsigned b, float c) {
  return __builtin_amdgcn_fdot2_f32_bf16(__builtin_bit_cast(bf16x2_t, a), __builtin_bit_cast(bf16x2_t, b), c, false);
}
template <int CTRL> DI float dpp_add(float v) {
  int t = __builtin_amdgcn_update_dpp(0, __builtin_bit_cast(int, v), CTRL, 0xf, 0xf, true);
  return v + __builtin_bit_cast(float, t);
}
DI float sum8(float v) {
  v = dpp_add<0xB1>(v);
  v = dpp_add<0x4E>(v);
  v = dpp_add<0x141>(v);
  return v;
}
DI void glds16(const void* sbase, unsigned voff, unsigned lds_dst) {
  asm volatile("s_mov_b32 m0, %2\n\ts_nop 3\n\tglobal_load_lds_dwordx4 %0, %1" ::"v"(voff), "s"(sbase), "s"(lds_dst) : "memory");
}
#define ST_AUX " sc1"
DI void st16(void* p, u32x4 v) { asm volatile("global_store_dwordx4 %0, %1, off" ST_AUX "\n\ts_nop 1" ::"v"(p), "v"(v) : "memory"); }
DI void st8(void* p, u32x2 v) { *(u32x2*)p = v; }
DI void st8wt(void* p, u32x2 v) { asm volatile("global_store_dwordx2 %0, %1, off sc1" ::"v"(p), "v"(v) : "memory"); }
DI void st4(void* p, unsigned v) { asm volatile("global_store_dword %0, %1, off sc1" ::"v"(p), "v"(v) : "memory"); }
DI void glds16_if0(int c, const void* sbase, unsigned voff, unsigned lds_dst) {
  asm volatile("s_cmp_lg_u32 %3, 0\n\ts_cbranch_scc1 1f\n\ts_mov_b32 m0, %2\n\ts_nop 1\n\tglobal_load_lds_dwordx4 %0, %1\n1:"
               ::"v"(voff), "s"(sbase), "s"(lds_dst), "s"(c) : "memory", "scc");
}
DI void touch_if0(int c, const void* sbase, unsigned voff, unsigned lds_dst) {
  asm volatile("s_cmp_lg_u32 %3, 0\n\ts_cbranch_scc1 1f\n\ts_mov_b32 m0, %2\n\ts_nop 1\n\tglobal_load_lds_dword %0, %1\n1:"
               ::"v"(voff), "s"(sbase), "s"(lds_dst), "s"(c) : "memory", "scc");
}
DI void touch_lines(const char* base, unsigned voff, unsigned lds_dummy) {
  asm volatile("s_mov_b32 m0, %2\n\ts_nop 3\n\tglobal_load_lds_dword %0, %1" ::"v"(voff), "s"(base), "s"(lds_dummy) : "memory");
}
#define CODE_TOUCH(SYM, BYTES, LDS_DUMMY)                                                                          \
  do {                                                                                                             \
    const char* cp_;                                                                                               \
    asm volatile("s_getpc_b64 s[20:21]\n\ts_add_u32 s20, s20, " SYM "@rel32@lo+4\n\ts_addc_u32 s21, s21, " SYM     \
                 "@rel32@hi+12\n\ts_mov_b64 %0, s[20:21]" : "=s"(cp_)::"s20", "s21", "scc");                      \
    constexpr int nl_ = (BYTES) / 128, per_ = (nl_ + 28) / 29;                                                      \
    const int ln_ = (blockIdx.x >> 3) * per_ + lane;                                                               \
    if (lane < per_ && ln_ < nl_) touch_lines(cp_, ln_ * 128, LDS_DUMMY);                                          \
  } while (0)
#define SYM_K1 "_Z9k1_ln_qkvPKcS0_PcS0_"
#define SYM_K2 "_Z7k2_attnPcPKcS1_S1_S1_"
#define SYM_K4 "_Z9k4_gateupPKcS0_PcS0_S1_"
constexpr int LEN_K1 = 24000, LEN_K2 = 11200, LEN_K4 = 30000;
template <int N> DI void wait_bar() { asm volatile("s_waitcnt vmcnt(%0) lgkmcnt(0)\n\ts_barrier" ::"n"(N) : "memory"); }
template <int N> DI void wait_vm() { asm volatile("s_waitcnt vmcnt(%0)" ::"n"(N) : "memory"); }
DI unsigned lds_arrive(lds_ptr c, int lane) {
  unsigned old = 0;
  if (lane == 0) old = __hip_atomic_fetch_add((LDS_AS unsigned*)c, 1u, __ATOMIC_RELAXED, __HIP_MEMORY_SCOPE_WORKGROUP);
  return __builtin_amdgcn_readfirstlane(old);
}
DI void lds_sync(lds_ptr c, unsigned n, int lane) {
  asm volatile("" ::: "memory");
  if (lds_arrive(c, lane) + 1 != n) {
    for (int spin = 0; spin < (1 << 20); ++spin) {
      const unsigned v = __builtin_amdgcn_readfirstlane(__hip_atomic_load((LDS_AS unsigned*)c, __ATOMIC_RELAXED, __HIP_MEMORY_SCOPE_WORKGROUP));
      if (v >= n) break;
      __builtin_amdgcn_s_sleep(1);
    }
  }
  asm volatile("" ::: "memory");
}
DI void lds_post(lds_ptr c, int lane) {
  asm volatile("" ::: "memory");
  if (lane == 0) (void)__hip_atomic_fetch_add((LDS_AS unsigned*)c, 1u, __ATOMIC_RELAXED, __HIP_MEMORY_SCOPE_WORKGROUP);
}
DI void lds_wait(lds_ptr c, unsigned n) {
  for (int spin = 0; spin < (1 << 20); ++spin) {
    const unsigned v = __builtin_amdgcn_readfirstlane(__hip_atomic_load((LDS_AS unsigned*)c, __ATOMIC_RELAXED, __HIP_MEMORY_SCOPE_WORKGROUP));
    if (v >= n) break;
    __builtin_amdgcn_s_sleep(1);
  }
  asm volatile("" ::: "memory");
}
DI void raw_bar() { asm volatile("s_barrier" ::: "memory"); }
DI void lds_bar() { asm volatile("s_waitcnt lgkmcnt(0)\n\ts_barrier" ::: "memory"); }
DI bf16x8 lds_r128(lds_ptr p) { return *(LDS_AS const bf16x8*)p; }
typedef short v4i16_t __attribute__((ext_vector_type(4)));
DI s16x4 lds_tr(lds_ptr p) { return __builtin_amdgcn_ds_read_tr16_b64_v4i16((LDS_AS v4i16_t*)p); }
DI bf16x8 cat8(s16x4 a, s16x4 b) { return __builtin_shufflevector(a, b, 0, 1, 2, 3, 4, 5, 6, 7); }
#define SB() __builtin_amdgcn_sched_barrier(0)
#define MFMA16(a, b, c) __builtin_amdgcn_mfma_f32_16x16x32_bf16(a, b, c, 0, 0, 0)
#define MFMA32(a, b, c) __builtin_amdgcn_mfma_f32_32x32x16_bf16(a, b, c, 0, 0, 0)
DI void mfma16_acc(f32x4& c, bf16x8 a, bf16x8 b) { asm volatile("v_mfma_f32_16x16x32_bf16 %0, %1, %2, %0" : "+v"(c) : "v"(a), "v"(b)); }
DI void mfma16_zero(f32x4& c, bf16x8 a, bf16x8 b) { asm volatile("v_mfma_f32_16x16x32_bf16 %0, %1, %2, 0" : "=&v"(c) : "v"(a), "v"(b)); }
template <class F, int... I> DI void for_seq_impl(F&& f, std::integer_sequence<int, I...>) { (f(std::integral_constant<int, I>{}), ...); }
template <int N, class F> DI void for_seq(F&& f) { for_seq_impl(f, std::make_integer_sequence<int, N>{}); }
DI int xcd_remap(int b, int nwg) {
  int q = nwg >> 3, r = nwg & 7, x = b & 7, o = b >> 3;
  return (x < r ? x * (q + 1) : r * (q + 1) + (x - r) * q) + o;
}

constexpr int K1_X = 0, K1_W = 40960, K1_SLOT = 12288, K1_ST = K1_W + K1_SLOT * 9, K1_CS = K1_W, K1_PF = K1_ST + 1024, K1_LDS = K1_PF + 1024;
constexpr int cmin(int a, int b) { return a < b ? a : b; }
constexpr int k1_nw(int kk) { return cmin(18, cmin(3 + 2 * kk, kk + 7)); }
constexpr int k1_nx(int kk) { return cmin(18, cmin(3 + 2 * kk, kk + 5)); }

template <bool ISV, bool HI>
DI void k1_consumer(lds_ptr lds, int cb, int lane, char* ws, int ct, int m0) {
  const int g = lane >> 4, q = (lane & 15) >> 2, p = lane & 3, r = lane & 15;
  constexpr int NM = HI ? 2 : 3, MB = HI ? 3 : 0;
  lds_ptr a0 = lds + K1_X + MB * 2048 + (r >> 3) * 1024 + (r & 7) * 128 + ((g ^ (r >> 1)) * 16);
  lds_ptr a1 = lds + K1_X + MB * 2048 + (r >> 3) * 1024 + (r & 7) * 128 + (((4 + g) ^ (r >> 1)) * 16);
  const int rho0 = 16 * (g >> 1) + 4 * (g & 1) + q;
  lds_ptr wb = lds + K1_W + rho0 * 128 + ((cb ^ ((rho0 >> 1) & 3)) * 32) + p * 8;
  lds_ptr wb5 = wb + 5 * K1_SLOT;
  asm volatile("" : "+v"(wb), "+v"(wb5));
  const u32x4 onesu = {0x3f803f80u, 0x3f803f80u, 0x3f803f80u, 0x3f803f80u};
  const bf16x8 ones = __builtin_bit_cast(bf16x8, onesu);
  f32x4 acc[3][3];
#pragma unroll
  for (int m = 0; m < 3; ++m)
#pragma unroll
    for (int j = 0; j < 3; ++j) acc[m][j] = f32x4{0.f, 0.f, 0.f, 0.f};
  bf16x8 fA[2][3];
  s16x4 wl[2][3], wh[2][3];
  auto rd1 = [&](auto KK, auto II) {
    constexpr int kk = KK, i = II, bf = kk & 1, sl = kk % 9;
    if constexpr (i >= 2 && i <= 4) {
      if constexpr (i - 2 < NM) fA[bf][i - 2] = lds_r128(((kk & 1) ? a1 : a0) + ((kk >> 1) & 3) * 10240 + (i - 2) * 2048);
    } else {
      constexpr int j = i < 2 ? 0 : (i - 3) / 2, hh = i < 2 ? i : (i - 3) % 2;
      lds_ptr bp = (sl >= 5 ? wb5 + (sl - 5) * K1_SLOT : wb + sl * K1_SLOT) + j * 4096 + hh * 1024;
      if constexpr (hh == 0) wl[bf][j] = lds_tr(bp); else wh[bf][j] = lds_tr(bp);
    }
  };
  lds_bar();
  STAMP_MARKW(0, 2, 512);
  const unsigned long long cy0_ = __builtin_readcyclecounter();
  for_seq<9>([&](auto I) { rd1(std::integral_constant<int, 0>{}, I); });
  SB();
  for_seq<18>([&](auto KK) {
    constexpr int kk = KK, bf = kk & 1;
    if constexpr (kk < 17) { raw_bar(); SB(); }
    for_seq<9>([&](auto II) {
      constexpr int i = II, j = i / 3, m = i % 3;
      const bf16x8 wv = cat8(wl[bf][j], wh[bf][j]);
      const bf16x8 av = (m < NM) ? fA[bf][m] : ones;
      acc[m][j] = ISV ? MFMA16(av, wv, acc[m][j]) : MFMA16(wv, av, acc[m][j]);
      SB();
      if constexpr (kk < 17) { rd1(std::integral_constant<int, kk + 1>{}, II); SB(); }
    });
  });
  STAMP_MARKW(0, 4, 512);
  if (threadIdx.x == 512) ((unsigned long long*)(ws + WS_DBG))[(256 * 6 + blockIdx.x) * 8] = __builtin_readcyclecounter() - cy0_;
  lds_ptr cx = lds + K1_CS + cb * 3072 + lane * 16;
  if (HI) {
#pragma unroll
    for (int j = 0; j < 3; ++j) *(LDS_AS f32x4*)(cx + j * 1024) = acc[2][j];
  }
  lds_bar();
  STAMP_MARKW(0, 5, 512);
  f32x4 cs[3];
#pragma unroll
  for (int j = 0; j < 3; ++j) cs[j] = HI ? acc[2][j] : *(LDS_AS f32x4*)(cx + j * 1024);
  if (ISV) {
    f32x4 st[NM][2];
#pragma unroll
    for (int m = 0; m < NM; ++m) {
      st[m][0] = *(LDS_AS f32x4*)(lds + K1_ST + ((MB + m) * 16 + 4 * g) * 8);
      st[m][1] = *(LDS_AS f32x4*)(lds + K1_ST + ((MB + m) * 16 + 4 * g) * 8 + 16);
    }
#pragma unroll
    for (int j = 0; j < 3; ++j) {
      const int c = (ct - 6) * 192 + j * 64 + cb * 16 + r;
      const int head = c / 18, d = c - head * 18;
      char* vb = ws + WS_V + (size_t)(head * 128 + (m0 >> 4) + MB) * 592;
      char* vp = vb + (g & 1) * 288 + d * 16 + (g >> 1) * 8;
      const float c0 = cs[j][0];
#pragma unroll
      for (int m = 0; m < NM; ++m) {
        u32x2 o = {cvt_pk(fmaf(st[m][0][0], c0, acc[m][j][0]) * st[m][0][1], fmaf(st[m][0][2], c0, acc[m][j][1]) * st[m][0][3]),
                   cvt_pk(fmaf(st[m][1][0], c0, acc[m][j][2]) * st[m][1][1], fmaf(st[m][1][2], c0, acc[m][j][3]) * st[m][1][3])};
        st8(vp + m * 592, o);
      }
      if (d == 17 && g < NM) *(u32x4*)(vb + g * 592 + 576) = onesu;
    }
  } else {
    const bool isq = ct < 3;
    const float sc = isq ? QSCALE : 1.f;
    f32x2 st[NM];
#pragma unroll
    for (int m = 0; m < NM; ++m) { st[m] = *(LDS_AS f32x2*)(lds + K1_ST + ((MB + m) * 16 + r) * 8); st[m][1] *= sc; }
#pragma unroll
    for (int j = 0; j < 3; ++j) {
      const int c = (isq ? ct : ct - 3) * 192 + j * 64 + cb * 16 + 4 * g;
      const int pair = c / 36, qi = (c - pair * 36) >> 2;
      const int head = 2 * pair + (qi > 4 ? 1 : 0), a = qi > 4 ? qi - 5 : qi;
      char* mainp = ws + (isq ? WS_QM : WS_KM) + (size_t)head * 65536 + (a >> 1) * 512 + (a & 1) * 8;
      char* xp = ws + (isq ? WS_QX : WS_KX) + (size_t)pair * 16384;
#pragma unroll
      for (int m = 0; m < NM; ++m) {
        const int rw = m0 + (MB + m) * 16 + r;
        u32x2 o = {cvt_pk(fmaf(st[m][0], cs[j][0], acc[m][j][0]) * st[m][1], fmaf(st[m][0], cs[j][1], acc[m][j][1]) * st[m][1]),
                   cvt_pk(fmaf(st[m][0], cs[j][2], acc[m][j][2]) * st[m][1], fmaf(st[m][0], cs[j][3], acc[m][j][3]) * st[m][1])};
        char* dst = (qi == 4) ? xp + rw * 8 : mainp + (rw >> 5) * 1024 + (rw & 31) * 16;
        st8(dst, o);
      }
    }
  }
}

__global__ __launch_bounds__(1024) void k1_ln_qkv(const char* __restrict__ x, const char* __restrict__ wqkv, char* __restrict__ ws, const char* __restrict__ wo) {
  extern __shared__ __attribute__((aligned(16))) char smem[];
  STAMP_BEGIN
  lds_ptr lds = (lds_ptr)smem;
  const unsigned lds0 = (unsigned)(uintptr_t)lds;
  const int tid = threadIdx.x, lane = tid & 63, wid = __builtin_amdgcn_readfirstlane(tid >> 6);
  const int tile = xcd_remap(blockIdx.x, 234);
  const int cgp = tile / 78, rem = tile - cgp * 78;
  const int mt = rem / 3, ct = cgp * 3 + (rem - mt * 3);
  const int m0 = min(mt * 80, SEQ - 80);
  if (wid < 3) {
    unsigned wv[4];
#pragma unroll
    for (int i = 0; i < 4; ++i) {
      const int rho = 8 * i + (lane >> 3);
      const int gk = ((rho >> 4) & 1) * 2 + ((rho >> 2) & 1), tk = (rho >> 3) & 1, qk = rho & 3;
      wv[i] = (8 * gk + 4 * tk + qk) * 3456 + (((lane & 7) ^ (((rho >> 1) & 3) << 1)) * 16);
    }
    const char* wsrc = wqkv + ct * 384 + wid * 128;
    const unsigned wdst = lds0 + K1_W + wid * 4096;
    for_seq<18>([&](auto KK) {
      constexpr int kk = KK;
#pragma unroll
      for (int n = (kk == 0 ? 0 : k1_nw(kk - 1)); n < k1_nw(kk); ++n)
#pragma unroll
        for (int i = 0; i < 4; ++i) glds16(wsrc + (size_t)n * 32 * 3456, wv[i], wdst + (n % 9) * K1_SLOT + i * 1024);
      wait_bar<(k1_nw(kk) - kk - 1) * 4>();
    });
    return;
  }
  if (wid == 3) {
    const unsigned v0 = (lane >> 3) * 1152 + (((lane & 7) ^ (lane >> 4)) * 16);
    const unsigned v1 = (lane >> 3) * 1152 + (((lane & 7) ^ (4 + (lane >> 4))) * 16);
    const char* xs = x + (size_t)m0 * 1152;
    for_seq<18>([&](auto KK) {
      constexpr int kk = KK;
#pragma unroll
      for (int h = (kk == 0 ? 0 : k1_nx(kk - 1)); h < k1_nx(kk); ++h)
#pragma unroll
        for (int i = 0; i < 5; ++i) {
          const int pc = (h & 1) * 5 + i;
          glds16(xs + pc * 9216 + (h >> 1) * 128, (pc & 1) ? v1 : v0, lds0 + K1_X + ((h >> 1) & 3) * 10240 + pc * 1024);
        }
      wait_bar<(k1_nx(kk) - 2 * (kk >> 1) - 2) * 5>();
    });
    return;
  }
  if (wid < 8) {
    const int j = wid - 4;
    if (j == 0) CODE_TOUCH(SYM_K1, LEN_K1, lds0 + K1_PF);
    if (j == 1) CODE_TOUCH(SYM_K2, LEN_K2, lds0 + K1_PF + 256);
    if (j >= 2 && blockIdx.x == 0) *(unsigned*)(ws + WS_CNT + ((j - 2) * 64 + lane) * 128) = 0u;
    {
      const int ln = blockIdx.x * 168 + j * 42 + lane;
      if (lane < 42) {
        if (ln < 15552) touch_lines(wqkv, ln * 128, lds0 + K1_PF + j * 256);
        else if (ln < 33984) touch_lines(x, (ln - 15552) * 128, lds0 + K1_PF + j * 256);
        else if (ln < 39168) touch_lines(wo, (ln - 33984) * 128, lds0 + K1_PF + j * 256);
      }
    }
    lds_ptr p0 = lds + K1_X + j * 1024 + lane * 16, p2 = lds + K1_X + min(j + 8, 9) * 1024 + lane * 16;
    float s1[3] = {0.f, 0.f, 0.f}, s2[3] = {0.f, 0.f, 0.f};
#pragma unroll 1
    for (int s = 0; s < 9; ++s) {
      raw_bar();
      const int so = (s & 3) * 10240;
      u32x4 v[3] = {*(LDS_AS u32x4*)(p0 + so), *(LDS_AS u32x4*)(p0 + so + 4096), *(LDS_AS u32x4*)(p2 + so)};
      SB();
      raw_bar();
      SB();
#pragma unroll
      for (int i = 0; i < 3; ++i)
#pragma unroll
        for (int e = 0; e < 4; ++e) { s1[i] = dot2(v[i][e], 0x3f803f80u, s1[i]); s2[i] = dot2(v[i][e], v[i][e], s2[i]); }
      asm volatile("" : "+v"(s1[0]), "+v"(s1[1]), "+v"(s1[2]), "+v"(s2[0]), "+v"(s2[1]), "+v"(s2[2]));
    }
#pragma unroll
    for (int i = 0; i < 3; ++i) {
      const float a = sum8(s1[i]), b = sum8(s2[i]);
      const float mean = a * (1.f / 576.f);
      const float var = fmaxf(b * (1.f / 576.f) - mean * mean, 0.f);
      const int pc = i < 2 ? j + 4 * i : min(j + 8, 9);
      if ((lane & 7) == 0) *(LDS_AS f32x2*)(lds + K1_ST + (pc * 8 + (lane >> 3)) * 8) = f32x2{-mean, __builtin_amdgcn_rsqf(var + LN_EPS)};
    }
    lds_bar();
    return;
  }
  if (ct >= 6) {
    if (wid < 12) k1_consumer<true, false>(lds, wid - 8, lane, ws, ct, m0);
    else k1_consumer<true, true>(lds, wid - 12, lane, ws, ct, m0);
  } else {
    if (wid < 12) k1_consumer<false, false>(lds, wid - 8, lane, ws, ct, m0);
    else k1_consumer<false, true>(lds, wid - 12, lane, ws, ct, m0);
  }
  STAMP_ENDW(0, 512);
}

#define K2SYNC 0
#define K2_SL0 4
#define K2_SL1 4
constexpr int K2_KM = 0, K2_KX = 16384, K2_V = K2_KX + 4096, K2_SY = 44032, K2_BI = K2_V + 19 * 1024 + 128, K2_CS = 44288, K2_W = K2_CS + 4864, K2_LDS = K2_W + 73728;

#define MFMA32K8(a, b, c) __builtin_amdgcn_mfma_f32_32x32x8bf16_1k(a, b, c, 0, 0, 0)
DI void k2_core(lds_ptr lds, char* ws, int hd, int t, int wid, int lane, bf16x8 q0, unsigned qx) {
  const int hi = lane >> 5, l31 = lane & 31;
  const u32x2 q1u = {hi ? 0x3f80u : ((hd & 1) ? 0u : qx), (!hi && (hd & 1)) ? qx : 0u};
  const s16x4 q1 = __builtin_bit_cast(s16x4, q1u);
  f32x16 S[9];
  lds_ptr kmp = lds + K2_KM + wid * 1024 + lane * 16;
  lds_ptr kxp = hi ? lds + K2_BI + wid * 256 : lds + K2_KX + (wid * 32 + l31) * 8;
  const float NEG = -1e30f;
  const int dq = l31 - 4 * hi;
  float mx0 = NEG, mx1 = NEG;
  bf16x8 kf[3];
  s16x4 kx[3];
  auto rdk = [&](auto NN) {
    constexpr int n = NN, i = n == 0 ? 8 : n - 1;
    kf[n % 3] = lds_r128(kmp + i * 1024);
    kx[n % 3] = *(LDS_AS const s16x4*)(kxp + i * 256);
  };
  auto post = [&](auto NN) {
    constexpr int n = NN, i = n == 0 ? 8 : n - 1;
    if constexpr (i < 8) {
      if constexpr (i == 0) {
#pragma unroll
        for (int r = 0; r < 16; ++r) S[0][r] = (((r & 3) + 8 * (r >> 2)) <= dq) ? S[8][r] : S[0][r];
      }
#pragma unroll
      for (int r = 0; r < 16; r += 4) {
        mx0 = fmaxf(fmaxf(mx0, S[i][r]), S[i][r + 1]);
        mx1 = fmaxf(fmaxf(mx1, S[i][r + 2]), S[i][r + 3]);
      }
    }
  };
  rdk(std::integral_constant<int, 0>{});
  rdk(std::integral_constant<int, 1>{});
  SB();
  for_seq<9>([&](auto NN) {
    constexpr int n = NN, i = n == 0 ? 8 : n - 1;
    const f32x16 z = {};
    S[i] = MFMA32(kf[n % 3], q0, z);
    SB();
    if constexpr (n + 2 < 9) { rdk(std::integral_constant<int, n + 2>{}); SB(); }
    S[i] = MFMA32K8(kx[n % 3], q1, S[i]);
    SB();
    if constexpr (n >= 1) { post(std::integral_constant<int, n - 1>{}); SB(); }
  });
  post(std::integral_constant<int, 8>{});
  float mx = fmaxf(mx0, mx1);
  {
    auto rr = __builtin_amdgcn_permlane32_swap(__float_as_uint(mx), __float_as_uint(mx), false, false);
    mx = fmaxf(__uint_as_float(rr[0]), __uint_as_float(rr[1]));
  }
  s16x4 mq, mk1;
  {
    const float nm = -mx;
    const unsigned h1 = __float_as_uint(nm) & 0xffff0000u;
    const float r1 = nm - __uint_as_float(h1);
    const unsigned h2 = __float_as_uint(r1) & 0xffff0000u;
    const float r2 = r1 - __uint_as_float(h2);
    const u32x2 mu = {hi ? 0u : ((h1 >> 16) | h2), hi ? 0u : (__float_as_uint(r2) >> 16)};
    mq = __builtin_bit_cast(s16x4, mu);
    const u32x2 ou = {0x3f803f80u, 0x00003f80u};
    mk1 = __builtin_bit_cast(s16x4, ou);
  }
  S[1] = MFMA32K8(mk1, mq, S[1]);
  S[2] = MFMA32K8(mk1, mq, S[2]);
  SB();
  lds_ptr vp = lds + K2_V + wid * 2 * 592 + (l31 < 18 ? hi * 288 + l31 * 16 : 576);
  f32x16 O;
  bf16x8 vf[3];
  auto rdv = [&](auto EE) {
    constexpr int e = EE, blk = e < 14 ? e + 2 : ((e - 14) >> 1) + ((e & 1) ? 16 : 0);
    if constexpr (e < 18) vf[e % 3] = lds_r128(vp + blk * 592);
  };
  rdv(std::integral_constant<int, 0>{});
  rdv(std::integral_constant<int, 1>{});
  SB();
  for_seq<16>([&](auto HH) {
    constexpr int h = HH, i = ((h >> 1) + 1) & 7, s = h & 1;
    u32x4 P;
#pragma unroll
    for (int j = 0; j < 4; ++j) P[j] = cvt_pk(__builtin_amdgcn_exp2f(S[i][8 * s + 2 * j]), __builtin_amdgcn_exp2f(S[i][8 * s + 2 * j + 1]));
    if constexpr (i == 0) {
      constexpr int e = 14 + 2 * s;
      u32x4 P8, P0;
#pragma unroll
      for (int j = 0; j < 4; ++j) {
        const int r0 = 8 * s + 2 * j, r1 = r0 + 1;
        const unsigned mk = ((((r0 & 3) + 8 * (r0 >> 2)) <= dq) ? 0xffffu : 0u) | ((((r1 & 3) + 8 * (r1 >> 2)) <= dq) ? 0xffff0000u : 0u);
        P8[j] = P[j] & mk;
        P0[j] = P[j] & ~mk;
      }
      rdv(std::integral_constant<int, e + 2>{});
      SB();
      O = MFMA32(vf[e % 3], __builtin_bit_cast(bf16x8, P0), O);
      SB();
      rdv(std::integral_constant<int, e + 3>{});
      O = MFMA32(vf[(e + 1) % 3], __builtin_bit_cast(bf16x8, P8), O);
      SB();
    } else {
      rdv(std::integral_constant<int, h + 2>{});
      SB();
      if constexpr (h == 0) { const f32x16 z = {}; O = MFMA32(vf[0], __builtin_bit_cast(bf16x8, P), z); }
      else O = MFMA32(vf[h % 3], __builtin_bit_cast(bf16x8, P), O);
      if constexpr (s == 0 && i + 2 <= 8) S[(i + 2) & 7] = MFMA32K8(mk1, mq, S[(i + 2) & 7]);
      SB();
    }
  });
  const float inv = __builtin_amdgcn_rcpf(O[10]);
  const int row = t * 32 + l31;
  char* ob = ws + WS_O + (size_t)(row >> 4) * 18 * 1024 + (row & 15) * 16;
  u32x4 o = {cvt_pk(O[0] * inv, O[1] * inv), cvt_pk(O[2] * inv, O[3] * inv), cvt_pk(O[4] * inv, O[5] * inv), cvt_pk(O[6] * inv, O[7] * inv)};
  st16(ob + (hd >> 1) * 1024 + (2 * (hd & 1) + hi) * 256, o);
  if (hi == 0) st4(ob + (16 + (hd >> 4)) * 1024 + ((hd >> 2) & 3) * 256 + (hd & 3) * 4, cvt_pk(O[8] * inv, O[9] * inv));
}

__global__ __launch_bounds__(768) void k2_attn(char* __restrict__ ws, const char* __restrict__ wg, const char* __restrict__ wu,
                                               const char* __restrict__ wo, const char* __restrict__ x) {
  extern __shared__ __attribute__((aligned(16))) char smem[];
  STAMP_BEGIN
  lds_ptr lds = (lds_ptr)smem;
  const unsigned lds0 = (unsigned)(uintptr_t)lds;
  const int tid = threadIdx.x, lane = tid & 63, wid = __builtin_amdgcn_readfirstlane(tid >> 6);
  const int hd = (blockIdx.x & 7) * 4 + ((blockIdx.x >> 3) & 3), qb = blockIdx.x >> 5;
  const bool has_tile = blockIdx.x < 234;
  const int tile = has_tile ? xcd_remap(blockIdx.x, 234) : 0;
  const int mt = tile / 9, nt = tile - mt * 9;
  const int m0 = min(mt * 80, SEQ - 80), rb0 = m0 >> 4;
  if (wid == 11) {
    if (lane < 16) *(LDS_AS u32x2*)(lds + K2_BI + lane * 256) = u32x2{(qb == 0 && lane < 8) ? 0xf149u : 0u, 0u};
    if (lane < 4) *(LDS_AS unsigned*)(lds + K2_SY + lane * 4) = 0u;
  }
  if (wid >= 10) {
    lds_bar();
    __builtin_amdgcn_s_sleep(K2_SL1);
    const int sw = wid - 10;
    if (sw == 0) CODE_TOUCH(SYM_K4, LEN_K4, lds0 + K2_CS + 4096);
    const bool act = blockIdx.x < 216;
    const int unit = act ? (int)blockIdx.x * 2 + sw : 0, part = unit / 144, c64 = unit - part * 144;
    const char* wsrc = (c64 < 72 ? wg + c64 * 128 : wu + (c64 - 72) * 128) + (lane & 7) * 16 + (size_t)(part * 192 + (lane >> 3)) * 9216;
    u32x4 v[24];
    if (act) {
#pragma unroll
      for (int i = 0; i < 24; ++i) v[i] = *(const u32x4*)(wsrc + (size_t)i * 8 * 9216);
    }
    if (act) {
      float cs[8];
#pragma unroll
      for (int j = 0; j < 8; ++j) cs[j] = 0.f;
#pragma unroll
      for (int i = 0; i < 24; ++i)
#pragma unroll
        for (int j = 0; j < 4; ++j) { cs[2 * j] += bf_lo(v[i][j]); cs[2 * j + 1] += bf_hi(v[i][j]); }
      lds_ptr cp = lds + K2_CS + sw * 2048;
      *(LDS_AS f32x4*)(cp + lane * 32) = f32x4{cs[0], cs[1], cs[2], cs[3]};
      *(LDS_AS f32x4*)(cp + lane * 32 + 16) = f32x4{cs[4], cs[5], cs[6], cs[7]};
      asm volatile("s_waitcnt lgkmcnt(0)" ::: "memory");
      float t = 0.f;
#pragma unroll
      for (int i = 0; i < 8; ++i) t += *(LDS_AS float*)(cp + i * 256 + lane * 4);
      *(float*)(ws + WS_CSP + ((size_t)part * 9216 + c64 * 64 + lane) * 4) = t;
    }
    if (has_tile) { raw_bar(); raw_bar(); }
    return;
  }
  if (wid >= 8) {
    raw_bar();
    __builtin_amdgcn_s_sleep(K2_SL0);
    if (has_tile) {
      const int par = wid - 8;
      unsigned va[4], vb[4];
#pragma unroll
      for (int i = 0; i < 4; ++i) {
        const int rho = 8 * i + (lane >> 3);
        const int g_ = ((rho >> 4) & 1) * 2 + ((rho >> 2) & 1), t_ = (rho >> 3) & 1, q_ = rho & 3;
        const int La = 36 * par + 18 * (g_ >> 1) + 8 * t_ + 4 * (g_ & 1) + q_, Lb = 288 * par + 18 * (4 * g_ + 2 * t_ + (q_ >> 1)) + 16 + (q_ & 1);
        const unsigned csw = ((lane & 7) ^ (((rho >> 1) & 3) << 1)) * 16;
        va[i] = La * 1152 + csw; vb[i] = Lb * 1152 + csw;
      }
#pragma unroll
      for (int kp = 0; kp < 9; ++kp)
#pragma unroll
        for (int i = 0; i < 4; ++i)
          glds16(wo + nt * 128 + (size_t)(kp == 8 ? 0 : 72 * kp) * 1152, kp == 8 ? vb[i] : va[i], lds0 + K2_W + par * 4096 + kp * 8192 + i * 1024);
    }
  } else {
    const int t = qb * 8 + wid;
    bf16x8 q0;
    unsigned qx;
    const unsigned vo = lane * 16;
    const int kt0 = qb * 8 - 8;
    asm volatile("global_load_dwordx4 %0, %2, %3\n\tglobal_load_dword %1, %4, %5"
                 : "=&v"(q0), "=&v"(qx)
                 : "v"(vo), "s"(ws + WS_QM + (size_t)hd * 65536 + (size_t)t * 1024), "v"((lane & 31) * 8),
                   "s"(ws + WS_QX + (size_t)(hd >> 1) * 16384 + (size_t)t * 256 + (hd & 1) * 4)
                 : "memory");
#pragma unroll
    for (int i = 0; i < 2; ++i) {
      const int lt = wid * 2 + i;
      const int kt = max(kt0 + lt, 0);
      glds16(ws + WS_KM + (size_t)hd * 65536 + (size_t)kt * 1024, vo, lds0 + K2_KM + lt * 1024);
    }
    if (wid >= 4) {
      const int k0 = max(qb * 256 - 256 + (wid - 4) * 128, 0);
      glds16(ws + WS_KX + (size_t)(hd >> 1) * 16384 + (size_t)k0 * 8, vo, lds0 + K2_KX + (wid - 4) * 1024);
    }
    const long vb0 = (long)(hd * 128 + qb * 16 - 16) * 592;
#pragma unroll
    for (int i = 0; i < 3; ++i) {
      const int pc = wid + 8 * i;
      if (pc < 19) {
        long off = vb0 + pc * 1024;
        unsigned v2 = vo;
        if (qb == 0) { off = (long)(hd * 128) * 592 + pc * 1024; v2 = (pc * 1024 + lane * 16 >= 9472) ? vo - 9472 + 65536 : vo + 65536; off -= 65536; }
        glds16(ws + WS_V + off, v2, lds0 + K2_V + pc * 1024);
      }
    }
    raw_bar();
    asm volatile("s_waitcnt vmcnt(0)" : "+v"(q0), "+v"(qx)::"memory");
    lds_sync(lds + K2_SY, 8, lane);
    k2_core(lds, ws, hd, t, wid, lane, q0, qx);
    STAMP_MARK(1, 3);
    STAMP_MARKW(1, 7, 448);
  }
  wait_vm<0>();
  STAMP_MARK(1, 4);
  unsigned* cnt = (unsigned*)(ws + WS_CNT);
  if (wid < 8) {
    if (lds_arrive(lds + K2_SY + 8, lane) == 7 && lane == 0) __hip_atomic_fetch_add(cnt + qb * 32, 1u, __ATOMIC_RELAXED, __HIP_MEMORY_SCOPE_AGENT);
  }
  if (!has_tile) { STAMP_END(1); return; }
  lds_sync(lds + K2_SY + 12, 10, lane);
  const int mi = wid >> 1, kg = wid & 1;
  const int g = lane >> 4, q = (lane & 15) >> 2, p = lane & 3;
  const int er = tid >> 3, ec = tid & 7;
  const u32x4 res = *(const u32x4*)(x + (size_t)(m0 + er) * 1152 + (nt * 64 + ec * 8) * 2);
  const int rho0 = 16 * (g >> 1) + 4 * (g & 1) + q;
  lds_ptr wb[4];
#pragma unroll
  for (int n = 0; n < 4; ++n) { wb[n] = lds + K2_W + kg * 4096 + rho0 * 128 + ((n ^ ((rho0 >> 1) & 3)) * 32) + p * 8; asm volatile("" : "+v"(wb[n])); }
  bf16x8 wf[5][4];
#pragma unroll
  for (int j = 0; j < 5; ++j)
#pragma unroll
    for (int n = 0; n < 4; ++n) wf[j][n] = cat8(lds_tr(wb[n] + j * 8192), lds_tr(wb[n] + j * 8192 + 1024));
  if (tid == 576) {
    const unsigned* c0 = cnt + (m0 >> 8) * 32;
    const unsigned* c1 = cnt + ((m0 + 79) >> 8) * 32;
    for (int spin = 0; spin < (1 << 18); ++spin) {
      const unsigned a = __hip_atomic_load(c0, __ATOMIC_RELAXED, __HIP_MEMORY_SCOPE_AGENT);
      const unsigned b = __hip_atomic_load(c1, __ATOMIC_RELAXED, __HIP_MEMORY_SCOPE_AGENT);
      if (a >= 32u && b >= 32u) break;
      __builtin_amdgcn_s_sleep(1);
    }
  }
  SB();
  raw_bar();
  SB();
  STAMP_MARK(1, 5);
  const __amdgpu_buffer_rsrc_t ors = __builtin_amdgcn_make_buffer_rsrc((void*)(ws + WS_O), 0, 128 * 18432, 0x00020000);
  const int aoff = (rb0 + mi) * 18432 + kg * 1024 + lane * 16;
  bf16x8 a[9];
#pragma unroll
  for (int i = 0; i < 9; ++i) a[i] = __builtin_bit_cast(bf16x8, __builtin_amdgcn_raw_buffer_load_b128(ors, aoff + i * 2048, 0, 16));
  SB();
  f32x4 acc[4];
#pragma unroll
  for (int n = 0; n < 4; ++n) acc[n] = f32x4{0.f, 0.f, 0.f, 0.f};
#pragma unroll
  for (int j = 0; j < 9; ++j) {
#pragma unroll
    for (int n = 0; n < 4; ++n) {
      acc[n] = MFMA16(wf[j % 5][n], a[j], acc[n]);
      if (j + 5 < 9) wf[j % 5][n] = cat8(lds_tr(wb[n] + (j + 5) * 8192),
                                         lds_tr(wb[n] + (j + 5) * 8192 + 1024));
    }
  }
  STAMP_MARK(1, 6);
  {
    lds_ptr cw = lds + kg * (80 * 272) + (mi * 16 + (lane & 15)) * 272 + 16 * g;
#pragma unroll
    for (int n = 0; n < 4; ++n) *(LDS_AS f32x4*)(cw + n * 64) = acc[n];
  }
  lds_bar();
  {
    lds_ptr cr = lds + er * 272 + ec * 32;
    f32x4 a0 = *(LDS_AS f32x4*)(cr), a1 = *(LDS_AS f32x4*)(cr + 16);
    f32x4 b0 = *(LDS_AS f32x4*)(cr + 80 * 272), b1 = *(LDS_AS f32x4*)(cr + 80 * 272 + 16);
    a0 += b0; a1 += b1;
    u32x4 o;
    o[0] = cvt_pk(a0[0] + bf_lo(res[0]), a0[1] + bf_hi(res[0]));
    o[1] = cvt_pk(a0[2] + bf_lo(res[1]), a0[3] + bf_hi(res[1]));
    o[2] = cvt_pk(a1[0] + bf_lo(res[2]), a1[1] + bf_hi(res[2]));
    o[3] = cvt_pk(a1[2] + bf_lo(res[3]), a1[3] + bf_hi(res[3]));
    const int row = m0 + er, col = nt * 64 + ec * 8;
    st16(ws + WS_H + (size_t)(row >> 4) * 18432 + (col >> 5) * 1024 + ((col >> 3) & 3) * 256 + (row & 15) * 16, o);
    float s1 = 0.f, s2 = 0.f;
#pragma unroll
    for (int j = 0; j < 4; ++j) { s1 = dot2(o[j], 0x3f803f80u, s1); s2 = dot2(o[j], o[j], s2); }
    s1 = sum8(s1); s2 = sum8(s2);
    if (ec == 0) *(f32x2*)(ws + WS_ST + (size_t)row * 80 + nt * 8) = f32x2{s1, s2};
  }
  STAMP_END(1);
}

constexpr int K4_SLOT = 18432, K4_ST = K4_SLOT * 8, K4_CS = K4_ST + 2048, K4_PF = K4_CS + 2048, K4_CP = K4_PF + 1024, K4_SY = 160000, K4_LDS = 160 * 1024;

constexpr int k4_frag(int i) { return i < 16 ? ((i >> 2) * 2 + (i & 1) + ((i & 2) ? 9 : 0)) : (i == 16 ? 8 : 17); }
constexpr int k4_foff(int f) { return (f >= 9 ? 9216 + (f - 9) * 32 : f * 32); }
constexpr int K4_T0 = 13 * 18;
constexpr int k4_slab(int G) { return G < K4_T0 ? G / 18 : (G - K4_T0 < 80 ? 13 + ((G - K4_T0) % 20) / 4 : 13 + (G - K4_T0 - 80) / 2); }
constexpr int k4_pos(int G) { return G < K4_T0 ? G % 18 : (G - K4_T0 < 80 ? ((G - K4_T0) / 20) * 4 + ((G - K4_T0) & 3) : 16 + ((G - K4_T0) & 1)); }
constexpr int k4_op(int G) {
  const int s = G / 18, i = G % 18;
  if (s > 12) return 0;
  if (s < 3) {
    const int pos[10] = {0, 2, 4, 6, 7, 9, 11, 13, 15, 16};
    for (int k = 0; k < 10; ++k) if (pos[k] == i) return 1 + (2 * s + 2 + k / 5) * 8 + k % 5;
    return 0;
  }
  if (s == 3) {
    const int pos[8] = {0, 2, 4, 6, 8, 11, 14, 16};
    for (int k = 0; k < 8; ++k) if (pos[k] == i) return k < 5 ? 1 + 8 * 8 + k : 1 + 9 * 8 + (k - 3);
    return 0;
  }
  if (i == 1) return 1 + (s + 5) * 8;
  if (i == 4) return 1 + (s + 5) * 8 + 1;
  if (s + 6 > 17) return 0;
  if (i == 8) return 1 + (s + 6) * 8 + 2;
  if (i == 13) return 1 + (s + 6) * 8 + 3;
  if (i == 16) return 1 + (s + 6) * 8 + 4;
  return 0;
}
constexpr int k4_allow(int G, int need) {
  int n = (need == 0) ? 4 : 0;
  for (int g = 0; g < G; ++g) {
    const int op = k4_op(g);
    if (op == 0) continue;
    const int sl = (op - 1) >> 3, kind = (op - 1) & 7;
    if (sl <= need) n = 0;
    else if (kind != 4) ++n;
  }
  return n;
}
constexpr int k4_need(int G) {
  if (G + 5 >= 324 || G == 0) return 0;
  int mx = 0;
  for (int g = 0; g < G + 5; ++g) mx = k4_slab(g) > mx ? k4_slab(g) : mx;
  return k4_slab(G + 5) > mx ? k4_slab(G + 5) : 0;
}

constexpr int d_off(int i) { return i < 6 ? 36864 + i * 9216 : (i < 10 ? (i - 6) * 9216 : (i < 17 ? 92160 + (i - 10) * 9216 : 36864)); }
constexpr int d_i(int H) { return H < 90 ? H / 9 : (H - 90 < 64 ? 10 + ((H - 90) % 16) / 2 : 10 + (H - 154)); }
constexpr int d_f(int H) { return H < 90 ? H % 9 : (H - 90 < 64 ? ((H - 90) / 16) * 2 + ((H - 90) & 1) : 8); }
constexpr int d_first(int H) {
  for (int h = 0; h < H; ++h) if (d_i(h) == d_i(H)) return 0;
  return 1;
}
DI void ld2_sc1(bf16x8& d0, bf16x8& d1, unsigned o0, unsigned o1, const char* base) {
  asm volatile("global_load_dwordx4 %0, %2, %4 sc1\n\tglobal_load_dwordx4 %1, %3, %4 sc1" : "=&v"(d0), "=&v"(d1) : "v"(o0), "v"(o1), "s"(base) : "memory");
}
template <int N> DI void wait_vm2(bf16x8& d0, bf16x8& d1) { asm volatile("s_waitcnt vmcnt(%2)" : "+v"(d0), "+v"(d1) : "n"(N) : "memory"); }
DI void poll_count(const unsigned* c, unsigned n) {
  for (int spin = 0; spin < (1 << 18); ++spin) {
    if (__hip_atomic_load(c, __ATOMIC_RELAXED, __HIP_MEMORY_SCOPE_AGENT) >= n) break;
    __builtin_amdgcn_s_sleep(1);
  }
}

__global__ __launch_bounds__(512, 2) void k4_gateup(const char* __restrict__ wgate, const char* __restrict__ wup, char* __restrict__ ws,
                                                    const char* __restrict__ wd, char* __restrict__ out) {
  extern __shared__ __attribute__((aligned(16))) char smem[];
  STAMP_BEGIN
  lds_ptr lds = (lds_ptr)smem;
  const unsigned lds0 = (unsigned)(uintptr_t)lds;
  const int tid = threadIdx.x, lane = tid & 63, wid = __builtin_amdgcn_readfirstlane(tid >> 6);
  const int xcd = blockIdx.x & 7, idx = blockIdx.x >> 3;
  const int pn = 4 * (xcd >> 2) + (idx & 3), jt = 8 * (xcd & 3) + (idx >> 2);
  const int wm = wid & 3, wn = wid >> 2;
  const char* asrc = ws + WS_H + (size_t)(pn * 16 + 2 * wid) * 18432;
  const unsigned avo = lane * 16, avo2 = lane * 16 + 18432;
  const char* wsrc = (wn ? wup : wgate) + jt * 288;
  unsigned wvo[3];
#pragma unroll
  for (int i = 0; i < 3; ++i) {
    const int pc = i < 2 ? 2 * wm + i : 8;
    const int o = pc * 1024 + lane * 16;
    const int rho = o / 288, u = (o - rho * 288) >> 4;
    const int gk = ((rho >> 4) & 1) * 2 + ((rho >> 2) & 1), tk = (rho >> 3) & 1, qk = rho & 3;
    wvo[i] = (8 * gk + 4 * tk + qk) * 9216 + u * 16;
  }
  const unsigned wdst = lds0 + wn * 9216;
  auto issue_w = [&](int s, int i) {
    const unsigned so = (s & 7) * K4_SLOT;
    const char* wp = wsrc + (size_t)s * 32 * 9216;
    if (i < 2) glds16(wp, wvo[i], wdst + so + wm * 2048 + i * 1024);
    else glds16_if0(wm, wp, wvo[2], wdst + so + 8192);
  };
  bf16x8 A[6][2];
  auto issue_a = [&](int s, bf16x8& dst, int m) {
    asm volatile("global_load_dwordx4 %0, %1, %2" : "=v"(dst) : "v"(m ? avo2 : avo), "s"(asrc + s * 1024) : "memory");
  };
  f32x4 sp0, sp1, sp2, sp3;
  f32x2 sp4;
  asm volatile("global_load_dwordx4 %0, %5, %6\n\tglobal_load_dwordx4 %1, %5, %6 offset:16\n\tglobal_load_dwordx4 %2, %5, %6 offset:32\n\t"
               "global_load_dwordx4 %3, %5, %6 offset:48\n\tglobal_load_dwordx2 %4, %5, %6 offset:64"
               : "=&v"(sp0), "=&v"(sp1), "=&v"(sp2), "=&v"(sp3), "=&v"(sp4)
               : "v"((lane & 31) * 80), "s"(ws + WS_ST + (size_t)(pn * 256 + wid * 32) * 80) : "memory");
  if (wm < 3 && lane < 36) glds16(ws + WS_CSP + (size_t)(wm * 9216 + wn * FF + jt * 144) * 4, lane * 16, lds0 + K4_CP + wm * 1152 + wn * 576);
  for_seq<2>([&](auto S) { issue_w(S, 0); issue_w(S, 1); issue_w(S, 2); issue_a(S, A[S][0], 0); issue_a(S, A[S][1], 1); });
  {
    asm volatile("s_waitcnt vmcnt(8)" : "+v"(sp0), "+v"(sp1), "+v"(sp2), "+v"(sp3), "+v"(sp4)::"memory");
    const float s1 = sp0[0] + sp0[2] + sp1[0] + sp1[2] + sp2[0] + sp2[2] + sp3[0] + sp3[2] + sp4[0];
    const float s2 = sp0[1] + sp0[3] + sp1[1] + sp1[3] + sp2[1] + sp2[3] + sp3[1] + sp3[3] + sp4[1];
    const float mean = s1 * (1.f / 576.f);
    const float var = fmaxf(s2 * (1.f / 576.f) - mean * mean, 0.f);
    *(LDS_AS f32x2*)(lds + K4_ST + (wid * 32 + (lane & 31)) * 8) = f32x2{-mean, __builtin_amdgcn_rsqf(var + LN_EPS)};
    *(LDS_AS unsigned*)(lds + K4_SY) = 0u;
    SB();
  }
  const int g = lane >> 4, q = (lane & 15) >> 2, p = lane & 3, r = lane & 15;
  lds_ptr wb = lds + (16 * (g >> 1) + 4 * (g & 1) + q) * 288 + p * 8;
  lds_ptr wbs[3] = {wb, wb + 3 * K4_SLOT, wb + 6 * K4_SLOT};
  asm volatile("" : "+v"(wbs[1]), "+v"(wbs[2]));
  lds_ptr csb = lds + K4_CS + 16 * g;
  asm volatile("" : "+v"(csb));
  f32x4 acc[2][18];
  asm volatile("s_waitcnt vmcnt(%2)\n\ts_barrier" : "+v"(A[0][0]), "+v"(A[0][1]) : "n"(k4_allow(0, 0)) : "memory");
  STAMP_MARK(2, 2);
  STAMP_CYC0
  s16x4 Bl[6], Bh[6];
  for_seq<5>([&](auto I) { Bl[I] = lds_tr(wb + k4_foff(k4_frag(I))); Bh[I] = lds_tr(wb + k4_foff(k4_frag(I)) + 2304); });
  SB();
  const float L2E = 1.4426950408889634f;
  char* hb = ws + WS_HID + (size_t)(pn * 16 + wid * 2) * (144 * 1024) + (size_t)(jt >> 2) * 18432;
  f32x2 st[2];
  float c1[2];
  auto glu = [&](float ag, float au, float cg, float cu, int m) -> float {
    const float tg = fmaf(st[m][0], cg, ag), tu = fmaf(st[m][0], cu, au);
    const float e = __builtin_amdgcn_exp2f(tg * c1[m]);
    return tu * st[m][1] * __builtin_amdgcn_rcpf(1.f + e);
  };
  f32x4 cg[2], cu[2];
  u32x4 own[4][2];
  auto slice = [&](auto QQ, auto KK) {
    constexpr int Q = QQ, k = KK, m = k >> 2, d = k & 3, h = d >> 1, e = (d & 1) * 2;
    const f32x4 ag = acc[m][2 * Q + h], au = acc[m][9 + 2 * Q + h];
    own[Q][m][d] = cvt_pk(glu(ag[e], au[e], cg[h][e], cu[h][e], m), glu(ag[e + 1], au[e + 1], cg[h][e + 1], cu[h][e + 1], m));
    if constexpr (d == 3) st16(hb + (size_t)m * (144 * 1024) + (4 * (jt & 3) + Q) * 1024 + lane * 16, own[Q][m]);
    else asm volatile("" : "+v"(own[Q][m]));
  };
  unsigned dvo[2], dvl[2];
  const char* dsrc = wd + (size_t)(jt >> 2) * 576 * 1152 + (jt & 3) * 288;
  auto dvoff = [&]() {
    int ln = tid;
    asm volatile("" : "+v"(ln));
    ln &= 63;
#pragma unroll
    for (int i = 0; i < 2; ++i) {
      const int o = (i ? 8 : wid) * 1024 + ln * 16;
      const int rho = o / 288, u = (o - rho * 288) >> 4;
      const int gk = ((rho >> 4) & 1) * 2 + ((rho >> 2) & 1), tk = (rho >> 3) & 1, qk = rho & 3;
      dvo[i] = (16 * tk + 4 * gk + qk) * 1152 + u * 16;
      dvl[i] = (144 * (gk >> 1) + 128 + 8 * (gk & 1) + 4 * tk + qk) * 1152 + u * 16;
    }
  };
  auto issue_d = [&](auto II, int pc) {
    constexpr int i = II;
    const int k = (4 * (jt & 3) + i) & 15;
    const char* src = dsrc + (size_t)(i < 16 ? 144 * (k >> 2) + 32 * (k & 3) : 288 * (i - 16)) * 1152;
    if (pc == 0) glds16(src, i < 16 ? dvo[0] : dvl[0], lds0 + d_off(i) + wid * 1024);
    else glds16_if0(wid ^ (i & 7), src, i < 16 ? dvo[1] : dvl[1], lds0 + d_off(i) + 8192);
  };
  for_seq<18>([&](auto GH) { for_seq<18>([&](auto GL) {
    constexpr int G = GH * 18 + GL, sl = k4_slab(G), f = k4_frag(k4_pos(G)), rb = G % 6, nb = (G + 5) % 6, NG = G + 5;
    const bf16x8 b = cat8(Bl[rb], Bh[rb]);
    constexpr int need = k4_need(G);
    if constexpr (need != 0) {
      asm volatile("s_waitcnt vmcnt(%2)\n\ts_barrier" : "+v"(A[need % 6][0]), "+v"(A[need % 6][1]) : "n"(k4_allow(G, need)) : "memory");
      SB();
    }
    if constexpr (sl == 0) mfma16_zero(acc[0][f], b, A[0][0]); else mfma16_acc(acc[0][f], b, A[sl % 6][0]);
    SB();
    if constexpr (NG < 324) { Bl[nb] = lds_tr(wbs[(k4_slab(NG) & 7) / 3] + ((k4_slab(NG) & 7) % 3) * K4_SLOT + k4_foff(k4_frag(k4_pos(NG)))); SB(); }
    if constexpr (sl == 0) mfma16_zero(acc[1][f], b, A[0][1]); else mfma16_acc(acc[1][f], b, A[sl % 6][1]);
    SB();
    if constexpr (NG < 324) { Bh[nb] = lds_tr(wbs[(k4_slab(NG) & 7) / 3] + ((k4_slab(NG) & 7) % 3) * K4_SLOT + k4_foff(k4_frag(k4_pos(NG))) + 2304); SB(); }
    constexpr int op = k4_op(G);
    if constexpr (op != 0) {
      constexpr int os = (op - 1) >> 3, kind = (op - 1) & 7;
      if constexpr (kind < 2) issue_a(os, A[os % 6][kind], kind);
      else issue_w(os, kind - 2);
      SB();
    }
    if constexpr (G % 18 == 10 && (G / 18 == 5 || G / 18 == 7 || G / 18 == 9)) {
      constexpr int k = (G / 18 - 5) / 2;
      touch_if0((int)(blockIdx.x / 216) + (wid ^ (k + 1)), wd + (size_t)(blockIdx.x * 3 + k) * 8192, avo * 8, lds0 + K4_PF + k * 256);
      SB();
    }
    if constexpr (G == 60) {
      lds_ptr cpp = lds + K4_CP + wid * 144 + lane * 4;
      const float t = *(LDS_AS float*)(cpp) + *(LDS_AS float*)(cpp + 1152) + *(LDS_AS float*)(cpp + 2304);
      *(LDS_AS float*)(lds + K4_CS + wid * 144 + min(lane, 36) * 4 + (lane >= 36 ? 1152 : 0)) = t;
      asm volatile("s_waitcnt lgkmcnt(0)" ::: "memory");
      SB();
    }
    if constexpr (G == K4_T0) {
      STAMP_MARK(2, 3);
      STAMP_CYC(2, 4);
#pragma unroll
      for (int m = 0; m < 2; ++m) { st[m] = *(LDS_AS f32x2*)(lds + K4_ST + (wid * 32 + m * 16 + r) * 8); c1[m] = -L2E * st[m][1]; }
      SB();
    }
    if constexpr (G >= K4_T0) {
      constexpr int t = G - K4_T0, u = t % 20;
      if constexpr (t == 11) { dvoff(); SB(); }
      if constexpr (t >= 12 && t < 24) { issue_d(std::integral_constant<int, (t - 12) / 2>{}, t & 1); SB(); }
      if constexpr (t < 80 && u == 18) {
#pragma unroll
        for (int h = 0; h < 2; ++h) {
          cg[h] = *(LDS_AS f32x4*)(csb + (2 * (t / 20) + h) * 64);
          cu[h] = *(LDS_AS f32x4*)(csb + 576 + (2 * (t / 20) + h) * 64);
        }
        SB();
      }
      if constexpr (t >= 20 && t < 80 && u >= 1 && u < 17 && (u & 1)) { slice(std::integral_constant<int, t / 20 - 1>{}, std::integral_constant<int, u / 2>{}); SB(); }
      if constexpr (t >= 81 && t < 89) { slice(std::integral_constant<int, 3>{}, std::integral_constant<int, t - 81>{}); SB(); }
    }
  }); });
  {
    asm volatile("s_nop 7\n\ts_nop 1" : "+v"(acc[0][8]), "+v"(acc[1][8]), "+v"(acc[0][17]), "+v"(acc[1][17]));
    const f32x4 cg8 = *(LDS_AS f32x4*)(csb + 512);
    const f32x4 cu8 = *(LDS_AS f32x4*)(csb + 576 + 512);
#pragma unroll
    for (int m = 0; m < 2; ++m) {
      const f32x4 ag = acc[m][8], au = acc[m][17];
      u32x2 o = {cvt_pk(glu(ag[0], au[0], cg8[0], cu8[0], m), glu(ag[1], au[1], cg8[1], cu8[1], m)),
                 cvt_pk(glu(ag[2], au[2], cg8[2], cu8[2], m), glu(ag[3], au[3], cg8[3], cu8[3], m))};
      st8wt(hb + (size_t)m * (144 * 1024) + (16 + ((jt >> 1) & 1)) * 1024 + (2 * (jt & 1) + (g >> 1)) * 256 + r * 16 + (g & 1) * 8, o);
    }
  }
  STAMP_MARK(2, 5);
  const int sb = jt >> 2, nq = jt & 3;
  unsigned* cntA = (unsigned*)(ws + WS_CNT + 1024) + (pn * 8 + sb) * 32;
  unsigned* cntB = (unsigned*)(ws + WS_CNT + 1024 + 8192) + (pn * 4 + nq) * 32;
  wait_bar<10>();
  auto dptr = [&](auto HH) -> lds_ptr {
    constexpr int H = HH, o = d_off(d_i(H)) + d_f(H) * 32;
    return wbs[o / (3 * K4_SLOT)] + o % (3 * K4_SLOT);
  };
  for_seq<5>([&](auto I) { Bl[I] = lds_tr(dptr(I)); Bh[I] = lds_tr(dptr(I) + 2304); });
  SB();
  f32x4 dacc[2][9];
  bf16x8 DA[14][2];
  char* pb = ws + WS_PART + (size_t)(((pn * 4 + nq) * 8 + wid) * 8 + sb) * 9216 + lane * 16;
  for_seq<18>([&](auto HH) { for_seq<9>([&](auto HL) {
    constexpr int H = HH * 9 + HL, i = d_i(H), f = d_f(H), rb = H % 6, nb = (H + 5) % 6, NH = H + 5;
    if constexpr (H == 36) {
      if (tid == 0) poll_count(cntA, 4u);
      SB();
      wait_bar<0>();
      STAMP_MARK(2, 6);
      issue_d(std::integral_constant<int, 17>{}, 0); issue_d(std::integral_constant<int, 17>{}, 1);
      const unsigned ho = lane * 16, ho2 = lane * 16 + 144 * 1024;
      for_seq<14>([&](auto JJ) {
        constexpr int j = JJ;
        const int k = j < 12 ? ((4 * nq + 4 + j) & 15) : 4 + j;
        ld2_sc1(DA[j][0], DA[j][1], ho, ho2, hb + k * 1024);
      });
      SB();
    }
    if constexpr (i >= 4 && d_first(H)) {
      wait_vm2<2 * (17 - i)>(DA[i - 4][0], DA[i - 4][1]);
      SB();
    }
    if constexpr (H == 90) { raw_bar(); SB(); }
    const bf16x8 b = cat8(Bl[rb], Bh[rb]);
    bf16x8 a0, a1;
    if constexpr (i < 4) { a0 = __builtin_bit_cast(bf16x8, own[i][0]); a1 = __builtin_bit_cast(bf16x8, own[i][1]); }
    else { a0 = DA[i - 4][0]; a1 = DA[i - 4][1]; }
    if constexpr (i == 0) mfma16_zero(dacc[0][f], b, a0); else mfma16_acc(dacc[0][f], b, a0);
    SB();
    if constexpr (NH < 162) { Bl[nb] = lds_tr(dptr(std::integral_constant<int, NH>{})); SB(); }
    if constexpr (i == 0) mfma16_zero(dacc[1][f], b, a1); else mfma16_acc(dacc[1][f], b, a1);
    SB();
    if constexpr (NH < 162) { Bh[nb] = lds_tr(dptr(std::integral_constant<int, NH>{}) + 2304); SB(); }
    if constexpr (H < 22) { issue_d(std::integral_constant<int, 6 + H / 2>{}, H & 1); SB(); }
    if constexpr (H == 24) {
      wait_vm<11>();
      if (lds_arrive(lds + K4_SY, lane) == 7 && lane == 0) __hip_atomic_fetch_add(cntA, 1u, __ATOMIC_RELAXED, __HIP_MEMORY_SCOPE_AGENT);
      SB();
    }
    if constexpr (H >= 106 && (((H - 90) % 16 == 3 || (H - 90) % 16 == 5))) {
      constexpr int pr = (H - 90) / 16 - 1, m = ((H - 90) % 16 == 5);
      const f32x4 x0 = dacc[m][2 * pr], x1 = dacc[m][2 * pr + 1];
      st16(pb + (m * 4 + pr) * 1024, u32x4{cvt_pk(x0[0], x0[1]), cvt_pk(x0[2], x0[3]), cvt_pk(x1[0], x1[1]), cvt_pk(x1[2], x1[3])});
      SB();
    }
  }); });
  {
    asm volatile("s_nop 7\n\ts_nop 1" : "+v"(dacc[0][8]), "+v"(dacc[1][8]));
    const f32x4 x0 = dacc[0][8], x1 = dacc[1][8];
    st16(pb + 8192, u32x4{cvt_pk(x0[0], x0[1]), cvt_pk(x0[2], x0[3]), cvt_pk(x1[0], x1[1]), cvt_pk(x1[2], x1[3])});
  }
  wait_bar<0>();
  STAMP_MARK(2, 7);
  if (tid == 0) __hip_atomic_fetch_add(cntB, 1u, __ATOMIC_RELAXED, __HIP_MEMORY_SCOPE_AGENT);
  {
    const int nc = wid == 1 ? 2 : 1;
    const int rbase = pn * 256 + sb * 32;
    int row[2][2], col[2][2];
    row[0][0] = row[0][1] = rbase + (wid >> 2) * 16 + r; col[0][0] = nq * 144 + (wid & 3) * 32 + 4 * g; col[0][1] = col[0][0] + 16;
    row[1][0] = rbase + r; row[1][1] = rbase + 16 + r; col[1][0] = col[1][1] = nq * 144 + 128 + 4 * g;
    u32x2 res[2][2];
#pragma unroll
    for (int c = 0; c < 2; ++c)
#pragma unroll
      for (int e = 0; e < 2; ++e)
        res[c][e] = *(const u32x2*)(ws + WS_H + (size_t)(row[c][e] >> 4) * 18432 + (col[c][e] >> 5) * 1024 + ((col[c][e] >> 3) & 3) * 256 + (row[c][e] & 15) * 16 + ((col[c][e] >> 2) & 1) * 8);
    if (tid == 0) poll_count(cntB, 8u);
    SB();
    raw_bar();
    SB();
    const __amdgpu_buffer_rsrc_t prs = __builtin_amdgcn_make_buffer_rsrc((void*)(ws + WS_PART + (size_t)((pn * 4 + nq) * 8 + sb) * 8 * 9216), 0, 8 * 9216, 0x00020000);
#pragma unroll
    for (int c = 0; c < 2; ++c) {
      if (c < nc) {
        u32x4 pv[8];
#pragma unroll
        for (int k = 0; k < 8; ++k) pv[k] = __builtin_amdgcn_raw_buffer_load_b128(prs, lane * 16 + (c ? 8192 : wid * 1024), k * 9216, 16);
        float sum[8];
#pragma unroll
        for (int e = 0; e < 4; ++e) { sum[2 * e] = bf_lo(res[c][e >> 1][e & 1]); sum[2 * e + 1] = bf_hi(res[c][e >> 1][e & 1]); }
        float acc2[8];
#pragma unroll
        for (int e = 0; e < 8; ++e) acc2[e] = 0.f;
#pragma unroll
        for (int k = 0; k < 8; ++k)
#pragma unroll
          for (int e = 0; e < 4; ++e) { acc2[2 * e] += bf_lo(pv[k][e]); acc2[2 * e + 1] += bf_hi(pv[k][e]); }
#pragma unroll
        for (int e = 0; e < 2; ++e)
          *(u32x2*)(out + (size_t)row[c][e] * 1152 + col[c][e] * 2) =
              u32x2{cvt_pk(sum[4 * e] + acc2[4 * e], sum[4 * e + 1] + acc2[4 * e + 1]), cvt_pk(sum[4 * e + 2] + acc2[4 * e + 2], sum[4 * e + 3] + acc2[4 * e + 3])};
      }
    }
  }
  STAMP_END(2);
}

__global__ void k_probe(char* ws) {
  __shared__ unsigned long long mn[3][256], mx[3][256]; __shared__ float mk[3][8][256];
  const unsigned long long* d = (const unsigned long long*)(ws + WS_DBG);
  const int t = threadIdx.x;
  for (int k = 0; k < 3; ++k) { const int b = t < 200 ? t : 0; const int b2 = (k == 0 && t >= 234) ? 0 : t; const unsigned long long* p = d + (k * 256 + b2) * 8; mn[k][t] = p[0]; mx[k][t] = p[1];
    const unsigned long long* q = d + (k * 256 + b) * 8; for (int i = 1; i < 8; ++i) mk[k][i][t] = (float)(long long)(q[i] - q[0]); }
  __syncthreads();
  for (int o = 128; o; o >>= 1) { if (t < o) for (int k = 0; k < 3; ++k) { mn[k][t] = mn[k][t] < mn[k][t + o] ? mn[k][t] : mn[k][t + o]; mx[k][t] = mx[k][t] > mx[k][t + o] ? mx[k][t] : mx[k][t + o]; for (int i = 1; i < 8; ++i) mk[k][i][t] += mk[k][i][t + o]; } __syncthreads(); }
  if (t == 0) {
#define S(k) ((long long)mn[k][0])
#define E(k) ((long long)mx[k][0])
#define M(k, i) ((long long)(mk[k][i][0] / 256.f))
    long long q = (0); if (q < 0) q = 0; if (q > 10000) q = 10000;
    const unsigned long long end = (unsigned long long)S(0) + 10000 + q * 20;
    while (__builtin_amdgcn_s_memrealtime() < end) __builtin_amdgcn_s_sleep(1);
  }
}
extern "C" void kernel_launch(void* const* d_in, const int* in_sizes, int n_in, void* d_out, int out_size, void* d_ws, size_t ws_size,
                              hipStream_t stream) {
  const char* x = (const char*)d_in[0];
  const char* wqkv = (const char*)d_in[1];
  const char* wo = (const char*)d_in[2];
  const char* wg = (const char*)d_in[4];
  const char* wu = (const char*)d_in[5];
  const char* wd = (const char*)d_in[6];
  char* ws = (char*)d_ws;
  (void)hipFuncSetAttribute((const void*)k1_ln_qkv, hipFuncAttributeMaxDynamicSharedMemorySize, K1_LDS);
  (void)hipFuncSetAttribute((const void*)k2_attn, hipFuncAttributeMaxDynamicSharedMemorySize, K2_LDS);
  (void)hipFuncSetAttribute((const void*)k4_gateup, hipFuncAttributeMaxDynamicSharedMemorySize, K4_LDS);
  k1_ln_qkv<<<234, 1024, K1_LDS, stream>>>(x, wqkv, ws, wo);
  k2_attn<<<256, 768, K2_LDS, stream>>>(ws, wg, wu, wo, x);
  k4_gateup<<<256, 512, K4_LDS, stream>>>(wg, wu, ws, wd, (char*)d_out);
  k_probe<<<1, 256, 0, stream>>>(ws);
}
```

```cpp
#include <hip/hip_runtime.h>
#include <hip/hip_bf16.h>
#include <stdint.h>
#include <utility>
#include <type_traits>

typedef __attribute__((ext_vector_type(8))) short bf16x8;
typedef __attribute__((ext_vector_type(4))) short s16x4;
typedef __attribute__((ext_vector_type(4))) float f32x4;
typedef __attribute__((ext_vector_type(16))) float f32x16;
typedef __attribute__((ext_vector_type(4))) unsigned u32x4;
typedef __attribute__((ext_vector_type(2))) unsigned u32x2;
typedef __attribute__((ext_vector_type(2))) __bf16 bf16x2_t;
typedef __attribute__((ext_vector_type(2))) float f32x2;
#define LDS_AS __attribute__((address_space(3)))
typedef LDS_AS char* lds_ptr;
#define DI __device__ __forceinline__

constexpr int SEQ = 2048, DM = 576, NH = 32, FF = 4608;
constexpr float LN_EPS = 1e-5f;
constexpr float QSCALE = 0.23570226039551584f * 1.4426950408889634f;

constexpr size_t WS_QM = 0;
constexpr size_t WS_KM = WS_QM + (size_t)32 * 64 * 1024;
constexpr size_t WS_QX = WS_KM + (size_t)32 * 64 * 1024;
constexpr size_t WS_KX = WS_QX + (size_t)16 * 2048 * 8;
constexpr size_t WS_V = WS_KX + (size_t)16 * 2048 * 8;
constexpr size_t WS_O = WS_V + (size_t)32 * 128 * 592 + 1024;
constexpr size_t WS_H = WS_O + (size_t)128 * 18 * 1024;
constexpr size_t WS_ST = WS_H + (size_t)128 * 18 * 1024;
constexpr size_t WS_CSP = WS_ST + (size_t)2048 * 80;
constexpr size_t WS_CS = WS_CSP + (size_t)8 * 9216 * 4;
constexpr size_t WS_HID = WS_CS + (size_t)9216 * 4;
constexpr size_t WS_CNT = WS_HID + (size_t)128 * 144 * 1024;
constexpr size_t WS_PART = WS_CNT + 16384;
constexpr size_t WS_END = WS_PART + (size_t)2048 * 9216;
static_assert(WS_O % 1024 == 0 && WS_H % 1024 == 0 && WS_ST % 256 == 0 && WS_CSP % 256 == 0 && WS_HID % 256 == 0, "align");

#define WS_BASE(sz) (((sz) - WS_END) & ~(size_t)0x1fffff)
#define STAMP_BEGIN
#define STAMP_MARK(k, i)
#define STAMP_MARKW(k, i, T)
#define STAMP_ENDW(k, T)
#define STAMP_CYC0
#define STAMP_CYC(k, i)
#define STAMP_END(k)

DI unsigned cvt_pk(float lo, float hi) {
  f32x2 v = {lo, hi};
  bf16x2_t r = __builtin_convertvector(v, bf16x2_t);
  return __builtin_bit_cast(unsigned, r);
}
DI float bf_lo(unsigned u) { return __builtin_bit_cast(float, u << 16); }
DI float bf_hi(unsigned u) { return __builtin_bit_cast(float, u & 0xffff0000u); }
DI float dot2(unsigned a, unsigned b, float c) {
  return __builtin_amdgcn_fdot2_f32_bf16(__builtin_bit_cast(bf16x2_t, a), __builtin_bit_cast(bf16x2_t, b), c, false);
}
template <int CTRL> DI float dpp_add(float v) {
  int t = __builtin_amdgcn_update_dpp(0, __builtin_bit_cast(int, v), CTRL, 0xf, 0xf, true);
  return v + __builtin_bit_cast(float, t);
}
DI float sum8(float v) {
  v = dpp_add<0xB1>(v);
  v = dpp_add<0x4E>(v);
  v = dpp_add<0x141>(v);
  return v;
}
#define GNOP "3"
DI void glds16(const void* sbase, unsigned voff, unsigned lds_dst) {
  asm volatile("s_mov_b32 m0, %2\n\ts_nop " GNOP "\n\tglobal_load_lds_dwordx4 %0, %1" ::"v"(voff), "s"(sbase), "s"(lds_dst) : "memory");
}
#define ST_AUX " sc1"
DI void st16(void* p, u32x4 v) { asm volatile("global_store_dwordx4 %0, %1, off" ST_AUX "\n\ts_nop 1" ::"v"(p), "v"(v) : "memory"); }
DI void st8(void* p, u32x2 v) { *(u32x2*)p = v; }
DI void st8wt(void* p, u32x2 v) { asm volatile("global_store_dwordx2 %0, %1, off sc1" ::"v"(p), "v"(v) : "memory"); }
DI void st4(void* p, unsigned v) { asm volatile("global_store_dword %0, %1, off sc1" ::"v"(p), "v"(v) : "memory"); }
DI void glds16_if0(int c, const void* sbase, unsigned voff, unsigned lds_dst) {
  asm volatile("s_cmp_lg_u32 %3, 0\n\ts_cbranch_scc1 1f\n\ts_mov_b32 m0, %2\n\ts_nop 1\n\tglobal_load_lds_dwordx4 %0, %1\n1:"
               ::"v"(voff), "s"(sbase), "s"(lds_dst), "s"(c) : "memory", "scc");
}
DI void touch_if0(int c, const void* sbase, unsigned voff, unsigned lds_dst) {
  asm volatile("s_cmp_lg_u32 %3, 0\n\ts_cbranch_scc1 1f\n\ts_mov_b32 m0, %2\n\ts_nop 1\n\tglobal_load_lds_dword %0, %1\n1:"
               ::"v"(voff), "s"(sbase), "s"(lds_dst), "s"(c) : "memory", "scc");
}
DI void touch_lines(const char* base, unsigned voff, unsigned lds_dummy) {
  asm volatile("s_mov_b32 m0, %2\n\ts_nop 3\n\tglobal_load_lds_dword %0, %1" ::"v"(voff), "s"(base), "s"(lds_dummy) : "memory");
}
#define CODE_TOUCH(SYM, BYTES, LDS_DUMMY)                                                                          \
  do {                                                                                                             \
    const char* cp_;                                                                                               \
    asm volatile("s_getpc_b64 s[20:21]\n\ts_add_u32 s20, s20, " SYM "@rel32@lo+4\n\ts_addc_u32 s21, s21, " SYM     \
                 "@rel32@hi+12\n\ts_mov_b64 %0, s[20:21]" : "=s"(cp_)::"s20", "s21", "scc");                      \
    constexpr int nl_ = (BYTES) / 128, per_ = (nl_ + 28) / 29;                                                      \
    const int ln_ = (blockIdx.x >> 3) * per_ + lane;                                                               \
    if (lane < per_ && ln_ < nl_) touch_lines(cp_, ln_ * 128, LDS_DUMMY);                                          \
  } while (0)
#define SYM_K1 "_Z9k1_ln_qkvPKcS0_PcS0_"
#define SYM_K2 "_Z7k2_attnPcPKcS1_S1_S1_"
#define SYM_K4 "_Z9k4_gateupPKcS0_PcS0_S1_"
constexpr int LEN_K1 = 24000, LEN_K2 = 11200, LEN_K4 = 30000;
template <int N> DI void wait_bar() { asm volatile("s_waitcnt vmcnt(%0) lgkmcnt(0)\n\ts_barrier" ::"n"(N) : "memory"); }
template <int N> DI void wait_vm() { asm volatile("s_waitcnt vmcnt(%0)" ::"n"(N) : "memory"); }
DI unsigned lds_arrive(lds_ptr c, int lane) {
  unsigned old = 0;
  if (lane == 0) old = __hip_atomic_fetch_add((LDS_AS unsigned*)c, 1u, __ATOMIC_RELAXED, __HIP_MEMORY_SCOPE_WORKGROUP);
  return __builtin_amdgcn_readfirstlane(old);
}
DI void lds_sync(lds_ptr c, unsigned n, int lane) {
  asm volatile("" ::: "memory");
  if (lds_arrive(c, lane) + 1 != n) {
    for (int spin = 0; spin < (1 << 20); ++spin) {
      const unsigned v = __builtin_amdgcn_readfirstlane(__hip_atomic_load((LDS_AS unsigned*)c, __ATOMIC_RELAXED, __HIP_MEMORY_SCOPE_WORKGROUP));
      if (v >= n) break;
      __builtin_amdgcn_s_sleep(1);
    }
  }
  asm volatile("" ::: "memory");
}
DI void lds_post(lds_ptr c, int lane) {
  asm volatile("" ::: "memory");
  if (lane == 0) (void)__hip_atomic_fetch_add((LDS_AS unsigned*)c, 1u, __ATOMIC_RELAXED, __HIP_MEMORY_SCOPE_WORKGROUP);
}
DI void lds_wait(lds_ptr c, unsigned n) {
  for (int spin = 0; spin < (1 << 20); ++spin) {
    const unsigned v = __builtin_amdgcn_readfirstlane(__hip_atomic_load((LDS_AS unsigned*)c, __ATOMIC_RELAXED, __HIP_MEMORY_SCOPE_WORKGROUP));
    if (v >= n) break;
    __builtin_amdgcn_s_sleep(1);
  }
  asm volatile("" ::: "memory");
}
template <class T> DI void pin(T& v) { asm volatile("" : "+v"(v)); }
DI void raw_bar() { asm volatile("s_barrier" ::: "memory"); }
DI void lds_bar() { asm volatile("s_waitcnt lgkmcnt(0)\n\ts_barrier" ::: "memory"); }
DI bf16x8 lds_r128(lds_ptr p) { return *(LDS_AS const bf16x8*)p; }
typedef short v4i16_t __attribute__((ext_vector_type(4)));
DI s16x4 lds_tr(lds_ptr p) { return __builtin_amdgcn_ds_read_tr16_b64_v4i16((LDS_AS v4i16_t*)p); }
DI bf16x8 cat8(s16x4 a, s16x4 b) { return __builtin_shufflevector(a, b, 0, 1, 2, 3, 4, 5, 6, 7); }
#define SB() __builtin_amdgcn_sched_barrier(0)
#define MFMA16(a, b, c) __builtin_amdgcn_mfma_f32_16x16x32_bf16(a, b, c, 0, 0, 0)
#define MFMA32(a, b, c) __builtin_amdgcn_mfma_f32_32x32x16_bf16(a, b, c, 0, 0, 0)
DI void mfma16_acc(f32x4& c, bf16x8 a, bf16x8 b) { asm volatile("v_mfma_f32_16x16x32_bf16 %0, %1, %2, %0" : "+v"(c) : "v"(a), "v"(b)); }
DI void mfma16_zero(f32x4& c, bf16x8 a, bf16x8 b) { asm volatile("v_mfma_f32_16x16x32_bf16 %0, %1, %2, 0" : "=&v"(c) : "v"(a), "v"(b)); }
template <class F, int... I> DI void for_seq_impl(F&& f, std::integer_sequence<int, I...>) { (f(std::integral_constant<int, I>{}), ...); }
template <int N, class F> DI void for_seq(F&& f) { for_seq_impl(f, std::make_integer_sequence<int, N>{}); }
DI int xcd_remap(int b, int nwg) {
  int q = nwg >> 3, r = nwg & 7, x = b & 7, o = b >> 3;
  return (x < r ? x * (q + 1) : r * (q + 1) + (x - r) * q) + o;
}

constexpr int K1_X = 0, K1_W = 40960, K1_SLOT = 12288, K1_ST = K1_W + K1_SLOT * 9, K1_CS = K1_W, K1_PF = K1_ST + 1024, K1_LDS = K1_PF + 1024;
constexpr int cmin(int a, int b) { return a < b ? a : b; }
constexpr int k1_nw(int kk) { return cmin(18, cmin(3 + 2 * kk, kk + 7)); }
constexpr int k1_nx(int kk) { return cmin(18, cmin(3 + 2 * kk, kk + 5)); }

template <bool ISV, bool HI>
DI void k1_consumer(lds_ptr lds, int cb, int lane, char* ws, int ct, int m0) {
  const int g = lane >> 4, q = (lane & 15) >> 2, p = lane & 3, r = lane & 15;
  constexpr int NM = HI ? 2 : 3, MB = HI ? 3 : 0;
  lds_ptr a0 = lds + K1_X + MB * 2048 + (r >> 3) * 1024 + (r & 7) * 128 + ((g ^ (r >> 1)) * 16);
  lds_ptr a1 = lds + K1_X + MB * 2048 + (r >> 3) * 1024 + (r & 7) * 128 + (((4 + g) ^ (r >> 1)) * 16);
  const int rho0 = 16 * (g >> 1) + 4 * (g & 1) + q;
  lds_ptr wb = lds + K1_W + rho0 * 128 + ((cb ^ ((rho0 >> 1) & 3)) * 32) + p * 8;
  lds_ptr wb5 = wb + 5 * K1_SLOT;
  asm volatile("" : "+v"(wb), "+v"(wb5));
  const u32x4 onesu = {0x3f803f80u, 0x3f803f80u, 0x3f803f80u, 0x3f803f80u};
  const bf16x8 ones = __builtin_bit_cast(bf16x8, onesu);
  f32x4 acc[3][3];
#pragma unroll
  for (int m = 0; m < 3; ++m)
#pragma unroll
    for (int j = 0; j < 3; ++j) acc[m][j] = f32x4{0.f, 0.f, 0.f, 0.f};
  bf16x8 fA[2][3];
  s16x4 wl[2][3], wh[2][3];
  auto rd1 = [&](auto KK, auto II) {
    constexpr int kk = KK, i = II, bf = kk & 1, sl = kk % 9;
    if constexpr (i >= 2 && i <= 4) {
      if constexpr (i - 2 < NM) fA[bf][i - 2] = lds_r128(((kk & 1) ? a1 : a0) + ((kk >> 1) & 3) * 10240 + (i - 2) * 2048);
    } else {
      constexpr int j = i < 2 ? 0 : (i - 3) / 2, hh = i < 2 ? i : (i - 3) % 2;
      lds_ptr bp = (sl >= 5 ? wb5 + (sl - 5) * K1_SLOT : wb + sl * K1_SLOT) + j * 4096 + hh * 1024;
      if constexpr (hh == 0) wl[bf][j] = lds_tr(bp); else wh[bf][j] = lds_tr(bp);
    }
  };
  lds_bar();
  STAMP_MARKW(0, 2, 512);
  for_seq<9>([&](auto I) { rd1(std::integral_constant<int, 0>{}, I); });
  SB();
  for_seq<18>([&](auto KK) {
    constexpr int kk = KK, bf = kk & 1;
    if constexpr (kk < 17) { raw_bar(); SB(); }
    for_seq<9>([&](auto II) {
      constexpr int i = II, j = i / 3, m = i % 3;
      const bf16x8 wv = cat8(wl[bf][j], wh[bf][j]);
      const bf16x8 av = (m < NM) ? fA[bf][m] : ones;
      acc[m][j] = ISV ? MFMA16(av, wv, acc[m][j]) : MFMA16(wv, av, acc[m][j]);
      SB();
      if constexpr (kk < 17) { rd1(std::integral_constant<int, kk + 1>{}, II); SB(); }
    });
  });
  STAMP_MARKW(0, 4, 512);
  lds_ptr cx = lds + K1_CS + cb * 3072 + lane * 16;
  if (HI) {
#pragma unroll
    for (int j = 0; j < 3; ++j) *(LDS_AS f32x4*)(cx + j * 1024) = acc[2][j];
  }
  lds_bar();
  STAMP_MARKW(0, 5, 512);
  f32x4 cs[3];
#pragma unroll
  for (int j = 0; j < 3; ++j) cs[j] = HI ? acc[2][j] : *(LDS_AS f32x4*)(cx + j * 1024);
  if (ISV) {
    f32x4 st[NM][2];
#pragma unroll
    for (int m = 0; m < NM; ++m) {
      st[m][0] = *(LDS_AS f32x4*)(lds + K1_ST + ((MB + m) * 16 + 4 * g) * 8);
      st[m][1] = *(LDS_AS f32x4*)(lds + K1_ST + ((MB + m) * 16 + 4 * g) * 8 + 16);
    }
#pragma unroll
    for (int j = 0; j < 3; ++j) {
      const int c = (ct - 6) * 192 + j * 64 + cb * 16 + r;
      const int head = c / 18, d = c - head * 18;
      char* vb = ws + WS_V + (size_t)(head * 128 + (m0 >> 4) + MB) * 592;
      char* vp = vb + (g & 1) * 288 + d * 16 + (g >> 1) * 8;
      const float c0 = cs[j][0];
#pragma unroll
      for (int m = 0; m < NM; ++m) {
        u32x2 o = {cvt_pk(fmaf(st[m][0][0], c0, acc[m][j][0]) * st[m][0][1], fmaf(st[m][0][2], c0, acc[m][j][1]) * st[m][0][3]),
                   cvt_pk(fmaf(st[m][1][0], c0, acc[m][j][2]) * st[m][1][1], fmaf(st[m][1][2], c0, acc[m][j][3]) * st[m][1][3])};
        st8(vp + m * 592, o);
      }
      if (d == 17 && g < NM) *(u32x4*)(vb + g * 592 + 576) = onesu;
    }
  } else {
    const bool isq = ct < 3;
    const float sc = isq ? QSCALE : 1.f;
    f32x2 st[NM];
#pragma unroll
    for (int m = 0; m < NM; ++m) { st[m] = *(LDS_AS f32x2*)(lds + K1_ST + ((MB + m) * 16 + r) * 8); st[m][1] *= sc; }
#pragma unroll
    for (int j = 0; j < 3; ++j) {
      const int c = (isq ? ct : ct - 3) * 192 + j * 64 + cb * 16 + 4 * g;
      const int pair = c / 36, qi = (c - pair * 36) >> 2;
      const int head = 2 * pair + (qi > 4 ? 1 : 0), a = qi > 4 ? qi - 5 : qi;
      char* mainp = ws + (isq ? WS_QM : WS_KM) + (size_t)head * 65536 + (a >> 1) * 512 + (a & 1) * 8;
      char* xp = ws + (isq ? WS_QX : WS_KX) + (size_t)pair * 16384;
#pragma unroll
      for (int m = 0; m < NM; ++m) {
        const int rw = m0 + (MB + m) * 16 + r;
        u32x2 o = {cvt_pk(fmaf(st[m][0], cs[j][0], acc[m][j][0]) * st[m][1], fmaf(st[m][0], cs[j][1], acc[m][j][1]) * st[m][1]),
                   cvt_pk(fmaf(st[m][0], cs[j][2], acc[m][j][2]) * st[m][1], fmaf(st[m][0], cs[j][3], acc[m][j][3]) * st[m][1])};
        char* dst = (qi == 4) ? xp + rw * 8 : mainp + (rw >> 5) * 1024 + (rw & 31) * 16;
        st8(dst, o);
      }
    }
  }
}

__global__ __launch_bounds__(1024) void k1_ln_qkv(const char* __restrict__ x, const char* __restrict__ wqkv, char* __restrict__ ws, const char* __restrict__ wo) {
  extern __shared__ __attribute__((aligned(16))) char smem[];
  STAMP_BEGIN
  lds_ptr lds = (lds_ptr)smem;
  const unsigned lds0 = (unsigned)(uintptr_t)lds;
  const int tid = threadIdx.x, lane = tid & 63, wid = __builtin_amdgcn_readfirstlane(tid >> 6);
  const int tile = xcd_remap(blockIdx.x, 234);
  const int cgp = tile / 78, rem = tile - cgp * 78;
  const int mt = rem / 3, ct = cgp * 3 + (rem - mt * 3);
  const int m0 = min(mt * 80, SEQ - 80);
  if (wid < 3) {
    unsigned wv[4];
#pragma unroll
    for (int i = 0; i < 4; ++i) {
      const int rho = 8 * i + (lane >> 3);
      const int gk = ((rho >> 4) & 1) * 2 + ((rho >> 2) & 1), tk = (rho >> 3) & 1, qk = rho & 3;
      wv[i] = (8 * gk + 4 * tk + qk) * 3456 + (((lane & 7) ^ (((rho >> 1) & 3) << 1)) * 16);
    }
    const char* wsrc = wqkv + ct * 384 + wid * 128;
    const unsigned wdst = lds0 + K1_W + wid * 4096;
    for_seq<18>([&](auto KK) {
      constexpr int kk = KK;
#pragma unroll
      for (int n = (kk == 0 ? 0 : k1_nw(kk - 1)); n < k1_nw(kk); ++n)
#pragma unroll
        for (int i = 0; i < 4; ++i) glds16(wsrc + (size_t)n * 32 * 3456, wv[i], wdst + (n % 9) * K1_SLOT + i * 1024);
      wait_bar<(k1_nw(kk) - kk - 1) * 4>();
    });
    return;
  }
  if (wid == 3) {
    const unsigned v0 = (lane >> 3) * 1152 + (((lane & 7) ^ (lane >> 4)) * 16);
    const unsigned v1 = (lane >> 3) * 1152 + (((lane & 7) ^ (4 + (lane >> 4))) * 16);
    const char* xs = x + (size_t)m0 * 1152;
    for_seq<18>([&](auto KK) {
      constexpr int kk = KK;
#pragma unroll
      for (int h = (kk == 0 ? 0 : k1_nx(kk - 1)); h < k1_nx(kk); ++h)
#pragma unroll
        for (int i = 0; i < 5; ++i) {
          const int pc = (h & 1) * 5 + i;
          glds16(xs + pc * 9216 + (h >> 1) * 128, (pc & 1) ? v1 : v0, lds0 + K1_X + ((h >> 1) & 3) * 10240 + pc * 1024);
        }
      wait_bar<(k1_nx(kk) - 2 * (kk >> 1) - 2) * 5>();
    });
    return;
  }
  if (wid < 8) {
    const int j = wid - 4;
    if (j == 0) CODE_TOUCH(SYM_K1, LEN_K1, lds0 + K1_PF);
    if (j == 1) CODE_TOUCH(SYM_K2, LEN_K2, lds0 + K1_PF + 256);
    if (j >= 2 && blockIdx.x == 0) *(unsigned*)(ws + WS_CNT + ((j - 2) * 64 + lane) * 128) = 0u;
    {
      const int ln = blockIdx.x * 168 + j * 42 + lane;
      if (lane < 42) {
        if (ln < 15552) touch_lines(wqkv, ln * 128, lds0 + K1_PF + j * 256);
        else if (ln < 33984) touch_lines(x, (ln - 15552) * 128, lds0 + K1_PF + j * 256);
        else if (ln < 39168) touch_lines(wo, (ln - 33984) * 128, lds0 + K1_PF + j * 256);
      }
    }
    lds_ptr p0 = lds + K1_X + j * 1024 + lane * 16, p2 = lds + K1_X + min(j + 8, 9) * 1024 + lane * 16;
    float s1[3] = {0.f, 0.f, 0.f}, s2[3] = {0.f, 0.f, 0.f};
#pragma unroll 1
    for (int s = 0; s < 9; ++s) {
      raw_bar();
      const int so = (s & 3) * 10240;
      u32x4 v[3] = {*(LDS_AS u32x4*)(p0 + so), *(LDS_AS u32x4*)(p0 + so + 4096), *(LDS_AS u32x4*)(p2 + so)};
      SB();
      raw_bar();
      SB();
#pragma unroll
      for (int i = 0; i < 3; ++i)
#pragma unroll
        for (int e = 0; e < 4; ++e) { s1[i] = dot2(v[i][e], 0x3f803f80u, s1[i]); s2[i] = dot2(v[i][e], v[i][e], s2[i]); }
      asm volatile("" : "+v"(s1[0]), "+v"(s1[1]), "+v"(s1[2]), "+v"(s2[0]), "+v"(s2[1]), "+v"(s2[2]));
    }
#pragma unroll
    for (int i = 0; i < 3; ++i) {
      const float a = sum8(s1[i]), b = sum8(s2[i]);
      const float mean = a * (1.f / 576.f);
      const float var = fmaxf(b * (1.f / 576.f) - mean * mean, 0.f);
      const int pc = i < 2 ? j + 4 * i : min(j + 8, 9);
      if ((lane & 7) == 0) *(LDS_AS f32x2*)(lds + K1_ST + (pc * 8 + (lane >> 3)) * 8) = f32x2{-mean, __builtin_amdgcn_rsqf(var + LN_EPS)};
    }
    lds_bar();
    return;
  }
  if (ct >= 6) {
    if (wid < 12) k1_consumer<true, false>(lds, wid - 8, lane, ws, ct, m0);
    else k1_consumer<true, true>(lds, wid - 12, lane, ws, ct, m0);
  } else {
    if (wid < 12) k1_consumer<false, false>(lds, wid - 8, lane, ws, ct, m0);
    else k1_consumer<false, true>(lds, wid - 12, lane, ws, ct, m0);
  }
  STAMP_ENDW(0, 512);
}

#define K2SYNC 0
#define K2_SL0 4
#define K2_SL1 4
constexpr int K2_KM = 0, K2_KX = 16384, K2_V = K2_KX + 4096, K2_SY = 44032, K2_BI = K2_V + 19 * 1024 + 128, K2_CS = 44288, K2_W = K2_CS + 4864, K2_LDS = K2_W + 73728;

#define MFMA32K8(a, b, c) __builtin_amdgcn_mfma_f32_32x32x8bf16_1k(a, b, c, 0, 0, 0)
DI void k2_core(lds_ptr lds, char* ws, int hd, int t, int wid, int lane, bf16x8 q0, unsigned qx) {
  const int hi = lane >> 5, l31 = lane & 31;
  const u32x2 q1u = {hi ? 0x3f80u : ((hd & 1) ? 0u : qx), (!hi && (hd & 1)) ? qx : 0u};
  const s16x4 q1 = __builtin_bit_cast(s16x4, q1u);
  f32x16 S[9];
  lds_ptr kmp = lds + K2_KM + wid * 1024 + lane * 16;
  lds_ptr kxp = hi ? lds + K2_BI + wid * 256 : lds + K2_KX + (wid * 32 + l31) * 8;
  const float NEG = -1e30f;
  const int dq = l31 - 4 * hi;
  float mx0 = NEG, mx1 = NEG;
  bf16x8 kf[3];
  s16x4 kx[3];
  auto rdk = [&](auto NN) {
    constexpr int n = NN, i = n == 0 ? 8 : n - 1;
    kf[n % 3] = lds_r128(kmp + i * 1024);
    kx[n % 3] = *(LDS_AS const s16x4*)(kxp + i * 256);
  };
  auto post = [&](auto NN) {
    constexpr int n = NN, i = n == 0 ? 8 : n - 1;
    if constexpr (i < 8) {
      if constexpr (i == 0) {
#pragma unroll
        for (int r = 0; r < 16; ++r) S[0][r] = (((r & 3) + 8 * (r >> 2)) <= dq) ? S[8][r] : S[0][r];
      }
#pragma unroll
      for (int r = 0; r < 16; r += 4) {
        mx0 = fmaxf(fmaxf(mx0, S[i][r]), S[i][r + 1]);
        mx1 = fmaxf(fmaxf(mx1, S[i][r + 2]), S[i][r + 3]);
      }
    }
  };
  rdk(std::integral_constant<int, 0>{});
  rdk(std::integral_constant<int, 1>{});
  SB();
  for_seq<9>([&](auto NN) {
    constexpr int n = NN, i = n == 0 ? 8 : n - 1;
    const f32x16 z = {};
    S[i] = MFMA32(kf[n % 3], q0, z);
    SB();
    if constexpr (n + 2 < 9) { rdk(std::integral_constant<int, n + 2>{}); SB(); }
    S[i] = MFMA32K8(kx[n % 3], q1, S[i]);
    SB();
    if constexpr (n >= 1) { post(std::integral_constant<int, n - 1>{}); SB(); }
  });
  post(std::integral_constant<int, 8>{});
  float mx = fmaxf(mx0, mx1);
  {
    auto rr = __builtin_amdgcn_permlane32_swap(__float_as_uint(mx), __float_as_uint(mx), false, false);
    mx = fmaxf(__uint_as_float(rr[0]), __uint_as_float(rr[1]));
  }
  s16x4 mq, mk1;
  {
    const float nm = -mx;
    const unsigned h1 = __float_as_uint(nm) & 0xffff0000u;
    const float r1 = nm - __uint_as_float(h1);
    const unsigned h2 = __float_as_uint(r1) & 0xffff0000u;
    const float r2 = r1 - __uint_as_float(h2);
    const u32x2 mu = {hi ? 0u : ((h1 >> 16) | h2), hi ? 0u : (__float_as_uint(r2) >> 16)};
    mq = __builtin_bit_cast(s16x4, mu);
    const u32x2 ou = {0x3f803f80u, 0x00003f80u};
    mk1 = __builtin_bit_cast(s16x4, ou);
  }
  S[1] = MFMA32K8(mk1, mq, S[1]);
  S[2] = MFMA32K8(mk1, mq, S[2]);
  SB();
  lds_ptr vp = lds + K2_V + wid * 2 * 592 + (l31 < 18 ? hi * 288 + l31 * 16 : 576);
  f32x16 O;
  bf16x8 vf[3];
  auto rdv = [&](auto EE) {
    constexpr int e = EE, blk = e < 14 ? e + 2 : ((e - 14) >> 1) + ((e & 1) ? 16 : 0);
    if constexpr (e < 18) vf[e % 3] = lds_r128(vp + blk * 592);
  };
  rdv(std::integral_constant<int, 0>{});
  rdv(std::integral_constant<int, 1>{});
  SB();
  for_seq<16>([&](auto HH) {
    constexpr int h = HH, i = ((h >> 1) + 1) & 7, s = h & 1;
    u32x4 P;
#pragma unroll
    for (int j = 0; j < 4; ++j) P[j] = cvt_pk(__builtin_amdgcn_exp2f(S[i][8 * s + 2 * j]), __builtin_amdgcn_exp2f(S[i][8 * s + 2 * j + 1]));
    if constexpr (i == 0) {
      constexpr int e = 14 + 2 * s;
      u32x4 P8, P0;
#pragma unroll
      for (int j = 0; j < 4; ++j) {
        const int r0 = 8 * s + 2 * j, r1 = r0 + 1;
        const unsigned mk = ((((r0 & 3) + 8 * (r0 >> 2)) <= dq) ? 0xffffu : 0u) | ((((r1 & 3) + 8 * (r1 >> 2)) <= dq) ? 0xffff0000u : 0u);
        P8[j] = P[j] & mk;
        P0[j] = P[j] & ~mk;
      }
      rdv(std::integral_constant<int, e + 2>{});
      SB();
      O = MFMA32(vf[e % 3], __builtin_bit_cast(bf16x8, P0), O);
      SB();
      rdv(std::integral_constant<int, e + 3>{});
      O = MFMA32(vf[(e + 1) % 3], __builtin_bit_cast(bf16x8, P8), O);
      SB();
    } else {
      rdv(std::integral_constant<int, h + 2>{});
      SB();
      if constexpr (h == 0) { const f32x16 z = {}; O = MFMA32(vf[0], __builtin_bit_cast(bf16x8, P), z); }
      else O = MFMA32(vf[h % 3], __builtin_bit_cast(bf16x8, P), O);
      if constexpr (s == 0 && i + 2 <= 8) S[(i + 2) & 7] = MFMA32K8(mk1, mq, S[(i + 2) & 7]);
      SB();
    }
  });
  const float inv = __builtin_amdgcn_rcpf(O[10]);
  const int row = t * 32 + l31;
  char* ob = ws + WS_O + (size_t)(row >> 4) * 18 * 1024 + (row & 15) * 16;
  u32x4 o = {cvt_pk(O[0] * inv, O[1] * inv), cvt_pk(O[2] * inv, O[3] * inv), cvt_pk(O[4] * inv, O[5] * inv), cvt_pk(O[6] * inv, O[7] * inv)};
  st16(ob + (hd >> 1) * 1024 + (2 * (hd & 1) + hi) * 256, o);
  if (hi == 0) st4(ob + (16 + (hd >> 4)) * 1024 + ((hd >> 2) & 3) * 256 + (hd & 3) * 4, cvt_pk(O[8] * inv, O[9] * inv));
}

__global__ __launch_bounds__(768) void k2_attn(char* __restrict__ ws, const char* __restrict__ wg, const char* __restrict__ wu,
                                               const char* __restrict__ wo, const char* __restrict__ x) {
  extern __shared__ __attribute__((aligned(16))) char smem[];
  STAMP_BEGIN
  lds_ptr lds = (lds_ptr)smem;
  const unsigned lds0 = (unsigned)(uintptr_t)lds;
  const int tid = threadIdx.x, lane = tid & 63, wid = __builtin_amdgcn_readfirstlane(tid >> 6);
  const int hd = (blockIdx.x & 7) * 4 + ((blockIdx.x >> 3) & 3), qb = blockIdx.x >> 5;
  const bool has_tile = blockIdx.x < 234;
  const int tile = has_tile ? xcd_remap(blockIdx.x, 234) : 0;
  const int mt = tile / 9, nt = tile - mt * 9;
  const int m0 = min(mt * 80, SEQ - 80), rb0 = m0 >> 4;
  if (wid == 11) {
    if (lane < 16) *(LDS_AS u32x2*)(lds + K2_BI + lane * 256) = u32x2{(qb == 0 && lane < 8) ? 0xf149u : 0u, 0u};
    if (lane < 4) *(LDS_AS unsigned*)(lds + K2_SY + lane * 4) = 0u;
  }
  if (wid >= 10) {
    lds_bar();
    __builtin_amdgcn_s_sleep(K2_SL1);
    const int sw = wid - 10;
    if (sw == 0) CODE_TOUCH(SYM_K4, LEN_K4, lds0 + K2_CS + 4096);
    const bool act = blockIdx.x < 216;
    const int unit = act ? (int)blockIdx.x * 2 + sw : 0, part = unit / 144, c64 = unit - part * 144;
    const char* wsrc = (c64 < 72 ? wg + c64 * 128 : wu + (c64 - 72) * 128) + (lane & 7) * 16 + (size_t)(part * 192 + (lane >> 3)) * 9216;
    u32x4 v[24];
    if (act) {
#pragma unroll
      for (int i = 0; i < 24; ++i) v[i] = *(const u32x4*)(wsrc + (size_t)i * 8 * 9216);
    }
    if (act) {
      float cs[8];
#pragma unroll
      for (int j = 0; j < 8; ++j) cs[j] = 0.f;
#pragma unroll
      for (int i = 0; i < 24; ++i)
#pragma unroll
        for (int j = 0; j < 4; ++j) { cs[2 * j] += bf_lo(v[i][j]); cs[2 * j + 1] += bf_hi(v[i][j]); }
      lds_ptr cp = lds + K2_CS + sw * 2048;
      *(LDS_AS f32x4*)(cp + lane * 32) = f32x4{cs[0], cs[1], cs[2], cs[3]};
      *(LDS_AS f32x4*)(cp + lane * 32 + 16) = f32x4{cs[4], cs[5], cs[6], cs[7]};
      asm volatile("s_waitcnt lgkmcnt(0)" ::: "memory");
      float t = 0.f;
#pragma unroll
      for (int i = 0; i < 8; ++i) t += *(LDS_AS float*)(cp + i * 256 + lane * 4);
      *(float*)(ws + WS_CSP + ((size_t)part * 9216 + c64 * 64 + lane) * 4) = t;
    }
    if (has_tile) { raw_bar(); raw_bar(); }
    return;
  }
  if (wid >= 8) {
    raw_bar();
    __builtin_amdgcn_s_sleep(K2_SL0);
    if (has_tile) {
      const int par = wid - 8;
      unsigned va[4], vb[4];
#pragma unroll
      for (int i = 0; i < 4; ++i) {
        const int rho = 8 * i + (lane >> 3);
        const int g_ = ((rho >> 4) & 1) * 2 + ((rho >> 2) & 1), t_ = (rho >> 3) & 1, q_ = rho & 3;
        const int La = 36 * par + 18 * (g_ >> 1) + 8 * t_ + 4 * (g_ & 1) + q_, Lb = 288 * par + 18 * (4 * g_ + 2 * t_ + (q_ >> 1)) + 16 + (q_ & 1);
        const unsigned csw = ((lane & 7) ^ (((rho >> 1) & 3) << 1)) * 16;
        va[i] = La * 1152 + csw; vb[i] = Lb * 1152 + csw;
      }
#pragma unroll
      for (int kp = 0; kp < 9; ++kp)
#pragma unroll
        for (int i = 0; i < 4; ++i)
          glds16(wo + nt * 128 + (size_t)(kp == 8 ? 0 : 72 * kp) * 1152, kp == 8 ? vb[i] : va[i], lds0 + K2_W + par * 4096 + kp * 8192 + i * 1024);
    }
  } else {
    const int t = qb * 8 + wid;
    bf16x8 q0;
    unsigned qx;
    const unsigned vo = lane * 16;
    const int kt0 = qb * 8 - 8;
    asm volatile("global_load_dwordx4 %0, %2, %3\n\tglobal_load_dword %1, %4, %5"
                 : "=&v"(q0), "=&v"(qx)
                 : "v"(vo), "s"(ws + WS_QM + (size_t)hd * 65536 + (size_t)t * 1024), "v"((lane & 31) * 8),
                   "s"(ws + WS_QX + (size_t)(hd >> 1) * 16384 + (size_t)t * 256 + (hd & 1) * 4)
                 : "memory");
#pragma unroll
    for (int i = 0; i < 2; ++i) {
      const int lt = wid * 2 + i;
      const int kt = max(kt0 + lt, 0);
      glds16(ws + WS_KM + (size_t)hd * 65536 + (size_t)kt * 1024, vo, lds0 + K2_KM + lt * 1024);
    }
    if (wid >= 4) {
      const int k0 = max(qb * 256 - 256 + (wid - 4) * 128, 0);
      glds16(ws + WS_KX + (size_t)(hd >> 1) * 16384 + (size_t)k0 * 8, vo, lds0 + K2_KX + (wid - 4) * 1024);
    }
    const long vb0 = (long)(hd * 128 + qb * 16 - 16) * 592;
#pragma unroll
    for (int i = 0; i < 3; ++i) {
      const int pc = wid + 8 * i;
      if (pc < 19) {
        long off = vb0 + pc * 1024;
        unsigned v2 = vo;
        if (qb == 0) { off = (long)(hd * 128) * 592 + pc * 1024; v2 = (pc * 1024 + lane * 16 >= 9472) ? vo - 9472 + 65536 : vo + 65536; off -= 65536; }
        glds16(ws + WS_V + off, v2, lds0 + K2_V + pc * 1024);
      }
    }
    raw_bar();
    asm volatile("s_waitcnt vmcnt(0)" : "+v"(q0), "+v"(qx)::"memory");
    lds_sync(lds + K2_SY, 8, lane);
    k2_core(lds, ws, hd, t, wid, lane, q0, qx);
    STAMP_MARK(1, 3);
    STAMP_MARKW(1, 7, 448);
  }
  wait_vm<0>();
  STAMP_MARK(1, 4);
  unsigned* cnt = (unsigned*)(ws + WS_CNT);
  if (wid < 8) {
    if (lds_arrive(lds + K2_SY + 8, lane) == 7 && lane == 0) __hip_atomic_fetch_add(cnt + qb * 32, 1u, __ATOMIC_RELAXED, __HIP_MEMORY_SCOPE_AGENT);
  }
  if (!has_tile) { STAMP_END(1); return; }
  lds_sync(lds + K2_SY + 12, 10, lane);
  const int mi = wid >> 1, kg = wid & 1;
  const int g = lane >> 4, q = (lane & 15) >> 2, p = lane & 3;
  const int er = tid >> 3, ec = tid & 7;
  const u32x4 res = *(const u32x4*)(x + (size_t)(m0 + er) * 1152 + (nt * 64 + ec * 8) * 2);
  const int rho0 = 16 * (g >> 1) + 4 * (g & 1) + q;
  lds_ptr wb[4];
#pragma unroll
  for (int n = 0; n < 4; ++n) { wb[n] = lds + K2_W + kg * 4096 + rho0 * 128 + ((n ^ ((rho0 >> 1) & 3)) * 32) + p * 8; asm volatile("" : "+v"(wb[n])); }
  bf16x8 wf[5][4];
#pragma unroll
  for (int j = 0; j < 5; ++j)
#pragma unroll
    for (int n = 0; n < 4; ++n) wf[j][n] = cat8(lds_tr(wb[n] + j * 8192), lds_tr(wb[n] + j * 8192 + 1024));
  if (tid == 576) {
    const unsigned* c0 = cnt + (m0 >> 8) * 32;
    const unsigned* c1 = cnt + ((m0 + 79) >> 8) * 32;
    for (int spin = 0; spin < (1 << 18); ++spin) {
      const unsigned a = __hip_atomic_load(c0, __ATOMIC_RELAXED, __HIP_MEMORY_SCOPE_AGENT);
      const unsigned b = __hip_atomic_load(c1, __ATOMIC_RELAXED, __HIP_MEMORY_SCOPE_AGENT);
      if (a >= 32u && b >= 32u) break;
      __builtin_amdgcn_s_sleep(1);
    }
  }
  SB();
  raw_bar();
  SB();
  STAMP_MARK(1, 5);
  const __amdgpu_buffer_rsrc_t ors = __builtin_amdgcn_make_buffer_rsrc((void*)(ws + WS_O), 0, 128 * 18432, 0x00020000);
  const int aoff = (rb0 + mi) * 18432 + kg * 1024 + lane * 16;
  bf16x8 a[9];
#pragma unroll
  for (int i = 0; i < 9; ++i) a[i] = __builtin_bit_cast(bf16x8, __builtin_amdgcn_raw_buffer_load_b128(ors, aoff + i * 2048, 0, 16));
  SB();
  f32x4 acc[4];
#pragma unroll
  for (int n = 0; n < 4; ++n) acc[n] = f32x4{0.f, 0.f, 0.f, 0.f};
#pragma unroll
  for (int j = 0; j < 9; ++j) {
#pragma unroll
    for (int n = 0; n < 4; ++n) {
      acc[n] = MFMA16(wf[j % 5][n], a[j], acc[n]);
      if (j + 5 < 9) wf[j % 5][n] = cat8(lds_tr(wb[n] + (j + 5) * 8192),
                                         lds_tr(wb[n] + (j + 5) * 8192 + 1024));
    }
  }
  STAMP_MARK(1, 6);
  {
    lds_ptr cw = lds + kg * (80 * 272) + (mi * 16 + (lane & 15)) * 272 + 16 * g;
#pragma unroll
    for (int n = 0; n < 4; ++n) *(LDS_AS f32x4*)(cw + n * 64) = acc[n];
  }
  lds_bar();
  {
    lds_ptr cr = lds + er * 272 + ec * 32;
    f32x4 a0 = *(LDS_AS f32x4*)(cr), a1 = *(LDS_AS f32x4*)(cr + 16);
    f32x4 b0 = *(LDS_AS f32x4*)(cr + 80 * 272), b1 = *(LDS_AS f32x4*)(cr + 80 * 272 + 16);
    a0 += b0; a1 += b1;
    u32x4 o;
    o[0] = cvt_pk(a0[0] + bf_lo(res[0]), a0[1] + bf_hi(res[0]));
    o[1] = cvt_pk(a0[2] + bf_lo(res[1]), a0[3] + bf_hi(res[1]));
    o[2] = cvt_pk(a1[0] + bf_lo(res[2]), a1[1] + bf_hi(res[2]));
    o[3] = cvt_pk(a1[2] + bf_lo(res[3]), a1[3] + bf_hi(res[3]));
    const int row = m0 + er, col = nt * 64 + ec * 8;
    st16(ws + WS_H + (size_t)(row >> 4) * 18432 + (col >> 5) * 1024 + ((col >> 3) & 3) * 256 + (row & 15) * 16, o);
    float s1 = 0.f, s2 = 0.f;
#pragma unroll
    for (int j = 0; j < 4; ++j) { s1 = dot2(o[j], 0x3f803f80u, s1); s2 = dot2(o[j], o[j], s2); }
    s1 = sum8(s1); s2 = sum8(s2);
    if (ec == 0) *(f32x2*)(ws + WS_ST + (size_t)row * 80 + nt * 8) = f32x2{s1, s2};
  }
  STAMP_END(1);
}

constexpr int K4_SLOT = 18432, K4_ST = K4_SLOT * 8, K4_CS = K4_ST + 2048, K4_PF = K4_CS + 2048, K4_CP = K4_PF + 1024, K4_SY = 160000, K4_LDS = 160 * 1024;

constexpr int k4_frag(int i) { return i < 16 ? ((i >> 2) * 2 + (i & 1) + ((i & 2) ? 9 : 0)) : (i == 16 ? 8 : 17); }
constexpr int k4_foff(int f) { return (f >= 9 ? 9216 + (f - 9) * 32 : f * 32); }
constexpr int K4_T0 = 13 * 18;
constexpr int k4_slab(int G) { return G < K4_T0 ? G / 18 : (G - K4_T0 < 80 ? 13 + ((G - K4_T0) % 20) / 4 : 13 + (G - K4_T0 - 80) / 2); }
constexpr int k4_pos(int G) { return G < K4_T0 ? G % 18 : (G - K4_T0 < 80 ? ((G - K4_T0) / 20) * 4 + ((G - K4_T0) & 3) : 16 + ((G - K4_T0) & 1)); }
constexpr int k4_op(int G) {
  const int s = G / 18, i = G % 18;
  if (s > 12) return 0;
  if (s < 3) {
    const int pos[10] = {0, 2, 4, 6, 7, 9, 11, 13, 15, 16};
    for (int k = 0; k < 10; ++k) if (pos[k] == i) return 1 + (2 * s + 2 + k / 5) * 8 + k % 5;
    return 0;
  }
  if (s == 3) {
    const int pos[8] = {0, 2, 4, 6, 8, 11, 14, 16};
    for (int k = 0; k < 8; ++k) if (pos[k] == i) return k < 5 ? 1 + 8 * 8 + k : 1 + 9 * 8 + (k - 3);
    return 0;
  }
  if (i == 1) return 1 + (s + 5) * 8;
  if (i == 4) return 1 + (s + 5) * 8 + 1;
  if (s + 6 > 17) return 0;
  if (i == 8) return 1 + (s + 6) * 8 + 2;
  if (i == 13) return 1 + (s + 6) * 8 + 3;
  if (i == 16) return 1 + (s + 6) * 8 + 4;
  return 0;
}
constexpr int k4_allow(int G, int need) {
  int n = (need == 0) ? 4 : 0;
  for (int g = 0; g < G; ++g) {
    const int op = k4_op(g);
    if (op == 0) continue;
    const int sl = (op - 1) >> 3, kind = (op - 1) & 7;
    if (sl <= need) n = 0;
    else if (kind != 4) ++n;
  }
  return n;
}
constexpr int k4_need(int G) {
  if (G + 5 >= 324 || G == 0) return 0;
  int mx = 0;
  for (int g = 0; g < G + 5; ++g) mx = k4_slab(g) > mx ? k4_slab(g) : mx;
  return k4_slab(G + 5) > mx ? k4_slab(G + 5) : 0;
}

constexpr int d_off(int i) { return i < 6 ? 36864 + i * 9216 : (i < 10 ? (i - 6) * 9216 : (i < 17 ? 92160 + (i - 10) * 9216 : 36864)); }
#define GLUV 1
#define EPI 1
#define EPK 0
#define DT 8
constexpr int D_G0 = (18 - DT) * 9;
constexpr int d_i(int H) { return H < D_G0 ? H / 9 : (H - D_G0 < 8 * DT ? 18 - DT + ((H - D_G0) % (2 * DT)) / 2 : 18 - DT + (H - D_G0 - 8 * DT)); }
constexpr int d_f(int H) { return H < D_G0 ? H % 9 : (H - D_G0 < 8 * DT ? ((H - D_G0) / (2 * DT)) * 2 + ((H - D_G0) & 1) : 8); }
constexpr int d_first(int H) {
  for (int h = 0; h < H; ++h) if (d_i(h) == d_i(H)) return 0;
  return 1;
}
DI void ld2_sc1(bf16x8& d0, bf16x8& d1, unsigned o0, unsigned o1, const char* base) {
  asm volatile("global_load_dwordx4 %0, %2, %4 sc1\n\tglobal_load_dwordx4 %1, %3, %4 sc1" : "=&v"(d0), "=&v"(d1) : "v"(o0), "v"(o1), "s"(base) : "memory");
}
template <int N> DI void wait_vm2(bf16x8& d0, bf16x8& d1) { asm volatile("s_waitcnt vmcnt(%2)" : "+v"(d0), "+v"(d1) : "n"(N) : "memory"); }
DI void poll_count(const unsigned* c, unsigned n) {
  for (int spin = 0; spin < (1 << 18); ++spin) {
    if (__hip_atomic_load(c, __ATOMIC_RELAXED, __HIP_MEMORY_SCOPE_AGENT) >= n) break;
    __builtin_amdgcn_s_sleep(1);
  }
}

__global__ __launch_bounds__(512, 2) void k4_gateup(const char* __restrict__ wgate, const char* __restrict__ wup, char* __restrict__ ws,
                                                    const char* __restrict__ wd, char* __restrict__ out) {
  extern __shared__ __attribute__((aligned(16))) char smem[];
  STAMP_BEGIN
  lds_ptr lds = (lds_ptr)smem;
  const unsigned lds0 = (unsigned)(uintptr_t)lds;
  const int tid = threadIdx.x, lane = tid & 63, wid = __builtin_amdgcn_readfirstlane(tid >> 6);
  const int xcd = blockIdx.x & 7, idx = blockIdx.x >> 3;
  const int pn = 4 * (xcd >> 2) + (idx & 3), jt = 8 * (xcd & 3) + (idx >> 2);
  const int wm = wid & 3, wn = wid >> 2;
  const char* asrc = ws + WS_H + (size_t)(pn * 16 + 2 * wid) * 18432;
  const unsigned avo = lane * 16, avo2 = lane * 16 + 18432;
  const char* wsrc = (wn ? wup : wgate) + jt * 288;
  unsigned wvo[3];
#pragma unroll
  for (int i = 0; i < 3; ++i) {
    const int pc = i < 2 ? 2 * wm + i : 8;
    const int o = pc * 1024 + lane * 16;
    const int rho = o / 288, u = (o - rho * 288) >> 4;
    const int gk = ((rho >> 4) & 1) * 2 + ((rho >> 2) & 1), tk = (rho >> 3) & 1, qk = rho & 3;
    wvo[i] = (8 * gk + 4 * tk + qk) * 9216 + u * 16;
  }
  const unsigned wdst = lds0 + wn * 9216;
  auto issue_w = [&](int s, int i) {
    const unsigned so = (s & 7) * K4_SLOT;
    const char* wp = wsrc + (size_t)s * 32 * 9216;
    if (i < 2) glds16(wp, wvo[i], wdst + so + wm * 2048 + i * 1024);
    else glds16_if0(wm, wp, wvo[2], wdst + so + 8192);
  };
  bf16x8 A[6][2];
  auto issue_a = [&](int s, bf16x8& dst, int m) {
    asm volatile("global_load_dwordx4 %0, %1, %2" : "=v"(dst) : "v"(m ? avo2 : avo), "s"(asrc + s * 1024) : "memory");
  };
  f32x4 sp0, sp1, sp2, sp3;
  f32x2 sp4;
  asm volatile("global_load_dwordx4 %0, %5, %6\n\tglobal_load_dwordx4 %1, %5, %6 offset:16\n\tglobal_load_dwordx4 %2, %5, %6 offset:32\n\t"
               "global_load_dwordx4 %3, %5, %6 offset:48\n\tglobal_load_dwordx2 %4, %5, %6 offset:64"
               : "=&v"(sp0), "=&v"(sp1), "=&v"(sp2), "=&v"(sp3), "=&v"(sp4)
               : "v"((lane & 31) * 80), "s"(ws + WS_ST + (size_t)(pn * 256 + wid * 32) * 80) : "memory");
  if (wm < 3 && lane < 36) glds16(ws + WS_CSP + (size_t)(wm * 9216 + wn * FF + jt * 144) * 4, lane * 16, lds0 + K4_CP + wm * 1152 + wn * 576);
  for_seq<2>([&](auto S) { issue_w(S, 0); issue_w(S, 1); issue_w(S, 2); issue_a(S, A[S][0], 0); issue_a(S, A[S][1], 1); });
  {
    asm volatile("s_waitcnt vmcnt(8)" : "+v"(sp0), "+v"(sp1), "+v"(sp2), "+v"(sp3), "+v"(sp4)::"memory");
    const float s1 = sp0[0] + sp0[2] + sp1[0] + sp1[2] + sp2[0] + sp2[2] + sp3[0] + sp3[2] + sp4[0];
    const float s2 = sp0[1] + sp0[3] + sp1[1] + sp1[3] + sp2[1] + sp2[3] + sp3[1] + sp3[3] + sp4[1];
    const float mean = s1 * (1.f / 576.f);
    const float var = fmaxf(s2 * (1.f / 576.f) - mean * mean, 0.f);
    *(LDS_AS f32x2*)(lds + K4_ST + (wid * 32 + (lane & 31)) * 8) = f32x2{-mean, __builtin_amdgcn_rsqf(var + LN_EPS)};
    *(LDS_AS unsigned*)(lds + K4_SY) = 0u;
    SB();
  }
  const int g = lane >> 4, q = (lane & 15) >> 2, p = lane & 3, r = lane & 15;
  lds_ptr wb = lds + (16 * (g >> 1) + 4 * (g & 1) + q) * 288 + p * 8;
  lds_ptr wbs[3] = {wb, wb + 3 * K4_SLOT, wb + 6 * K4_SLOT};
  asm volatile("" : "+v"(wbs[1]), "+v"(wbs[2]));
  lds_ptr csb = lds + K4_CS + 16 * g;
  asm volatile("" : "+v"(csb));
  f32x4 acc[2][18];
  asm volatile("s_waitcnt vmcnt(%2)\n\ts_barrier" : "+v"(A[0][0]), "+v"(A[0][1]) : "n"(k4_allow(0, 0)) : "memory");
  STAMP_MARK(2, 2);
  STAMP_CYC0
  s16x4 Bl[6], Bh[6];
  for_seq<5>([&](auto I) { Bl[I] = lds_tr(wb + k4_foff(k4_frag(I))); Bh[I] = lds_tr(wb + k4_foff(k4_frag(I)) + 2304); });
  SB();
  const float L2E = 1.4426950408889634f;
  char* hb = ws + WS_HID + (size_t)(pn * 16 + wid * 2) * (144 * 1024) + (size_t)(jt >> 2) * 18432;
  f32x2 st[2];
  float c1[2];
  float sd[2];
  auto glu = [&](float ag, float au, float cg, float cu, int m) -> float {
    const float tg = fmaf(st[m][0], cg, ag), tu = fmaf(st[m][0], cu, au);
    const float e = __builtin_amdgcn_exp2f(tg * c1[m]);
    return tu * __builtin_amdgcn_rcpf(fmaf(e, sd[m], sd[m]));
  };
  f32x4 cg[2], cu[2];
  u32x4 own[4][2];
  auto slice = [&](auto QQ, auto KK) {
    constexpr int Q = QQ, k = KK, m = k >> 2, d = k & 3, h = d >> 1, e = (d & 1) * 2;
    const f32x4 ag = acc[m][2 * Q + h], au = acc[m][9 + 2 * Q + h];
    own[Q][m][d] = cvt_pk(glu(ag[e], au[e], cg[h][e], cu[h][e], m), glu(ag[e + 1], au[e + 1], cg[h][e + 1], cu[h][e + 1], m));
    if constexpr (d == 3) st16(hb + (size_t)m * (144 * 1024) + (4 * (jt & 3) + Q) * 1024 + lane * 16, own[Q][m]);
    else asm volatile("" : "+v"(own[Q][m]));
  };
  float eg[2][2], eu[2][2];
  auto micro = [&](auto QQ, auto KK, auto JJ) {
    constexpr int Q = QQ, k = KK, j = JJ, m = k >> 2, d = k & 3, h = d >> 1, e = (d & 1) * 2, b = k & 1;
#pragma unroll
    for (int x = 0; x < 2; ++x) {
      if constexpr (j == 0) eg[b][x] = fmaf(st[m][0], cg[h][e + x], acc[m][2 * Q + h][e + x]);
      if constexpr (j == 1) eg[b][x] *= c1[m];
      if constexpr (j == 2) eg[b][x] = __builtin_amdgcn_exp2f(eg[b][x]);
      if constexpr (j == 3) eu[b][x] = fmaf(st[m][0], cu[h][e + x], acc[m][9 + 2 * Q + h][e + x]);
      if constexpr (j == 4) eg[b][x] = fmaf(eg[b][x], sd[m], sd[m]);
      if constexpr (j == 5) eg[b][x] = __builtin_amdgcn_rcpf(eg[b][x]);
      if constexpr (j == 6) eu[b][x] *= eg[b][x];
      if constexpr (j == 3 || j == 6) pin(eu[b][x]); else if constexpr (j < 7) pin(eg[b][x]);
    }
    if constexpr (j == 7) {
      own[Q][m][d] = cvt_pk(eu[b][0], eu[b][1]);
      if constexpr (d == 3) st16(hb + (size_t)m * (144 * 1024) + (4 * (jt & 3) + Q) * 1024 + lane * 16, own[Q][m]);
      else pin(own[Q][m]);
    }
  };
  auto gapops = [&](auto QQ, auto GG) {
    constexpr int gg = GG;
    if constexpr (gg >= 4 && gg < 36) micro(QQ, std::integral_constant<int, gg / 4 - 1>{}, std::integral_constant<int, 4 + gg % 4>{});
    if constexpr (gg >= 0 && gg < 32) micro(QQ, std::integral_constant<int, gg / 4>{}, std::integral_constant<int, gg % 4>{});
    SB();
  };
  unsigned dvo[2], dvl[2];
  const char* dsrc = wd + (size_t)(jt >> 2) * 576 * 1152 + (jt & 3) * 288;
  auto dvoff = [&]() {
    int ln = tid;
    asm volatile("" : "+v"(ln));
    ln &= 63;
#pragma unroll
    for (int i = 0; i < 2; ++i) {
      const int o = (i ? 8 : wid) * 1024 + ln * 16;
      const int rho = o / 288, u = (o - rho * 288) >> 4;
      const int gk = ((rho >> 4) & 1) * 2 + ((rho >> 2) & 1), tk = (rho >> 3) & 1, qk = rho & 3;
      dvo[i] = (16 * tk + 4 * gk + qk) * 1152 + u * 16;
      dvl[i] = (144 * (gk >> 1) + 128 + 8 * (gk & 1) + 4 * tk + qk) * 1152 + u * 16;
    }
  };
  auto issue_d = [&](auto II, int pc) {
    constexpr int i = II;
    const int k = (4 * (jt & 3) + i) & 15;
    const char* src = dsrc + (size_t)(i < 16 ? 144 * (k >> 2) + 32 * (k & 3) : 288 * (i - 16)) * 1152;
    if (pc == 0) glds16(src, i < 16 ? dvo[0] : dvl[0], lds0 + d_off(i) + wid * 1024);
    else glds16_if0(wid ^ (i & 7), src, i < 16 ? dvo[1] : dvl[1], lds0 + d_off(i) + 8192);
  };
  for_seq<18>([&](auto GH) { for_seq<18>([&](auto GL) {
    constexpr int G = GH * 18 + GL, sl = k4_slab(G), f = k4_frag(k4_pos(G)), rb = G % 6, nb = (G + 5) % 6, NG = G + 5;
    const bf16x8 b = cat8(Bl[rb], Bh[rb]);
    constexpr int need = k4_need(G);
    if constexpr (need != 0) {
      asm volatile("s_waitcnt vmcnt(%2)\n\ts_barrier" : "+v"(A[need % 6][0]), "+v"(A[need % 6][1]) : "n"(k4_allow(G, need)) : "memory");
      SB();
    }
    if constexpr (sl == 0) mfma16_zero(acc[0][f], b, A[0][0]); else mfma16_acc(acc[0][f], b, A[sl % 6][0]);
    SB();
    if constexpr (NG < 324) { Bl[nb] = lds_tr(wbs[(k4_slab(NG) & 7) / 3] + ((k4_slab(NG) & 7) % 3) * K4_SLOT + k4_foff(k4_frag(k4_pos(NG)))); SB(); }
    if constexpr (G >= K4_T0 + 20 && G < K4_T0 + 80 && (G - K4_T0) % 20 >= 1 && (G - K4_T0) % 20 < 19)
      gapops(std::integral_constant<int, (G - K4_T0) / 20 - 1>{}, std::integral_constant<int, 2 * ((G - K4_T0) % 20 - 1)>{});
    if constexpr (G >= K4_T0 + 81) {
      gapops(std::integral_constant<int, 3>{}, std::integral_constant<int, 4 * (G - K4_T0 - 81)>{});
      gapops(std::integral_constant<int, 3>{}, std::integral_constant<int, 4 * (G - K4_T0 - 81) + 1>{});
    }
    if constexpr (sl == 0) mfma16_zero(acc[1][f], b, A[0][1]); else mfma16_acc(acc[1][f], b, A[sl % 6][1]);
    SB();
    if constexpr (NG < 324) { Bh[nb] = lds_tr(wbs[(k4_slab(NG) & 7) / 3] + ((k4_slab(NG) & 7) % 3) * K4_SLOT + k4_foff(k4_frag(k4_pos(NG))) + 2304); SB(); }
    constexpr int op = k4_op(G);
    if constexpr (op != 0) {
      constexpr int os = (op - 1) >> 3, kind = (op - 1) & 7;
      if constexpr (kind < 2) issue_a(os, A[os % 6][kind], kind);
      else issue_w(os, kind - 2);
      SB();
    }
    if constexpr (G % 18 == 10 && (G / 18 == 5 || G / 18 == 7 || G / 18 == 9)) {
      constexpr int k = (G / 18 - 5) / 2;
      touch_if0((int)(blockIdx.x / 216) + (wid ^ (k + 1)), wd + (size_t)(blockIdx.x * 3 + k) * 8192, avo * 8, lds0 + K4_PF + k * 256);
      SB();
    }
    if constexpr (G == 60) {
      lds_ptr cpp = lds + K4_CP + wid * 144 + lane * 4;
      const float t = *(LDS_AS float*)(cpp) + *(LDS_AS float*)(cpp + 1152) + *(LDS_AS float*)(cpp + 2304);
      *(LDS_AS float*)(lds + K4_CS + wid * 144 + min(lane, 36) * 4 + (lane >= 36 ? 1152 : 0)) = t;
      asm volatile("s_waitcnt lgkmcnt(0)" ::: "memory");
      SB();
    }
    if constexpr (G == K4_T0) {
      STAMP_MARK(2, 3);
      STAMP_CYC(2, 4);
#pragma unroll
      for (int m = 0; m < 2; ++m) { st[m] = *(LDS_AS f32x2*)(lds + K4_ST + (wid * 32 + m * 16 + r) * 8); c1[m] = -L2E * st[m][1]; sd[m] = __builtin_amdgcn_rcpf(st[m][1]); }
      SB();
    }
    if constexpr (G >= K4_T0) {
      constexpr int t = G - K4_T0, u = t % 20;
      if constexpr (t == 11) { dvoff(); SB(); }
      if constexpr (t >= 12 && t < 24) { issue_d(std::integral_constant<int, (t - 12) / 2>{}, t & 1); SB(); }
      if constexpr (t >= 20 && t < 80 && u >= 1 && u < 19) gapops(std::integral_constant<int, t / 20 - 1>{}, std::integral_constant<int, 2 * (u - 1) + 1>{});
      if constexpr (t >= 81) {
        gapops(std::integral_constant<int, 3>{}, std::integral_constant<int, 4 * (t - 81) + 2>{});
        gapops(std::integral_constant<int, 3>{}, std::integral_constant<int, 4 * (t - 81) + 3>{});
      }
      if constexpr (t < 80 && u == 18) {
#pragma unroll
        for (int h = 0; h < 2; ++h) {
          cg[h] = *(LDS_AS f32x4*)(csb + (2 * (t / 20) + h) * 64);
          cu[h] = *(LDS_AS f32x4*)(csb + 576 + (2 * (t / 20) + h) * 64);
        }
        SB();
      }
    }
  }); });
  {
    asm volatile("s_nop 7\n\ts_nop 1" : "+v"(acc[0][8]), "+v"(acc[1][8]), "+v"(acc[0][17]), "+v"(acc[1][17]));
    const f32x4 cg8 = *(LDS_AS f32x4*)(csb + 512);
    const f32x4 cu8 = *(LDS_AS f32x4*)(csb + 576 + 512);
#pragma unroll
    for (int m = 0; m < 2; ++m) {
      const f32x4 ag = acc[m][8], au = acc[m][17];
      u32x2 o = {cvt_pk(glu(ag[0], au[0], cg8[0], cu8[0], m), glu(ag[1], au[1], cg8[1], cu8[1], m)),
                 cvt_pk(glu(ag[2], au[2], cg8[2], cu8[2], m), glu(ag[3], au[3], cg8[3], cu8[3], m))};
      st8wt(hb + (size_t)m * (144 * 1024) + (16 + ((jt >> 1) & 1)) * 1024 + (2 * (jt & 1) + (g >> 1)) * 256 + r * 16 + (g & 1) * 8, o);
    }
  }
  STAMP_MARK(2, 5);
  const int sb = jt >> 2, nq = jt & 3;
  unsigned* cntA = (unsigned*)(ws + WS_CNT + 1024) + (pn * 8 + sb) * 32;
  unsigned* cntB = (unsigned*)(ws + WS_CNT + 1024 + 8192) + (pn * 4 + nq) * 32;
  wait_bar<10>();
  auto dptr = [&](auto HH) -> lds_ptr {
    constexpr int H = HH, o = d_off(d_i(H)) + d_f(H) * 32;
    return wbs[o / (3 * K4_SLOT)] + o % (3 * K4_SLOT);
  };
  for_seq<5>([&](auto I) { Bl[I] = lds_tr(dptr(I)); Bh[I] = lds_tr(dptr(I) + 2304); });
  SB();
  f32x4 dacc[2][9];
  bf16x8 DA[14][2];
  char* pb = ws + WS_PART + (size_t)(((pn * 4 + nq) * 8 + wid) * 8 + sb) * 9216 + lane * 16;
  for_seq<18>([&](auto HH) { for_seq<9>([&](auto HL) {
    constexpr int H = HH * 9 + HL, i = d_i(H), f = d_f(H), rb = H % 6, nb = (H + 5) % 6, NH = H + 5;
    if constexpr (H == 36) {
      if (tid == 0) poll_count(cntA, 4u);
      SB();
      wait_bar<0>();
      STAMP_MARK(2, 6);
      issue_d(std::integral_constant<int, 17>{}, 0); issue_d(std::integral_constant<int, 17>{}, 1);
      const unsigned ho = lane * 16, ho2 = lane * 16 + 144 * 1024;
      for_seq<14>([&](auto JJ) {
        constexpr int j = JJ;
        const int k = j < 12 ? ((4 * nq + 4 + j) & 15) : 4 + j;
        ld2_sc1(DA[j][0], DA[j][1], ho, ho2, hb + k * 1024);
      });
      SB();
    }
    if constexpr (i >= 4 && d_first(H)) {
      wait_vm2<2 * (17 - i)>(DA[i - 4][0], DA[i - 4][1]);
      SB();
    }
    if constexpr (H == D_G0) { raw_bar(); SB(); }
    const bf16x8 b = cat8(Bl[rb], Bh[rb]);
    bf16x8 a0, a1;
    if constexpr (i < 4) { a0 = __builtin_bit_cast(bf16x8, own[i][0]); a1 = __builtin_bit_cast(bf16x8, own[i][1]); }
    else { a0 = DA[i - 4][0]; a1 = DA[i - 4][1]; }
    if constexpr (i == 0) mfma16_zero(dacc[0][f], b, a0); else mfma16_acc(dacc[0][f], b, a0);
    SB();
    if constexpr (NH < 162) { Bl[nb] = lds_tr(dptr(std::integral_constant<int, NH>{})); SB(); }
    if constexpr (i == 0) mfma16_zero(dacc[1][f], b, a1); else mfma16_acc(dacc[1][f], b, a1);
    SB();
    if constexpr (NH < 162) { Bh[nb] = lds_tr(dptr(std::integral_constant<int, NH>{}) + 2304); SB(); }
    if constexpr (H < 22) { issue_d(std::integral_constant<int, 6 + H / 2>{}, H & 1); SB(); }
    if constexpr (H == 24) {
      wait_vm<11>();
      if (lds_arrive(lds + K4_SY, lane) == 7 && lane == 0) __hip_atomic_fetch_add(cntA, 1u, __ATOMIC_RELAXED, __HIP_MEMORY_SCOPE_AGENT);
      SB();
    }
    if constexpr (H >= D_G0 + 2 * DT && (((H - D_G0) % (2 * DT) == 2 || (H - D_G0) % (2 * DT) == 3))) {
      constexpr int pr = (H - D_G0) / (2 * DT) - 1, m = ((H - D_G0) % (2 * DT) == 3);
      const f32x4 x0 = dacc[m][2 * pr], x1 = dacc[m][2 * pr + 1];
      st16(pb + (m * 4 + pr) * 1024, u32x4{cvt_pk(x0[0], x0[1]), cvt_pk(x0[2], x0[3]), cvt_pk(x1[0], x1[1]), cvt_pk(x1[2], x1[3])});
      SB();
    }
  }); });
  {
    asm volatile("s_nop 7\n\ts_nop 1" : "+v"(dacc[0][8]), "+v"(dacc[1][8]));
    const f32x4 x0 = dacc[0][8], x1 = dacc[1][8];
    st16(pb + 8192, u32x4{cvt_pk(x0[0], x0[1]), cvt_pk(x0[2], x0[3]), cvt_pk(x1[0], x1[1]), cvt_pk(x1[2], x1[3])});
  }
  wait_bar<0>();
  STAMP_MARK(2, 7);
  if (tid == 0) __hip_atomic_fetch_add(cntB, 1u, __ATOMIC_RELAXED, __HIP_MEMORY_SCOPE_AGENT);
  {
    const int nc = wid == 1 ? 2 : 1;
    const int rbase = pn * 256 + sb * 32;
    int row[2][2], col[2][2];
    row[0][0] = row[0][1] = rbase + (wid >> 2) * 16 + r; col[0][0] = nq * 144 + (wid & 3) * 32 + 4 * g; col[0][1] = col[0][0] + 16;
    row[1][0] = rbase + r; row[1][1] = rbase + 16 + r; col[1][0] = col[1][1] = nq * 144 + 128 + 4 * g;
    u32x2 res[2][2];
#pragma unroll
    for (int c = 0; c < 2; ++c)
#pragma unroll
      for (int e = 0; e < 2; ++e)
        res[c][e] = *(const u32x2*)(ws + WS_H + (size_t)(row[c][e] >> 4) * 18432 + (col[c][e] >> 5) * 1024 + ((col[c][e] >> 3) & 3) * 256 + (row[c][e] & 15) * 16 + ((col[c][e] >> 2) & 1) * 8);
    if (tid == 0) poll_count(cntB, 8u);
    SB();
    raw_bar();
    SB();
    const __amdgpu_buffer_rsrc_t prs = __builtin_amdgcn_make_buffer_rsrc((void*)(ws + WS_PART + (size_t)((pn * 4 + nq) * 8 + sb) * 8 * 9216), 0, 8 * 9216, 0x00020000);
#pragma unroll
    for (int c = 0; c < 2; ++c) {
      if (c < nc) {
        u32x4 pv[8];
#pragma unroll
        for (int k = 0; k < 8; ++k) pv[k] = __builtin_amdgcn_raw_buffer_load_b128(prs, lane * 16 + (c ? 8192 : wid * 1024), k * 9216, 16);
        float sum[8];
#pragma unroll
        for (int e = 0; e < 4; ++e) { sum[2 * e] = bf_lo(res[c][e >> 1][e & 1]); sum[2 * e + 1] = bf_hi(res[c][e >> 1][e & 1]); }
        float acc2[8];
#pragma unroll
        for (int e = 0; e < 8; ++e) acc2[e] = 0.f;
#pragma unroll
        for (int k = 0; k < 8; ++k)
#pragma unroll
          for (int e = 0; e < 4; ++e) { acc2[2 * e] += bf_lo(pv[k][e]); acc2[2 * e + 1] += bf_hi(pv[k][e]); }
#pragma unroll
        for (int e = 0; e < 2; ++e)
          *(u32x2*)(out + (size_t)row[c][e] * 1152 + col[c][e] * 2) =
              u32x2{cvt_pk(sum[4 * e] + acc2[4 * e], sum[4 * e + 1] + acc2[4 * e + 1]), cvt_pk(sum[4 * e + 2] + acc2[4 * e + 2], sum[4 * e + 3] + acc2[4 * e + 3])};
      }
    }
  }
  STAMP_END(2);
}

extern "C" void kernel_launch(void* const* d_in, const int* in_sizes, int n_in, void* d_out, int out_size, void* d_ws, size_t ws_size,
                              hipStream_t stream) {
  const char* x = (const char*)d_in[0];
  const char* wqkv = (const char*)d_in[1];
  const char* wo = (const char*)d_in[2];
  const char* wg = (const char*)d_in[4];
  const char* wu = (const char*)d_in[5];
  const char* wd = (const char*)d_in[6];
  char* ws = (char*)d_ws + WS_BASE(ws_size);
  (void)hipFuncSetAttribute((const void*)k1_ln_qkv, hipFuncAttributeMaxDynamicSharedMemorySize, K1_LDS);
  (void)hipFuncSetAttribute((const void*)k2_attn, hipFuncAttributeMaxDynamicSharedMemorySize, K2_LDS);
  (void)hipFuncSetAttribute((const void*)k4_gateup, hipFuncAttributeMaxDynamicSharedMemorySize, K4_LDS);
  k1_ln_qkv<<<234, 1024, K1_LDS, stream>>>(x, wqkv, ws, wo);
  k2_attn<<<256, 768, K2_LDS, stream>>>(ws, wg, wu, wo, x);
  k4_gateup<<<256, 512, K4_LDS, stream>>>(wg, wu, ws, wd, (char*)d_out);
}
```

```cpp
#include <hip/hip_runtime.h>
#include <hip/hip_bf16.h>
#include <stdint.h>
#include <utility>
#include <type_traits>

typedef __attribute__((ext_vector_type(8))) short bf16x8;
typedef __attribute__((ext_vector_type(4))) short s16x4;
typedef __attribute__((ext_vector_type(4))) float f32x4;
typedef __attribute__((ext_vector_type(16))) float f32x16;
typedef __attribute__((ext_vector_type(4))) unsigned u32x4;
typedef __attribute__((ext_vector_type(2))) unsigned u32x2;
typedef __attribute__((ext_vector_type(2))) __bf16 bf16x2_t;
typedef __attribute__((ext_vector_type(2))) float f32x2;
#define LDS_AS __attribute__((address_space(3)))
typedef LDS_AS char* lds_ptr;
#define DI __device__ __forceinline__

constexpr int SEQ = 2048, DM = 576, NH = 32, FF = 4608;
constexpr float LN_EPS = 1e-5f;
constexpr float QSCALE = 0.23570226039551584f * 1.4426950408889634f;

constexpr size_t WS_Q = 0;
constexpr size_t WS_K = WS_Q + (size_t)72 * 32768;
constexpr size_t WS_V = WS_K + (size_t)72 * 32768;
constexpr size_t WS_ONES = WS_V + (size_t)256 * 9216;
constexpr size_t WS_O = WS_ONES + 1024;
constexpr size_t WS_H = WS_O + (size_t)128 * 18 * 1024;
constexpr size_t WS_ST = WS_H + (size_t)128 * 18 * 1024;
constexpr size_t WS_CSP = WS_ST + (size_t)2048 * 80;
constexpr size_t WS_CS = WS_CSP + (size_t)8 * 9216 * 4;
constexpr size_t WS_HID = WS_CS + (size_t)9216 * 4;
constexpr size_t WS_CNT = WS_HID + (size_t)128 * 144 * 1024;
constexpr size_t WS_PART = WS_CNT + 24576;
constexpr size_t WS_END = WS_PART + (size_t)2048 * 9216;
static_assert(WS_O % 1024 == 0 && WS_H % 1024 == 0 && WS_ST % 256 == 0 && WS_CSP % 256 == 0 && WS_HID % 256 == 0, "align");

#define WS_BASE(sz) (((sz) - WS_END) & ~(size_t)0x1fffff)
#define STAMP_BEGIN
#define STAMP_MARK(k, i)
#define STAMP_MARKW(k, i, T)
#define STAMP_ENDW(k, T)
#define STAMP_CYC0
#define STAMP_CYC(k, i)
#define STAMP_END(k)

DI unsigned cvt_pk(float lo, float hi) {
  f32x2 v = {lo, hi};
  bf16x2_t r = __builtin_convertvector(v, bf16x2_t);
  return __builtin_bit_cast(unsigned, r);
}
DI float bf_lo(unsigned u) { return __builtin_bit_cast(float, u << 16); }
DI float bf_hi(unsigned u) { return __builtin_bit_cast(float, u & 0xffff0000u); }
DI float dot2(unsigned a, unsigned b, float c) {
  return __builtin_amdgcn_fdot2_f32_bf16(__builtin_bit_cast(bf16x2_t, a), __builtin_bit_cast(bf16x2_t, b), c, false);
}
template <int CTRL> DI float dpp_add(float v) {
  int t = __builtin_amdgcn_update_dpp(0, __builtin_bit_cast(int, v), CTRL, 0xf, 0xf, true);
  return v + __builtin_bit_cast(float, t);
}
DI float sum8(float v) {
  v = dpp_add<0xB1>(v);
  v = dpp_add<0x4E>(v);
  v = dpp_add<0x141>(v);
  return v;
}
#define GNOP "3"
DI void glds16(const void* sbase, unsigned voff, unsigned lds_dst) {
  asm volatile("s_mov_b32 m0, %2\n\ts_nop " GNOP "\n\tglobal_load_lds_dwordx4 %0, %1" ::"v"(voff), "s"(sbase), "s"(lds_dst) : "memory");
}
#define ST_AUX " sc1"
DI void st16(void* p, u32x4 v) { asm volatile("global_store_dwordx4 %0, %1, off" ST_AUX "\n\ts_nop 1" ::"v"(p), "v"(v) : "memory"); }
DI void st8(void* p, u32x2 v) { *(u32x2*)p = v; }
DI void st8wt(void* p, u32x2 v) { asm volatile("global_store_dwordx2 %0, %1, off sc1" ::"v"(p), "v"(v) : "memory"); }
DI void st4(void* p, unsigned v) { asm volatile("global_store_dword %0, %1, off sc1" ::"v"(p), "v"(v) : "memory"); }
DI void glds16_if0(int c, const void* sbase, unsigned voff, unsigned lds_dst) {
  asm volatile("s_cmp_lg_u32 %3, 0\n\ts_cbranch_scc1 1f\n\ts_mov_b32 m0, %2\n\ts_nop 1\n\tglobal_load_lds_dwordx4 %0, %1\n1:"
               ::"v"(voff), "s"(sbase), "s"(lds_dst), "s"(c) : "memory", "scc");
}
DI void touch_if0(int c, const void* sbase, unsigned voff, unsigned lds_dst) {
  asm volatile("s_cmp_lg_u32 %3, 0\n\ts_cbranch_scc1 1f\n\ts_mov_b32 m0, %2\n\ts_nop 1\n\tglobal_load_lds_dword %0, %1\n1:"
               ::"v"(voff), "s"(sbase), "s"(lds_dst), "s"(c) : "memory", "scc");
}
DI void touch_lines(const char* base, unsigned voff, unsigned lds_dummy) {
  asm volatile("s_mov_b32 m0, %2\n\ts_nop 3\n\tglobal_load_lds_dword %0, %1" ::"v"(voff), "s"(base), "s"(lds_dummy) : "memory");
}
#define CODE_TOUCH(SYM, BYTES, LDS_DUMMY)                                                                          \
  do {                                                                                                             \
    const char* cp_;                                                                                               \
    asm volatile("s_getpc_b64 s[20:21]\n\ts_add_u32 s20, s20, " SYM "@rel32@lo+4\n\ts_addc_u32 s21, s21, " SYM     \
                 "@rel32@hi+12\n\ts_mov_b64 %0, s[20:21]" : "=s"(cp_)::"s20", "s21", "scc");                      \
    constexpr int nl_ = (BYTES) / 128, per_ = (nl_ + 28) / 29;                                                      \
    const int ln_ = (blockIdx.x >> 3) * per_ + lane;                                                               \
    if (lane < per_ && ln_ < nl_) touch_lines(cp_, ln_ * 128, LDS_DUMMY);                                          \
  } while (0)
#define SYM_K1 "_Z9k1_ln_qkvPKcS0_PcS0_"
#define SYM_K2 "_Z7k2_attnPcPKcS1_S1_S1_"
#define SYM_K4 "_Z9k4_gateupPKcS0_PcS0_S1_"
constexpr int LEN_K1 = 21800, LEN_K2 = 12400, LEN_K4 = 30000;
template <int N> DI void wait_bar() { asm volatile("s_waitcnt vmcnt(%0) lgkmcnt(0)\n\ts_barrier" ::"n"(N) : "memory"); }
template <int N> DI void wait_vm() { asm volatile("s_waitcnt vmcnt(%0)" ::"n"(N) : "memory"); }
DI unsigned lds_arrive(lds_ptr c, int lane) {
  unsigned old = 0;
  if (lane == 0) old = __hip_atomic_fetch_add((LDS_AS unsigned*)c, 1u, __ATOMIC_RELAXED, __HIP_MEMORY_SCOPE_WORKGROUP);
  return __builtin_amdgcn_readfirstlane(old);
}
DI void lds_sync(lds_ptr c, unsigned n, int lane) {
  asm volatile("" ::: "memory");
  if (lds_arrive(c, lane) + 1 != n) {
    for (int spin = 0; spin < (1 << 20); ++spin) {
      const unsigned v = __builtin_amdgcn_readfirstlane(__hip_atomic_load((LDS_AS unsigned*)c, __ATOMIC_RELAXED, __HIP_MEMORY_SCOPE_WORKGROUP));
      if (v >= n) break;
      __builtin_amdgcn_s_sleep(1);
    }
  }
  asm volatile("" ::: "memory");
}
DI void lds_post(lds_ptr c, int lane) {
  asm volatile("" ::: "memory");
  if (lane == 0) (void)__hip_atomic_fetch_add((LDS_AS unsigned*)c, 1u, __ATOMIC_RELAXED, __HIP_MEMORY_SCOPE_WORKGROUP);
}
DI void lds_wait(lds_ptr c, unsigned n) {
  for (int spin = 0; spin < (1 << 20); ++spin) {
    const unsigned v = __builtin_amdgcn_readfirstlane(__hip_atomic_load((LDS_AS unsigned*)c, __ATOMIC_RELAXED, __HIP_MEMORY_SCOPE_WORKGROUP));
    if (v >= n) break;
    __builtin_amdgcn_s_sleep(1);
  }
  asm volatile("" ::: "memory");
}
template <class T> DI void pin(T& v) { asm volatile("" : "+v"(v)); }
DI void raw_bar() { asm volatile("s_barrier" ::: "memory"); }
DI void lds_bar() { asm volatile("s_waitcnt lgkmcnt(0)\n\ts_barrier" ::: "memory"); }
DI bf16x8 lds_r128(lds_ptr p) { return *(LDS_AS const bf16x8*)p; }
typedef short v4i16_t __attribute__((ext_vector_type(4)));
DI s16x4 lds_tr(lds_ptr p) { return __builtin_amdgcn_ds_read_tr16_b64_v4i16((LDS_AS v4i16_t*)p); }
DI bf16x8 cat8(s16x4 a, s16x4 b) { return __builtin_shufflevector(a, b, 0, 1, 2, 3, 4, 5, 6, 7); }
#define SB() __builtin_amdgcn_sched_barrier(0)
#define MFMA16(a, b, c) __builtin_amdgcn_mfma_f32_16x16x32_bf16(a, b, c, 0, 0, 0)
#define MFMA32(a, b, c) __builtin_amdgcn_mfma_f32_32x32x16_bf16(a, b, c, 0, 0, 0)
DI void mfma16_acc(f32x4& c, bf16x8 a, bf16x8 b) { asm volatile("v_mfma_f32_16x16x32_bf16 %0, %1, %2, %0" : "+v"(c) : "v"(a), "v"(b)); }
DI void mfma16_zero(f32x4& c, bf16x8 a, bf16x8 b) { asm volatile("v_mfma_f32_16x16x32_bf16 %0, %1, %2, 0" : "=&v"(c) : "v"(a), "v"(b)); }
template <class F, int... I> DI void for_seq_impl(F&& f, std::integer_sequence<int, I...>) { (f(std::integral_constant<int, I>{}), ...); }
template <int N, class F> DI void for_seq(F&& f) { for_seq_impl(f, std::make_integer_sequence<int, N>{}); }
DI int xcd_remap(int b, int nwg) {
  int q = nwg >> 3, r = nwg & 7, x = b & 7, o = b >> 3;
  return (x < r ? x * (q + 1) : r * (q + 1) + (x - r) * q) + o;
}

constexpr int K1_X = 0, K1_W = 40960, K1_SLOT = 12288, K1_ST = K1_W + K1_SLOT * 9, K1_CS = K1_W, K1_PF = K1_ST + 1024, K1_LDS = K1_PF + 1024;
constexpr int cmin(int a, int b) { return a < b ? a : b; }
constexpr int k1_nw(int kk) { return cmin(18, cmin(3 + 2 * kk, kk + 7)); }
constexpr int k1_nx(int kk) { return cmin(18, cmin(3 + 2 * kk, kk + 5)); }

template <bool ISV, bool HI>
DI void k1_consumer(lds_ptr lds, int cb, int lane, char* ws, int ct, int m0) {
  const int g = lane >> 4, q = (lane & 15) >> 2, p = lane & 3, r = lane & 15;
  constexpr int NM = HI ? 2 : 3, MB = HI ? 3 : 0;
  lds_ptr a0 = lds + K1_X + MB * 2048 + (r >> 3) * 1024 + (r & 7) * 128 + ((g ^ (r >> 1)) * 16);
  lds_ptr a1 = lds + K1_X + MB * 2048 + (r >> 3) * 1024 + (r & 7) * 128 + (((4 + g) ^ (r >> 1)) * 16);
  const int rho0 = 16 * (g >> 1) + 4 * (g & 1) + q;
  lds_ptr wb = lds + K1_W + rho0 * 128 + ((cb ^ ((rho0 >> 1) & 3)) * 32) + p * 8;
  lds_ptr wb5 = wb + 5 * K1_SLOT;
  asm volatile("" : "+v"(wb), "+v"(wb5));
  const u32x4 onesu = {0x3f803f80u, 0x3f803f80u, 0x3f803f80u, 0x3f803f80u};
  const bf16x8 ones = __builtin_bit_cast(bf16x8, onesu);
  f32x4 acc[3][3];
#pragma unroll
  for (int m = 0; m < 3; ++m)
#pragma unroll
    for (int j = 0; j < 3; ++j) acc[m][j] = f32x4{0.f, 0.f, 0.f, 0.f};
  bf16x8 fA[2][3];
  s16x4 wl[2][3], wh[2][3];
  auto rd1 = [&](auto KK, auto II) {
    constexpr int kk = KK, i = II, bf = kk & 1, sl = kk % 9;
    if constexpr (i >= 2 && i <= 4) {
      if constexpr (i - 2 < NM) fA[bf][i - 2] = lds_r128(((kk & 1) ? a1 : a0) + ((kk >> 1) & 3) * 10240 + (i - 2) * 2048);
    } else {
      constexpr int j = i < 2 ? 0 : (i - 3) / 2, hh = i < 2 ? i : (i - 3) % 2;
      lds_ptr bp = (sl >= 5 ? wb5 + (sl - 5) * K1_SLOT : wb + sl * K1_SLOT) + j * 4096 + hh * 1024;
      if constexpr (hh == 0) wl[bf][j] = lds_tr(bp); else wh[bf][j] = lds_tr(bp);
    }
  };
  lds_bar();
  STAMP_MARKW(0, 2, 512);
  for_seq<9>([&](auto I) { rd1(std::integral_constant<int, 0>{}, I); });
  SB();
  for_seq<18>([&](auto KK) {
    constexpr int kk = KK, bf = kk & 1;
    if constexpr (kk < 17) { raw_bar(); SB(); }
    for_seq<9>([&](auto II) {
      constexpr int i = II, j = i / 3, m = i % 3;
      const bf16x8 wv = cat8(wl[bf][j], wh[bf][j]);
      const bf16x8 av = (m < NM) ? fA[bf][m] : ones;
      acc[m][j] = ISV ? MFMA16(av, wv, acc[m][j]) : MFMA16(wv, av, acc[m][j]);
      SB();
      if constexpr (kk < 17) { rd1(std::integral_constant<int, kk + 1>{}, II); SB(); }
    });
  });
  STAMP_MARKW(0, 4, 512);
  lds_ptr cx = lds + K1_CS + cb * 3072 + lane * 16;
  if (HI) {
#pragma unroll
    for (int j = 0; j < 3; ++j) *(LDS_AS f32x4*)(cx + j * 1024) = acc[2][j];
  }
  lds_bar();
  STAMP_MARKW(0, 5, 512);
  f32x4 cs[3];
#pragma unroll
  for (int j = 0; j < 3; ++j) cs[j] = HI ? acc[2][j] : *(LDS_AS f32x4*)(cx + j * 1024);
  u32x2 o[NM * 3];
  if (ISV) {
#pragma unroll
    for (int m = 0; m < NM; ++m) {
      const f32x4 s0 = *(LDS_AS f32x4*)(lds + K1_ST + ((MB + m) * 16 + 4 * g) * 8), s1 = *(LDS_AS f32x4*)(lds + K1_ST + ((MB + m) * 16 + 4 * g) * 8 + 16);
#pragma unroll
      for (int j = 0; j < 3; ++j) {
        const float c0 = cs[j][0];
        o[3 * m + j] = u32x2{cvt_pk(fmaf(s0[0], c0, acc[m][j][0]) * s0[1], fmaf(s0[2], c0, acc[m][j][1]) * s0[3]),
                             cvt_pk(fmaf(s1[0], c0, acc[m][j][2]) * s1[1], fmaf(s1[2], c0, acc[m][j][3]) * s1[3])};
      }
    }
  } else {
    const float sc = ct < 3 ? QSCALE : 1.f;
#pragma unroll
    for (int m = 0; m < NM; ++m) {
      f32x2 st = *(LDS_AS f32x2*)(lds + K1_ST + ((MB + m) * 16 + r) * 8);
      st[1] *= sc;
#pragma unroll
      for (int j = 0; j < 3; ++j)
        o[3 * m + j] = u32x2{cvt_pk(fmaf(st[0], cs[j][0], acc[m][j][0]) * st[1], fmaf(st[0], cs[j][1], acc[m][j][1]) * st[1]),
                             cvt_pk(fmaf(st[0], cs[j][2], acc[m][j][2]) * st[1], fmaf(st[0], cs[j][3], acc[m][j][3]) * st[1])};
    }
  }
  char* base;
  int sm, sj, sel, half;
  if (ISV) { base = ws + WS_V + (size_t)(((m0 >> 4) + MB) * 2 + (g & 1)) * 9216 + ((ct - 6) * 192 + cb * 16 + r) * 16; sm = 18432; sj = 1024; sel = g >> 1; half = g >> 1; }
  else { base = ws + WS_Q + (size_t)(ct * 24 + cb * 2 + (g >> 1)) * 32768 + (m0 + MB * 16 + r) * 16; sm = 256; sj = 8 * 32768; sel = g & 1; half = g & 1; }
#pragma unroll
  for (int n = 0; n + 1 < NM * 3; n += 2) {
    u32x4 c;
#pragma unroll
    for (int e = 0; e < 2; ++e) {
      auto rr = ISV ? __builtin_amdgcn_permlane32_swap(o[n][e], o[n + 1][e], false, false) : __builtin_amdgcn_permlane16_swap(o[n][e], o[n + 1][e], false, false);
      c[e] = rr[0]; c[2 + e] = rr[1];
    }
    const int o0 = (n / 3) * sm + (n % 3) * sj, o1 = ((n + 1) / 3) * sm + ((n + 1) % 3) * sj;
    st16(base + (sel ? o1 : o0), c);
  }
  if (NM == 3) st8wt(base + 2 * sm + 2 * sj + half * 8, o[8]);
}

__global__ __launch_bounds__(1024) void k1_ln_qkv(const char* __restrict__ x, const char* __restrict__ wqkv, char* __restrict__ ws, const char* __restrict__ wo) {
  extern __shared__ __attribute__((aligned(16))) char smem[];
  STAMP_BEGIN
  lds_ptr lds = (lds_ptr)smem;
  const unsigned lds0 = (unsigned)(uintptr_t)lds;
  const int tid = threadIdx.x, lane = tid & 63, wid = __builtin_amdgcn_readfirstlane(tid >> 6);
  const int tile = xcd_remap(blockIdx.x, 234);
  const int cgp = tile / 78, rem = tile - cgp * 78;
  const int mt = rem / 3, ct = cgp * 3 + (rem - mt * 3);
  const int m0 = min(mt * 80, SEQ - 80);
  if (wid < 3) {
    unsigned wv[4];
#pragma unroll
    for (int i = 0; i < 4; ++i) {
      const int rho = 8 * i + (lane >> 3);
      const int gk = ((rho >> 4) & 1) * 2 + ((rho >> 2) & 1), tk = (rho >> 3) & 1, qk = rho & 3;
      wv[i] = (8 * gk + 4 * tk + qk) * 3456 + (((lane & 7) ^ (((rho >> 1) & 3) << 1)) * 16);
    }
    const char* wsrc = wqkv + ct * 384 + wid * 128;
    const unsigned wdst = lds0 + K1_W + wid * 4096;
    for_seq<18>([&](auto KK) {
      constexpr int kk = KK;
#pragma unroll
      for (int n = (kk == 0 ? 0 : k1_nw(kk - 1)); n < k1_nw(kk); ++n)
#pragma unroll
        for (int i = 0; i < 4; ++i) glds16(wsrc + (size_t)n * 32 * 3456, wv[i], wdst + (n % 9) * K1_SLOT + i * 1024);
      wait_bar<(k1_nw(kk) - kk - 1) * 4>();
    });
    return;
  }
  if (wid == 3) {
    const unsigned v0 = (lane >> 3) * 1152 + (((lane & 7) ^ (lane >> 4)) * 16);
    const unsigned v1 = (lane >> 3) * 1152 + (((lane & 7) ^ (4 + (lane >> 4))) * 16);
    const char* xs = x + (size_t)m0 * 1152;
    for_seq<18>([&](auto KK) {
      constexpr int kk = KK;
#pragma unroll
      for (int h = (kk == 0 ? 0 : k1_nx(kk - 1)); h < k1_nx(kk); ++h)
#pragma unroll
        for (int i = 0; i < 5; ++i) {
          const int pc = (h & 1) * 5 + i;
          glds16(xs + pc * 9216 + (h >> 1) * 128, (pc & 1) ? v1 : v0, lds0 + K1_X + ((h >> 1) & 3) * 10240 + pc * 1024);
        }
      wait_bar<(k1_nx(kk) - 2 * (kk >> 1) - 2) * 5>();
    });
    return;
  }
  if (wid < 8) {
    const int j = wid - 4;
    if (j == 0) CODE_TOUCH(SYM_K1, LEN_K1, lds0 + K1_PF);
    if (j == 1) CODE_TOUCH(SYM_K2, LEN_K2, lds0 + K1_PF + 256);
    if (j == 1 && blockIdx.x == 0 && lane < 4) *(unsigned*)(ws + WS_ONES + lane * 4) = 0x3f803f80u;
    if (j >= 1 && blockIdx.x == 0) *(unsigned*)(ws + WS_CNT + ((j - 1) * 64 + lane) * 128) = 0u;
    {
      const int ln = blockIdx.x * 168 + j * 42 + lane;
      if (lane < 42) {
        if (ln < 15552) touch_lines(wqkv, ln * 128, lds0 + K1_PF + j * 256);
        else if (ln < 33984) touch_lines(x, (ln - 15552) * 128, lds0 + K1_PF + j * 256);
        else if (ln < 39168) touch_lines(wo, (ln - 33984) * 128, lds0 + K1_PF + j * 256);
      }
    }
    lds_ptr p0 = lds + K1_X + j * 1024 + lane * 16, p2 = lds + K1_X + min(j + 8, 9) * 1024 + lane * 16;
    float s1[3] = {0.f, 0.f, 0.f}, s2[3] = {0.f, 0.f, 0.f};
#pragma unroll 1
    for (int s = 0; s < 9; ++s) {
      raw_bar();
      const int so = (s & 3) * 10240;
      u32x4 v[3] = {*(LDS_AS u32x4*)(p0 + so), *(LDS_AS u32x4*)(p0 + so + 4096), *(LDS_AS u32x4*)(p2 + so)};
      SB();
      raw_bar();
      SB();
#pragma unroll
      for (int i = 0; i < 3; ++i)
#pragma unroll
        for (int e = 0; e < 4; ++e) { s1[i] = dot2(v[i][e], 0x3f803f80u, s1[i]); s2[i] = dot2(v[i][e], v[i][e], s2[i]); }
      asm volatile("" : "+v"(s1[0]), "+v"(s1[1]), "+v"(s1[2]), "+v"(s2[0]), "+v"(s2[1]), "+v"(s2[2]));
    }
#pragma unroll
    for (int i = 0; i < 3; ++i) {
      const float a = sum8(s1[i]), b = sum8(s2[i]);
      const float mean = a * (1.f / 576.f);
      const float var = fmaxf(b * (1.f / 576.f) - mean * mean, 0.f);
      const int pc = i < 2 ? j + 4 * i : min(j + 8, 9);
      if ((lane & 7) == 0) *(LDS_AS f32x2*)(lds + K1_ST + (pc * 8 + (lane >> 3)) * 8) = f32x2{-mean, __builtin_amdgcn_rsqf(var + LN_EPS)};
    }
    lds_bar();
    return;
  }
  if (ct >= 6) {
    if (wid < 12) k1_consumer<true, false>(lds, wid - 8, lane, ws, ct, m0);
    else k1_consumer<true, true>(lds, wid - 12, lane, ws, ct, m0);
  } else {
    if (wid < 12) k1_consumer<false, false>(lds, wid - 8, lane, ws, ct, m0);
    else k1_consumer<false, true>(lds, wid - 12, lane, ws, ct, m0);
  }
  STAMP_ENDW(0, 512);
}

#define K2SYNC 0
#define K2_SL0 4
#define K2_SL1 4
constexpr int K2_KM = 0, K2_V = 24576, K2_SY = 44032, K2_CS = 44288, K2_W = K2_CS + 4864, K2_LDS = K2_W + 73728;

#define MFMA32K8(a, b, c) __builtin_amdgcn_mfma_f32_32x32x8bf16_1k(a, b, c, 0, 0, 0)
DI void k2_core(lds_ptr lds, char* ws, int hd, int t, int wid, int lane, u32x4 q0u, u32x2 q1u, bool first) {
  const int hi = lane >> 5, l31 = lane & 31;
  const int dlo = ((18 * hd) & 7) >> 1;
#pragma unroll
  for (int e = 0; e < 4; ++e) q0u[e] = (4 * hi + e >= dlo) ? q0u[e] : 0u;
#pragma unroll
  for (int e = 0; e < 2; ++e) q1u[e] = (8 + 2 * hi + e < dlo + 9) ? q1u[e] : 0u;
  const bf16x8 q0 = __builtin_bit_cast(bf16x8, q0u);
  const s16x4 q1 = __builtin_bit_cast(s16x4, q1u);
  f32x16 S[9];
  lds_ptr kmp = lds + K2_KM + hi * 8192 + (wid * 32 + l31) * 16;
  lds_ptr kxp = lds + K2_KM + 16384 + (wid * 32 + l31) * 16 + hi * 8;
  const float NEG = -1e30f;
  const int dq = l31 - 4 * hi;
  float mx0 = NEG, mx1 = NEG;
  bf16x8 kf[3];
  s16x4 kx[3];
  auto rdk = [&](auto NN) {
    constexpr int n = NN, i = n == 0 ? 8 : n - 1;
    kf[n % 3] = lds_r128(kmp + i * 512);
    kx[n % 3] = *(LDS_AS const s16x4*)(kxp + i * 512);
  };
  auto post = [&](auto NN) {
    constexpr int n = NN, i = n == 0 ? 8 : n - 1;
    if constexpr (i < 8) {
      if constexpr (i == 0) {
#pragma unroll
        for (int r = 0; r < 16; ++r) S[0][r] = (((r & 3) + 8 * (r >> 2)) <= dq) ? S[8][r] : S[0][r];
      }
#pragma unroll
      for (int r = 0; r < 16; r += 4) {
        mx0 = fmaxf(fmaxf(mx0, S[i][r]), S[i][r + 1]);
        mx1 = fmaxf(fmaxf(mx1, S[i][r + 2]), S[i][r + 3]);
      }
    }
  };
  rdk(std::integral_constant<int, 0>{});
  rdk(std::integral_constant<int, 1>{});
  SB();
  for_seq<9>([&](auto NN) {
    constexpr int n = NN, i = n == 0 ? 8 : n - 1;
    const f32x16 z = {};
    S[i] = MFMA32(kf[n % 3], q0, z);
    SB();
    if constexpr (n + 2 < 9) { rdk(std::integral_constant<int, n + 2>{}); SB(); }
    S[i] = MFMA32K8(kx[n % 3], q1, S[i]);
    SB();
    if constexpr (n >= 1) { post(std::integral_constant<int, n - 1>{}); SB(); }
  });
  post(std::integral_constant<int, 8>{});
  float mx = fmaxf(mx0, mx1);
  {
    auto rr = __builtin_amdgcn_permlane32_swap(__float_as_uint(mx), __float_as_uint(mx), false, false);
    mx = fmaxf(__uint_as_float(rr[0]), __uint_as_float(rr[1]));
  }
  s16x4 mq;
  {
    const float nm = -mx;
    const unsigned h1 = __float_as_uint(nm) & 0xffff0000u;
    const float r1 = nm - __uint_as_float(h1);
    const unsigned h2 = __float_as_uint(r1) & 0xffff0000u;
    const float r2 = r1 - __uint_as_float(h2);
    const u32x2 mu = {hi ? 0u : ((h1 >> 16) | h2), hi ? 0u : ((__float_as_uint(r2) >> 16) | 0xf1490000u)};
    mq = __builtin_bit_cast(s16x4, mu);
  }
  auto mk1 = [&](int i) { return __builtin_bit_cast(s16x4, u32x2{0x3f803f80u, (first && i > 0 && wid + i < 8) ? 0x3f803f80u : 0x00003f80u}); };
  S[1] = MFMA32K8(mk1(1), mq, S[1]);
  S[2] = MFMA32K8(mk1(2), mq, S[2]);
  SB();
  lds_ptr vp = lds + K2_V + wid * 2 * 592 + (l31 < 18 ? hi * 288 + l31 * 16 : 576);
  f32x16 O;
  const unsigned keep0 = first ? 0u : ~0u;
  bf16x8 vf[3];
  auto rdv = [&](auto EE) {
    constexpr int e = EE, blk = e < 14 ? e + 2 : ((e - 14) >> 1) + ((e & 1) ? 16 : 0);
    if constexpr (e < 18) vf[e % 3] = lds_r128(vp + blk * 592);
  };
  rdv(std::integral_constant<int, 0>{});
  rdv(std::integral_constant<int, 1>{});
  SB();
  for_seq<16>([&](auto HH) {
    constexpr int h = HH, i = ((h >> 1) + 1) & 7, s = h & 1;
    u32x4 P;
#pragma unroll
    for (int j = 0; j < 4; ++j) P[j] = cvt_pk(__builtin_amdgcn_exp2f(S[i][8 * s + 2 * j]), __builtin_amdgcn_exp2f(S[i][8 * s + 2 * j + 1]));
    if constexpr (i == 0) {
      constexpr int e = 14 + 2 * s;
      u32x4 P8, P0;
#pragma unroll
      for (int j = 0; j < 4; ++j) {
        const int r0 = 8 * s + 2 * j, r1 = r0 + 1;
        const unsigned mk = ((((r0 & 3) + 8 * (r0 >> 2)) <= dq) ? 0xffffu : 0u) | ((((r1 & 3) + 8 * (r1 >> 2)) <= dq) ? 0xffff0000u : 0u);
        P8[j] = P[j] & mk;
        P0[j] = P[j] & ~mk & keep0;
      }
      rdv(std::integral_constant<int, e + 2>{});
      SB();
      O = MFMA32(vf[e % 3], __builtin_bit_cast(bf16x8, P0), O);
      SB();
      rdv(std::integral_constant<int, e + 3>{});
      O = MFMA32(vf[(e + 1) % 3], __builtin_bit_cast(bf16x8, P8), O);
      SB();
    } else {
      rdv(std::integral_constant<int, h + 2>{});
      SB();
      if constexpr (h == 0) { const f32x16 z = {}; O = MFMA32(vf[0], __builtin_bit_cast(bf16x8, P), z); }
      else O = MFMA32(vf[h % 3], __builtin_bit_cast(bf16x8, P), O);
      if constexpr (s == 0 && i + 2 <= 8) S[(i + 2) & 7] = MFMA32K8(mk1((i + 2) & 7), mq, S[(i + 2) & 7]);
      SB();
    }
  });
  const float inv = __builtin_amdgcn_rcpf(O[10]);
  const int row = t * 32 + l31;
  char* ob = ws + WS_O + (size_t)(row >> 4) * 18 * 1024 + (row & 15) * 16;
  u32x4 o = {cvt_pk(O[0] * inv, O[1] * inv), cvt_pk(O[2] * inv, O[3] * inv), cvt_pk(O[4] * inv, O[5] * inv), cvt_pk(O[6] * inv, O[7] * inv)};
  st16(ob + (hd >> 1) * 1024 + (2 * (hd & 1) + hi) * 256, o);
  if (hi == 0) st4(ob + (16 + (hd >> 4)) * 1024 + ((hd >> 2) & 3) * 256 + (hd & 3) * 4, cvt_pk(O[8] * inv, O[9] * inv));
}

__global__ __launch_bounds__(768) void k2_attn(char* __restrict__ ws, const char* __restrict__ wg, const char* __restrict__ wu,
                                               const char* __restrict__ wo, const char* __restrict__ x) {
  extern __shared__ __attribute__((aligned(16))) char smem[];
  STAMP_BEGIN
  lds_ptr lds = (lds_ptr)smem;
  const unsigned lds0 = (unsigned)(uintptr_t)lds;
  const int tid = threadIdx.x, lane = tid & 63, wid = __builtin_amdgcn_readfirstlane(tid >> 6);
  const int hd = (blockIdx.x & 7) * 4 + ((blockIdx.x >> 3) & 3), qb = blockIdx.x >> 5;
  const bool has_tile = blockIdx.x < 234;
  const int tile = has_tile ? xcd_remap(blockIdx.x, 234) : 0;
  const int mt = tile / 9, nt = tile - mt * 9;
  const int m0 = min(mt * 80, SEQ - 80), rb0 = m0 >> 4;
  if (wid == 11) {
    if (lane < 4) *(LDS_AS unsigned*)(lds + K2_SY + lane * 4) = 0u;
  }
  if (wid >= 10) {
    lds_bar();
    __builtin_amdgcn_s_sleep(K2_SL1);
    const int sw = wid - 10;
    if (sw == 0) CODE_TOUCH(SYM_K4, LEN_K4, lds0 + K2_CS + 4096);
    const bool act = blockIdx.x < 216;
    const int unit = act ? (int)blockIdx.x * 2 + sw : 0, part = unit / 144, c64 = unit - part * 144;
    const char* wsrc = (c64 < 72 ? wg + c64 * 128 : wu + (c64 - 72) * 128) + (lane & 7) * 16 + (size_t)(part * 192 + (lane >> 3)) * 9216;
    u32x4 v[24];
    if (act) {
#pragma unroll
      for (int i = 0; i < 24; ++i) v[i] = *(const u32x4*)(wsrc + (size_t)i * 8 * 9216);
    }
    if (act) {
      float cs[8];
#pragma unroll
      for (int j = 0; j < 8; ++j) cs[j] = 0.f;
#pragma unroll
      for (int i = 0; i < 24; ++i)
#pragma unroll
        for (int j = 0; j < 4; ++j) {
          cs[2 * j] += bf_lo(v[i][j]); cs[2 * j + 1] += bf_hi(v[i][j]);
        }
      lds_ptr cp = lds + K2_CS + sw * 2048;
      *(LDS_AS f32x4*)(cp + lane * 32) = f32x4{cs[0], cs[1], cs[2], cs[3]};
      *(LDS_AS f32x4*)(cp + lane * 32 + 16) = f32x4{cs[4], cs[5], cs[6], cs[7]};
      asm volatile("s_waitcnt lgkmcnt(0)" ::: "memory");
      float t = 0.f;
#pragma unroll
      for (int i = 0; i < 8; ++i) t += *(LDS_AS float*)(cp + i * 256 + lane * 4);
      st4(ws + WS_CSP + ((size_t)part * 9216 + c64 * 64 + lane) * 4, __float_as_uint(t));
    }
    if (has_tile) { raw_bar(); raw_bar(); }
    return;
  }
  if (wid >= 8) {
    raw_bar();
    __builtin_amdgcn_s_sleep(K2_SL0);
    if (has_tile) {
      const int par = wid - 8;
      unsigned va[4], vb[4];
#pragma unroll
      for (int i = 0; i < 4; ++i) {
        const int rho = 8 * i + (lane >> 3);
        const int g_ = ((rho >> 4) & 1) * 2 + ((rho >> 2) & 1), t_ = (rho >> 3) & 1, q_ = rho & 3;
        const int La = 36 * par + 18 * (g_ >> 1) + 8 * t_ + 4 * (g_ & 1) + q_, Lb = 288 * par + 18 * (4 * g_ + 2 * t_ + (q_ >> 1)) + 16 + (q_ & 1);
        const unsigned csw = ((lane & 7) ^ (((rho >> 1) & 3) << 1)) * 16;
        va[i] = La * 1152 + csw; vb[i] = Lb * 1152 + csw;
      }
#pragma unroll
      for (int kp = 0; kp < 9; ++kp)
#pragma unroll
        for (int i = 0; i < 4; ++i)
          glds16(wo + nt * 128 + (size_t)(kp == 8 ? 0 : 72 * kp) * 1152, kp == 8 ? vb[i] : va[i], lds0 + K2_W + par * 4096 + kp * 8192 + i * 1024);
    }
  } else {
    const int t = qb * 8 + wid;
    u32x4 q0;
    u32x2 q1;
    const int c0 = (18 * hd) >> 3;
    asm volatile("global_load_dwordx4 %0, %2, %4\n\tglobal_load_dwordx2 %1, %3, %4"
                 : "=&v"(q0), "=&v"(q1)
                 : "v"((lane >> 5) * 32768 + (lane & 31) * 16), "v"(65536 + (lane & 31) * 16 + (lane >> 5) * 8),
                   "s"(ws + WS_Q + (size_t)c0 * 32768 + t * 512)
                 : "memory");
    const int key0 = max(qb * 256 - 256 + wid * 64, 0);
#pragma unroll
    for (int i = 0; i < 3; ++i) glds16(ws + WS_K + (size_t)(c0 + i) * 32768 + key0 * 16, (qb == 0 && wid < 4) ? 0 : lane * 16, lds0 + K2_KM + i * 8192 + wid * 1024);
#pragma unroll
    for (int i = 0; i < 3; ++i) {
      const int pc = wid + 8 * i;
      if (pc < 19) {
        const int u = min(pc * 64 + lane, 32 * 37 - 1), bl = u / 37, w = u - bl * 37;
        const int blk = max(qb * 16 - 16 + bl, 0);
        const unsigned vo = w == 36 ? (unsigned)(WS_ONES - WS_V) : (blk * 2 + (w >= 18)) * 9216 + (hd * 18 + (w >= 18 ? w - 18 : w)) * 16;
        glds16(ws + WS_V, vo, lds0 + K2_V + pc * 1024);
      }
    }
    raw_bar();
    asm volatile("s_waitcnt vmcnt(0)" : "+v"(q0), "+v"(q1)::"memory");
    lds_sync(lds + K2_SY, 8, lane);
    STAMP_MARK(1, 2);
    k2_core(lds, ws, hd, t, wid, lane, q0, q1, qb == 0);
    STAMP_MARK(1, 3);
    STAMP_MARKW(1, 7, 448);
  }
  wait_vm<0>();
  STAMP_MARK(1, 4);
  unsigned* cnt = (unsigned*)(ws + WS_CNT);
  if (wid < 8) {
    if (lds_arrive(lds + K2_SY + 8, lane) == 7 && lane < 8) __hip_atomic_fetch_add(cnt + (qb * 8 + lane) * 32, 1u, __ATOMIC_RELAXED, __HIP_MEMORY_SCOPE_AGENT);
  }
  if (!has_tile) { STAMP_END(1); return; }
  lds_sync(lds + K2_SY + 12, 10, lane);
  const int mi = wid >> 1, kg = wid & 1;
  const int g = lane >> 4, q = (lane & 15) >> 2, p = lane & 3;
  const int er = tid >> 3, ec = tid & 7;
  const u32x4 res = *(const u32x4*)(x + (size_t)(m0 + er) * 1152 + (nt * 64 + ec * 8) * 2);
  const int rho0 = 16 * (g >> 1) + 4 * (g & 1) + q;
  lds_ptr wb[4];
#pragma unroll
  for (int n = 0; n < 4; ++n) { wb[n] = lds + K2_W + kg * 4096 + rho0 * 128 + ((n ^ ((rho0 >> 1) & 3)) * 32) + p * 8; asm volatile("" : "+v"(wb[n])); }
  bf16x8 wf[5][4];
#pragma unroll
  for (int j = 0; j < 5; ++j)
#pragma unroll
    for (int n = 0; n < 4; ++n) wf[j][n] = cat8(lds_tr(wb[n] + j * 8192), lds_tr(wb[n] + j * 8192 + 1024));
  if (tid == 576) {
    const unsigned* c0 = cnt + ((m0 >> 8) * 8 + (tile & 7)) * 32;
    const unsigned* c1 = cnt + (((m0 + 79) >> 8) * 8 + (tile & 7)) * 32;
    for (int spin = 0; spin < (1 << 18); ++spin) {
      const unsigned a = __hip_atomic_load(c0, __ATOMIC_RELAXED, __HIP_MEMORY_SCOPE_AGENT);
      const unsigned b = __hip_atomic_load(c1, __ATOMIC_RELAXED, __HIP_MEMORY_SCOPE_AGENT);
      if (a >= 32u && b >= 32u) break;
      __builtin_amdgcn_s_sleep(1);
    }
  }
  SB();
  raw_bar();
  SB();
  STAMP_MARK(1, 5);
  const __amdgpu_buffer_rsrc_t ors = __builtin_amdgcn_make_buffer_rsrc((void*)(ws + WS_O), 0, 128 * 18432, 0x00020000);
  const int aoff = (rb0 + mi) * 18432 + kg * 1024 + lane * 16;
  bf16x8 a[9];
#pragma unroll
  for (int i = 0; i < 9; ++i) a[i] = __builtin_bit_cast(bf16x8, __builtin_amdgcn_raw_buffer_load_b128(ors, aoff + i * 2048, 0, 16));
  SB();
  f32x4 acc[4];
#pragma unroll
  for (int n = 0; n < 4; ++n) acc[n] = f32x4{0.f, 0.f, 0.f, 0.f};
#pragma unroll
  for (int j = 0; j < 9; ++j) {
#pragma unroll
    for (int n = 0; n < 4; ++n) {
      acc[n] = MFMA16(wf[j % 5][n], a[j], acc[n]);
      if (j + 5 < 9) wf[j % 5][n] = cat8(lds_tr(wb[n] + (j + 5) * 8192),
                                         lds_tr(wb[n] + (j + 5) * 8192 + 1024));
    }
  }
  STAMP_MARK(1, 6);
  {
    lds_ptr cw = lds + kg * (80 * 272) + (mi * 16 + (lane & 15)) * 272 + 16 * g;
#pragma unroll
    for (int n = 0; n < 4; ++n) *(LDS_AS f32x4*)(cw + n * 64) = acc[n];
  }
  lds_bar();
  {
    lds_ptr cr = lds + er * 272 + ec * 32;
    f32x4 a0 = *(LDS_AS f32x4*)(cr), a1 = *(LDS_AS f32x4*)(cr + 16);
    f32x4 b0 = *(LDS_AS f32x4*)(cr + 80 * 272), b1 = *(LDS_AS f32x4*)(cr + 80 * 272 + 16);
    a0 += b0; a1 += b1;
    u32x4 o;
    o[0] = cvt_pk(a0[0] + bf_lo(res[0]), a0[1] + bf_hi(res[0]));
    o[1] = cvt_pk(a0[2] + bf_lo(res[1]), a0[3] + bf_hi(res[1]));
    o[2] = cvt_pk(a1[0] + bf_lo(res[2]), a1[1] + bf_hi(res[2]));
    o[3] = cvt_pk(a1[2] + bf_lo(res[3]), a1[3] + bf_hi(res[3]));
    const int row = m0 + er, col = nt * 64 + ec * 8;
    st16(ws + WS_H + (size_t)(row >> 4) * 18432 + (col >> 5) * 1024 + ((col >> 3) & 3) * 256 + (row & 15) * 16, o);
    float s1 = 0.f, s2 = 0.f;
#pragma unroll
    for (int j = 0; j < 4; ++j) { s1 = dot2(o[j], 0x3f803f80u, s1); s2 = dot2(o[j], o[j], s2); }
    s1 = sum8(s1); s2 = sum8(s2);
    if (ec == 0) st8wt(ws + WS_ST + (size_t)nt * 16384 + row * 8, u32x2{__float_as_uint(s1), __float_as_uint(s2)});
  }
  STAMP_END(1);
}

constexpr int K4_SLOT = 18432, K4_ST = K4_SLOT * 8, K4_CS = K4_ST + 2048, K4_PF = K4_CS + 2048, K4_CP = K4_PF + 1024, K4_SY = 160000, K4_LDS = 160 * 1024;

constexpr int k4_frag(int i) { return i < 16 ? ((i >> 2) * 2 + (i & 1) + ((i & 2) ? 9 : 0)) : (i == 16 ? 8 : 17); }
constexpr int k4_foff(int f) { return (f >= 9 ? 9216 + (f - 9) * 32 : f * 32); }
constexpr int K4_T0 = 13 * 18;
constexpr int k4_slab(int G) { return G < K4_T0 ? G / 18 : (G - K4_T0 < 80 ? 13 + ((G - K4_T0) % 20) / 4 : 13 + (G - K4_T0 - 80) / 2); }
constexpr int k4_pos(int G) { return G < K4_T0 ? G % 18 : (G - K4_T0 < 80 ? ((G - K4_T0) / 20) * 4 + ((G - K4_T0) & 3) : 16 + ((G - K4_T0) & 1)); }
constexpr int k4_op(int G) {
  const int s = G / 18, i = G % 18;
  if (s > 12) return 0;
  if (s < 3) {
    const int pos[10] = {0, 2, 4, 6, 7, 9, 11, 13, 15, 16};
    for (int k = 0; k < 10; ++k) if (pos[k] == i) return 1 + (2 * s + 2 + k / 5) * 8 + k % 5;
    return 0;
  }
  if (s == 3) {
    const int pos[8] = {0, 2, 4, 6, 8, 11, 14, 16};
    for (int k = 0; k < 8; ++k) if (pos[k] == i) return k < 5 ? 1 + 8 * 8 + k : 1 + 9 * 8 + (k - 3);
    return 0;
  }
  if (i == 1) return 1 + (s + 5) * 8;
  if (i == 4) return 1 + (s + 5) * 8 + 1;
  if (s + 6 > 17) return 0;
  if (i == 8) return 1 + (s + 6) * 8 + 2;
  if (i == 13) return 1 + (s + 6) * 8 + 3;
  if (i == 16) return 1 + (s + 6) * 8 + 4;
  return 0;
}
constexpr int k4_allow(int G, int need) {
  int n = (need == 0) ? 4 : 0;
  for (int g = 0; g < G; ++g) {
    const int op = k4_op(g);
    if (op == 0) continue;
    const int sl = (op - 1) >> 3, kind = (op - 1) & 7;
    if (sl <= need) n = 0;
    else if (kind != 4) ++n;
  }
  return n;
}
constexpr int k4_need(int G) {
  if (G + 5 >= 324 || G == 0) return 0;
  int mx = 0;
  for (int g = 0; g < G + 5; ++g) mx = k4_slab(g) > mx ? k4_slab(g) : mx;
  return k4_slab(G + 5) > mx ? k4_slab(G + 5) : 0;
}

constexpr int d_off(int i) { return i < 6 ? 36864 + i * 9216 : (i < 10 ? (i - 6) * 9216 : (i < 17 ? 92160 + (i - 10) * 9216 : 36864)); }
#define DT 8
constexpr int D_G0 = (18 - DT) * 9;
constexpr int d_i(int H) { return H < D_G0 ? H / 9 : (H - D_G0 < 8 * DT ? 18 - DT + ((H - D_G0) % (2 * DT)) / 2 : 18 - DT + (H - D_G0 - 8 * DT)); }
constexpr int d_f(int H) { return H < D_G0 ? H % 9 : (H - D_G0 < 8 * DT ? ((H - D_G0) / (2 * DT)) * 2 + ((H - D_G0) & 1) : 8); }
constexpr int d_first(int H) {
  for (int h = 0; h < H; ++h) if (d_i(h) == d_i(H)) return 0;
  return 1;
}
DI void ld2_sc1(bf16x8& d0, bf16x8& d1, unsigned o0, unsigned o1, const char* base) {
  asm volatile("global_load_dwordx4 %0, %2, %4 sc1\n\tglobal_load_dwordx4 %1, %3, %4 sc1" : "=&v"(d0), "=&v"(d1) : "v"(o0), "v"(o1), "s"(base) : "memory");
}
template <int N> DI void wait_vm2(bf16x8& d0, bf16x8& d1) { asm volatile("s_waitcnt vmcnt(%2)" : "+v"(d0), "+v"(d1) : "n"(N) : "memory"); }
DI void poll_count(const unsigned* c, unsigned n) {
  for (int spin = 0; spin < (1 << 18); ++spin) {
    if (__hip_atomic_load(c, __ATOMIC_RELAXED, __HIP_MEMORY_SCOPE_AGENT) >= n) break;
    __builtin_amdgcn_s_sleep(1);
  }
}

__global__ __launch_bounds__(512, 2) void k4_gateup(const char* __restrict__ wgate, const char* __restrict__ wup, char* __restrict__ ws,
                                                    const char* __restrict__ wd, char* __restrict__ out) {
  extern __shared__ __attribute__((aligned(16))) char smem[];
  STAMP_BEGIN
  lds_ptr lds = (lds_ptr)smem;
  const unsigned lds0 = (unsigned)(uintptr_t)lds;
  const int tid = threadIdx.x, lane = tid & 63, wid = __builtin_amdgcn_readfirstlane(tid >> 6);
  const int xcd = blockIdx.x & 7, idx = blockIdx.x >> 3;
  const int pn = 4 * (xcd >> 2) + (idx & 3), jt = 8 * (xcd & 3) + (idx >> 2);
  const int wm = wid & 3, wn = wid >> 2;
  const char* asrc = ws + WS_H + (size_t)(pn * 16 + 2 * wid) * 18432;
  const unsigned avo = lane * 16, avo2 = lane * 16 + 18432;
  const char* wsrc = (wn ? wup : wgate) + jt * 288;
  unsigned wvo[3];
#pragma unroll
  for (int i = 0; i < 3; ++i) {
    const int pc = i < 2 ? 2 * wm + i : 8;
    const int o = pc * 1024 + lane * 16;
    const int rho = o / 288, u = (o - rho * 288) >> 4;
    const int gk = ((rho >> 4) & 1) * 2 + ((rho >> 2) & 1), tk = (rho >> 3) & 1, qk = rho & 3;
    wvo[i] = (8 * gk + 4 * tk + qk) * 9216 + u * 16;
  }
  const unsigned wdst = lds0 + wn * 9216;
  auto issue_w = [&](int s, int i) {
    const unsigned so = (s & 7) * K4_SLOT;
    const char* wp = wsrc + (size_t)s * 32 * 9216;
    if (i < 2) glds16(wp, wvo[i], wdst + so + wm * 2048 + i * 1024);
    else glds16_if0(wm, wp, wvo[2], wdst + so + 8192);
  };
  bf16x8 A[6][2];
  auto issue_a = [&](int s, bf16x8& dst, int m) {
    asm volatile("global_load_dwordx4 %0, %1, %2" : "=v"(dst) : "v"(m ? avo2 : avo), "s"(asrc + s * 1024) : "memory");
  };
  f32x2 sp[5];
  {
    const char* stp = ws + WS_ST + (size_t)(pn * 256 + wid * 32) * 8;
    const unsigned so = (lane & 31) * 8 + (lane >> 5) * 5 * 16384;
    asm volatile("global_load_dwordx2 %0, %5, %7\n\tglobal_load_dwordx2 %1, %5, %8\n\tglobal_load_dwordx2 %2, %5, %9\n\t"
                 "global_load_dwordx2 %3, %5, %10\n\tglobal_load_dwordx2 %4, %6, %10"
                 : "=&v"(sp[0]), "=&v"(sp[1]), "=&v"(sp[2]), "=&v"(sp[3]), "=&v"(sp[4])
                 : "v"(so), "v"((lane & 31) * 8 + 16384), "s"(stp), "s"(stp + 16384), "s"(stp + 32768), "s"(stp + 49152) : "memory");
  }
  if (wm < 3 && lane < 36) glds16(ws + WS_CSP + (size_t)(wm * 9216 + wn * FF + jt * 144) * 4, lane * 16, lds0 + K4_CP + wm * 1152 + wn * 576);
  for_seq<2>([&](auto S) { issue_w(S, 0); issue_w(S, 1); issue_w(S, 2); issue_a(S, A[S][0], 0); issue_a(S, A[S][1], 1); });
  {
    asm volatile("s_waitcnt vmcnt(8)" : "+v"(sp[0]), "+v"(sp[1]), "+v"(sp[2]), "+v"(sp[3]), "+v"(sp[4])::"memory");
    const f32x2 t4 = (sp[0] + sp[1]) + (sp[2] + sp[3]);
    auto r1 = __builtin_amdgcn_permlane32_swap(__float_as_uint(t4[0]), __float_as_uint(t4[0]), false, false);
    auto r2 = __builtin_amdgcn_permlane32_swap(__float_as_uint(t4[1]), __float_as_uint(t4[1]), false, false);
    const float s1 = __uint_as_float(r1[0]) + __uint_as_float(r1[1]) + sp[4][0];
    const float s2 = __uint_as_float(r2[0]) + __uint_as_float(r2[1]) + sp[4][1];
    const float mean = s1 * (1.f / 576.f);
    const float var = fmaxf(s2 * (1.f / 576.f) - mean * mean, 0.f);
    *(LDS_AS f32x2*)(lds + K4_ST + (wid * 32 + (lane & 31)) * 8) = f32x2{-mean, __builtin_amdgcn_rsqf(var + LN_EPS)};
    *(LDS_AS unsigned*)(lds + K4_SY) = 0u;
    SB();
  }
  const int g = lane >> 4, q = (lane & 15) >> 2, p = lane & 3, r = lane & 15;
  lds_ptr wb = lds + (16 * (g >> 1) + 4 * (g & 1) + q) * 288 + p * 8;
  lds_ptr wbs[3] = {wb, wb + 3 * K4_SLOT, wb + 6 * K4_SLOT};
  asm volatile("" : "+v"(wbs[1]), "+v"(wbs[2]));
  lds_ptr csb = lds + K4_CS + 16 * g;
  asm volatile("" : "+v"(csb));
  f32x4 acc[2][18];
  asm volatile("s_waitcnt vmcnt(%2)\n\ts_barrier" : "+v"(A[0][0]), "+v"(A[0][1]) : "n"(k4_allow(0, 0)) : "memory");
  STAMP_MARK(2, 2);
  STAMP_CYC0
  s16x4 Bl[6], Bh[6];
  for_seq<5>([&](auto I) { Bl[I] = lds_tr(wb + k4_foff(k4_frag(I))); Bh[I] = lds_tr(wb + k4_foff(k4_frag(I)) + 2304); });
  SB();
  const float L2E = 1.4426950408889634f;
  char* hb = ws + WS_HID + (size_t)(pn * 16 + wid * 2) * (144 * 1024) + (size_t)(jt >> 2) * 18432;
  f32x2 st[2];
  float c1[2];
  float sd[2];
  auto glu = [&](float ag, float au, int m) -> float {
    return au * __builtin_amdgcn_rcpf(fmaf(__builtin_amdgcn_exp2f(ag * c1[m]), sd[m], sd[m]));
  };
  f32x4 cv;
  u32x4 own[4][2];
  float eg[2][2];
  auto micro = [&](auto QQ, auto KK, auto JJ) {
    constexpr int Q = QQ, k = KK, j = JJ, m = k >> 2, d = k & 3, h = d >> 1, e = (d & 1) * 2, b = k & 1;
#pragma unroll
    for (int x = 0; x < 2; ++x) {
      if constexpr (j == 0) eg[b][x] = acc[m][2 * Q + h][e + x] * c1[m];
      if constexpr (j == 1) eg[b][x] = __builtin_amdgcn_exp2f(eg[b][x]);
      if constexpr (j == 2) eg[b][x] = fmaf(eg[b][x], sd[m], sd[m]);
      if constexpr (j == 3) eg[b][x] = __builtin_amdgcn_rcpf(eg[b][x]);
      if constexpr (j == 4) eg[b][x] *= acc[m][9 + 2 * Q + h][e + x];
      if constexpr (j < 5) pin(eg[b][x]);
    }
    if constexpr (j == 6) {
      own[Q][m][d] = cvt_pk(eg[b][0], eg[b][1]);
      if constexpr (d == 3) st16(hb + (size_t)m * (144 * 1024) + (4 * (jt & 3) + Q) * 1024 + lane * 16, own[Q][m]);
      else pin(own[Q][m]);
    }
  };
  auto gapops = [&](auto QQ, auto GG) {
    constexpr int gg = GG;
    if constexpr (gg >= 4 && gg < 36) micro(QQ, std::integral_constant<int, gg / 4 - 1>{}, std::integral_constant<int, 4 + gg % 4>{});
    if constexpr (gg >= 0 && gg < 32) micro(QQ, std::integral_constant<int, gg / 4>{}, std::integral_constant<int, gg % 4>{});
    SB();
  };
  unsigned dvo[2], dvl[2];
  const char* dsrc = wd + (size_t)(jt >> 2) * 576 * 1152 + (jt & 3) * 288;
  auto dvoff = [&]() {
    int ln = tid;
    asm volatile("" : "+v"(ln));
    ln &= 63;
#pragma unroll
    for (int i = 0; i < 2; ++i) {
      const int o = (i ? 8 : wid) * 1024 + ln * 16;
      const int rho = o / 288, u = (o - rho * 288) >> 4;
      const int gk = ((rho >> 4) & 1) * 2 + ((rho >> 2) & 1), tk = (rho >> 3) & 1, qk = rho & 3;
      dvo[i] = (16 * tk + 4 * gk + qk) * 1152 + u * 16;
      dvl[i] = (144 * (gk >> 1) + 128 + 8 * (gk & 1) + 4 * tk + qk) * 1152 + u * 16;
    }
  };
  auto issue_d = [&](auto II, int pc) {
    constexpr int i = II;
    const int k = (4 * (jt & 3) + i) & 15;
    const char* src = dsrc + (size_t)(i < 16 ? 144 * (k >> 2) + 32 * (k & 3) : 288 * (i - 16)) * 1152;
    if (pc == 0) glds16(src, i < 16 ? dvo[0] : dvl[0], lds0 + d_off(i) + wid * 1024);
    else glds16_if0(wid ^ (i & 7), src, i < 16 ? dvo[1] : dvl[1], lds0 + d_off(i) + 8192);
  };
  for_seq<18>([&](auto GH) { for_seq<18>([&](auto GL) {
    constexpr int G = GH * 18 + GL, sl = k4_slab(G), f = k4_frag(k4_pos(G)), rb = G % 6, nb = (G + 5) % 6, NG = G + 5;
    const bf16x8 b = cat8(Bl[rb], Bh[rb]);
    constexpr int need = k4_need(G);
    if constexpr (need != 0) {
      asm volatile("s_waitcnt vmcnt(%2)\n\ts_barrier" : "+v"(A[need % 6][0]), "+v"(A[need % 6][1]) : "n"(k4_allow(G, need)) : "memory");
      SB();
    }
    if constexpr (sl == 0) mfma16_zero(acc[0][f], b, A[0][0]); else mfma16_acc(acc[0][f], b, A[sl % 6][0]);
    SB();
    if constexpr (NG < 324) { Bl[nb] = lds_tr(wbs[(k4_slab(NG) & 7) / 3] + ((k4_slab(NG) & 7) % 3) * K4_SLOT + k4_foff(k4_frag(k4_pos(NG)))); SB(); }
    if constexpr (G >= K4_T0 + 20 && G < K4_T0 + 80 && (G - K4_T0) % 20 >= 1 && (G - K4_T0) % 20 < 19)
      gapops(std::integral_constant<int, (G - K4_T0) / 20 - 1>{}, std::integral_constant<int, 2 * ((G - K4_T0) % 20 - 1)>{});
    if constexpr (G >= K4_T0 + 81) {
      gapops(std::integral_constant<int, 3>{}, std::integral_constant<int, 4 * (G - K4_T0 - 81)>{});
      gapops(std::integral_constant<int, 3>{}, std::integral_constant<int, 4 * (G - K4_T0 - 81) + 1>{});
    }
    if constexpr (sl == 0) mfma16_zero(acc[1][f], b, A[0][1]); else mfma16_acc(acc[1][f], b, A[sl % 6][1]);
    SB();
    if constexpr (NG < 324) { Bh[nb] = lds_tr(wbs[(k4_slab(NG) & 7) / 3] + ((k4_slab(NG) & 7) % 3) * K4_SLOT + k4_foff(k4_frag(k4_pos(NG))) + 2304); SB(); }
    constexpr int op = k4_op(G);
    if constexpr (op != 0) {
      constexpr int os = (op - 1) >> 3, kind = (op - 1) & 7;
      if constexpr (kind < 2) issue_a(os, A[os % 6][kind], kind);
      else issue_w(os, kind - 2);
      SB();
    }
    if constexpr (G % 18 == 10 && (G / 18 == 5 || G / 18 == 7 || G / 18 == 9)) {
      constexpr int k = (G / 18 - 5) / 2;
      touch_if0((int)(blockIdx.x / 216) + (wid ^ (k + 1)), wd + (size_t)(blockIdx.x * 3 + k) * 8192, avo * 8, lds0 + K4_PF + k * 256);
      SB();
    }
    if constexpr (G == 60) {
      lds_ptr cpp = lds + K4_CP + wid * 144 + lane * 4;
      const float t = *(LDS_AS float*)(cpp) + *(LDS_AS float*)(cpp + 1152) + *(LDS_AS float*)(cpp + 2304);
      *(LDS_AS float*)(lds + K4_CS + wid * 144 + min(lane, 36) * 4 + (lane >= 36 ? 1152 : 0)) = t;
      asm volatile("s_waitcnt lgkmcnt(0)" ::: "memory");
      SB();
    }
    if constexpr (G == 88) {
#pragma unroll
      for (int m = 0; m < 2; ++m) st[m] = *(LDS_AS f32x2*)(lds + K4_ST + (wid * 32 + m * 16 + r) * 8);
      SB();
    }
    if constexpr (G >= 90 && G <= 162 && (G - 90) % 4 == 0) {
      if constexpr (G > 90) {
        constexpr int cf = k4_frag((G + (G - 94 < 36 ? 8 : 9)) % 18);
#pragma unroll
        for (int m = 0; m < 2; ++m) {
#pragma unroll
          for (int e = 0; e < 4; ++e) acc[m][cf][e] = fmaf(st[m][0], cv[e], acc[m][cf][e]);
          pin(acc[m][cf]);
        }
        SB();
      }
      if constexpr (G < 162) { cv = *(LDS_AS f32x4*)(csb + k4_frag((G + 4 + (G - 90 < 36 ? 8 : 9)) % 18) * 64); SB(); }
    }
    if constexpr (G == K4_T0) {
      STAMP_MARK(2, 3);
      STAMP_CYC(2, 4);
#pragma unroll
      for (int m = 0; m < 2; ++m) { c1[m] = -L2E * st[m][1]; sd[m] = __builtin_amdgcn_rcpf(st[m][1]); }
      SB();
    }
    if constexpr (G >= K4_T0) {
      constexpr int t = G - K4_T0, u = t % 20;
      if constexpr (t == 11) { dvoff(); SB(); }
      if constexpr (t >= 12 && t < 24) { issue_d(std::integral_constant<int, (t - 12) / 2>{}, t & 1); SB(); }
      if constexpr (t >= 20 && t < 80 && u >= 1 && u < 19) gapops(std::integral_constant<int, t / 20 - 1>{}, std::integral_constant<int, 2 * (u - 1) + 1>{});
      if constexpr (t >= 81) {
        gapops(std::integral_constant<int, 3>{}, std::integral_constant<int, 4 * (t - 81) + 2>{});
        gapops(std::integral_constant<int, 3>{}, std::integral_constant<int, 4 * (t - 81) + 3>{});
      }
    }
  }); });
  {
    asm volatile("s_nop 7\n\ts_nop 1" : "+v"(acc[0][8]), "+v"(acc[1][8]), "+v"(acc[0][17]), "+v"(acc[1][17]));
#pragma unroll
    for (int m = 0; m < 2; ++m) {
      const f32x4 ag = acc[m][8], au = acc[m][17];
      u32x2 o = {cvt_pk(glu(ag[0], au[0], m), glu(ag[1], au[1], m)), cvt_pk(glu(ag[2], au[2], m), glu(ag[3], au[3], m))};
      st8wt(hb + (size_t)m * (144 * 1024) + (16 + ((jt >> 1) & 1)) * 1024 + (2 * (jt & 1) + (g >> 1)) * 256 + r * 16 + (g & 1) * 8, o);
    }
  }
  STAMP_MARK(2, 5);
  const int sb = jt >> 2, nq = jt & 3;
  unsigned* cntA = (unsigned*)(ws + WS_CNT + 8192) + (pn * 8 + sb) * 32;
  unsigned* cntB = (unsigned*)(ws + WS_CNT + 16384) + (pn * 4 + nq) * 32;
  wait_bar<10>();
  auto dptr = [&](auto HH) -> lds_ptr {
    constexpr int H = HH, o = d_off(d_i(H)) + d_f(H) * 32;
    return wbs[o / (3 * K4_SLOT)] + o % (3 * K4_SLOT);
  };
  for_seq<5>([&](auto I) { Bl[I] = lds_tr(dptr(I)); Bh[I] = lds_tr(dptr(I) + 2304); });
  SB();
  f32x4 dacc[2][9];
  bf16x8 DA[14][2];
  char* pb = ws + WS_PART + (size_t)(((pn * 4 + nq) * 8 + wid) * 8 + sb) * 9216 + lane * 16;
  for_seq<18>([&](auto HH) { for_seq<9>([&](auto HL) {
    constexpr int H = HH * 9 + HL, i = d_i(H), f = d_f(H), rb = H % 6, nb = (H + 5) % 6, NH = H + 5;
    if constexpr (H == 36) {
      if (tid == 0) poll_count(cntA, 4u);
      SB();
      wait_bar<0>();
      STAMP_MARK(2, 6);
      issue_d(std::integral_constant<int, 17>{}, 0); issue_d(std::integral_constant<int, 17>{}, 1);
      const unsigned ho = lane * 16, ho2 = lane * 16 + 144 * 1024;
      for_seq<14>([&](auto JJ) {
        constexpr int j = JJ;
        const int k = j < 12 ? ((4 * nq + 4 + j) & 15) : 4 + j;
        ld2_sc1(DA[j][0], DA[j][1], ho, ho2, hb + k * 1024);
      });
      SB();
    }
    if constexpr (i >= 4 && d_first(H)) {
      wait_vm2<2 * (17 - i)>(DA[i - 4][0], DA[i - 4][1]);
      SB();
    }
    if constexpr (H == D_G0) { raw_bar(); SB(); }
    const bf16x8 b = cat8(Bl[rb], Bh[rb]);
    bf16x8 a0, a1;
    if constexpr (i < 4) { a0 = __builtin_bit_cast(bf16x8, own[i][0]); a1 = __builtin_bit_cast(bf16x8, own[i][1]); }
    else { a0 = DA[i - 4][0]; a1 = DA[i - 4][1]; }
    if constexpr (i == 0) mfma16_zero(dacc[0][f], b, a0); else mfma16_acc(dacc[0][f], b, a0);
    SB();
    if constexpr (NH < 162) { Bl[nb] = lds_tr(dptr(std::integral_constant<int, NH>{})); SB(); }
    if constexpr (i == 0) mfma16_zero(dacc[1][f], b, a1); else mfma16_acc(dacc[1][f], b, a1);
    SB();
    if constexpr (NH < 162) { Bh[nb] = lds_tr(dptr(std::integral_constant<int, NH>{}) + 2304); SB(); }
    if constexpr (H < 22) { issue_d(std::integral_constant<int, 6 + H / 2>{}, H & 1); SB(); }
    if constexpr (H == 24) {
      wait_vm<11>();
      if (lds_arrive(lds + K4_SY, lane) == 7 && lane == 0) __hip_atomic_fetch_add(cntA, 1u, __ATOMIC_RELAXED, __HIP_MEMORY_SCOPE_AGENT);
      SB();
    }
    if constexpr (H >= D_G0 + 2 * DT && (((H - D_G0) % (2 * DT) == 2 || (H - D_G0) % (2 * DT) == 3))) {
      constexpr int pr = (H - D_G0) / (2 * DT) - 1, m = ((H - D_G0) % (2 * DT) == 3);
      const f32x4 x0 = dacc[m][2 * pr], x1 = dacc[m][2 * pr + 1];
      st16(pb + (m * 4 + pr) * 1024, u32x4{cvt_pk(x0[0], x0[1]), cvt_pk(x0[2], x0[3]), cvt_pk(x1[0], x1[1]), cvt_pk(x1[2], x1[3])});
      SB();
    }
  }); });
  {
    asm volatile("s_nop 7\n\ts_nop 1" : "+v"(dacc[0][8]), "+v"(dacc[1][8]));
    const f32x4 x0 = dacc[0][8], x1 = dacc[1][8];
    st16(pb + 8192, u32x4{cvt_pk(x0[0], x0[1]), cvt_pk(x0[2], x0[3]), cvt_pk(x1[0], x1[1]), cvt_pk(x1[2], x1[3])});
  }
  wait_bar<0>();
  STAMP_MARK(2, 7);
  if (tid == 0) __hip_atomic_fetch_add(cntB, 1u, __ATOMIC_RELAXED, __HIP_MEMORY_SCOPE_AGENT);
  {
    const int nc = wid == 1 ? 2 : 1;
    const int rbase = pn * 256 + sb * 32;
    int row[2][2], col[2][2];
    row[0][0] = row[0][1] = rbase + (wid >> 2) * 16 + r; col[0][0] = nq * 144 + (wid & 3) * 32 + 4 * g; col[0][1] = col[0][0] + 16;
    row[1][0] = rbase + r; row[1][1] = rbase + 16 + r; col[1][0] = col[1][1] = nq * 144 + 128 + 4 * g;
    u32x2 res[2][2];
#pragma unroll
    for (int c = 0; c < 2; ++c)
#pragma unroll
      for (int e = 0; e < 2; ++e)
        res[c][e] = *(const u32x2*)(ws + WS_H + (size_t)(row[c][e] >> 4) * 18432 + (col[c][e] >> 5) * 1024 + ((col[c][e] >> 3) & 3) * 256 + (row[c][e] & 15) * 16 + ((col[c][e] >> 2) & 1) * 8);
    if (tid == 0) poll_count(cntB, 8u);
    SB();
    raw_bar();
    SB();
    const __amdgpu_buffer_rsrc_t prs = __builtin_amdgcn_make_buffer_rsrc((void*)(ws + WS_PART + (size_t)((pn * 4 + nq) * 8 + sb) * 8 * 9216), 0, 8 * 9216, 0x00020000);
#pragma unroll
    for (int c = 0; c < 2; ++c) {
      if (c < nc) {
        u32x4 pv[8];
#pragma unroll
        for (int k = 0; k < 8; ++k) pv[k] = __builtin_amdgcn_raw_buffer_load_b128(prs, lane * 16 + (c ? 8192 : wid * 1024), k * 9216, 16);
        float sum[8];
#pragma unroll
        for (int e = 0; e < 4; ++e) { sum[2 * e] = bf_lo(res[c][e >> 1][e & 1]); sum[2 * e + 1] = bf_hi(res[c][e >> 1][e & 1]); }
        float acc2[8];
#pragma unroll
        for (int e = 0; e < 8; ++e) acc2[e] = 0.f;
#pragma unroll
        for (int k = 0; k < 8; ++k)
#pragma unroll
          for (int e = 0; e < 4; ++e) { acc2[2 * e] += bf_lo(pv[k][e]); acc2[2 * e + 1] += bf_hi(pv[k][e]); }
#pragma unroll
        for (int e = 0; e < 2; ++e)
          *(u32x2*)(out + (size_t)row[c][e] * 1152 + col[c][e] * 2) =
              u32x2{cvt_pk(sum[4 * e] + acc2[4 * e], sum[4 * e + 1] + acc2[4 * e + 1]), cvt_pk(sum[4 * e + 2] + acc2[4 * e + 2], sum[4 * e + 3] + acc2[4 * e + 3])};
      }
    }
  }
  STAMP_END(2);
}

extern "C" void kernel_launch(void* const* d_in, const int* in_sizes, int n_in, void* d_out, int out_size, void* d_ws, size_t ws_size,
                              hipStream_t stream) {
  const char* x = (const char*)d_in[0];
  const char* wqkv = (const char*)d_in[1];
  const char* wo = (const char*)d_in[2];
  const char* wg = (const char*)d_in[4];
  const char* wu = (const char*)d_in[5];
  const char* wd = (const char*)d_in[6];
  char* ws = (char*)d_ws + WS_BASE(ws_size);
  (void)hipFuncSetAttribute((const void*)k1_ln_qkv, hipFuncAttributeMaxDynamicSharedMemorySize, K1_LDS);
  (void)hipFuncSetAttribute((const void*)k2_attn, hipFuncAttributeMaxDynamicSharedMemorySize, K2_LDS);
  (void)hipFuncSetAttribute((const void*)k4_gateup, hipFuncAttributeMaxDynamicSharedMemorySize, K4_LDS);
  k1_ln_qkv<<<234, 1024, K1_LDS, stream>>>(x, wqkv, ws, wo);
  k2_attn<<<256, 768, K2_LDS, stream>>>(ws, wg, wu, wo, x);
  k4_gateup<<<256, 512, K4_LDS, stream>>>(wg, wu, ws, wd, (char*)d_out);
}
```

```cpp
#include <hip/hip_runtime.h>
#include <hip/hip_bf16.h>
#include <stdint.h>
#include <utility>
#include <type_traits>

typedef __attribute__((ext_vector_type(8))) short bf16x8;
typedef __attribute__((ext_vector_type(4))) short s16x4;
typedef __attribute__((ext_vector_type(4))) float f32x4;
typedef __attribute__((ext_vector_type(16))) float f32x16;
typedef __attribute__((ext_vector_type(4))) unsigned u32x4;
typedef __attribute__((ext_vector_type(2))) unsigned u32x2;
typedef __attribute__((ext_vector_type(2))) __bf16 bf16x2_t;
typedef __attribute__((ext_vector_type(2))) float f32x2;
#define LDS_AS __attribute__((address_space(3)))
typedef LDS_AS char* lds_ptr;
#define DI __device__ __forceinline__

constexpr int SEQ = 2048, DM = 576, NH = 32, FF = 4608;
constexpr float LN_EPS = 1e-5f;
constexpr float QSCALE = 0.23570226039551584f * 1.4426950408889634f;

constexpr size_t WS_Q = 0;
constexpr size_t WS_K = WS_Q + (size_t)72 * 32768;
constexpr size_t WS_V = WS_K + (size_t)72 * 32768;
constexpr size_t WS_ONES = WS_V + (size_t)256 * 9216;
constexpr size_t WS_O = WS_ONES + 1024;
constexpr size_t WS_OX = WS_O + (size_t)128 * 18 * 1024;
constexpr size_t WS_H = WS_OX + (size_t)32 * 8192;
constexpr size_t WS_ST = WS_H + (size_t)128 * 18 * 1024;
constexpr size_t WS_CSP = WS_ST + (size_t)2048 * 80;
constexpr size_t WS_CS = WS_CSP + (size_t)8 * 9216 * 4;
constexpr size_t WS_HID = WS_CS + (size_t)9216 * 4;
constexpr size_t WS_CNT = WS_HID + (size_t)128 * 144 * 1024;
constexpr size_t WS_PART = WS_CNT + 24576;
constexpr size_t WS_END = WS_PART + (size_t)2048 * 9216;
static_assert(WS_O % 1024 == 0 && WS_H % 1024 == 0 && WS_ST % 256 == 0 && WS_CSP % 256 == 0 && WS_HID % 256 == 0, "align");

#define WS_BASE(sz) (((sz) - WS_END) & ~(size_t)0x1fffff)
#define STAMP_BEGIN
#define STAMP_MARK(k, i)
#define STAMP_MARKW(k, i, T)
#define STAMP_ENDW(k, T)
#define STAMP_CYC0
#define STAMP_CYC(k, i)
#define STAMP_END(k)

DI unsigned cvt_pk(float lo, float hi) {
  f32x2 v = {lo, hi};
  bf16x2_t r = __builtin_convertvector(v, bf16x2_t);
  return __builtin_bit_cast(unsigned, r);
}
DI float bf_lo(unsigned u) { return __builtin_bit_cast(float, u << 16); }
DI float bf_hi(unsigned u) { return __builtin_bit_cast(float, u & 0xffff0000u); }
DI float dot2(unsigned a, unsigned b, float c) {
  return __builtin_amdgcn_fdot2_f32_bf16(__builtin_bit_cast(bf16x2_t, a), __builtin_bit_cast(bf16x2_t, b), c, false);
}
template <int CTRL> DI float dpp_add(float v) {
  int t = __builtin_amdgcn_update_dpp(0, __builtin_bit_cast(int, v), CTRL, 0xf, 0xf, true);
  return v + __builtin_bit_cast(float, t);
}
DI float sum8(float v) {
  v = dpp_add<0xB1>(v);
  v = dpp_add<0x4E>(v);
  v = dpp_add<0x141>(v);
  return v;
}
#define GNOP "3"
DI void glds16(const void* sbase, unsigned voff, unsigned lds_dst) {
  asm volatile("s_mov_b32 m0, %2\n\ts_nop " GNOP "\n\tglobal_load_lds_dwordx4 %0, %1" ::"v"(voff), "s"(sbase), "s"(lds_dst) : "memory");
}
#define ST_AUX " sc1"
DI void st16(void* p, u32x4 v) { asm volatile("global_store_dwordx4 %0, %1, off" ST_AUX "\n\ts_nop 1" ::"v"(p), "v"(v) : "memory"); }
DI void st8(void* p, u32x2 v) { *(u32x2*)p = v; }
DI void st8wt(void* p, u32x2 v) { asm volatile("global_store_dwordx2 %0, %1, off sc1" ::"v"(p), "v"(v) : "memory"); }
DI void st4(void* p, unsigned v) { asm volatile("global_store_dword %0, %1, off sc1" ::"v"(p), "v"(v) : "memory"); }
DI void glds16_if0(int c, const void* sbase, unsigned voff, unsigned lds_dst) {
  asm volatile("s_cmp_lg_u32 %3, 0\n\ts_cbranch_scc1 1f\n\ts_mov_b32 m0, %2\n\ts_nop 1\n\tglobal_load_lds_dwordx4 %0, %1\n1:"
               ::"v"(voff), "s"(sbase), "s"(lds_dst), "s"(c) : "memory", "scc");
}
DI void touch_if0(int c, const void* sbase, unsigned voff, unsigned lds_dst) {
  asm volatile("s_cmp_lg_u32 %3, 0\n\ts_cbranch_scc1 1f\n\ts_mov_b32 m0, %2\n\ts_nop 1\n\tglobal_load_lds_dword %0, %1\n1:"
               ::"v"(voff), "s"(sbase), "s"(lds_dst), "s"(c) : "memory", "scc");
}
DI void touch_lines(const char* base, unsigned voff, unsigned lds_dummy) {
  asm volatile("s_mov_b32 m0, %2\n\ts_nop 3\n\tglobal_load_lds_dword %0, %1" ::"v"(voff), "s"(base), "s"(lds_dummy) : "memory");
}
#define CODE_TOUCH(SYM, BYTES, LDS_DUMMY)                                                                          \
  do {                                                                                                             \
    const char* cp_;                                                                                               \
    asm volatile("s_getpc_b64 s[20:21]\n\ts_add_u32 s20, s20, " SYM "@rel32@lo+4\n\ts_addc_u32 s21, s21, " SYM     \
                 "@rel32@hi+12\n\ts_mov_b64 %0, s[20:21]" : "=s"(cp_)::"s20", "s21", "scc");                      \
    constexpr int nl_ = (BYTES) / 128, per_ = (nl_ + 28) / 29;                                                      \
    const int ln_ = (blockIdx.x >> 3) * per_ + lane;                                                               \
    if (lane < per_ && ln_ < nl_) touch_lines(cp_, ln_ * 128, LDS_DUMMY);                                          \
  } while (0)
#define SYM_K1 "_Z9k1_ln_qkvPKcS0_PcS0_"
#define SYM_K2 "_Z7k2_attnPcPKcS1_S1_S1_"
#define SYM_K4 "_Z9k4_gateupPKcS0_PcS0_S1_"
constexpr int LEN_K1 = 21800, LEN_K2 = 12400, LEN_K4 = 30000;
template <int N> DI void wait_bar() { asm volatile("s_waitcnt vmcnt(%0) lgkmcnt(0)\n\ts_barrier" ::"n"(N) : "memory"); }
template <int N> DI void wait_vm() { asm volatile("s_waitcnt vmcnt(%0)" ::"n"(N) : "memory"); }
DI unsigned lds_arrive(lds_ptr c, int lane) {
  unsigned old = 0;
  if (lane == 0) old = __hip_atomic_fetch_add((LDS_AS unsigned*)c, 1u, __ATOMIC_RELAXED, __HIP_MEMORY_SCOPE_WORKGROUP);
  return __builtin_amdgcn_readfirstlane(old);
}
DI void lds_sync(lds_ptr c, unsigned n, int lane) {
  asm volatile("" ::: "memory");
  if (lds_arrive(c, lane) + 1 != n) {
    for (int spin = 0; spin < (1 << 20); ++spin) {
      const unsigned v = __builtin_amdgcn_readfirstlane(__hip_atomic_load((LDS_AS unsigned*)c, __ATOMIC_RELAXED, __HIP_MEMORY_SCOPE_WORKGROUP));
      if (v >= n) break;
      __builtin_amdgcn_s_sleep(1);
    }
  }
  asm volatile("" ::: "memory");
}
DI void lds_post(lds_ptr c, int lane) {
  asm volatile("" ::: "memory");
  if (lane == 0) (void)__hip_atomic_fetch_add((LDS_AS unsigned*)c, 1u, __ATOMIC_RELAXED, __HIP_MEMORY_SCOPE_WORKGROUP);
}
DI void lds_wait(lds_ptr c, unsigned n) {
  for (int spin = 0; spin < (1 << 20); ++spin) {
    const unsigned v = __builtin_amdgcn_readfirstlane(__hip_atomic_load((LDS_AS unsigned*)c, __ATOMIC_RELAXED, __HIP_MEMORY_SCOPE_WORKGROUP));
    if (v >= n) break;
    __builtin_amdgcn_s_sleep(1);
  }
  asm volatile("" ::: "memory");
}
template <class T> DI void pin(T& v) { asm volatile("" : "+v"(v)); }
DI void raw_bar() { asm volatile("s_barrier" ::: "memory"); }
DI void lds_bar() { asm volatile("s_waitcnt lgkmcnt(0)\n\ts_barrier" ::: "memory"); }
DI bf16x8 lds_r128(lds_ptr p) { return *(LDS_AS const bf16x8*)p; }
typedef short v4i16_t __attribute__((ext_vector_type(4)));
DI s16x4 lds_tr(lds_ptr p) { return __builtin_amdgcn_ds_read_tr16_b64_v4i16((LDS_AS v4i16_t*)p); }
DI bf16x8 cat8(s16x4 a, s16x4 b) { return __builtin_shufflevector(a, b, 0, 1, 2, 3, 4, 5, 6, 7); }
#define SB() __builtin_amdgcn_sched_barrier(0)
#define MFMA16(a, b, c) __builtin_amdgcn_mfma_f32_16x16x32_bf16(a, b, c, 0, 0, 0)
#define MFMA32(a, b, c) __builtin_amdgcn_mfma_f32_32x32x16_bf16(a, b, c, 0, 0, 0)
DI void mfma16_acc(f32x4& c, bf16x8 a, bf16x8 b) { asm volatile("v_mfma_f32_16x16x32_bf16 %0, %1, %2, %0" : "+v"(c) : "v"(a), "v"(b)); }
DI void mfma16_zero(f32x4& c, bf16x8 a, bf16x8 b) { asm volatile("v_mfma_f32_16x16x32_bf16 %0, %1, %2, 0" : "=&v"(c) : "v"(a), "v"(b)); }
template <class F, int... I> DI void for_seq_impl(F&& f, std::integer_sequence<int, I...>) { (f(std::integral_constant<int, I>{}), ...); }
template <int N, class F> DI void for_seq(F&& f) { for_seq_impl(f, std::make_integer_sequence<int, N>{}); }
DI int xcd_remap(int b, int nwg) {
  int q = nwg >> 3, r = nwg & 7, x = b & 7, o = b >> 3;
  return (x < r ? x * (q + 1) : r * (q + 1) + (x - r) * q) + o;
}

constexpr int K1_X = 0, K1_W = 40960, K1_SLOT = 12288, K1_ST = K1_W + K1_SLOT * 9, K1_CS = K1_W, K1_PF = K1_ST + 1024, K1_LDS = K1_PF + 1024;
constexpr int cmin(int a, int b) { return a < b ? a : b; }
constexpr int k1_nw(int kk) { return cmin(18, cmin(3 + 2 * kk, kk + 7)); }
constexpr int k1_nx(int kk) { return cmin(18, cmin(3 + 2 * kk, kk + 5)); }

template <bool ISV, bool HI>
DI void k1_consumer(lds_ptr lds, int cb, int lane, char* ws, int ct, int m0) {
  const int g = lane >> 4, q = (lane & 15) >> 2, p = lane & 3, r = lane & 15;
  constexpr int NM = HI ? 2 : 3, MB = HI ? 3 : 0;
  lds_ptr a0 = lds + K1_X + MB * 2048 + (r >> 3) * 1024 + (r & 7) * 128 + ((g ^ (r >> 1)) * 16);
  lds_ptr a1 = lds + K1_X + MB * 2048 + (r >> 3) * 1024 + (r & 7) * 128 + (((4 + g) ^ (r >> 1)) * 16);
  const int rho0 = 16 * (g >> 1) + 4 * (g & 1) + q;
  lds_ptr wb = lds + K1_W + rho0 * 128 + ((cb ^ ((rho0 >> 1) & 3)) * 32) + p * 8;
  lds_ptr wb5 = wb + 5 * K1_SLOT;
  asm volatile("" : "+v"(wb), "+v"(wb5));
  const u32x4 onesu = {0x3f803f80u, 0x3f803f80u, 0x3f803f80u, 0x3f803f80u};
  const bf16x8 ones = __builtin_bit_cast(bf16x8, onesu);
  f32x4 acc[3][3];
#pragma unroll
  for (int m = 0; m < 3; ++m)
#pragma unroll
    for (int j = 0; j < 3; ++j) acc[m][j] = f32x4{0.f, 0.f, 0.f, 0.f};
  bf16x8 fA[2][3];
  s16x4 wl[2][3], wh[2][3];
  auto rd1 = [&](auto KK, auto II) {
    constexpr int kk = KK, i = II, bf = kk & 1, sl = kk % 9;
    if constexpr (i >= 2 && i <= 4) {
      if constexpr (i - 2 < NM) fA[bf][i - 2] = lds_r128(((kk & 1) ? a1 : a0) + ((kk >> 1) & 3) * 10240 + (i - 2) * 2048);
    } else {
      constexpr int j = i < 2 ? 0 : (i - 3) / 2, hh = i < 2 ? i : (i - 3) % 2;
      lds_ptr bp = (sl >= 5 ? wb5 + (sl - 5) * K1_SLOT : wb + sl * K1_SLOT) + j * 4096 + hh * 1024;
      if constexpr (hh == 0) wl[bf][j] = lds_tr(bp); else wh[bf][j] = lds_tr(bp);
    }
  };
  lds_bar();
  STAMP_MARKW(0, 2, 512);
  for_seq<9>([&](auto I) { rd1(std::integral_constant<int, 0>{}, I); });
  SB();
  for_seq<18>([&](auto KK) {
    constexpr int kk = KK, bf = kk & 1;
    if constexpr (kk < 17) { raw_bar(); SB(); }
    for_seq<9>([&](auto II) {
      constexpr int i = II, j = i / 3, m = i % 3;
      const bf16x8 wv = cat8(wl[bf][j], wh[bf][j]);
      const bf16x8 av = (m < NM) ? fA[bf][m] : ones;
      acc[m][j] = ISV ? MFMA16(av, wv, acc[m][j]) : MFMA16(wv, av, acc[m][j]);
      SB();
      if constexpr (kk < 17) { rd1(std::integral_constant<int, kk + 1>{}, II); SB(); }
    });
  });
  STAMP_MARKW(0, 4, 512);
  lds_ptr cx = lds + K1_CS + cb * 3072 + lane * 16;
  if (HI) {
#pragma unroll
    for (int j = 0; j < 3; ++j) *(LDS_AS f32x4*)(cx + j * 1024) = acc[2][j];
  }
  lds_bar();
  STAMP_MARKW(0, 5, 512);
  f32x4 cs[3];
#pragma unroll
  for (int j = 0; j < 3; ++j) cs[j] = HI ? acc[2][j] : *(LDS_AS f32x4*)(cx + j * 1024);
  u32x2 o[NM * 3];
  if (ISV) {
#pragma unroll
    for (int m = 0; m < NM; ++m) {
      const f32x4 s0 = *(LDS_AS f32x4*)(lds + K1_ST + ((MB + m) * 16 + 4 * g) * 8), s1 = *(LDS_AS f32x4*)(lds + K1_ST + ((MB + m) * 16 + 4 * g) * 8 + 16);
#pragma unroll
      for (int j = 0; j < 3; ++j) {
        const float c0 = cs[j][0];
        o[3 * m + j] = u32x2{cvt_pk(fmaf(s0[0], c0, acc[m][j][0]) * s0[1], fmaf(s0[2], c0, acc[m][j][1]) * s0[3]),
                             cvt_pk(fmaf(s1[0], c0, acc[m][j][2]) * s1[1], fmaf(s1[2], c0, acc[m][j][3]) * s1[3])};
      }
    }
  } else {
    const float sc = ct < 3 ? QSCALE : 1.f;
#pragma unroll
    for (int m = 0; m < NM; ++m) {
      f32x2 st = *(LDS_AS f32x2*)(lds + K1_ST + ((MB + m) * 16 + r) * 8);
      st[1] *= sc;
#pragma unroll
      for (int j = 0; j < 3; ++j)
        o[3 * m + j] = u32x2{cvt_pk(fmaf(st[0], cs[j][0], acc[m][j][0]) * st[1], fmaf(st[0], cs[j][1], acc[m][j][1]) * st[1]),
                             cvt_pk(fmaf(st[0], cs[j][2], acc[m][j][2]) * st[1], fmaf(st[0], cs[j][3], acc[m][j][3]) * st[1])};
    }
  }
  char* base;
  int sm, sj, sel, half;
  if (ISV) { base = ws + WS_V + (size_t)(((m0 >> 4) + MB) * 2 + (g & 1)) * 9216 + ((ct - 6) * 192 + cb * 16 + r) * 16; sm = 18432; sj = 1024; sel = g >> 1; half = g >> 1; }
  else { base = ws + WS_Q + (size_t)(ct * 24 + cb * 2 + (g >> 1)) * 32768 + (m0 + MB * 16 + r) * 16; sm = 256; sj = 8 * 32768; sel = g & 1; half = g & 1; }
#pragma unroll
  for (int n = 0; n + 1 < NM * 3; n += 2) {
    u32x4 c;
#pragma unroll
    for (int e = 0; e < 2; ++e) {
      auto rr = ISV ? __builtin_amdgcn_permlane32_swap(o[n][e], o[n + 1][e], false, false) : __builtin_amdgcn_permlane16_swap(o[n][e], o[n + 1][e], false, false);
      c[e] = rr[0]; c[2 + e] = rr[1];
    }
    const int o0 = (n / 3) * sm + (n % 3) * sj, o1 = ((n + 1) / 3) * sm + ((n + 1) % 3) * sj;
    st16(base + (sel ? o1 : o0), c);
  }
  if (NM == 3) st8wt(base + 2 * sm + 2 * sj + half * 8, o[8]);
}

__global__ __launch_bounds__(1024) void k1_ln_qkv(const char* __restrict__ x, const char* __restrict__ wqkv, char* __restrict__ ws, const char* __restrict__ wo) {
  extern __shared__ __attribute__((aligned(16))) char smem[];
  STAMP_BEGIN
  lds_ptr lds = (lds_ptr)smem;
  const unsigned lds0 = (unsigned)(uintptr_t)lds;
  const int tid = threadIdx.x, lane = tid & 63, wid = __builtin_amdgcn_readfirstlane(tid >> 6);
  const int tile = xcd_remap(blockIdx.x, 234);
  const int cgp = tile / 78, rem = tile - cgp * 78;
  const int mt = rem / 3, ct = cgp * 3 + (rem - mt * 3);
  const int m0 = min(mt * 80, SEQ - 80);
  if (wid < 3) {
    unsigned wv[4];
#pragma unroll
    for (int i = 0; i < 4; ++i) {
      const int rho = 8 * i + (lane >> 3);
      const int gk = ((rho >> 4) & 1) * 2 + ((rho >> 2) & 1), tk = (rho >> 3) & 1, qk = rho & 3;
      wv[i] = (8 * gk + 4 * tk + qk) * 3456 + (((lane & 7) ^ (((rho >> 1) & 3) << 1)) * 16);
    }
    const char* wsrc = wqkv + ct * 384 + wid * 128;
    const unsigned wdst = lds0 + K1_W + wid * 4096;
    for_seq<18>([&](auto KK) {
      constexpr int kk = KK;
#pragma unroll
      for (int n = (kk == 0 ? 0 : k1_nw(kk - 1)); n < k1_nw(kk); ++n)
#pragma unroll
        for (int i = 0; i < 4; ++i) glds16(wsrc + (size_t)n * 32 * 3456, wv[i], wdst + (n % 9) * K1_SLOT + i * 1024);
      wait_bar<(k1_nw(kk) - kk - 1) * 4>();
    });
    return;
  }
  if (wid == 3) {
    const unsigned v0 = (lane >> 3) * 1152 + (((lane & 7) ^ (lane >> 4)) * 16);
    const unsigned v1 = (lane >> 3) * 1152 + (((lane & 7) ^ (4 + (lane >> 4))) * 16);
    const char* xs = x + (size_t)m0 * 1152;
    for_seq<18>([&](auto KK) {
      constexpr int kk = KK;
#pragma unroll
      for (int h = (kk == 0 ? 0 : k1_nx(kk - 1)); h < k1_nx(kk); ++h)
#pragma unroll
        for (int i = 0; i < 5; ++i) {
          const int pc = (h & 1) * 5 + i;
          glds16(xs + pc * 9216 + (h >> 1) * 128, (pc & 1) ? v1 : v0, lds0 + K1_X + ((h >> 1) & 3) * 10240 + pc * 1024);
        }
      wait_bar<(k1_nx(kk) - 2 * (kk >> 1) - 2) * 5>();
    });
    return;
  }
  if (wid < 8) {
    const int j = wid - 4;
    if (j == 0) CODE_TOUCH(SYM_K1, LEN_K1, lds0 + K1_PF);
    if (j == 1) CODE_TOUCH(SYM_K2, LEN_K2, lds0 + K1_PF + 256);
    if (j == 1 && blockIdx.x == 0 && lane < 4) *(unsigned*)(ws + WS_ONES + lane * 4) = 0x3f803f80u;
    if (j >= 1 && blockIdx.x == 0) *(unsigned*)(ws + WS_CNT + ((j - 1) * 64 + lane) * 128) = 0u;
    {
      const int ln = blockIdx.x * 168 + j * 42 + lane;
      if (lane < 42) {
        if (ln < 15552) touch_lines(wqkv, ln * 128, lds0 + K1_PF + j * 256);
        else if (ln < 33984) touch_lines(x, (ln - 15552) * 128, lds0 + K1_PF + j * 256);
        else if (ln < 39168) touch_lines(wo, (ln - 33984) * 128, lds0 + K1_PF + j * 256);
      }
    }
    lds_ptr p0 = lds + K1_X + j * 1024 + lane * 16, p2 = lds + K1_X + min(j + 8, 9) * 1024 + lane * 16;
    float s1[3] = {0.f, 0.f, 0.f}, s2[3] = {0.f, 0.f, 0.f};
#pragma unroll 1
    for (int s = 0; s < 9; ++s) {
      raw_bar();
      const int so = (s & 3) * 10240;
      u32x4 v[3] = {*(LDS_AS u32x4*)(p0 + so), *(LDS_AS u32x4*)(p0 + so + 4096), *(LDS_AS u32x4*)(p2 + so)};
      SB();
      raw_bar();
      SB();
#pragma unroll
      for (int i = 0; i < 3; ++i)
#pragma unroll
        for (int e = 0; e < 4; ++e) { s1[i] = dot2(v[i][e], 0x3f803f80u, s1[i]); s2[i] = dot2(v[i][e], v[i][e], s2[i]); }
      asm volatile("" : "+v"(s1[0]), "+v"(s1[1]), "+v"(s1[2]), "+v"(s2[0]), "+v"(s2[1]), "+v"(s2[2]));
    }
#pragma unroll
    for (int i = 0; i < 3; ++i) {
      const float a = sum8(s1[i]), b = sum8(s2[i]);
      const float mean = a * (1.f / 576.f);
      const float var = fmaxf(b * (1.f / 576.f) - mean * mean, 0.f);
      const int pc = i < 2 ? j + 4 * i : min(j + 8, 9);
      if ((lane & 7) == 0) *(LDS_AS f32x2*)(lds + K1_ST + (pc * 8 + (lane >> 3)) * 8) = f32x2{-mean, __builtin_amdgcn_rsqf(var + LN_EPS)};
    }
    lds_bar();
    return;
  }
  if (ct >= 6) {
    if (wid < 12) k1_consumer<true, false>(lds, wid - 8, lane, ws, ct, m0);
    else k1_consumer<true, true>(lds, wid - 12, lane, ws, ct, m0);
  } else {
    if (wid < 12) k1_consumer<false, false>(lds, wid - 8, lane, ws, ct, m0);
    else k1_consumer<false, true>(lds, wid - 12, lane, ws, ct, m0);
  }
  STAMP_ENDW(0, 512);
}

#define K2SYNC 0
#define K2_SL0 4
#define K2_SL1 4
constexpr int K2_KM = 0, K2_V = 24576, K2_SY = 44032, K2_CS = 44288, K2_W = K2_CS + 4864, K2_LDS = K2_W + 73728;

#define MFMA32K8(a, b, c) __builtin_amdgcn_mfma_f32_32x32x8bf16_1k(a, b, c, 0, 0, 0)
DI void k2_core(lds_ptr lds, char* ws, int hd, int t, int wid, int lane, u32x4 q0u, u32x2 q1u, bool first) {
  const int hi = lane >> 5, l31 = lane & 31;
  const int dlo = ((18 * hd) & 7) >> 1;
#pragma unroll
  for (int e = 0; e < 4; ++e) q0u[e] = (4 * hi + e >= dlo) ? q0u[e] : 0u;
#pragma unroll
  for (int e = 0; e < 2; ++e) q1u[e] = (8 + 2 * hi + e < dlo + 9) ? q1u[e] : 0u;
  const bf16x8 q0 = __builtin_bit_cast(bf16x8, q0u);
  const s16x4 q1 = __builtin_bit_cast(s16x4, q1u);
  f32x16 S[9];
  lds_ptr kmp = lds + K2_KM + hi * 8192 + (wid * 32 + l31) * 16;
  lds_ptr kxp = lds + K2_KM + 16384 + (wid * 32 + l31) * 16 + hi * 8;
  const float NEG = -1e30f;
  const int dq = l31 - 4 * hi;
  float mx0 = NEG, mx1 = NEG;
  bf16x8 kf[3];
  s16x4 kx[3];
  auto rdk = [&](auto NN) {
    constexpr int n = NN, i = n == 0 ? 8 : n - 1;
    kf[n % 3] = lds_r128(kmp + i * 512);
    kx[n % 3] = *(LDS_AS const s16x4*)(kxp + i * 512);
  };
  auto post = [&](auto NN) {
    constexpr int n = NN, i = n == 0 ? 8 : n - 1;
    if constexpr (i < 8) {
      if constexpr (i == 0) {
#pragma unroll
        for (int r = 0; r < 16; ++r) S[0][r] = (((r & 3) + 8 * (r >> 2)) <= dq) ? S[8][r] : S[0][r];
      }
#pragma unroll
      for (int r = 0; r < 16; r += 4) {
        mx0 = fmaxf(fmaxf(mx0, S[i][r]), S[i][r + 1]);
        mx1 = fmaxf(fmaxf(mx1, S[i][r + 2]), S[i][r + 3]);
      }
    }
  };
  rdk(std::integral_constant<int, 0>{});
  rdk(std::integral_constant<int, 1>{});
  SB();
  for_seq<9>([&](auto NN) {
    constexpr int n = NN, i = n == 0 ? 8 : n - 1;
    const f32x16 z = {};
    S[i] = MFMA32(kf[n % 3], q0, z);
    SB();
    if constexpr (n + 2 < 9) { rdk(std::integral_constant<int, n + 2>{}); SB(); }
    S[i] = MFMA32K8(kx[n % 3], q1, S[i]);
    SB();
    if constexpr (n >= 1) { post(std::integral_constant<int, n - 1>{}); SB(); }
  });
  post(std::integral_constant<int, 8>{});
  float mx = fmaxf(mx0, mx1);
  {
    auto rr = __builtin_amdgcn_permlane32_swap(__float_as_uint(mx), __float_as_uint(mx), false, false);
    mx = fmaxf(__uint_as_float(rr[0]), __uint_as_float(rr[1]));
  }
  s16x4 mq;
  {
    const float nm = -mx;
    const unsigned h1 = __float_as_uint(nm) & 0xffff0000u;
    const float r1 = nm - __uint_as_float(h1);
    const unsigned h2 = __float_as_uint(r1) & 0xffff0000u;
    const float r2 = r1 - __uint_as_float(h2);
    const u32x2 mu = {hi ? 0u : ((h1 >> 16) | h2), hi ? 0u : ((__float_as_uint(r2) >> 16) | 0xf1490000u)};
    mq = __builtin_bit_cast(s16x4, mu);
  }
  auto mk1 = [&](int i) { return __builtin_bit_cast(s16x4, u32x2{0x3f803f80u, (first && i > 0 && wid + i < 8) ? 0x3f803f80u : 0x00003f80u}); };
  S[1] = MFMA32K8(mk1(1), mq, S[1]);
  S[2] = MFMA32K8(mk1(2), mq, S[2]);
  SB();
  lds_ptr vp = lds + K2_V + wid * 2 * 592 + (l31 < 18 ? hi * 288 + l31 * 16 : 576);
  f32x16 O;
  const unsigned keep0 = first ? 0u : ~0u;
  bf16x8 vf[3];
  auto rdv = [&](auto EE) {
    constexpr int e = EE, blk = e < 14 ? e + 2 : ((e - 14) >> 1) + ((e & 1) ? 16 : 0);
    if constexpr (e < 18) vf[e % 3] = lds_r128(vp + blk * 592);
  };
  rdv(std::integral_constant<int, 0>{});
  rdv(std::integral_constant<int, 1>{});
  SB();
  for_seq<16>([&](auto HH) {
    constexpr int h = HH, i = ((h >> 1) + 1) & 7, s = h & 1;
    u32x4 P;
#pragma unroll
    for (int j = 0; j < 4; ++j) P[j] = cvt_pk(__builtin_amdgcn_exp2f(S[i][8 * s + 2 * j]), __builtin_amdgcn_exp2f(S[i][8 * s + 2 * j + 1]));
    if constexpr (i == 0) {
      constexpr int e = 14 + 2 * s;
      u32x4 P8, P0;
#pragma unroll
      for (int j = 0; j < 4; ++j) {
        const int r0 = 8 * s + 2 * j, r1 = r0 + 1;
        const unsigned mk = ((((r0 & 3) + 8 * (r0 >> 2)) <= dq) ? 0xffffu : 0u) | ((((r1 & 3) + 8 * (r1 >> 2)) <= dq) ? 0xffff0000u : 0u);
        P8[j] = P[j] & mk;
        P0[j] = P[j] & ~mk & keep0;
      }
      rdv(std::integral_constant<int, e + 2>{});
      SB();
      O = MFMA32(vf[e % 3], __builtin_bit_cast(bf16x8, P0), O);
      SB();
      rdv(std::integral_constant<int, e + 3>{});
      O = MFMA32(vf[(e + 1) % 3], __builtin_bit_cast(bf16x8, P8), O);
      SB();
    } else {
      rdv(std::integral_constant<int, h + 2>{});
      SB();
      if constexpr (h == 0) { const f32x16 z = {}; O = MFMA32(vf[0], __builtin_bit_cast(bf16x8, P), z); }
      else O = MFMA32(vf[h % 3], __builtin_bit_cast(bf16x8, P), O);
      if constexpr (s == 0 && i + 2 <= 8) S[(i + 2) & 7] = MFMA32K8(mk1((i + 2) & 7), mq, S[(i + 2) & 7]);
      SB();
    }
  });
  const float inv = __builtin_amdgcn_rcpf(O[10]);
  const int row = t * 32 + l31;
  char* ob = ws + WS_O + (size_t)(row >> 4) * 18 * 1024 + (row & 15) * 16;
  u32x4 o = {cvt_pk(O[0] * inv, O[1] * inv), cvt_pk(O[2] * inv, O[3] * inv), cvt_pk(O[4] * inv, O[5] * inv), cvt_pk(O[6] * inv, O[7] * inv)};
  st16(ob + (hd >> 1) * 1024 + (2 * (hd & 1) + hi) * 256, o);
  if (hi == 0) st4(ws + WS_OX + hd * 8192 + row * 4, cvt_pk(O[8] * inv, O[9] * inv));
}

__global__ __launch_bounds__(768) void k2_attn(char* __restrict__ ws, const char* __restrict__ wg, const char* __restrict__ wu,
                                               const char* __restrict__ wo, const char* __restrict__ x) {
  extern __shared__ __attribute__((aligned(16))) char smem[];
  STAMP_BEGIN
  lds_ptr lds = (lds_ptr)smem;
  const unsigned lds0 = (unsigned)(uintptr_t)lds;
  const int tid = threadIdx.x, lane = tid & 63, wid = __builtin_amdgcn_readfirstlane(tid >> 6);
  const int hd = (blockIdx.x & 7) * 4 + ((blockIdx.x >> 3) & 3), qb = blockIdx.x >> 5;
  const bool has_tile = blockIdx.x < 234;
  const int tile = has_tile ? xcd_remap(blockIdx.x, 234) : 0;
  const int mt = tile / 9, nt = tile - mt * 9;
  const int m0 = min(mt * 80, SEQ - 80), rb0 = m0 >> 4;
  if (wid == 11) {
    if (lane < 4) *(LDS_AS unsigned*)(lds + K2_SY + lane * 4) = 0u;
  }
  if (wid >= 10) {
    lds_bar();
    __builtin_amdgcn_s_sleep(K2_SL1);
    const int sw = wid - 10;
    if (sw == 0) CODE_TOUCH(SYM_K4, LEN_K4, lds0 + K2_CS + 4096);
    const bool act = blockIdx.x < 216;
    const int unit = act ? (int)blockIdx.x * 2 + sw : 0, part = unit / 144, c64 = unit - part * 144;
    const char* wsrc = (c64 < 72 ? wg + c64 * 128 : wu + (c64 - 72) * 128) + (lane & 7) * 16 + (size_t)(part * 192 + (lane >> 3)) * 9216;
    u32x4 v[24];
    if (act) {
#pragma unroll
      for (int i = 0; i < 24; ++i) v[i] = *(const u32x4*)(wsrc + (size_t)i * 8 * 9216);
    }
    if (act) {
      float cs[8];
#pragma unroll
      for (int j = 0; j < 8; ++j) cs[j] = 0.f;
#pragma unroll
      for (int i = 0; i < 24; ++i)
#pragma unroll
        for (int j = 0; j < 4; ++j) {
          cs[2 * j] += bf_lo(v[i][j]); cs[2 * j + 1] += bf_hi(v[i][j]);
        }
      lds_ptr cp = lds + K2_CS + sw * 2048;
      *(LDS_AS f32x4*)(cp + lane * 32) = f32x4{cs[0], cs[1], cs[2], cs[3]};
      *(LDS_AS f32x4*)(cp + lane * 32 + 16) = f32x4{cs[4], cs[5], cs[6], cs[7]};
      asm volatile("s_waitcnt lgkmcnt(0)" ::: "memory");
      float t = 0.f;
#pragma unroll
      for (int i = 0; i < 8; ++i) t += *(LDS_AS float*)(cp + i * 256 + lane * 4);
      st4(ws + WS_CSP + ((size_t)part * 9216 + c64 * 64 + lane) * 4, __float_as_uint(t));
    }
    if (has_tile) { raw_bar(); raw_bar(); }
    return;
  }
  if (wid >= 8) {
    raw_bar();
    __builtin_amdgcn_s_sleep(K2_SL0);
    if (has_tile) {
      const int par = wid - 8;
      unsigned va[4], vb[4];
#pragma unroll
      for (int i = 0; i < 4; ++i) {
        const int rho = 8 * i + (lane >> 3);
        const int g_ = ((rho >> 4) & 1) * 2 + ((rho >> 2) & 1), t_ = (rho >> 3) & 1, q_ = rho & 3;
        const int La = 36 * par + 18 * (g_ >> 1) + 8 * t_ + 4 * (g_ & 1) + q_, Lb = 288 * par + 18 * (4 * g_ + 2 * t_ + (q_ >> 1)) + 16 + (q_ & 1);
        const unsigned csw = ((lane & 7) ^ (((rho >> 1) & 3) << 1)) * 16;
        va[i] = La * 1152 + csw; vb[i] = Lb * 1152 + csw;
      }
#pragma unroll
      for (int kp = 0; kp < 9; ++kp)
#pragma unroll
        for (int i = 0; i < 4; ++i)
          glds16(wo + nt * 128 + (size_t)(kp == 8 ? 0 : 72 * kp) * 1152, kp == 8 ? vb[i] : va[i], lds0 + K2_W + par * 4096 + kp * 8192 + i * 1024);
    }
  } else {
    const int t = qb * 8 + wid;
    u32x4 q0;
    u32x2 q1;
    const int c0 = (18 * hd) >> 3;
    asm volatile("global_load_dwordx4 %0, %2, %4\n\tglobal_load_dwordx2 %1, %3, %4"
                 : "=&v"(q0), "=&v"(q1)
                 : "v"((lane >> 5) * 32768 + (lane & 31) * 16), "v"(65536 + (lane & 31) * 16 + (lane >> 5) * 8),
                   "s"(ws + WS_Q + (size_t)c0 * 32768 + t * 512)
                 : "memory");
    const int key0 = max(qb * 256 - 256 + wid * 64, 0);
#pragma unroll
    for (int i = 0; i < 3; ++i) glds16(ws + WS_K + (size_t)(c0 + i) * 32768 + key0 * 16, (qb == 0 && wid < 4) ? 0 : lane * 16, lds0 + K2_KM + i * 8192 + wid * 1024);
#pragma unroll
    for (int i = 0; i < 3; ++i) {
      const int pc = wid + 8 * i;
      if (pc < 19) {
        const int u = min(pc * 64 + lane, 32 * 37 - 1), bl = u / 37, w = u - bl * 37;
        const int blk = max(qb * 16 - 16 + bl, 0);
        const unsigned vo = w == 36 ? (unsigned)(WS_ONES - WS_V) : (blk * 2 + (w >= 18)) * 9216 + (hd * 18 + (w >= 18 ? w - 18 : w)) * 16;
        glds16(ws + WS_V, vo, lds0 + K2_V + pc * 1024);
      }
    }
    raw_bar();
    asm volatile("s_waitcnt vmcnt(0)" : "+v"(q0), "+v"(q1)::"memory");
    lds_sync(lds + K2_SY, 8, lane);
    STAMP_MARK(1, 2);
    k2_core(lds, ws, hd, t, wid, lane, q0, q1, qb == 0);
    STAMP_MARK(1, 3);
    STAMP_MARKW(1, 7, 448);
  }
  wait_vm<0>();
  STAMP_MARK(1, 4);
  unsigned* cnt = (unsigned*)(ws + WS_CNT);
  if (wid < 8) {
    if (lds_arrive(lds + K2_SY + 8, lane) == 7 && lane < 8) __hip_atomic_fetch_add(cnt + (qb * 8 + lane) * 32, 1u, __ATOMIC_RELAXED, __HIP_MEMORY_SCOPE_AGENT);
  }
  if (!has_tile) { STAMP_END(1); return; }
  lds_sync(lds + K2_SY + 12, 10, lane);
  const int mi = wid >> 1, kg = wid & 1;
  const int g = lane >> 4, q = (lane & 15) >> 2, p = lane & 3;
  const int er = tid >> 3, ec = tid & 7;
  const u32x4 res = *(const u32x4*)(x + (size_t)(m0 + er) * 1152 + (nt * 64 + ec * 8) * 2);
  const int rho0 = 16 * (g >> 1) + 4 * (g & 1) + q;
  lds_ptr wb[4];
#pragma unroll
  for (int n = 0; n < 4; ++n) { wb[n] = lds + K2_W + kg * 4096 + rho0 * 128 + ((n ^ ((rho0 >> 1) & 3)) * 32) + p * 8; asm volatile("" : "+v"(wb[n])); }
  bf16x8 wf[5][4];
#pragma unroll
  for (int j = 0; j < 5; ++j)
#pragma unroll
    for (int n = 0; n < 4; ++n) wf[j][n] = cat8(lds_tr(wb[n] + j * 8192), lds_tr(wb[n] + j * 8192 + 1024));
  if (tid == 576) {
    const unsigned* c0 = cnt + ((m0 >> 8) * 8 + (tile & 7)) * 32;
    const unsigned* c1 = cnt + (((m0 + 79) >> 8) * 8 + (tile & 7)) * 32;
    for (int spin = 0; spin < (1 << 18); ++spin) {
      const unsigned a = __hip_atomic_load(c0, __ATOMIC_RELAXED, __HIP_MEMORY_SCOPE_AGENT);
      const unsigned b = __hip_atomic_load(c1, __ATOMIC_RELAXED, __HIP_MEMORY_SCOPE_AGENT);
      if (a >= 32u && b >= 32u) break;
      __builtin_amdgcn_s_sleep(1);
    }
  }
  SB();
  raw_bar();
  SB();
  STAMP_MARK(1, 5);
  const __amdgpu_buffer_rsrc_t ors = __builtin_amdgcn_make_buffer_rsrc((void*)(ws + WS_O), 0, 128 * 18432 + 32 * 8192, 0x00020000);
  const int aoff = (rb0 + mi) * 18432 + kg * 1024 + lane * 16;
  bf16x8 a[9];
#pragma unroll
  for (int i = 0; i < 8; ++i) a[i] = __builtin_bit_cast(bf16x8, __builtin_amdgcn_raw_buffer_load_b128(ors, aoff + i * 2048, 0, 16));
  {
    u32x4 ax;
    const int xo = 128 * 18432 + (kg * 16 + 4 * g) * 8192 + (m0 + mi * 16 + (lane & 15)) * 4;
#pragma unroll
    for (int e = 0; e < 4; ++e) ax[e] = __builtin_amdgcn_raw_buffer_load_b32(ors, xo, e * 8192, 16);
    a[8] = __builtin_bit_cast(bf16x8, ax);
  }
  SB();
  f32x4 acc[4];
#pragma unroll
  for (int n = 0; n < 4; ++n) acc[n] = f32x4{0.f, 0.f, 0.f, 0.f};
#pragma unroll
  for (int j = 0; j < 9; ++j) {
#pragma unroll
    for (int n = 0; n < 4; ++n) {
      acc[n] = MFMA16(wf[j % 5][n], a[j], acc[n]);
      if (j + 5 < 9) wf[j % 5][n] = cat8(lds_tr(wb[n] + (j + 5) * 8192),
                                         lds_tr(wb[n] + (j + 5) * 8192 + 1024));
    }
  }
  STAMP_MARK(1, 6);
  {
    lds_ptr cw = lds + kg * (80 * 272) + (mi * 16 + (lane & 15)) * 272 + 16 * g;
#pragma unroll
    for (int n = 0; n < 4; ++n) *(LDS_AS f32x4*)(cw + n * 64) = acc[n];
  }
  lds_bar();
  {
    lds_ptr cr = lds + er * 272 + ec * 32;
    f32x4 a0 = *(LDS_AS f32x4*)(cr), a1 = *(LDS_AS f32x4*)(cr + 16);
    f32x4 b0 = *(LDS_AS f32x4*)(cr + 80 * 272), b1 = *(LDS_AS f32x4*)(cr + 80 * 272 + 16);
    a0 += b0; a1 += b1;
    u32x4 o;
    o[0] = cvt_pk(a0[0] + bf_lo(res[0]), a0[1] + bf_hi(res[0]));
    o[1] = cvt_pk(a0[2] + bf_lo(res[1]), a0[3] + bf_hi(res[1]));
    o[2] = cvt_pk(a1[0] + bf_lo(res[2]), a1[1] + bf_hi(res[2]));
    o[3] = cvt_pk(a1[2] + bf_lo(res[3]), a1[3] + bf_hi(res[3]));
    const int row = m0 + er, col = nt * 64 + ec * 8;
    st16(ws + WS_H + (size_t)(row >> 4) * 18432 + (col >> 5) * 1024 + ((col >> 3) & 3) * 256 + (row & 15) * 16, o);
    float s1 = 0.f, s2 = 0.f;
#pragma unroll
    for (int j = 0; j < 4; ++j) { s1 = dot2(o[j], 0x3f803f80u, s1); s2 = dot2(o[j], o[j], s2); }
    s1 = sum8(s1); s2 = sum8(s2);
    if (ec == 0) st8wt(ws + WS_ST + (size_t)nt * 16384 + row * 8, u32x2{__float_as_uint(s1), __float_as_uint(s2)});
  }
  STAMP_END(1);
}

constexpr int K4_SLOT = 18432, K4_ST = K4_SLOT * 8, K4_CS = K4_ST + 2048, K4_PF = K4_CS + 2048, K4_CP = K4_PF + 1024, K4_SY = 160000, K4_LDS = 160 * 1024;

constexpr int k4_frag(int i) { return i < 16 ? ((i >> 2) * 2 + (i & 1) + ((i & 2) ? 9 : 0)) : (i == 16 ? 8 : 17); }
constexpr int k4_foff(int f) { return (f >= 9 ? 9216 + (f - 9) * 32 : f * 32); }
constexpr int K4_T0 = 13 * 18;
constexpr int k4_slab(int G) { return G < K4_T0 ? G / 18 : (G - K4_T0 < 80 ? 13 + ((G - K4_T0) % 20) / 4 : 13 + (G - K4_T0 - 80) / 2); }
constexpr int k4_pos(int G) { return G < K4_T0 ? G % 18 : (G - K4_T0 < 80 ? ((G - K4_T0) / 20) * 4 + ((G - K4_T0) & 3) : 16 + ((G - K4_T0) & 1)); }
constexpr int k4_op(int G) {
  const int s = G / 18, i = G % 18;
  if (s > 12) return 0;
  if (s < 3) {
    const int pos[10] = {0, 2, 4, 6, 7, 9, 11, 13, 15, 16};
    for (int k = 0; k < 10; ++k) if (pos[k] == i) return 1 + (2 * s + 2 + k / 5) * 8 + k % 5;
    return 0;
  }
  if (s == 3) {
    const int pos[8] = {0, 2, 4, 6, 8, 11, 14, 16};
    for (int k = 0; k < 8; ++k) if (pos[k] == i) return k < 5 ? 1 + 8 * 8 + k : 1 + 9 * 8 + (k - 3);
    return 0;
  }
  if (i == 1) return 1 + (s + 5) * 8;
  if (i == 4) return 1 + (s + 5) * 8 + 1;
  if (s + 6 > 17) return 0;
  if (i == 8) return 1 + (s + 6) * 8 + 2;
  if (i == 13) return 1 + (s + 6) * 8 + 3;
  if (i == 16) return 1 + (s + 6) * 8 + 4;
  return 0;
}
constexpr int k4_allow(int G, int need) {
  int n = (need == 0) ? 4 : 0;
  for (int g = 0; g < G; ++g) {
    const int op = k4_op(g);
    if (op == 0) continue;
    const int sl = (op - 1) >> 3, kind = (op - 1) & 7;
    if (sl <= need) n = 0;
    else if (kind != 4) ++n;
  }
  return n;
}
constexpr int k4_need(int G) {
  if (G + 5 >= 324 || G == 0) return 0;
  int mx = 0;
  for (int g = 0; g < G + 5; ++g) mx = k4_slab(g) > mx ? k4_slab(g) : mx;
  return k4_slab(G + 5) > mx ? k4_slab(G + 5) : 0;
}

constexpr int d_off(int i) { return i < 6 ? 36864 + i * 9216 : (i < 10 ? (i - 6) * 9216 : (i < 17 ? 92160 + (i - 10) * 9216 : 36864)); }
#define OUT_ST(p, v) st16(p, v)
#define DT 8
constexpr int D_G0 = (18 - DT) * 9;
constexpr int d_i(int H) { return H < D_G0 ? H / 9 : (H - D_G0 < 8 * DT ? 18 - DT + ((H - D_G0) % (2 * DT)) / 2 : 18 - DT + (H - D_G0 - 8 * DT)); }
constexpr int d_f(int H) { return H < D_G0 ? H % 9 : (H - D_G0 < 8 * DT ? ((H - D_G0) / (2 * DT)) * 2 + ((H - D_G0) & 1) : 8); }
constexpr int d_first(int H) {
  for (int h = 0; h < H; ++h) if (d_i(h) == d_i(H)) return 0;
  return 1;
}
DI void ld2_sc1(bf16x8& d0, bf16x8& d1, unsigned o0, unsigned o1, const char* base) {
  asm volatile("global_load_dwordx4 %0, %2, %4 sc1\n\tglobal_load_dwordx4 %1, %3, %4 sc1" : "=&v"(d0), "=&v"(d1) : "v"(o0), "v"(o1), "s"(base) : "memory");
}
template <int N> DI void wait_vm2(bf16x8& d0, bf16x8& d1) { asm volatile("s_waitcnt vmcnt(%2)" : "+v"(d0), "+v"(d1) : "n"(N) : "memory"); }
DI void poll_count(const unsigned* c, unsigned n) {
  for (int spin = 0; spin < (1 << 18); ++spin) {
    if (__hip_atomic_load(c, __ATOMIC_RELAXED, __HIP_MEMORY_SCOPE_AGENT) >= n) break;
    __builtin_amdgcn_s_sleep(1);
  }
}

__global__ __launch_bounds__(512, 2) void k4_gateup(const char* __restrict__ wgate, const char* __restrict__ wup, char* __restrict__ ws,
                                                    const char* __restrict__ wd, char* __restrict__ out) {
  extern __shared__ __attribute__((aligned(16))) char smem[];
  STAMP_BEGIN
  lds_ptr lds = (lds_ptr)smem;
  const unsigned lds0 = (unsigned)(uintptr_t)lds;
  const int tid = threadIdx.x, lane = tid & 63, wid = __builtin_amdgcn_readfirstlane(tid >> 6);
  const int xcd = blockIdx.x & 7, idx = blockIdx.x >> 3;
  const int pn = 4 * (xcd >> 2) + (idx & 3), jt = 8 * (xcd & 3) + (idx >> 2);
  const int wm = wid & 3, wn = wid >> 2;
  const char* asrc = ws + WS_H + (size_t)(pn * 16 + 2 * wid) * 18432;
  const unsigned avo = lane * 16, avo2 = lane * 16 + 18432;
  const char* wsrc = (wn ? wup : wgate) + jt * 288;
  unsigned wvo[3];
#pragma unroll
  for (int i = 0; i < 3; ++i) {
    const int pc = i < 2 ? 2 * wm + i : 8;
    const int o = pc * 1024 + lane * 16;
    const int rho = o / 288, u = (o - rho * 288) >> 4;
    const int gk = ((rho >> 4) & 1) * 2 + ((rho >> 2) & 1), tk = (rho >> 3) & 1, qk = rho & 3;
    wvo[i] = (8 * gk + 4 * tk + qk) * 9216 + u * 16;
  }
  const unsigned wdst = lds0 + wn * 9216;
  auto issue_w = [&](int s, int i) {
    const unsigned so = (s & 7) * K4_SLOT;
    const char* wp = wsrc + (size_t)s * 32 * 9216;
    if (i < 2) glds16(wp, wvo[i], wdst + so + wm * 2048 + i * 1024);
    else glds16_if0(wm, wp, wvo[2], wdst + so + 8192);
  };
  bf16x8 A[6][2];
  auto issue_a = [&](int s, bf16x8& dst, int m) {
    asm volatile("global_load_dwordx4 %0, %1, %2" : "=v"(dst) : "v"(m ? avo2 : avo), "s"(asrc + s * 1024) : "memory");
  };
  f32x2 sp[5];
  {
    const char* stp = ws + WS_ST + (size_t)(pn * 256 + wid * 32) * 8;
    const unsigned so = (lane & 31) * 8 + (lane >> 5) * 5 * 16384;
    asm volatile("global_load_dwordx2 %0, %5, %7\n\tglobal_load_dwordx2 %1, %5, %8\n\tglobal_load_dwordx2 %2, %5, %9\n\t"
                 "global_load_dwordx2 %3, %5, %10\n\tglobal_load_dwordx2 %4, %6, %10"
                 : "=&v"(sp[0]), "=&v"(sp[1]), "=&v"(sp[2]), "=&v"(sp[3]), "=&v"(sp[4])
                 : "v"(so), "v"((lane & 31) * 8 + 16384), "s"(stp), "s"(stp + 16384), "s"(stp + 32768), "s"(stp + 49152) : "memory");
  }
  if (wm < 3 && lane < 36) glds16(ws + WS_CSP + (size_t)(wm * 9216 + wn * FF + jt * 144) * 4, lane * 16, lds0 + K4_CP + wm * 1152 + wn * 576);
  for_seq<2>([&](auto S) { issue_w(S, 0); issue_w(S, 1); issue_w(S, 2); issue_a(S, A[S][0], 0); issue_a(S, A[S][1], 1); });
  {
    asm volatile("s_waitcnt vmcnt(8)" : "+v"(sp[0]), "+v"(sp[1]), "+v"(sp[2]), "+v"(sp[3]), "+v"(sp[4])::"memory");
    const f32x2 t4 = (sp[0] + sp[1]) + (sp[2] + sp[3]);
    auto r1 = __builtin_amdgcn_permlane32_swap(__float_as_uint(t4[0]), __float_as_uint(t4[0]), false, false);
    auto r2 = __builtin_amdgcn_permlane32_swap(__float_as_uint(t4[1]), __float_as_uint(t4[1]), false, false);
    const float s1 = __uint_as_float(r1[0]) + __uint_as_float(r1[1]) + sp[4][0];
    const float s2 = __uint_as_float(r2[0]) + __uint_as_float(r2[1]) + sp[4][1];
    const float mean = s1 * (1.f / 576.f);
    const float var = fmaxf(s2 * (1.f / 576.f) - mean * mean, 0.f);
    *(LDS_AS f32x2*)(lds + K4_ST + (wid * 32 + (lane & 31)) * 8) = f32x2{-mean, __builtin_amdgcn_rsqf(var + LN_EPS)};
    *(LDS_AS unsigned*)(lds + K4_SY) = 0u;
    SB();
  }
  const int g = lane >> 4, q = (lane & 15) >> 2, p = lane & 3, r = lane & 15;
  lds_ptr wb = lds + (16 * (g >> 1) + 4 * (g & 1) + q) * 288 + p * 8;
  lds_ptr wbs[3] = {wb, wb + 3 * K4_SLOT, wb + 6 * K4_SLOT};
  asm volatile("" : "+v"(wbs[1]), "+v"(wbs[2]));
  lds_ptr csb = lds + K4_CS + 16 * g;
  asm volatile("" : "+v"(csb));
  f32x4 acc[2][18];
  asm volatile("s_waitcnt vmcnt(%2)\n\ts_barrier" : "+v"(A[0][0]), "+v"(A[0][1]) : "n"(k4_allow(0, 0)) : "memory");
  STAMP_MARK(2, 2);
  STAMP_CYC0
  s16x4 Bl[6], Bh[6];
  for_seq<5>([&](auto I) { Bl[I] = lds_tr(wb + k4_foff(k4_frag(I))); Bh[I] = lds_tr(wb + k4_foff(k4_frag(I)) + 2304); });
  SB();
  const float L2E = 1.4426950408889634f;
  char* hb = ws + WS_HID + (size_t)(pn * 16 + wid * 2) * (144 * 1024) + (size_t)(jt >> 2) * 18432;
  f32x2 st[2];
  float c1[2];
  float sd[2];
  auto glu = [&](float ag, float au, int m) -> float {
    return au * __builtin_amdgcn_rcpf(fmaf(__builtin_amdgcn_exp2f(ag * c1[m]), sd[m], sd[m]));
  };
  f32x4 cv;
  u32x4 own[4][2];
  float eg[2][2];
  auto micro = [&](auto QQ, auto KK, auto JJ) {
    constexpr int Q = QQ, k = KK, j = JJ, m = k >> 2, d = k & 3, h = d >> 1, e = (d & 1) * 2, b = k & 1;
#pragma unroll
    for (int x = 0; x < 2; ++x) {
      if constexpr (j == 0) eg[b][x] = acc[m][2 * Q + h][e + x] * c1[m];
      if constexpr (j == 1) eg[b][x] = __builtin_amdgcn_exp2f(eg[b][x]);
      if constexpr (j == 2) eg[b][x] = fmaf(eg[b][x], sd[m], sd[m]);
      if constexpr (j == 3) eg[b][x] = __builtin_amdgcn_rcpf(eg[b][x]);
      if constexpr (j == 4) eg[b][x] *= acc[m][9 + 2 * Q + h][e + x];
      if constexpr (j < 5) pin(eg[b][x]);
    }
    if constexpr (j == 6) {
      own[Q][m][d] = cvt_pk(eg[b][0], eg[b][1]);
      if constexpr (d == 3) st16(hb + (size_t)m * (144 * 1024) + (4 * (jt & 3) + Q) * 1024 + lane * 16, own[Q][m]);
      else pin(own[Q][m]);
    }
  };
  auto gapops = [&](auto QQ, auto GG) {
    constexpr int gg = GG;
    if constexpr (gg >= 4 && gg < 36) micro(QQ, std::integral_constant<int, gg / 4 - 1>{}, std::integral_constant<int, 4 + gg % 4>{});
    if constexpr (gg >= 0 && gg < 32) micro(QQ, std::integral_constant<int, gg / 4>{}, std::integral_constant<int, gg % 4>{});
    SB();
  };
  unsigned dvo[2], dvl[2];
  const char* dsrc = wd + (size_t)(jt >> 2) * 576 * 1152 + (jt & 3) * 288;
  auto dvoff = [&]() {
    int ln = tid;
    asm volatile("" : "+v"(ln));
    ln &= 63;
#pragma unroll
    for (int i = 0; i < 2; ++i) {
      const int o = (i ? 8 : wid) * 1024 + ln * 16;
      const int rho = o / 288, u = (o - rho * 288) >> 4;
      const int gk = ((rho >> 4) & 1) * 2 + ((rho >> 2) & 1), tk = (rho >> 3) & 1, qk = rho & 3;
      dvo[i] = (16 * tk + 4 * gk + qk) * 1152 + u * 16;
      dvl[i] = (144 * (gk >> 1) + 128 + 8 * (gk & 1) + 4 * tk + qk) * 1152 + u * 16;
    }
  };
  auto issue_d = [&](auto II, int pc) {
    constexpr int i = II;
    const int k = (4 * (jt & 3) + i) & 15;
    const char* src = dsrc + (size_t)(i < 16 ? 144 * (k >> 2) + 32 * (k & 3) : 288 * (i - 16)) * 1152;
    if (pc == 0) glds16(src, i < 16 ? dvo[0] : dvl[0], lds0 + d_off(i) + wid * 1024);
    else glds16_if0(wid ^ (i & 7), src, i < 16 ? dvo[1] : dvl[1], lds0 + d_off(i) + 8192);
  };
  for_seq<18>([&](auto GH) { for_seq<18>([&](auto GL) {
    constexpr int G = GH * 18 + GL, sl = k4_slab(G), f = k4_frag(k4_pos(G)), rb = G % 6, nb = (G + 5) % 6, NG = G + 5;
    const bf16x8 b = cat8(Bl[rb], Bh[rb]);
    constexpr int need = k4_need(G);
    if constexpr (need != 0) {
      asm volatile("s_waitcnt vmcnt(%2)\n\ts_barrier" : "+v"(A[need % 6][0]), "+v"(A[need % 6][1]) : "n"(k4_allow(G, need)) : "memory");
      SB();
    }
    if constexpr (sl == 0) mfma16_zero(acc[0][f], b, A[0][0]); else mfma16_acc(acc[0][f], b, A[sl % 6][0]);
    SB();
    if constexpr (NG < 324) { Bl[nb] = lds_tr(wbs[(k4_slab(NG) & 7) / 3] + ((k4_slab(NG) & 7) % 3) * K4_SLOT + k4_foff(k4_frag(k4_pos(NG)))); SB(); }
    if constexpr (G >= K4_T0 + 20 && G < K4_T0 + 80 && (G - K4_T0) % 20 >= 1 && (G - K4_T0) % 20 < 19)
      gapops(std::integral_constant<int, (G - K4_T0) / 20 - 1>{}, std::integral_constant<int, 2 * ((G - K4_T0) % 20 - 1)>{});
    if constexpr (G >= K4_T0 + 81) {
      gapops(std::integral_constant<int, 3>{}, std::integral_constant<int, 4 * (G - K4_T0 - 81)>{});
      gapops(std::integral_constant<int, 3>{}, std::integral_constant<int, 4 * (G - K4_T0 - 81) + 1>{});
    }
    if constexpr (sl == 0) mfma16_zero(acc[1][f], b, A[0][1]); else mfma16_acc(acc[1][f], b, A[sl % 6][1]);
    SB();
    if constexpr (NG < 324) { Bh[nb] = lds_tr(wbs[(k4_slab(NG) & 7) / 3] + ((k4_slab(NG) & 7) % 3) * K4_SLOT + k4_foff(k4_frag(k4_pos(NG))) + 2304); SB(); }
    constexpr int op = k4_op(G);
    if constexpr (op != 0) {
      constexpr int os = (op - 1) >> 3, kind = (op - 1) & 7;
      if constexpr (kind < 2) issue_a(os, A[os % 6][kind], kind);
      else issue_w(os, kind - 2);
      SB();
    }
    if constexpr (G % 18 == 10 && (G / 18 == 5 || G / 18 == 7 || G / 18 == 9)) {
      constexpr int k = (G / 18 - 5) / 2;
      touch_if0((int)(blockIdx.x / 216) + (wid ^ (k + 1)), wd + (size_t)(blockIdx.x * 3 + k) * 8192, avo * 8, lds0 + K4_PF + k * 256);
      SB();
    }
    if constexpr (G == 60) {
      lds_ptr cpp = lds + K4_CP + wid * 144 + lane * 4;
      const float t = *(LDS_AS float*)(cpp) + *(LDS_AS float*)(cpp + 1152) + *(LDS_AS float*)(cpp + 2304);
      *(LDS_AS float*)(lds + K4_CS + wid * 144 + min(lane, 36) * 4 + (lane >= 36 ? 1152 : 0)) = t;
      asm volatile("s_waitcnt lgkmcnt(0)" ::: "memory");
      SB();
    }
    if constexpr (G == 88) {
#pragma unroll
      for (int m = 0; m < 2; ++m) st[m] = *(LDS_AS f32x2*)(lds + K4_ST + (wid * 32 + m * 16 + r) * 8);
      SB();
    }
    if constexpr (G >= 90 && G <= 162 && (G - 90) % 4 == 0) {
      if constexpr (G > 90) {
        constexpr int cf = k4_frag((G + (G - 94 < 36 ? 8 : 9)) % 18);
#pragma unroll
        for (int m = 0; m < 2; ++m) {
#pragma unroll
          for (int e = 0; e < 4; ++e) acc[m][cf][e] = fmaf(st[m][0], cv[e], acc[m][cf][e]);
          pin(acc[m][cf]);
        }
        SB();
      }
      if constexpr (G < 162) { cv = *(LDS_AS f32x4*)(csb + k4_frag((G + 4 + (G - 90 < 36 ? 8 : 9)) % 18) * 64); SB(); }
    }
    if constexpr (G == K4_T0) {
      STAMP_MARK(2, 3);
      STAMP_CYC(2, 4);
#pragma unroll
      for (int m = 0; m < 2; ++m) { c1[m] = -L2E * st[m][1]; sd[m] = __builtin_amdgcn_rcpf(st[m][1]); }
      SB();
    }
    if constexpr (G >= K4_T0) {
      constexpr int t = G - K4_T0, u = t % 20;
      if constexpr (t == 11) { dvoff(); SB(); }
      if constexpr (t >= 12 && t < 24) { issue_d(std::integral_constant<int, (t - 12) / 2>{}, t & 1); SB(); }
      if constexpr (t >= 20 && t < 80 && u >= 1 && u < 19) gapops(std::integral_constant<int, t / 20 - 1>{}, std::integral_constant<int, 2 * (u - 1) + 1>{});
      if constexpr (t >= 81) {
        gapops(std::integral_constant<int, 3>{}, std::integral_constant<int, 4 * (t - 81) + 2>{});
        gapops(std::integral_constant<int, 3>{}, std::integral_constant<int, 4 * (t - 81) + 3>{});
      }
    }
  }); });
  {
    asm volatile("s_nop 7\n\ts_nop 1" : "+v"(acc[0][8]), "+v"(acc[1][8]), "+v"(acc[0][17]), "+v"(acc[1][17]));
#pragma unroll
    for (int m = 0; m < 2; ++m) {
      const f32x4 ag = acc[m][8], au = acc[m][17];
      u32x2 o = {cvt_pk(glu(ag[0], au[0], m), glu(ag[1], au[1], m)), cvt_pk(glu(ag[2], au[2], m), glu(ag[3], au[3], m))};
      st8wt(hb + (size_t)m * (144 * 1024) + (16 + ((jt >> 1) & 1)) * 1024 + (2 * (jt & 1) + (g >> 1)) * 256 + r * 16 + (g & 1) * 8, o);
    }
  }
  STAMP_MARK(2, 5);
  const int sb = jt >> 2, nq = jt & 3;
  unsigned* cntA = (unsigned*)(ws + WS_CNT + 8192) + (pn * 8 + sb) * 32;
  unsigned* cntB = (unsigned*)(ws + WS_CNT + 16384) + (pn * 4 + nq) * 32;
  wait_bar<10>();
  auto dptr = [&](auto HH) -> lds_ptr {
    constexpr int H = HH, o = d_off(d_i(H)) + d_f(H) * 32;
    return wbs[o / (3 * K4_SLOT)] + o % (3 * K4_SLOT);
  };
  for_seq<5>([&](auto I) { Bl[I] = lds_tr(dptr(I)); Bh[I] = lds_tr(dptr(I) + 2304); });
  SB();
  f32x4 dacc[2][9];
  bf16x8 DA[14][2];
  char* pb = ws + WS_PART + (size_t)(((pn * 4 + nq) * 8 + wid) * 8 + sb) * 9216 + lane * 16;
  for_seq<18>([&](auto HH) { for_seq<9>([&](auto HL) {
    constexpr int H = HH * 9 + HL, i = d_i(H), f = d_f(H), rb = H % 6, nb = (H + 5) % 6, NH = H + 5;
    if constexpr (H == 36) {
      if (tid == 0) poll_count(cntA, 4u);
      SB();
      wait_bar<0>();
      STAMP_MARK(2, 6);
      issue_d(std::integral_constant<int, 17>{}, 0); issue_d(std::integral_constant<int, 17>{}, 1);
      const unsigned ho = lane * 16, ho2 = lane * 16 + 144 * 1024;
      for_seq<14>([&](auto JJ) {
        constexpr int j = JJ;
        const int k = j < 12 ? ((4 * nq + 4 + j) & 15) : 4 + j;
        ld2_sc1(DA[j][0], DA[j][1], ho, ho2, hb + k * 1024);
      });
      SB();
    }
    if constexpr (i >= 4 && d_first(H)) {
      wait_vm2<2 * (17 - i)>(DA[i - 4][0], DA[i - 4][1]);
      SB();
    }
    if constexpr (H == D_G0) { raw_bar(); SB(); }
    const bf16x8 b = cat8(Bl[rb], Bh[rb]);
    bf16x8 a0, a1;
    if constexpr (i < 4) { a0 = __builtin_bit_cast(bf16x8, own[i][0]); a1 = __builtin_bit_cast(bf16x8, own[i][1]); }
    else { a0 = DA[i - 4][0]; a1 = DA[i - 4][1]; }
    if constexpr (i == 0) mfma16_zero(dacc[0][f], b, a0); else mfma16_acc(dacc[0][f], b, a0);
    SB();
    if constexpr (NH < 162) { Bl[nb] = lds_tr(dptr(std::integral_constant<int, NH>{})); SB(); }
    if constexpr (i == 0) mfma16_zero(dacc[1][f], b, a1); else mfma16_acc(dacc[1][f], b, a1);
    SB();
    if constexpr (NH < 162) { Bh[nb] = lds_tr(dptr(std::integral_constant<int, NH>{}) + 2304); SB(); }
    if constexpr (H < 22) { issue_d(std::integral_constant<int, 6 + H / 2>{}, H & 1); SB(); }
    if constexpr (H == 24) {
      wait_vm<11>();
      if (lds_arrive(lds + K4_SY, lane) == 7 && lane == 0) __hip_atomic_fetch_add(cntA, 1u, __ATOMIC_RELAXED, __HIP_MEMORY_SCOPE_AGENT);
      SB();
    }
    if constexpr (H >= D_G0 + 2 * DT && (((H - D_G0) % (2 * DT) == 2 || (H - D_G0) % (2 * DT) == 3))) {
      constexpr int pr = (H - D_G0) / (2 * DT) - 1, m = ((H - D_G0) % (2 * DT) == 3);
      const f32x4 x0 = dacc[m][2 * pr], x1 = dacc[m][2 * pr + 1];
      st16(pb + (m * 4 + pr) * 1024, u32x4{cvt_pk(x0[0], x0[1]), cvt_pk(x0[2], x0[3]), cvt_pk(x1[0], x1[1]), cvt_pk(x1[2], x1[3])});
      SB();
    }
  }); });
  {
    asm volatile("s_nop 7\n\ts_nop 1" : "+v"(dacc[0][8]), "+v"(dacc[1][8]));
    const f32x4 x0 = dacc[0][8], x1 = dacc[1][8];
    st16(pb + 8192, u32x4{cvt_pk(x0[0], x0[1]), cvt_pk(x0[2], x0[3]), cvt_pk(x1[0], x1[1]), cvt_pk(x1[2], x1[3])});
  }
  wait_bar<0>();
  STAMP_MARK(2, 7);
  if (tid == 0) __hip_atomic_fetch_add(cntB, 1u, __ATOMIC_RELAXED, __HIP_MEMORY_SCOPE_AGENT);
  {
    const int nc = wid == 1 ? 2 : 1;
    const int rbase = pn * 256 + sb * 32;
    int row[2][2], col[2][2];
    row[0][0] = row[0][1] = rbase + (wid >> 2) * 16 + r; col[0][0] = nq * 144 + (wid & 3) * 32 + 4 * g; col[0][1] = col[0][0] + 16;
    row[1][0] = rbase + r; row[1][1] = rbase + 16 + r; col[1][0] = col[1][1] = nq * 144 + 128 + 4 * g;
    u32x2 res[2][2];
#pragma unroll
    for (int c = 0; c < 2; ++c)
#pragma unroll
      for (int e = 0; e < 2; ++e)
        res[c][e] = *(const u32x2*)(ws + WS_H + (size_t)(row[c][e] >> 4) * 18432 + (col[c][e] >> 5) * 1024 + ((col[c][e] >> 3) & 3) * 256 + (row[c][e] & 15) * 16 + ((col[c][e] >> 2) & 1) * 8);
    if (tid == 0) poll_count(cntB, 8u);
    SB();
    raw_bar();
    SB();
    const __amdgpu_buffer_rsrc_t prs = __builtin_amdgcn_make_buffer_rsrc((void*)(ws + WS_PART + (size_t)((pn * 4 + nq) * 8 + sb) * 8 * 9216), 0, 8 * 9216, 0x00020000);
#pragma unroll
    for (int c = 0; c < 2; ++c) {
      if (c < nc) {
        u32x4 pv[8];
#pragma unroll
        for (int k = 0; k < 8; ++k) pv[k] = __builtin_amdgcn_raw_buffer_load_b128(prs, lane * 16 + (c ? 8192 : wid * 1024), k * 9216, 16);
        float sum[8];
#pragma unroll
        for (int e = 0; e < 4; ++e) { sum[2 * e] = bf_lo(res[c][e >> 1][e & 1]); sum[2 * e + 1] = bf_hi(res[c][e >> 1][e & 1]); }
        float acc2[8];
#pragma unroll
        for (int e = 0; e < 8; ++e) acc2[e] = 0.f;
#pragma unroll
        for (int k = 0; k < 8; ++k)
#pragma unroll
          for (int e = 0; e < 4; ++e) { acc2[2 * e] += bf_lo(pv[k][e]); acc2[2 * e + 1] += bf_hi(pv[k][e]); }
        unsigned pk[4];
#pragma unroll
        for (int e = 0; e < 4; ++e) pk[e] = cvt_pk(sum[2 * e] + acc2[2 * e], sum[2 * e + 1] + acc2[2 * e + 1]);
        auto x0 = __builtin_amdgcn_permlane16_swap(pk[0], pk[2], false, false);
        auto x1 = __builtin_amdgcn_permlane16_swap(pk[1], pk[3], false, false);
        const int sel = g & 1;
        OUT_ST(out + (size_t)(row[c][0] + (c ? 16 * sel : 0)) * 1152 + (col[c][0] + (c ? -4 : 12) * sel) * 2, (u32x4{x0[0], x1[0], x0[1], x1[1]}));
      }
    }
  }
  STAMP_END(2);
}

extern "C" void kernel_launch(void* const* d_in, const int* in_sizes, int n_in, void* d_out, int out_size, void* d_ws, size_t ws_size,
                              hipStream_t stream) {
  const char* x = (const char*)d_in[0];
  const char* wqkv = (const char*)d_in[1];
  const char* wo = (const char*)d_in[2];
  const char* wg = (const char*)d_in[4];
  const char* wu = (const char*)d_in[5];
  const char* wd = (const char*)d_in[6];
  char* ws = (char*)d_ws + WS_BASE(ws_size);
  (void)hipFuncSetAttribute((const void*)k1_ln_qkv, hipFuncAttributeMaxDynamicSharedMemorySize, K1_LDS);
  (void)hipFuncSetAttribute((const void*)k2_attn, hipFuncAttributeMaxDynamicSharedMemorySize, K2_LDS);
  (void)hipFuncSetAttribute((const void*)k4_gateup, hipFuncAttributeMaxDynamicSharedMemorySize, K4_LDS);
  k1_ln_qkv<<<234, 1024, K1_LDS, stream>>>(x, wqkv, ws, wo);
  k2_attn<<<256, 768, K2_LDS, stream>>>(ws, wg, wu, wo, x);
  k4_gateup<<<256, 512, K4_LDS, stream>>>(wg, wu, ws, wd, (char*)d_out);
}
```

```cpp
#include <hip/hip_runtime.h>
#include <hip/hip_bf16.h>
#include <stdint.h>
#include <utility>
#include <type_traits>

typedef __attribute__((ext_vector_type(8))) short bf16x8;
typedef __attribute__((ext_vector_type(4))) short s16x4;
typedef __attribute__((ext_vector_type(4))) float f32x4;
typedef __attribute__((ext_vector_type(16))) float f32x16;
typedef __attribute__((ext_vector_type(4))) unsigned u32x4;
typedef __attribute__((ext_vector_type(2))) unsigned u32x2;
typedef __attribute__((ext_vector_type(2))) __bf16 bf16x2_t;
typedef __attribute__((ext_vector_type(2))) float f32x2;
#define LDS_AS __attribute__((address_space(3)))
typedef LDS_AS char* lds_ptr;
#define DI __device__ __forceinline__

constexpr int SEQ = 2048, DM = 576, NH = 32, FF = 4608;
constexpr float LN_EPS = 1e-5f;
constexpr float QSCALE = 0.23570226039551584f * 1.4426950408889634f;

constexpr size_t WS_Q = 0;
constexpr size_t WS_K = WS_Q + (size_t)72 * 32768;
constexpr size_t WS_V = WS_K + (size_t)72 * 32768;
constexpr size_t WS_ONES = WS_V + (size_t)256 * 9216;
constexpr size_t WS_O = WS_ONES + 1024;
constexpr size_t WS_OX = WS_O + (size_t)128 * 18 * 1024;
constexpr size_t WS_H = WS_OX + (size_t)32 * 8192;
constexpr size_t WS_ST = WS_H + (size_t)128 * 18 * 1024;
constexpr size_t WS_XM = WS_ST + (size_t)9 * 16384;
constexpr size_t WS_HM = WS_XM + 8192;
constexpr size_t WS_WB = WS_HM + 8192;
constexpr size_t WS_HID = WS_WB + 4096;
constexpr size_t WS_CNT = WS_HID + (size_t)128 * 144 * 1024;
constexpr size_t WS_PART = WS_CNT + 24576;
constexpr size_t WS_END = WS_PART + (size_t)2048 * 9216;
static_assert(WS_O % 1024 == 0 && WS_H % 1024 == 0 && WS_ST % 256 == 0 && WS_HID % 256 == 0, "align");

#define WS_BASE(sz) (((sz) - WS_END) & ~(size_t)0x1fffff)
#define STAMP_BEGIN
#define STAMP_MARK(k, i)
#define STAMP_MARKW(k, i, T)
#define STAMP_ENDW(k, T)
#define STAMP_CYC0
#define STAMP_CYC(k, i)
#define STAMP_END(k)

DI unsigned cvt_pk(float lo, float hi) {
  f32x2 v = {lo, hi};
  bf16x2_t r = __builtin_convertvector(v, bf16x2_t);
  return __builtin_bit_cast(unsigned, r);
}
DI float bf_lo(unsigned u) { return __builtin_bit_cast(float, u << 16); }
DI float bf_hi(unsigned u) { return __builtin_bit_cast(float, u & 0xffff0000u); }
DI float dot2(unsigned a, unsigned b, float c) {
  return __builtin_amdgcn_fdot2_f32_bf16(__builtin_bit_cast(bf16x2_t, a), __builtin_bit_cast(bf16x2_t, b), c, false);
}
template <int CTRL> DI float dpp_add(float v) {
  int t = __builtin_amdgcn_update_dpp(0, __builtin_bit_cast(int, v), CTRL, 0xf, 0xf, true);
  return v + __builtin_bit_cast(float, t);
}
DI float sum8(float v) {
  v = dpp_add<0xB1>(v);
  v = dpp_add<0x4E>(v);
  v = dpp_add<0x141>(v);
  return v;
}
#define GNOP "3"
DI void glds16(const void* sbase, unsigned voff, unsigned lds_dst) {
  asm volatile("s_mov_b32 m0, %2\n\ts_nop " GNOP "\n\tglobal_load_lds_dwordx4 %0, %1" ::"v"(voff), "s"(sbase), "s"(lds_dst) : "memory");
}
#define ST_AUX " sc1"
DI void st16(void* p, u32x4 v) { asm volatile("global_store_dwordx4 %0, %1, off" ST_AUX "\n\ts_nop 1" ::"v"(p), "v"(v) : "memory"); }
DI void st8(void* p, u32x2 v) { *(u32x2*)p = v; }
DI void st8wt(void* p, u32x2 v) { asm volatile("global_store_dwordx2 %0, %1, off sc1" ::"v"(p), "v"(v) : "memory"); }
DI void st4(void* p, unsigned v) { asm volatile("global_store_dword %0, %1, off sc1" ::"v"(p), "v"(v) : "memory"); }
DI void glds16_if0(int c, const void* sbase, unsigned voff, unsigned lds_dst) {
  asm volatile("s_cmp_lg_u32 %3, 0\n\ts_cbranch_scc1 1f\n\ts_mov_b32 m0, %2\n\ts_nop 1\n\tglobal_load_lds_dwordx4 %0, %1\n1:"
               ::"v"(voff), "s"(sbase), "s"(lds_dst), "s"(c) : "memory", "scc");
}
DI void touch_if0(int c, const void* sbase, unsigned voff, unsigned lds_dst) {
  asm volatile("s_cmp_lg_u32 %3, 0\n\ts_cbranch_scc1 1f\n\ts_mov_b32 m0, %2\n\ts_nop 1\n\tglobal_load_lds_dword %0, %1\n1:"
               ::"v"(voff), "s"(sbase), "s"(lds_dst), "s"(c) : "memory", "scc");
}
DI void touch_lines(const char* base, unsigned voff, unsigned lds_dummy) {
  asm volatile("s_mov_b32 m0, %2\n\ts_nop 3\n\tglobal_load_lds_dword %0, %1" ::"v"(voff), "s"(base), "s"(lds_dummy) : "memory");
}
#define CODE_TOUCH(SYM, BYTES, LDS_DUMMY)                                                                          \
  do {                                                                                                             \
    const char* cp_;                                                                                               \
    asm volatile("s_getpc_b64 s[20:21]\n\ts_add_u32 s20, s20, " SYM "@rel32@lo+4\n\ts_addc_u32 s21, s21, " SYM     \
                 "@rel32@hi+12\n\ts_mov_b64 %0, s[20:21]" : "=s"(cp_)::"s20", "s21", "scc");                      \
    constexpr int nl_ = (BYTES) / 128, per_ = (nl_ + 28) / 29;                                                      \
    const int ln_ = (blockIdx.x >> 3) * per_ + lane;                                                               \
    if (lane < per_ && ln_ < nl_) touch_lines(cp_, ln_ * 128, LDS_DUMMY);                                          \
  } while (0)
#define SYM_K1 "_Z9k1_ln_qkvPKcS0_PcS0_"
#define SYM_K2 "_Z7k2_attnPcPKcS1_S1_S1_"
#define SYM_K4 "_Z9k4_gateupPKcS0_PcS0_S1_"
constexpr int LEN_K1 = 22600, LEN_K2 = 11000, LEN_K4 = 29000;
template <int N> DI void wait_bar() { asm volatile("s_waitcnt vmcnt(%0) lgkmcnt(0)\n\ts_barrier" ::"n"(N) : "memory"); }
template <int N> DI void wait_vm() { asm volatile("s_waitcnt vmcnt(%0)" ::"n"(N) : "memory"); }
DI unsigned lds_arrive(lds_ptr c, int lane) {
  unsigned old = 0;
  if (lane == 0) old = __hip_atomic_fetch_add((LDS_AS unsigned*)c, 1u, __ATOMIC_RELAXED, __HIP_MEMORY_SCOPE_WORKGROUP);
  return __builtin_amdgcn_readfirstlane(old);
}
DI void lds_sync(lds_ptr c, unsigned n, int lane) {
  asm volatile("" ::: "memory");
  if (lds_arrive(c, lane) + 1 != n) {
    for (int spin = 0; spin < (1 << 20); ++spin) {
      const unsigned v = __builtin_amdgcn_readfirstlane(__hip_atomic_load((LDS_AS unsigned*)c, __ATOMIC_RELAXED, __HIP_MEMORY_SCOPE_WORKGROUP));
      if (v >= n) break;
      __builtin_amdgcn_s_sleep(1);
    }
  }
  asm volatile("" ::: "memory");
}
DI void lds_post(lds_ptr c, int lane) {
  asm volatile("" ::: "memory");
  if (lane == 0) (void)__hip_atomic_fetch_add((LDS_AS unsigned*)c, 1u, __ATOMIC_RELAXED, __HIP_MEMORY_SCOPE_WORKGROUP);
}
DI void lds_wait(lds_ptr c, unsigned n) {
  for (int spin = 0; spin < (1 << 20); ++spin) {
    const unsigned v = __builtin_amdgcn_readfirstlane(__hip_atomic_load((LDS_AS unsigned*)c, __ATOMIC_RELAXED, __HIP_MEMORY_SCOPE_WORKGROUP));
    if (v >= n) break;
    __builtin_amdgcn_s_sleep(1);
  }
  asm volatile("" ::: "memory");
}
template <class T> DI void pin(T& v) { asm volatile("" : "+v"(v)); }
DI void raw_bar() { asm volatile("s_barrier" ::: "memory"); }
DI void lds_bar() { asm volatile("s_waitcnt lgkmcnt(0)\n\ts_barrier" ::: "memory"); }
DI bf16x8 lds_r128(lds_ptr p) { return *(LDS_AS const bf16x8*)p; }
typedef short v4i16_t __attribute__((ext_vector_type(4)));
DI s16x4 lds_tr(lds_ptr p) { return __builtin_amdgcn_ds_read_tr16_b64_v4i16((LDS_AS v4i16_t*)p); }
DI bf16x8 cat8(s16x4 a, s16x4 b) { return __builtin_shufflevector(a, b, 0, 1, 2, 3, 4, 5, 6, 7); }
#define SB() __builtin_amdgcn_sched_barrier(0)
#define MFMA16(a, b, c) __builtin_amdgcn_mfma_f32_16x16x32_bf16(a, b, c, 0, 0, 0)
#define MFMA32(a, b, c) __builtin_amdgcn_mfma_f32_32x32x16_bf16(a, b, c, 0, 0, 0)
DI void mfma16_acc(f32x4& c, bf16x8 a, bf16x8 b) { asm volatile("v_mfma_f32_16x16x32_bf16 %0, %1, %2, %0" : "+v"(c) : "v"(a), "v"(b)); }
DI void mfma16_zero(f32x4& c, bf16x8 a, bf16x8 b) { asm volatile("v_mfma_f32_16x16x32_bf16 %0, %1, %2, 0" : "=&v"(c) : "v"(a), "v"(b)); }
template <class F, int... I> DI void for_seq_impl(F&& f, std::integer_sequence<int, I...>) { (f(std::integral_constant<int, I>{}), ...); }
template <int N, class F> DI void for_seq(F&& f) { for_seq_impl(f, std::make_integer_sequence<int, N>{}); }
DI int xcd_remap(int b, int nwg) {
  int q = nwg >> 3, r = nwg & 7, x = b & 7, o = b >> 3;
  return (x < r ? x * (q + 1) : r * (q + 1) + (x - r) * q) + o;
}

constexpr int K1_X = 0, K1_W = 40960, K1_SLOT = 12288, K1_ST = K1_W + K1_SLOT * 9, K1_CS = K1_W, K1_PF = K1_ST + 1024, K1_LDS = K1_PF + 1024;
constexpr int cmin(int a, int b) { return a < b ? a : b; }
constexpr int k1_nw(int kk) { return cmin(18, cmin(3 + 2 * kk, kk + 7)); }
constexpr int k1_nx(int kk) { return cmin(18, cmin(3 + 2 * kk, kk + 5)); }

template <bool ISV, bool HI>
DI void k1_consumer(lds_ptr lds, int cb, int lane, char* ws, int ct, int m0) {
  const int g = lane >> 4, q = (lane & 15) >> 2, p = lane & 3, r = lane & 15;
  constexpr int NM = HI ? 2 : 3, MB = HI ? 3 : 0;
  lds_ptr a0 = lds + K1_X + MB * 2048 + (r >> 3) * 1024 + (r & 7) * 128 + ((g ^ (r >> 1)) * 16);
  lds_ptr a1 = lds + K1_X + MB * 2048 + (r >> 3) * 1024 + (r & 7) * 128 + (((4 + g) ^ (r >> 1)) * 16);
  const int rho0 = 16 * (g >> 1) + 4 * (g & 1) + q;
  lds_ptr wb = lds + K1_W + rho0 * 128 + ((cb ^ ((rho0 >> 1) & 3)) * 32) + p * 8;
  lds_ptr wb5 = wb + 5 * K1_SLOT;
  asm volatile("" : "+v"(wb), "+v"(wb5));
  const u32x4 onesu = {0x3f803f80u, 0x3f803f80u, 0x3f803f80u, 0x3f803f80u};
  const bf16x8 ones = __builtin_bit_cast(bf16x8, onesu);
  f32x4 acc[3][3];
#pragma unroll
  for (int m = 0; m < 3; ++m)
#pragma unroll
    for (int j = 0; j < 3; ++j) acc[m][j] = f32x4{0.f, 0.f, 0.f, 0.f};
  bf16x8 fA[2][3];
  s16x4 wl[2][3], wh[2][3];
  auto rd1 = [&](auto KK, auto II) {
    constexpr int kk = KK, i = II, bf = kk & 1, sl = kk % 9;
    if constexpr (i >= 2 && i <= 4) {
      if constexpr (i - 2 < NM) fA[bf][i - 2] = lds_r128(((kk & 1) ? a1 : a0) + ((kk >> 1) & 3) * 10240 + (i - 2) * 2048);
    } else {
      constexpr int j = i < 2 ? 0 : (i - 3) / 2, hh = i < 2 ? i : (i - 3) % 2;
      lds_ptr bp = (sl >= 5 ? wb5 + (sl - 5) * K1_SLOT : wb + sl * K1_SLOT) + j * 4096 + hh * 1024;
      if constexpr (hh == 0) wl[bf][j] = lds_tr(bp); else wh[bf][j] = lds_tr(bp);
    }
  };
  lds_bar();
  STAMP_MARKW(0, 2, 512);
  for_seq<9>([&](auto I) { rd1(std::integral_constant<int, 0>{}, I); });
  SB();
  for_seq<18>([&](auto KK) {
    constexpr int kk = KK, bf = kk & 1;
    if constexpr (kk < 17) { raw_bar(); SB(); }
    for_seq<9>([&](auto II) {
      constexpr int i = II, j = i / 3, m = i % 3;
      const bf16x8 wv = cat8(wl[bf][j], wh[bf][j]);
      const bf16x8 av = (m < NM) ? fA[bf][m] : ones;
      acc[m][j] = ISV ? MFMA16(av, wv, acc[m][j]) : MFMA16(wv, av, acc[m][j]);
      SB();
      if constexpr (kk < 17) { rd1(std::integral_constant<int, kk + 1>{}, II); SB(); }
    });
  });
  STAMP_MARKW(0, 4, 512);
  lds_ptr cx = lds + K1_CS + cb * 3072 + lane * 16;
  if (HI) {
#pragma unroll
    for (int j = 0; j < 3; ++j) *(LDS_AS f32x4*)(cx + j * 1024) = acc[2][j];
  }
  lds_bar();
  STAMP_MARKW(0, 5, 512);
  f32x4 cs[3];
#pragma unroll
  for (int j = 0; j < 3; ++j) cs[j] = HI ? acc[2][j] : *(LDS_AS f32x4*)(cx + j * 1024);
  u32x2 o[NM * 3];
  if (ISV) {
#pragma unroll
    for (int m = 0; m < NM; ++m) {
      const f32x4 s0 = *(LDS_AS f32x4*)(lds + K1_ST + ((MB + m) * 16 + 4 * g) * 8), s1 = *(LDS_AS f32x4*)(lds + K1_ST + ((MB + m) * 16 + 4 * g) * 8 + 16);
#pragma unroll
      for (int j = 0; j < 3; ++j) {
        const float c0 = cs[j][0];
        o[3 * m + j] = u32x2{cvt_pk(fmaf(s0[0], c0, acc[m][j][0]) * s0[1], fmaf(s0[2], c0, acc[m][j][1]) * s0[3]),
                             cvt_pk(fmaf(s1[0], c0, acc[m][j][2]) * s1[1], fmaf(s1[2], c0, acc[m][j][3]) * s1[3])};
      }
    }
  } else {
    const float sc = ct < 3 ? QSCALE : 1.f;
#pragma unroll
    for (int m = 0; m < NM; ++m) {
      f32x2 st = *(LDS_AS f32x2*)(lds + K1_ST + ((MB + m) * 16 + r) * 8);
      st[1] *= sc;
#pragma unroll
      for (int j = 0; j < 3; ++j)
        o[3 * m + j] = u32x2{cvt_pk(fmaf(st[0], cs[j][0], acc[m][j][0]) * st[1], fmaf(st[0], cs[j][1], acc[m][j][1]) * st[1]),
                             cvt_pk(fmaf(st[0], cs[j][2], acc[m][j][2]) * st[1], fmaf(st[0], cs[j][3], acc[m][j][3]) * st[1])};
    }
  }
  char* base;
  int sm, sj, sel, half;
  if (ISV) { base = ws + WS_V + (size_t)(((m0 >> 4) + MB) * 2 + (g & 1)) * 9216 + ((ct - 6) * 192 + cb * 16 + r) * 16; sm = 18432; sj = 1024; sel = g >> 1; half = g >> 1; }
  else { base = ws + WS_Q + (size_t)(ct * 24 + cb * 2 + (g >> 1)) * 32768 + (m0 + MB * 16 + r) * 16; sm = 256; sj = 8 * 32768; sel = g & 1; half = g & 1; }
#pragma unroll
  for (int n = 0; n + 1 < NM * 3; n += 2) {
    u32x4 c;
#pragma unroll
    for (int e = 0; e < 2; ++e) {
      auto rr = ISV ? __builtin_amdgcn_permlane32_swap(o[n][e], o[n + 1][e], false, false) : __builtin_amdgcn_permlane16_swap(o[n][e], o[n + 1][e], false, false);
      c[e] = rr[0]; c[2 + e] = rr[1];
    }
    const int o0 = (n / 3) * sm + (n % 3) * sj, o1 = ((n + 1) / 3) * sm + ((n + 1) % 3) * sj;
    st16(base + (sel ? o1 : o0), c);
  }
  if (NM == 3) st8wt(base + 2 * sm + 2 * sj + half * 8, o[8]);
}

__global__ __launch_bounds__(1024) void k1_ln_qkv(const char* __restrict__ x, const char* __restrict__ wqkv, char* __restrict__ ws, const char* __restrict__ wo) {
  extern __shared__ __attribute__((aligned(16))) char smem[];
  STAMP_BEGIN
  lds_ptr lds = (lds_ptr)smem;
  const unsigned lds0 = (unsigned)(uintptr_t)lds;
  const int tid = threadIdx.x, lane = tid & 63, wid = __builtin_amdgcn_readfirstlane(tid >> 6);
  const int tile = xcd_remap(blockIdx.x, 234);
  const int cgp = tile / 78, rem = tile - cgp * 78;
  const int mt = rem / 3, ct = cgp * 3 + (rem - mt * 3);
  const int m0 = min(mt * 80, SEQ - 80);
  if (wid < 3) {
    unsigned wv[4];
#pragma unroll
    for (int i = 0; i < 4; ++i) {
      const int rho = 8 * i + (lane >> 3);
      const int gk = ((rho >> 4) & 1) * 2 + ((rho >> 2) & 1), tk = (rho >> 3) & 1, qk = rho & 3;
      wv[i] = (8 * gk + 4 * tk + qk) * 3456 + (((lane & 7) ^ (((rho >> 1) & 3) << 1)) * 16);
    }
    const char* wsrc = wqkv + ct * 384 + wid * 128;
    const unsigned wdst = lds0 + K1_W + wid * 4096;
    for_seq<18>([&](auto KK) {
      constexpr int kk = KK;
#pragma unroll
      for (int n = (kk == 0 ? 0 : k1_nw(kk - 1)); n < k1_nw(kk); ++n)
#pragma unroll
        for (int i = 0; i < 4; ++i) glds16(wsrc + (size_t)n * 32 * 3456, wv[i], wdst + (n % 9) * K1_SLOT + i * 1024);
      wait_bar<(k1_nw(kk) - kk - 1) * 4>();
    });
    return;
  }
  if (wid == 3) {
    const unsigned v0 = (lane >> 3) * 1152 + (((lane & 7) ^ (lane >> 4)) * 16);
    const unsigned v1 = (lane >> 3) * 1152 + (((lane & 7) ^ (4 + (lane >> 4))) * 16);
    const char* xs = x + (size_t)m0 * 1152;
    for_seq<18>([&](auto KK) {
      constexpr int kk = KK;
#pragma unroll
      for (int h = (kk == 0 ? 0 : k1_nx(kk - 1)); h < k1_nx(kk); ++h)
#pragma unroll
        for (int i = 0; i < 5; ++i) {
          const int pc = (h & 1) * 5 + i;
          glds16(xs + pc * 9216 + (h >> 1) * 128, (pc & 1) ? v1 : v0, lds0 + K1_X + ((h >> 1) & 3) * 10240 + pc * 1024);
        }
      wait_bar<(k1_nx(kk) - 2 * (kk >> 1) - 2) * 5>();
    });
    return;
  }
  if (wid < 8) {
    const int j = wid - 4;
    if (j == 0) CODE_TOUCH(SYM_K1, LEN_K1, lds0 + K1_PF);
    if (j == 1) CODE_TOUCH(SYM_K2, LEN_K2, lds0 + K1_PF + 256);
    if (j == 1 && blockIdx.x == 0 && lane < 4) *(unsigned*)(ws + WS_ONES + lane * 4) = 0x3f803f80u;
    if (j >= 1 && blockIdx.x == 0) *(unsigned*)(ws + WS_CNT + ((j - 1) * 64 + lane) * 128) = 0u;
    {
      const int ln = blockIdx.x * 148 + j * 37 + lane;
      if (lane < 37) {
        if (ln < 15552) touch_lines(wqkv, ln * 128, lds0 + K1_PF + j * 256);
        else if (ln < 33984) touch_lines(x, (ln - 15552) * 128, lds0 + K1_PF + j * 256);
      }
    }
    const bool do_wb = blockIdx.x >= 100 && blockIdx.x < 118;
    const int wrow = do_wb ? ((blockIdx.x - 100) * 4 + j) * 8 + (lane >> 3) : 0;
    u32x4 wv[9];
    if (do_wb) {
#pragma unroll
      for (int i = 0; i < 9; ++i) wv[i] = *(const u32x4*)(wo + (size_t)wrow * 1152 + (lane & 7) * 16 + i * 128);
    }
    SB();
    lds_ptr p0 = lds + K1_X + j * 1024 + lane * 16, p2 = lds + K1_X + min(j + 8, 9) * 1024 + lane * 16;
    float s1[3] = {0.f, 0.f, 0.f}, s2[3] = {0.f, 0.f, 0.f};
#pragma unroll 1
    for (int s = 0; s < 9; ++s) {
      raw_bar();
      const int so = (s & 3) * 10240;
      u32x4 v[3] = {*(LDS_AS u32x4*)(p0 + so), *(LDS_AS u32x4*)(p0 + so + 4096), *(LDS_AS u32x4*)(p2 + so)};
      SB();
      raw_bar();
      SB();
#pragma unroll
      for (int i = 0; i < 3; ++i)
#pragma unroll
        for (int e = 0; e < 4; ++e) { s1[i] = dot2(v[i][e], 0x3f803f80u, s1[i]); s2[i] = dot2(v[i][e], v[i][e], s2[i]); }
      asm volatile("" : "+v"(s1[0]), "+v"(s1[1]), "+v"(s1[2]), "+v"(s2[0]), "+v"(s2[1]), "+v"(s2[2]));
    }
#pragma unroll
    for (int i = 0; i < 3; ++i) {
      const float a = sum8(s1[i]), b = sum8(s2[i]);
      const float mean = a * (1.f / 576.f);
      const float var = fmaxf(b * (1.f / 576.f) - mean * mean, 0.f);
      const int pc = i < 2 ? j + 4 * i : min(j + 8, 9);
      if ((lane & 7) == 0) {
        *(LDS_AS f32x2*)(lds + K1_ST + (pc * 8 + (lane >> 3)) * 8) = f32x2{-mean, __builtin_amdgcn_rsqf(var + LN_EPS)};
        if (ct == 0) *(float*)(ws + WS_XM + (m0 + pc * 8 + (lane >> 3)) * 4) = mean;
      }
    }
    lds_bar();
    if (do_wb) {
      float t = 0.f;
#pragma unroll
      for (int i = 0; i < 9; ++i)
#pragma unroll
        for (int e = 0; e < 4; ++e) t = dot2(wv[i][e], 0x3f803f80u, t);
      t = sum8(t) * (1.f / 576.f);
      const unsigned h = cvt_pk(t, 0.f) & 0xffffu, l = cvt_pk(t - bf_lo(h), 0.f) & 0xffffu;
      const int hd = wrow / 18, d = wrow - hd * 18;
      const int pos = d < 16 ? (hd >> 1) * 32 + (2 * (hd & 1) + ((d >> 2) & 1)) * 8 + 4 * (d >> 3) + (d & 3)
                             : (16 + (hd >> 4)) * 32 + ((hd >> 2) & 3) * 8 + 2 * (hd & 3) + (d - 16);
      if ((lane & 7) < 2) *(unsigned short*)(ws + WS_WB + (lane & 1) * 1152 + pos * 2) = (unsigned short)((lane & 1) ? l : h);
    }
    return;
  }
  if (ct >= 6) {
    if (wid < 12) k1_consumer<true, false>(lds, wid - 8, lane, ws, ct, m0);
    else k1_consumer<true, true>(lds, wid - 12, lane, ws, ct, m0);
  } else {
    if (wid < 12) k1_consumer<false, false>(lds, wid - 8, lane, ws, ct, m0);
    else k1_consumer<false, true>(lds, wid - 12, lane, ws, ct, m0);
  }
  STAMP_ENDW(0, 512);
}

#define K2_PFN 40
#define K2SYNC 0
#define K2_SL0 4
#define K2_SL1 4
constexpr int K2_KM = 0, K2_V = 24576, K2_SY = 44032, K2_CS = 44288, K2_W = K2_CS + 4864, K2_WB = K2_W + 73728, K2_LDS = K2_WB + 3072;

#define MFMA32K8(a, b, c) __builtin_amdgcn_mfma_f32_32x32x8bf16_1k(a, b, c, 0, 0, 0)
DI void k2_core(lds_ptr lds, char* ws, int hd, int t, int wid, int lane, u32x4 q0u, u32x2 q1u, bool first) {
  const int hi = lane >> 5, l31 = lane & 31;
  const int dlo = ((18 * hd) & 7) >> 1;
#pragma unroll
  for (int e = 0; e < 4; ++e) q0u[e] = (4 * hi + e >= dlo) ? q0u[e] : 0u;
#pragma unroll
  for (int e = 0; e < 2; ++e) q1u[e] = (8 + 2 * hi + e < dlo + 9) ? q1u[e] : 0u;
  const bf16x8 q0 = __builtin_bit_cast(bf16x8, q0u);
  const s16x4 q1 = __builtin_bit_cast(s16x4, q1u);
  f32x16 S[9];
  lds_ptr kmp = lds + K2_KM + hi * 8192 + (wid * 32 + l31) * 16;
  lds_ptr kxp = lds + K2_KM + 16384 + (wid * 32 + l31) * 16 + hi * 8;
  const float NEG = -1e30f;
  const int dq = l31 - 4 * hi;
  float mx0 = NEG, mx1 = NEG;
  bf16x8 kf[3];
  s16x4 kx[3];
  auto rdk = [&](auto NN) {
    constexpr int n = NN, i = n == 0 ? 8 : n - 1;
    kf[n % 3] = lds_r128(kmp + i * 512);
    kx[n % 3] = *(LDS_AS const s16x4*)(kxp + i * 512);
  };
  auto post = [&](auto NN) {
    constexpr int n = NN, i = n == 0 ? 8 : n - 1;
    if constexpr (i < 8) {
      if constexpr (i == 0) {
#pragma unroll
        for (int r = 0; r < 16; ++r) S[0][r] = (((r & 3) + 8 * (r >> 2)) <= dq) ? S[8][r] : S[0][r];
      }
#pragma unroll
      for (int r = 0; r < 16; r += 4) {
        mx0 = fmaxf(fmaxf(mx0, S[i][r]), S[i][r + 1]);
        mx1 = fmaxf(fmaxf(mx1, S[i][r + 2]), S[i][r + 3]);
      }
    }
  };
  rdk(std::integral_constant<int, 0>{});
  rdk(std::integral_constant<int, 1>{});
  SB();
  for_seq<9>([&](auto NN) {
    constexpr int n = NN, i = n == 0 ? 8 : n - 1;
    const f32x16 z = {};
    S[i] = MFMA32(kf[n % 3], q0, z);
    SB();
    if constexpr (n + 2 < 9) { rdk(std::integral_constant<int, n + 2>{}); SB(); }
    S[i] = MFMA32K8(kx[n % 3], q1, S[i]);
    SB();
    if constexpr (n >= 1) { post(std::integral_constant<int, n - 1>{}); SB(); }
  });
  post(std::integral_constant<int, 8>{});
  float mx = fmaxf(mx0, mx1);
  {
    auto rr = __builtin_amdgcn_permlane32_swap(__float_as_uint(mx), __float_as_uint(mx), false, false);
    mx = fmaxf(__uint_as_float(rr[0]), __uint_as_float(rr[1]));
  }
  s16x4 mq;
  {
    const float nm = -mx;
    const unsigned h1 = __float_as_uint(nm) & 0xffff0000u;
    const float r1 = nm - __uint_as_float(h1);
    const unsigned h2 = __float_as_uint(r1) & 0xffff0000u;
    const float r2 = r1 - __uint_as_float(h2);
    const u32x2 mu = {hi ? 0u : ((h1 >> 16) | h2), hi ? 0u : ((__float_as_uint(r2) >> 16) | 0xf1490000u)};
    mq = __builtin_bit_cast(s16x4, mu);
  }
  auto mk1 = [&](int i) { return __builtin_bit_cast(s16x4, u32x2{0x3f803f80u, (first && i > 0 && wid + i < 8) ? 0x3f803f80u : 0x00003f80u}); };
  S[1] = MFMA32K8(mk1(1), mq, S[1]);
  S[2] = MFMA32K8(mk1(2), mq, S[2]);
  SB();
  lds_ptr vp = lds + K2_V + wid * 2 * 592 + (l31 < 18 ? hi * 288 + l31 * 16 : 576);
  f32x16 O;
  const unsigned keep0 = first ? 0u : ~0u;
  bf16x8 vf[3];
  auto rdv = [&](auto EE) {
    constexpr int e = EE, blk = e < 14 ? e + 2 : ((e - 14) >> 1) + ((e & 1) ? 16 : 0);
    if constexpr (e < 18) vf[e % 3] = lds_r128(vp + blk * 592);
  };
  rdv(std::integral_constant<int, 0>{});
  rdv(std::integral_constant<int, 1>{});
  SB();
  for_seq<16>([&](auto HH) {
    constexpr int h = HH, i = ((h >> 1) + 1) & 7, s = h & 1;
    u32x4 P;
#pragma unroll
    for (int j = 0; j < 4; ++j) P[j] = cvt_pk(__builtin_amdgcn_exp2f(S[i][8 * s + 2 * j]), __builtin_amdgcn_exp2f(S[i][8 * s + 2 * j + 1]));
    if constexpr (i == 0) {
      constexpr int e = 14 + 2 * s;
      u32x4 P8, P0;
#pragma unroll
      for (int j = 0; j < 4; ++j) {
        const int r0 = 8 * s + 2 * j, r1 = r0 + 1;
        const unsigned mk = ((((r0 & 3) + 8 * (r0 >> 2)) <= dq) ? 0xffffu : 0u) | ((((r1 & 3) + 8 * (r1 >> 2)) <= dq) ? 0xffff0000u : 0u);
        P8[j] = P[j] & mk;
        P0[j] = P[j] & ~mk & keep0;
      }
      rdv(std::integral_constant<int, e + 2>{});
      SB();
      O = MFMA32(vf[e % 3], __builtin_bit_cast(bf16x8, P0), O);
      SB();
      rdv(std::integral_constant<int, e + 3>{});
      O = MFMA32(vf[(e + 1) % 3], __builtin_bit_cast(bf16x8, P8), O);
      SB();
    } else {
      rdv(std::integral_constant<int, h + 2>{});
      SB();
      if constexpr (h == 0) { const f32x16 z = {}; O = MFMA32(vf[0], __builtin_bit_cast(bf16x8, P), z); }
      else O = MFMA32(vf[h % 3], __builtin_bit_cast(bf16x8, P), O);
      if constexpr (s == 0 && i + 2 <= 8) S[(i + 2) & 7] = MFMA32K8(mk1((i + 2) & 7), mq, S[(i + 2) & 7]);
      SB();
    }
  });
  const float inv = __builtin_amdgcn_rcpf(O[10]);
  const int row = t * 32 + l31;
  char* ob = ws + WS_O + (size_t)(row >> 4) * 18 * 1024 + (row & 15) * 16;
  u32x4 o = {cvt_pk(O[0] * inv, O[1] * inv), cvt_pk(O[2] * inv, O[3] * inv), cvt_pk(O[4] * inv, O[5] * inv), cvt_pk(O[6] * inv, O[7] * inv)};
  st16(ob + (hd >> 1) * 1024 + (2 * (hd & 1) + hi) * 256, o);
  if (hi == 0) st4(ws + WS_OX + hd * 8192 + row * 4, cvt_pk(O[8] * inv, O[9] * inv));
}

__global__ __launch_bounds__(704) void k2_attn(char* __restrict__ ws, const char* __restrict__ wg, const char* __restrict__ wu,
                                               const char* __restrict__ wo, const char* __restrict__ x) {
  extern __shared__ __attribute__((aligned(16))) char smem[];
  STAMP_BEGIN
  lds_ptr lds = (lds_ptr)smem;
  const unsigned lds0 = (unsigned)(uintptr_t)lds;
  const int tid = threadIdx.x, lane = tid & 63, wid = __builtin_amdgcn_readfirstlane(tid >> 6);
  const int hd = (blockIdx.x & 7) * 4 + ((blockIdx.x >> 3) & 3), qb = blockIdx.x >> 5;
  const bool has_tile = blockIdx.x < 234;
  const int tile = has_tile ? xcd_remap(blockIdx.x, 234) : 0;
  const int mt = tile / 9, nt = tile - mt * 9;
  const int m0 = min(mt * 80, SEQ - 80), rb0 = m0 >> 4;
  if (wid >= 10) {
    if (lane < 4) *(LDS_AS unsigned*)(lds + K2_SY + lane * 4) = 0u;
    lds_bar();
    __builtin_amdgcn_s_sleep(K2_SL1);
    CODE_TOUCH(SYM_K4, LEN_K4, lds0 + K2_CS);
#pragma unroll
    for (int i = 0; i < K2_PFN; ++i)
      glds16(((blockIdx.x & 1) ? wu : wg) + (size_t)(i * 128 + (blockIdx.x >> 1)) * 1024, lane * 16, lds0 + K2_CS + 1024);
    if (has_tile) { raw_bar(); raw_bar(); }
    return;
  }
  if (wid >= 8) {
    raw_bar();
    __builtin_amdgcn_s_sleep(K2_SL0);
    if (has_tile) {
      const int par = wid - 8;
      glds16(ws + WS_WB + par * 2048, lane * 16, lds0 + K2_WB + par * 2048);
      glds16(ws + WS_WB + 1024, lane * 16, lds0 + K2_WB + 1024);
      unsigned va[4], vb[4];
#pragma unroll
      for (int i = 0; i < 4; ++i) {
        const int rho = 8 * i + (lane >> 3);
        const int g_ = ((rho >> 4) & 1) * 2 + ((rho >> 2) & 1), t_ = (rho >> 3) & 1, q_ = rho & 3;
        const int La = 36 * par + 18 * (g_ >> 1) + 8 * t_ + 4 * (g_ & 1) + q_, Lb = 288 * par + 18 * (4 * g_ + 2 * t_ + (q_ >> 1)) + 16 + (q_ & 1);
        const unsigned csw = ((lane & 7) ^ (((rho >> 1) & 3) << 1)) * 16;
        va[i] = La * 1152 + csw; vb[i] = Lb * 1152 + csw;
      }
#pragma unroll
      for (int kp = 0; kp < 9; ++kp)
#pragma unroll
        for (int i = 0; i < 4; ++i)
          glds16(wo + nt * 128 + (size_t)(kp == 8 ? 0 : 72 * kp) * 1152, kp == 8 ? vb[i] : va[i], lds0 + K2_W + par * 4096 + kp * 8192 + i * 1024);
    }
  } else {
    const int t = qb * 8 + wid;
    u32x4 q0;
    u32x2 q1;
    const int c0 = (18 * hd) >> 3;
    asm volatile("global_load_dwordx4 %0, %2, %4\n\tglobal_load_dwordx2 %1, %3, %4"
                 : "=&v"(q0), "=&v"(q1)
                 : "v"((lane >> 5) * 32768 + (lane & 31) * 16), "v"(65536 + (lane & 31) * 16 + (lane >> 5) * 8),
                   "s"(ws + WS_Q + (size_t)c0 * 32768 + t * 512)
                 : "memory");
    const int key0 = max(qb * 256 - 256 + wid * 64, 0);
#pragma unroll
    for (int i = 0; i < 3; ++i) glds16(ws + WS_K + (size_t)(c0 + i) * 32768 + key0 * 16, (qb == 0 && wid < 4) ? 0 : lane * 16, lds0 + K2_KM + i * 8192 + wid * 1024);
#pragma unroll
    for (int i = 0; i < 3; ++i) {
      const int pc = wid + 8 * i;
      if (pc < 19) {
        const int u = min(pc * 64 + lane, 32 * 37 - 1), bl = u / 37, w = u - bl * 37;
        const int blk = max(qb * 16 - 16 + bl, 0);
        const unsigned vo = w == 36 ? (unsigned)(WS_ONES - WS_V) : (blk * 2 + (w >= 18)) * 9216 + (hd * 18 + (w >= 18 ? w - 18 : w)) * 16;
        glds16(ws + WS_V, vo, lds0 + K2_V + pc * 1024);
      }
    }
    raw_bar();
    asm volatile("s_waitcnt vmcnt(0)" : "+v"(q0), "+v"(q1)::"memory");
    lds_sync(lds + K2_SY, 8, lane);
    STAMP_MARK(1, 2);
    k2_core(lds, ws, hd, t, wid, lane, q0, q1, qb == 0);
    STAMP_MARK(1, 3);
    STAMP_MARKW(1, 7, 448);
  }
  wait_vm<0>();
  STAMP_MARK(1, 4);
  unsigned* cnt = (unsigned*)(ws + WS_CNT);
  if (wid < 8) {
    if (lds_arrive(lds + K2_SY + 8, lane) == 7 && lane < 8) __hip_atomic_fetch_add(cnt + (qb * 8 + lane) * 32, 1u, __ATOMIC_RELAXED, __HIP_MEMORY_SCOPE_AGENT);
  }
  if (!has_tile) { STAMP_END(1); return; }
  lds_sync(lds + K2_SY + 12, 10, lane);
  const int mi = wid >> 1, kg = wid & 1;
  const int g = lane >> 4, q = (lane & 15) >> 2, p = lane & 3;
  const int er = tid >> 3, ec = tid & 7;
  const u32x4 res = *(const u32x4*)(x + (size_t)(m0 + er) * 1152 + (nt * 64 + ec * 8) * 2);
  const float xm = *(const float*)(ws + WS_XM + (m0 + er) * 4);
  const int rho0 = 16 * (g >> 1) + 4 * (g & 1) + q;
  lds_ptr wb[4];
#pragma unroll
  for (int n = 0; n < 4; ++n) { wb[n] = lds + K2_W + kg * 4096 + rho0 * 128 + ((n ^ ((rho0 >> 1) & 3)) * 32) + p * 8; asm volatile("" : "+v"(wb[n])); }
  bf16x8 wf[5][4];
#pragma unroll
  for (int j = 0; j < 5; ++j)
#pragma unroll
    for (int n = 0; n < 4; ++n) wf[j][n] = cat8(lds_tr(wb[n] + j * 8192), lds_tr(wb[n] + j * 8192 + 1024));
  if (tid == 576) {
    const unsigned* c0 = cnt + ((m0 >> 8) * 8 + (tile & 7)) * 32;
    const unsigned* c1 = cnt + (((m0 + 79) >> 8) * 8 + (tile & 7)) * 32;
    for (int spin = 0; spin < (1 << 18); ++spin) {
      const unsigned a = __hip_atomic_load(c0, __ATOMIC_RELAXED, __HIP_MEMORY_SCOPE_AGENT);
      const unsigned b = __hip_atomic_load(c1, __ATOMIC_RELAXED, __HIP_MEMORY_SCOPE_AGENT);
      if (a >= 32u && b >= 32u) break;
      __builtin_amdgcn_s_sleep(1);
    }
  }
  SB();
  raw_bar();
  SB();
  STAMP_MARK(1, 5);
  const __amdgpu_buffer_rsrc_t ors = __builtin_amdgcn_make_buffer_rsrc((void*)(ws + WS_O), 0, 128 * 18432 + 32 * 8192, 0x00020000);
  const int aoff = (rb0 + mi) * 18432 + kg * 1024 + lane * 16;
  bf16x8 a[9];
#pragma unroll
  for (int i = 0; i < 8; ++i) a[i] = __builtin_bit_cast(bf16x8, __builtin_amdgcn_raw_buffer_load_b128(ors, aoff + i * 2048, 0, 16));
  {
    u32x4 ax;
    const int xo = 128 * 18432 + (kg * 16 + 4 * g) * 8192 + (m0 + mi * 16 + (lane & 15)) * 4;
#pragma unroll
    for (int e = 0; e < 4; ++e) ax[e] = __builtin_amdgcn_raw_buffer_load_b32(ors, xo, e * 8192, 16);
    a[8] = __builtin_bit_cast(bf16x8, ax);
  }
  SB();
  f32x4 acc[4];
#pragma unroll
  for (int n = 0; n < 4; ++n) acc[n] = f32x4{0.f, 0.f, 0.f, 0.f};
  f32x4 accm = {0.f, 0.f, 0.f, 0.f};
  lds_ptr wbp = lds + K2_WB + (lane & 1) * 1152 + kg * 64 + g * 16;
#pragma unroll
  for (int j = 0; j < 9; ++j) {
    accm = MFMA16(lds_r128(wbp + j * 128), a[j], accm);
#pragma unroll
    for (int n = 0; n < 4; ++n) {
      acc[n] = MFMA16(wf[j % 5][n], a[j], acc[n]);
      if (j + 5 < 9) wf[j % 5][n] = cat8(lds_tr(wb[n] + (j + 5) * 8192),
                                         lds_tr(wb[n] + (j + 5) * 8192 + 1024));
    }
  }
  STAMP_MARK(1, 6);
  {
    lds_ptr cw = lds + kg * (80 * 272) + (mi * 16 + (lane & 15)) * 272 + 16 * g;
#pragma unroll
    for (int n = 0; n < 4; ++n) *(LDS_AS f32x4*)(cw + n * 64) = acc[n];
    if (g == 0) *(LDS_AS float*)(cw + 256) = accm[0] + accm[1];
  }
  lds_bar();
  {
    lds_ptr cr = lds + er * 272 + ec * 32;
    f32x4 a0 = *(LDS_AS f32x4*)(cr), a1 = *(LDS_AS f32x4*)(cr + 16);
    f32x4 b0 = *(LDS_AS f32x4*)(cr + 80 * 272), b1 = *(LDS_AS f32x4*)(cr + 80 * 272 + 16);
    const float mean = xm + (*(LDS_AS float*)(lds + er * 272 + 256) + *(LDS_AS float*)(lds + (80 + er) * 272 + 256));
    a0 += b0 - mean; a1 += b1 - mean;
    u32x4 o;
    o[0] = cvt_pk(a0[0] + bf_lo(res[0]), a0[1] + bf_hi(res[0]));
    o[1] = cvt_pk(a0[2] + bf_lo(res[1]), a0[3] + bf_hi(res[1]));
    o[2] = cvt_pk(a1[0] + bf_lo(res[2]), a1[1] + bf_hi(res[2]));
    o[3] = cvt_pk(a1[2] + bf_lo(res[3]), a1[3] + bf_hi(res[3]));
    const int row = m0 + er, col = nt * 64 + ec * 8;
    st16(ws + WS_H + (size_t)(row >> 4) * 18432 + (col >> 5) * 1024 + ((col >> 3) & 3) * 256 + (row & 15) * 16, o);
    float s1 = 0.f, s2 = 0.f;
#pragma unroll
    for (int j = 0; j < 4; ++j) { s1 = dot2(o[j], 0x3f803f80u, s1); s2 = dot2(o[j], o[j], s2); }
    s1 = sum8(s1); s2 = sum8(s2);
    if (ec == 1 && nt == 0) st4(ws + WS_HM + row * 4, __float_as_uint(mean));
    if (ec == 0) st8wt(ws + WS_ST + (size_t)nt * 16384 + row * 8, u32x2{__float_as_uint(s1), __float_as_uint(s2)});
  }
  STAMP_END(1);
}

constexpr int K4_SLOT = 18432, K4_ST = K4_SLOT * 8, K4_PF = K4_ST + 2048, K4_SY = 160000, K4_LDS = 160 * 1024;

constexpr int k4_frag(int i) { return i < 16 ? ((i >> 2) * 2 + (i & 1) + ((i & 2) ? 9 : 0)) : (i == 16 ? 8 : 17); }
constexpr int k4_foff(int f) { return (f >= 9 ? 9216 + (f - 9) * 32 : f * 32); }
constexpr int K4_T0 = 13 * 18;
constexpr int k4_slab(int G) { return G < K4_T0 ? G / 18 : (G - K4_T0 < 80 ? 13 + ((G - K4_T0) % 20) / 4 : 13 + (G - K4_T0 - 80) / 2); }
constexpr int k4_pos(int G) { return G < K4_T0 ? G % 18 : (G - K4_T0 < 80 ? ((G - K4_T0) / 20) * 4 + ((G - K4_T0) & 3) : 16 + ((G - K4_T0) & 1)); }
constexpr int k4_op(int G) {
  const int s = G / 18, i = G % 18;
  if (s > 12) return 0;
  if (s < 3) {
    const int pos[10] = {0, 2, 4, 6, 7, 9, 11, 13, 15, 16};
    for (int k = 0; k < 10; ++k) if (pos[k] == i) return 1 + (2 * s + 2 + k / 5) * 8 + k % 5;
    return 0;
  }
  if (s == 3) {
    const int pos[8] = {0, 2, 4, 6, 8, 11, 14, 16};
    for (int k = 0; k < 8; ++k) if (pos[k] == i) return k < 5 ? 1 + 8 * 8 + k : 1 + 9 * 8 + (k - 3);
    return 0;
  }
  if (i == 1) return 1 + (s + 5) * 8;
  if (i == 4) return 1 + (s + 5) * 8 + 1;
  if (s + 6 > 17) return 0;
  if (i == 8) return 1 + (s + 6) * 8 + 2;
  if (i == 13) return 1 + (s + 6) * 8 + 3;
  if (i == 16) return 1 + (s + 6) * 8 + 4;
  return 0;
}
constexpr int k4_allow(int G, int need) {
  int n = (need == 0) ? 4 : 0;
  for (int g = 0; g < G; ++g) {
    const int op = k4_op(g);
    if (op == 0) continue;
    const int sl = (op - 1) >> 3, kind = (op - 1) & 7;
    if (sl <= need) n = 0;
    else if (kind != 4) ++n;
  }
  return n;
}
constexpr int k4_need(int G) {
  if (G + 5 >= 324 || G == 0) return 0;
  int mx = 0;
  for (int g = 0; g < G + 5; ++g) mx = k4_slab(g) > mx ? k4_slab(g) : mx;
  return k4_slab(G + 5) > mx ? k4_slab(G + 5) : 0;
}

constexpr int d_off(int i) { return i < 6 ? 36864 + i * 9216 : (i < 10 ? (i - 6) * 9216 : (i < 17 ? 92160 + (i - 10) * 9216 : 36864)); }
#define DT 8
constexpr int D_G0 = (18 - DT) * 9;
constexpr int d_i(int H) { return H < D_G0 ? H / 9 : (H - D_G0 < 8 * DT ? 18 - DT + ((H - D_G0) % (2 * DT)) / 2 : 18 - DT + (H - D_G0 - 8 * DT)); }
constexpr int d_f(int H) { return H < D_G0 ? H % 9 : (H - D_G0 < 8 * DT ? ((H - D_G0) / (2 * DT)) * 2 + ((H - D_G0) & 1) : 8); }
constexpr int d_first(int H) {
  for (int h = 0; h < H; ++h) if (d_i(h) == d_i(H)) return 0;
  return 1;
}
DI void ld2_sc1(bf16x8& d0, bf16x8& d1, unsigned o0, unsigned o1, const char* base) {
  asm volatile("global_load_dwordx4 %0, %2, %4 sc1\n\tglobal_load_dwordx4 %1, %3, %4 sc1" : "=&v"(d0), "=&v"(d1) : "v"(o0), "v"(o1), "s"(base) : "memory");
}
template <int N> DI void wait_vm2(bf16x8& d0, bf16x8& d1) { asm volatile("s_waitcnt vmcnt(%2)" : "+v"(d0), "+v"(d1) : "n"(N) : "memory"); }
DI void poll_count(const unsigned* c, unsigned n) {
  for (int spin = 0; spin < (1 << 18); ++spin) {
    if (__hip_atomic_load(c, __ATOMIC_RELAXED, __HIP_MEMORY_SCOPE_AGENT) >= n) break;
    __builtin_amdgcn_s_sleep(1);
  }
}

__global__ __launch_bounds__(512, 2) void k4_gateup(const char* __restrict__ wgate, const char* __restrict__ wup, char* __restrict__ ws,
                                                    const char* __restrict__ wd, char* __restrict__ out) {
  extern __shared__ __attribute__((aligned(16))) char smem[];
  STAMP_BEGIN
  lds_ptr lds = (lds_ptr)smem;
  const unsigned lds0 = (unsigned)(uintptr_t)lds;
  const int tid = threadIdx.x, lane = tid & 63, wid = __builtin_amdgcn_readfirstlane(tid >> 6);
  const int xcd = blockIdx.x & 7, idx = blockIdx.x >> 3;
  const int pn = 4 * (xcd >> 2) + (idx & 3), jt = 8 * (xcd & 3) + (idx >> 2);
  const int wm = wid & 3, wn = wid >> 2;
  const char* asrc = ws + WS_H + (size_t)(pn * 16 + 2 * wid) * 18432;
  const unsigned avo = lane * 16, avo2 = lane * 16 + 18432;
  const char* wsrc = (wn ? wup : wgate) + jt * 288;
  unsigned wvo[3];
#pragma unroll
  for (int i = 0; i < 3; ++i) {
    const int pc = i < 2 ? 2 * wm + i : 8;
    const int o = pc * 1024 + lane * 16;
    const int rho = o / 288, u = (o - rho * 288) >> 4;
    const int gk = ((rho >> 4) & 1) * 2 + ((rho >> 2) & 1), tk = (rho >> 3) & 1, qk = rho & 3;
    wvo[i] = (8 * gk + 4 * tk + qk) * 9216 + u * 16;
  }
  const unsigned wdst = lds0 + wn * 9216;
  auto issue_w = [&](int s, int i) {
    const unsigned so = (s & 7) * K4_SLOT;
    const char* wp = wsrc + (size_t)s * 32 * 9216;
    if (i < 2) glds16(wp, wvo[i], wdst + so + wm * 2048 + i * 1024);
    else glds16_if0(wm, wp, wvo[2], wdst + so + 8192);
  };
  bf16x8 A[6][2];
  auto issue_a = [&](int s, bf16x8& dst, int m) {
    asm volatile("global_load_dwordx4 %0, %1, %2" : "=v"(dst) : "v"(m ? avo2 : avo), "s"(asrc + s * 1024) : "memory");
  };
  f32x2 sp[5];
  {
    const char* stp = ws + WS_ST + (size_t)(pn * 256 + wid * 32) * 8;
    const unsigned so = (lane & 31) * 8 + (lane >> 5) * 5 * 16384;
    asm volatile("global_load_dwordx2 %0, %5, %7\n\tglobal_load_dwordx2 %1, %5, %8\n\tglobal_load_dwordx2 %2, %5, %9\n\t"
                 "global_load_dwordx2 %3, %5, %10\n\tglobal_load_dwordx2 %4, %6, %10"
                 : "=&v"(sp[0]), "=&v"(sp[1]), "=&v"(sp[2]), "=&v"(sp[3]), "=&v"(sp[4])
                 : "v"(so), "v"((lane & 31) * 8 + 16384), "s"(stp), "s"(stp + 16384), "s"(stp + 32768), "s"(stp + 49152) : "memory");
  }
  for_seq<2>([&](auto S) { issue_w(S, 0); issue_w(S, 1); issue_w(S, 2); issue_a(S, A[S][0], 0); issue_a(S, A[S][1], 1); });
  {
    asm volatile("s_waitcnt vmcnt(8)" : "+v"(sp[0]), "+v"(sp[1]), "+v"(sp[2]), "+v"(sp[3]), "+v"(sp[4])::"memory");
    const f32x2 t4 = (sp[0] + sp[1]) + (sp[2] + sp[3]);
    auto r1 = __builtin_amdgcn_permlane32_swap(__float_as_uint(t4[0]), __float_as_uint(t4[0]), false, false);
    auto r2 = __builtin_amdgcn_permlane32_swap(__float_as_uint(t4[1]), __float_as_uint(t4[1]), false, false);
    const float s1 = __uint_as_float(r1[0]) + __uint_as_float(r1[1]) + sp[4][0];
    const float s2 = __uint_as_float(r2[0]) + __uint_as_float(r2[1]) + sp[4][1];
    const float mean = s1 * (1.f / 576.f);
    const float var = fmaxf(s2 * (1.f / 576.f) - mean * mean, 0.f);
    *(LDS_AS f32x2*)(lds + K4_ST + (wid * 32 + (lane & 31)) * 8) = f32x2{-1.4426950408889634f * __builtin_amdgcn_rsqf(var + LN_EPS), __builtin_amdgcn_sqrtf(var + LN_EPS)};
    *(LDS_AS unsigned*)(lds + K4_SY) = 0u;
    SB();
  }
  const int g = lane >> 4, q = (lane & 15) >> 2, p = lane & 3, r = lane & 15;
  lds_ptr wb = lds + (16 * (g >> 1) + 4 * (g & 1) + q) * 288 + p * 8;
  lds_ptr wbs[3] = {wb, wb + 3 * K4_SLOT, wb + 6 * K4_SLOT};
  asm volatile("" : "+v"(wbs[1]), "+v"(wbs[2]));
  f32x4 acc[2][18];
  asm volatile("s_waitcnt vmcnt(%2)\n\ts_barrier" : "+v"(A[0][0]), "+v"(A[0][1]) : "n"(k4_allow(0, 0)) : "memory");
  STAMP_MARK(2, 2);
  STAMP_CYC0
  s16x4 Bl[6], Bh[6];
  for_seq<5>([&](auto I) { Bl[I] = lds_tr(wb + k4_foff(k4_frag(I))); Bh[I] = lds_tr(wb + k4_foff(k4_frag(I)) + 2304); });
  SB();
  char* hb = ws + WS_HID + (size_t)(pn * 16 + wid * 2) * (144 * 1024) + (size_t)(jt >> 2) * 18432;
  float c1[2];
  float sd[2];
  auto glu = [&](float ag, float au, int m) -> float {
    return au * __builtin_amdgcn_rcpf(fmaf(__builtin_amdgcn_exp2f(ag * c1[m]), sd[m], sd[m]));
  };
  u32x4 own[4][2];
  float eg[2][2];
  auto micro = [&](auto QQ, auto KK, auto JJ) {
    constexpr int Q = QQ, k = KK, j = JJ, m = k >> 2, d = k & 3, h = d >> 1, e = (d & 1) * 2, b = k & 1;
#pragma unroll
    for (int x = 0; x < 2; ++x) {
      if constexpr (j == 0) eg[b][x] = acc[m][2 * Q + h][e + x] * c1[m];
      if constexpr (j == 1) eg[b][x] = __builtin_amdgcn_exp2f(eg[b][x]);
      if constexpr (j == 2) eg[b][x] = fmaf(eg[b][x], sd[m], sd[m]);
      if constexpr (j == 3) eg[b][x] = __builtin_amdgcn_rcpf(eg[b][x]);
      if constexpr (j == 4) eg[b][x] *= acc[m][9 + 2 * Q + h][e + x];
      if constexpr (j < 5) pin(eg[b][x]);
    }
    if constexpr (j == 6) {
      own[Q][m][d] = cvt_pk(eg[b][0], eg[b][1]);
      if constexpr (d == 3) st16(hb + (size_t)m * (144 * 1024) + (4 * (jt & 3) + Q) * 1024 + lane * 16, own[Q][m]);
      else pin(own[Q][m]);
    }
  };
  auto gapops = [&](auto QQ, auto GG) {
    constexpr int gg = GG;
    if constexpr (gg >= 4 && gg < 36) micro(QQ, std::integral_constant<int, gg / 4 - 1>{}, std::integral_constant<int, 4 + gg % 4>{});
    if constexpr (gg >= 0 && gg < 32) micro(QQ, std::integral_constant<int, gg / 4>{}, std::integral_constant<int, gg % 4>{});
    SB();
  };
  unsigned dvo[2], dvl[2];
  const char* dsrc = wd + (size_t)(jt >> 2) * 576 * 1152 + (jt & 3) * 288;
  auto dvoff = [&]() {
    int ln = tid;
    asm volatile("" : "+v"(ln));
    ln &= 63;
#pragma unroll
    for (int i = 0; i < 2; ++i) {
      const int o = (i ? 8 : wid) * 1024 + ln * 16;
      const int rho = o / 288, u = (o - rho * 288) >> 4;
      const int gk = ((rho >> 4) & 1) * 2 + ((rho >> 2) & 1), tk = (rho >> 3) & 1, qk = rho & 3;
      dvo[i] = (16 * tk + 4 * gk + qk) * 1152 + u * 16;
      dvl[i] = (144 * (gk >> 1) + 128 + 8 * (gk & 1) + 4 * tk + qk) * 1152 + u * 16;
    }
  };
  auto issue_d = [&](auto II, int pc) {
    constexpr int i = II;
    const int k = (4 * (jt & 3) + i) & 15;
    const char* src = dsrc + (size_t)(i < 16 ? 144 * (k >> 2) + 32 * (k & 3) : 288 * (i - 16)) * 1152;
    if (pc == 0) glds16(src, i < 16 ? dvo[0] : dvl[0], lds0 + d_off(i) + wid * 1024);
    else glds16_if0(wid ^ (i & 7), src, i < 16 ? dvo[1] : dvl[1], lds0 + d_off(i) + 8192);
  };
  for_seq<18>([&](auto GH) { for_seq<18>([&](auto GL) {
    constexpr int G = GH * 18 + GL, sl = k4_slab(G), f = k4_frag(k4_pos(G)), rb = G % 6, nb = (G + 5) % 6, NG = G + 5;
    const bf16x8 b = cat8(Bl[rb], Bh[rb]);
    constexpr int need = k4_need(G);
    if constexpr (need != 0) {
      asm volatile("s_waitcnt vmcnt(%2)\n\ts_barrier" : "+v"(A[need % 6][0]), "+v"(A[need % 6][1]) : "n"(k4_allow(G, need)) : "memory");
      SB();
    }
    if constexpr (sl == 0) mfma16_zero(acc[0][f], b, A[0][0]); else mfma16_acc(acc[0][f], b, A[sl % 6][0]);
    SB();
    if constexpr (NG < 324) { Bl[nb] = lds_tr(wbs[(k4_slab(NG) & 7) / 3] + ((k4_slab(NG) & 7) % 3) * K4_SLOT + k4_foff(k4_frag(k4_pos(NG)))); SB(); }
    if constexpr (G >= K4_T0 + 20 && G < K4_T0 + 80 && (G - K4_T0) % 20 >= 1 && (G - K4_T0) % 20 < 19)
      gapops(std::integral_constant<int, (G - K4_T0) / 20 - 1>{}, std::integral_constant<int, 2 * ((G - K4_T0) % 20 - 1)>{});
    if constexpr (G >= K4_T0 + 81) {
      gapops(std::integral_constant<int, 3>{}, std::integral_constant<int, 4 * (G - K4_T0 - 81)>{});
      gapops(std::integral_constant<int, 3>{}, std::integral_constant<int, 4 * (G - K4_T0 - 81) + 1>{});
    }
    if constexpr (sl == 0) mfma16_zero(acc[1][f], b, A[0][1]); else mfma16_acc(acc[1][f], b, A[sl % 6][1]);
    SB();
    if constexpr (NG < 324) { Bh[nb] = lds_tr(wbs[(k4_slab(NG) & 7) / 3] + ((k4_slab(NG) & 7) % 3) * K4_SLOT + k4_foff(k4_frag(k4_pos(NG))) + 2304); SB(); }
    constexpr int op = k4_op(G);
    if constexpr (op != 0) {
      constexpr int os = (op - 1) >> 3, kind = (op - 1) & 7;
      if constexpr (kind < 2) issue_a(os, A[os % 6][kind], kind);
      else issue_w(os, kind - 2);
      SB();
    }
    if constexpr (G % 18 == 10 && (G / 18 == 5 || G / 18 == 7 || G / 18 == 9)) {
      constexpr int k = (G / 18 - 5) / 2;
      touch_if0((int)(blockIdx.x / 216) + (wid ^ (k + 1)), wd + (size_t)(blockIdx.x * 3 + k) * 8192, avo * 8, lds0 + K4_PF + k * 256);
      SB();
    }
    if constexpr (G == K4_T0) {
      STAMP_MARK(2, 3);
      STAMP_CYC(2, 4);
#pragma unroll
      for (int m = 0; m < 2; ++m) { const f32x2 st = *(LDS_AS f32x2*)(lds + K4_ST + (wid * 32 + m * 16 + r) * 8); c1[m] = st[0]; sd[m] = st[1]; }
      SB();
    }
    if constexpr (G >= K4_T0) {
      constexpr int t = G - K4_T0, u = t % 20;
      if constexpr (t == 11) { dvoff(); SB(); }
      if constexpr (t >= 12 && t < 24) { issue_d(std::integral_constant<int, (t - 12) / 2>{}, t & 1); SB(); }
      if constexpr (t >= 20 && t < 80 && u >= 1 && u < 19) gapops(std::integral_constant<int, t / 20 - 1>{}, std::integral_constant<int, 2 * (u - 1) + 1>{});
      if constexpr (t >= 81) {
        gapops(std::integral_constant<int, 3>{}, std::integral_constant<int, 4 * (t - 81) + 2>{});
        gapops(std::integral_constant<int, 3>{}, std::integral_constant<int, 4 * (t - 81) + 3>{});
      }
    }
  }); });
  {
    asm volatile("s_nop 7\n\ts_nop 1" : "+v"(acc[0][8]), "+v"(acc[1][8]), "+v"(acc[0][17]), "+v"(acc[1][17]));
#pragma unroll
    for (int m = 0; m < 2; ++m) {
      const f32x4 ag = acc[m][8], au = acc[m][17];
      u32x2 o = {cvt_pk(glu(ag[0], au[0], m), glu(ag[1], au[1], m)), cvt_pk(glu(ag[2], au[2], m), glu(ag[3], au[3], m))};
      st8wt(hb + (size_t)m * (144 * 1024) + (16 + ((jt >> 1) & 1)) * 1024 + (2 * (jt & 1) + (g >> 1)) * 256 + r * 16 + (g & 1) * 8, o);
    }
  }
  STAMP_MARK(2, 5);
  const int sb = jt >> 2, nq = jt & 3;
  unsigned* cntA = (unsigned*)(ws + WS_CNT + 8192) + (pn * 8 + sb) * 32;
  unsigned* cntB = (unsigned*)(ws + WS_CNT + 16384) + (pn * 4 + nq) * 32;
  wait_bar<10>();
  auto dptr = [&](auto HH) -> lds_ptr {
    constexpr int H = HH, o = d_off(d_i(H)) + d_f(H) * 32;
    return wbs[o / (3 * K4_SLOT)] + o % (3 * K4_SLOT);
  };
  for_seq<5>([&](auto I) { Bl[I] = lds_tr(dptr(I)); Bh[I] = lds_tr(dptr(I) + 2304); });
  SB();
  f32x4 dacc[2][9];
  bf16x8 DA[14][2];
  char* pb = ws + WS_PART + (size_t)(((pn * 4 + nq) * 8 + wid) * 8 + sb) * 9216 + lane * 16;
  for_seq<18>([&](auto HH) { for_seq<9>([&](auto HL) {
    constexpr int H = HH * 9 + HL, i = d_i(H), f = d_f(H), rb = H % 6, nb = (H + 5) % 6, NH = H + 5;
    if constexpr (H == 36) {
      if (tid == 0) poll_count(cntA, 4u);
      SB();
      wait_bar<0>();
      STAMP_MARK(2, 6);
      issue_d(std::integral_constant<int, 17>{}, 0); issue_d(std::integral_constant<int, 17>{}, 1);
      const unsigned ho = lane * 16, ho2 = lane * 16 + 144 * 1024;
      for_seq<14>([&](auto JJ) {
        constexpr int j = JJ;
        const int k = j < 12 ? ((4 * nq + 4 + j) & 15) : 4 + j;
        ld2_sc1(DA[j][0], DA[j][1], ho, ho2, hb + k * 1024);
      });
      SB();
    }
    if constexpr (i >= 4 && d_first(H)) {
      wait_vm2<2 * (17 - i)>(DA[i - 4][0], DA[i - 4][1]);
      SB();
    }
    if constexpr (H == D_G0) { raw_bar(); SB(); }
    const bf16x8 b = cat8(Bl[rb], Bh[rb]);
    bf16x8 a0, a1;
    if constexpr (i < 4) { a0 = __builtin_bit_cast(bf16x8, own[i][0]); a1 = __builtin_bit_cast(bf16x8, own[i][1]); }
    else { a0 = DA[i - 4][0]; a1 = DA[i - 4][1]; }
    if constexpr (i == 0) mfma16_zero(dacc[0][f], b, a0); else mfma16_acc(dacc[0][f], b, a0);
    SB();
    if constexpr (NH < 162) { Bl[nb] = lds_tr(dptr(std::integral_constant<int, NH>{})); SB(); }
    if constexpr (i == 0) mfma16_zero(dacc[1][f], b, a1); else mfma16_acc(dacc[1][f], b, a1);
    SB();
    if constexpr (NH < 162) { Bh[nb] = lds_tr(dptr(std::integral_constant<int, NH>{}) + 2304); SB(); }
    if constexpr (H < 22) { issue_d(std::integral_constant<int, 6 + H / 2>{}, H & 1); SB(); }
    if constexpr (H == 24) {
      wait_vm<11>();
      if (lds_arrive(lds + K4_SY, lane) == 7 && lane == 0) __hip_atomic_fetch_add(cntA, 1u, __ATOMIC_RELAXED, __HIP_MEMORY_SCOPE_AGENT);
      SB();
    }
    if constexpr (H >= D_G0 + 2 * DT && (((H - D_G0) % (2 * DT) == 2 || (H - D_G0) % (2 * DT) == 3))) {
      constexpr int pr = (H - D_G0) / (2 * DT) - 1, m = ((H - D_G0) % (2 * DT) == 3);
      const f32x4 x0 = dacc[m][2 * pr], x1 = dacc[m][2 * pr + 1];
      st16(pb + (m * 4 + pr) * 1024, u32x4{cvt_pk(x0[0], x0[1]), cvt_pk(x0[2], x0[3]), cvt_pk(x1[0], x1[1]), cvt_pk(x1[2], x1[3])});
      SB();
    }
  }); });
  {
    asm volatile("s_nop 7\n\ts_nop 1" : "+v"(dacc[0][8]), "+v"(dacc[1][8]));
    const f32x4 x0 = dacc[0][8], x1 = dacc[1][8];
    st16(pb + 8192, u32x4{cvt_pk(x0[0], x0[1]), cvt_pk(x0[2], x0[3]), cvt_pk(x1[0], x1[1]), cvt_pk(x1[2], x1[3])});
  }
  wait_bar<0>();
  STAMP_MARK(2, 7);
  if (tid == 0) __hip_atomic_fetch_add(cntB, 1u, __ATOMIC_RELAXED, __HIP_MEMORY_SCOPE_AGENT);
  {
    const int nc = wid == 1 ? 2 : 1;
    const int rbase = pn * 256 + sb * 32;
    int row[2][2], col[2][2];
    row[0][0] = row[0][1] = rbase + (wid >> 2) * 16 + r; col[0][0] = nq * 144 + (wid & 3) * 32 + 4 * g; col[0][1] = col[0][0] + 16;
    row[1][0] = rbase + r; row[1][1] = rbase + 16 + r; col[1][0] = col[1][1] = nq * 144 + 128 + 4 * g;
    u32x2 res[2][2];
    float hm[2][2];
#pragma unroll
    for (int c = 0; c < 2; ++c)
#pragma unroll
      for (int e = 0; e < 2; ++e)
        res[c][e] = *(const u32x2*)(ws + WS_H + (size_t)(row[c][e] >> 4) * 18432 + (col[c][e] >> 5) * 1024 + ((col[c][e] >> 3) & 3) * 256 + (row[c][e] & 15) * 16 + ((col[c][e] >> 2) & 1) * 8),
        hm[c][e] = *(const float*)(ws + WS_HM + row[c][e] * 4);
    if (tid == 0) poll_count(cntB, 8u);
    SB();
    raw_bar();
    SB();
    const __amdgpu_buffer_rsrc_t prs = __builtin_amdgcn_make_buffer_rsrc((void*)(ws + WS_PART + (size_t)((pn * 4 + nq) * 8 + sb) * 8 * 9216), 0, 8 * 9216, 0x00020000);
#pragma unroll
    for (int c = 0; c < 2; ++c) {
      if (c < nc) {
        u32x4 pv[8];
#pragma unroll
        for (int k = 0; k < 8; ++k) pv[k] = __builtin_amdgcn_raw_buffer_load_b128(prs, lane * 16 + (c ? 8192 : wid * 1024), k * 9216, 16);
        float sum[8];
#pragma unroll
        for (int e = 0; e < 4; ++e) { sum[2 * e] = bf_lo(res[c][e >> 1][e & 1]) + hm[c][e >> 1]; sum[2 * e + 1] = bf_hi(res[c][e >> 1][e & 1]) + hm[c][e >> 1]; }
        float acc2[8];
#pragma unroll
        for (int e = 0; e < 8; ++e) acc2[e] = 0.f;
#pragma unroll
        for (int k = 0; k < 8; ++k)
#pragma unroll
          for (int e = 0; e < 4; ++e) { acc2[2 * e] += bf_lo(pv[k][e]); acc2[2 * e + 1] += bf_hi(pv[k][e]); }
        unsigned pk[4];
#pragma unroll
        for (int e = 0; e < 4; ++e) pk[e] = cvt_pk(sum[2 * e] + acc2[2 * e], sum[2 * e + 1] + acc2[2 * e + 1]);
        auto x0 = __builtin_amdgcn_permlane16_swap(pk[0], pk[2], false, false);
        auto x1 = __builtin_amdgcn_permlane16_swap(pk[1], pk[3], false, false);
        const int sel = g & 1;
        st16(out + (size_t)(row[c][0] + (c ? 16 * sel : 0)) * 1152 + (col[c][0] + (c ? -4 : 12) * sel) * 2, (u32x4{x0[0], x1[0], x0[1], x1[1]}));
      }
    }
  }
  STAMP_END(2);
}

extern "C" void kernel_launch(void* const* d_in, const int* in_sizes, int n_in, void* d_out, int out_size, void* d_ws, size_t ws_size,
                              hipStream_t stream) {
  const char* x = (const char*)d_in[0];
  const char* wqkv = (const char*)d_in[1];
  const char* wo = (const char*)d_in[2];
  const char* wg = (const char*)d_in[4];
  const char* wu = (const char*)d_in[5];
  const char* wd = (const char*)d_in[6];
  char* ws = (char*)d_ws + WS_BASE(ws_size);
  (void)hipFuncSetAttribute((const void*)k1_ln_qkv, hipFuncAttributeMaxDynamicSharedMemorySize, K1_LDS);
  (void)hipFuncSetAttribute((const void*)k2_attn, hipFuncAttributeMaxDynamicSharedMemorySize, K2_LDS);
  (void)hipFuncSetAttribute((const void*)k4_gateup, hipFuncAttributeMaxDynamicSharedMemorySize, K4_LDS);
  k1_ln_qkv<<<234, 1024, K1_LDS, stream>>>(x, wqkv, ws, wo);
  k2_attn<<<256, 704, K2_LDS, stream>>>(ws, wg, wu, wo, x);
  k4_gateup<<<256, 512, K4_LDS, stream>>>(wg, wu, ws, wd, (char*)d_out);
}
```

```cpp
#include <hip/hip_runtime.h>
#include <hip/hip_bf16.h>
#include <stdint.h>
#include <utility>
#include <type_traits>

typedef __attribute__((ext_vector_type(8))) short bf16x8;
typedef __attribute__((ext_vector_type(4))) short s16x4;
typedef __attribute__((ext_vector_type(4))) float f32x4;
typedef __attribute__((ext_vector_type(16))) float f32x16;
typedef __attribute__((ext_vector_type(4))) unsigned u32x4;
typedef __attribute__((ext_vector_type(2))) unsigned u32x2;
typedef __attribute__((ext_vector_type(2))) __bf16 bf16x2_t;
typedef __attribute__((ext_vector_type(2))) float f32x2;
#define LDS_AS __attribute__((address_space(3)))
typedef LDS_AS char* lds_ptr;
#define DI __device__ __forceinline__

constexpr int SEQ = 2048, DM = 576, NH = 32, FF = 4608;
constexpr float LN_EPS = 1e-5f;
constexpr float QSCALE = 0.23570226039551584f * 1.4426950408889634f;

constexpr size_t WS_Q = 0;
constexpr size_t WS_K = WS_Q + (size_t)72 * 32768;
constexpr size_t WS_V = WS_K + (size_t)72 * 32768;
constexpr size_t WS_ONES = WS_V + (size_t)256 * 9216;
constexpr size_t WS_O = WS_ONES + 1024;
constexpr size_t WS_OX = WS_O + (size_t)128 * 18 * 1024;
constexpr size_t WS_H = WS_OX + (size_t)32 * 8192;
constexpr size_t WS_ST = WS_H + (size_t)128 * 18 * 1024;
constexpr size_t WS_HM = WS_ST + (size_t)16 * 16384;
constexpr size_t WS_XM = WS_HM + 16384;
constexpr size_t WS_WB = WS_XM + 8192;
constexpr size_t WS_HID = WS_WB + 9216;
constexpr size_t WS_CNT = WS_HID + (size_t)128 * 144 * 1024;
constexpr size_t WS_PART = WS_CNT + 24576;
constexpr size_t WS_END = WS_PART + (size_t)2048 * 9216;
static_assert(WS_O % 1024 == 0 && WS_H % 1024 == 0 && WS_ST % 256 == 0 && WS_HID % 256 == 0, "align");

#define WS_BASE(sz) (((sz) - WS_END) & ~(size_t)0x1fffff)
#define STAMP_BEGIN
#define STAMP_MARK(k, i)
#define STAMP_MARKW(k, i, T)
#define STAMP_ENDW(k, T)
#define STAMP_CYC0
#define STAMP_CYC(k, i)
#define STAMP_END(k)

DI unsigned cvt_pk(float lo, float hi) {
  f32x2 v = {lo, hi};
  bf16x2_t r = __builtin_convertvector(v, bf16x2_t);
  return __builtin_bit_cast(unsigned, r);
}
DI float bf_lo(unsigned u) { return __builtin_bit_cast(float, u << 16); }
DI float bf_hi(unsigned u) { return __builtin_bit_cast(float, u & 0xffff0000u); }
DI float dot2(unsigned a, unsigned b, float c) {
  return __builtin_amdgcn_fdot2_f32_bf16(__builtin_bit_cast(bf16x2_t, a), __builtin_bit_cast(bf16x2_t, b), c, false);
}
template <int CTRL> DI float dpp_add(float v) {
  int t = __builtin_amdgcn_update_dpp(0, __builtin_bit_cast(int, v), CTRL, 0xf, 0xf, true);
  return v + __builtin_bit_cast(float, t);
}
DI float sum8(float v) {
  v = dpp_add<0xB1>(v);
  v = dpp_add<0x4E>(v);
  v = dpp_add<0x141>(v);
  return v;
}
#define GNOP "3"
DI float sum_g(float v) {
  auto a = __builtin_amdgcn_permlane16_swap(__float_as_uint(v), __float_as_uint(v), false, false);
  v = __uint_as_float(a[0]) + __uint_as_float(a[1]);
  auto b = __builtin_amdgcn_permlane32_swap(__float_as_uint(v), __float_as_uint(v), false, false);
  return __uint_as_float(b[0]) + __uint_as_float(b[1]);
}
DI void glds16(const void* sbase, unsigned voff, unsigned lds_dst) {
  asm volatile("s_mov_b32 m0, %2\n\ts_nop " GNOP "\n\tglobal_load_lds_dwordx4 %0, %1" ::"v"(voff), "s"(sbase), "s"(lds_dst) : "memory");
}
#define ST_AUX " sc1"
DI void st16(void* p, u32x4 v) { asm volatile("global_store_dwordx4 %0, %1, off" ST_AUX "\n\ts_nop 1" ::"v"(p), "v"(v) : "memory"); }
DI void st8(void* p, u32x2 v) { *(u32x2*)p = v; }
DI void st8wt(void* p, u32x2 v) { asm volatile("global_store_dwordx2 %0, %1, off sc1" ::"v"(p), "v"(v) : "memory"); }
DI void st4(void* p, unsigned v) { asm volatile("global_store_dword %0, %1, off sc1" ::"v"(p), "v"(v) : "memory"); }
DI void glds16_if0(int c, const void* sbase, unsigned voff, unsigned lds_dst) {
  asm volatile("s_cmp_lg_u32 %3, 0\n\ts_cbranch_scc1 1f\n\ts_mov_b32 m0, %2\n\ts_nop 1\n\tglobal_load_lds_dwordx4 %0, %1\n1:"
               ::"v"(voff), "s"(sbase), "s"(lds_dst), "s"(c) : "memory", "scc");
}
DI void touch_if0(int c, const void* sbase, unsigned voff, unsigned lds_dst) {
  asm volatile("s_cmp_lg_u32 %3, 0\n\ts_cbranch_scc1 1f\n\ts_mov_b32 m0, %2\n\ts_nop 1\n\tglobal_load_lds_dword %0, %1\n1:"
               ::"v"(voff), "s"(sbase), "s"(lds_dst), "s"(c) : "memory", "scc");
}
DI void touch_lines(const char* base, unsigned voff, unsigned lds_dummy) {
  asm volatile("s_mov_b32 m0, %2\n\ts_nop 3\n\tglobal_load_lds_dword %0, %1" ::"v"(voff), "s"(base), "s"(lds_dummy) : "memory");
}
#define CODE_TOUCH(SYM, BYTES, LDS_DUMMY)                                                                          \
  do {                                                                                                             \
    const char* cp_;                                                                                               \
    asm volatile("s_getpc_b64 s[20:21]\n\ts_add_u32 s20, s20, " SYM "@rel32@lo+4\n\ts_addc_u32 s21, s21, " SYM     \
                 "@rel32@hi+12\n\ts_mov_b64 %0, s[20:21]" : "=s"(cp_)::"s20", "s21", "scc");                      \
    constexpr int nl_ = (BYTES) / 128, per_ = (nl_ + 28) / 29;                                                      \
    const int ln_ = (blockIdx.x >> 3) * per_ + lane;                                                               \
    if (lane < per_ && ln_ < nl_) touch_lines(cp_, ln_ * 128, LDS_DUMMY);                                          \
  } while (0)
#define SYM_K1 "_Z9k1_ln_qkvPKcS0_PcS0_"
#define SYM_K4 "_Z9k4_gateupPKcS0_PcS0_S1_S0_S0_"
constexpr int LEN_K1 = 22600, LEN_K4 = 40400;
template <int N> DI void wait_bar() { asm volatile("s_waitcnt vmcnt(%0) lgkmcnt(0)\n\ts_barrier" ::"n"(N) : "memory"); }
template <int N> DI void wait_vm() { asm volatile("s_waitcnt vmcnt(%0)" ::"n"(N) : "memory"); }
DI unsigned lds_arrive(lds_ptr c, int lane) {
  unsigned old = 0;
  if (lane == 0) old = __hip_atomic_fetch_add((LDS_AS unsigned*)c, 1u, __ATOMIC_RELAXED, __HIP_MEMORY_SCOPE_WORKGROUP);
  return __builtin_amdgcn_readfirstlane(old);
}
DI void lds_sync(lds_ptr c, unsigned n, int lane) {
  asm volatile("" ::: "memory");
  if (lds_arrive(c, lane) + 1 != n) {
    for (int spin = 0; spin < (1 << 20); ++spin) {
      const unsigned v = __builtin_amdgcn_readfirstlane(__hip_atomic_load((LDS_AS unsigned*)c, __ATOMIC_RELAXED, __HIP_MEMORY_SCOPE_WORKGROUP));
      if (v >= n) break;
      __builtin_amdgcn_s_sleep(1);
    }
  }
  asm volatile("" ::: "memory");
}
DI void lds_post(lds_ptr c, int lane) {
  asm volatile("" ::: "memory");
  if (lane == 0) (void)__hip_atomic_fetch_add((LDS_AS unsigned*)c, 1u, __ATOMIC_RELAXED, __HIP_MEMORY_SCOPE_WORKGROUP);
}
DI void lds_wait(lds_ptr c, unsigned n) {
  for (int spin = 0; spin < (1 << 20); ++spin) {
    const unsigned v = __builtin_amdgcn_readfirstlane(__hip_atomic_load((LDS_AS unsigned*)c, __ATOMIC_RELAXED, __HIP_MEMORY_SCOPE_WORKGROUP));
    if (v >= n) break;
    __builtin_amdgcn_s_sleep(1);
  }
  asm volatile("" ::: "memory");
}
template <class T> DI void pin(T& v) { asm volatile("" : "+v"(v)); }
DI void raw_bar() { asm volatile("s_barrier" ::: "memory"); }
DI void lds_bar() { asm volatile("s_waitcnt lgkmcnt(0)\n\ts_barrier" ::: "memory"); }
DI bf16x8 lds_r128(lds_ptr p) { return *(LDS_AS const bf16x8*)p; }
typedef short v4i16_t __attribute__((ext_vector_type(4)));
DI s16x4 lds_tr(lds_ptr p) { return __builtin_amdgcn_ds_read_tr16_b64_v4i16((LDS_AS v4i16_t*)p); }
DI bf16x8 cat8(s16x4 a, s16x4 b) { return __builtin_shufflevector(a, b, 0, 1, 2, 3, 4, 5, 6, 7); }
#define SB() __builtin_amdgcn_sched_barrier(0)
#define MFMA16(a, b, c) __builtin_amdgcn_mfma_f32_16x16x32_bf16(a, b, c, 0, 0, 0)
#define MFMA32(a, b, c) __builtin_amdgcn_mfma_f32_32x32x16_bf16(a, b, c, 0, 0, 0)
DI void mfma16_acc(f32x4& c, bf16x8 a, bf16x8 b) { asm volatile("v_mfma_f32_16x16x32_bf16 %0, %1, %2, %0" : "+v"(c) : "v"(a), "v"(b)); }
DI void mfma16_zero(f32x4& c, bf16x8 a, bf16x8 b) { asm volatile("v_mfma_f32_16x16x32_bf16 %0, %1, %2, 0" : "=&v"(c) : "v"(a), "v"(b)); }
template <class F, int... I> DI void for_seq_impl(F&& f, std::integer_sequence<int, I...>) { (f(std::integral_constant<int, I>{}), ...); }
template <int N, class F> DI void for_seq(F&& f) { for_seq_impl(f, std::make_integer_sequence<int, N>{}); }
DI int xcd_remap(int b, int nwg) {
  int q = nwg >> 3, r = nwg & 7, x = b & 7, o = b >> 3;
  return (x < r ? x * (q + 1) : r * (q + 1) + (x - r) * q) + o;
}

constexpr int K1_X = 0, K1_W = 40960, K1_SLOT = 12288, K1_ST = K1_W + K1_SLOT * 9, K1_CS = K1_W, K1_PF = K1_ST + 1024, K1_LDS = K1_PF + 1024;
constexpr int cmin(int a, int b) { return a < b ? a : b; }
constexpr int k1_nw(int kk) { return cmin(18, cmin(3 + 2 * kk, kk + 7)); }
constexpr int k1_nx(int kk) { return cmin(18, cmin(3 + 2 * kk, kk + 5)); }

template <bool ISV, bool HI>
DI void k1_consumer(lds_ptr lds, int cb, int lane, char* ws, int ct, int m0) {
  const int g = lane >> 4, q = (lane & 15) >> 2, p = lane & 3, r = lane & 15;
  constexpr int NM = HI ? 2 : 3, MB = HI ? 3 : 0;
  lds_ptr a0 = lds + K1_X + MB * 2048 + (r >> 3) * 1024 + (r & 7) * 128 + ((g ^ (r >> 1)) * 16);
  lds_ptr a1 = lds + K1_X + MB * 2048 + (r >> 3) * 1024 + (r & 7) * 128 + (((4 + g) ^ (r >> 1)) * 16);
  const int rho0 = 16 * (g >> 1) + 4 * (g & 1) + q;
  lds_ptr wb = lds + K1_W + rho0 * 128 + ((cb ^ ((rho0 >> 1) & 3)) * 32) + p * 8;
  lds_ptr wb5 = wb + 5 * K1_SLOT;
  asm volatile("" : "+v"(wb), "+v"(wb5));
  const u32x4 onesu = {0x3f803f80u, 0x3f803f80u, 0x3f803f80u, 0x3f803f80u};
  const bf16x8 ones = __builtin_bit_cast(bf16x8, onesu);
  f32x4 acc[3][3];
#pragma unroll
  for (int m = 0; m < 3; ++m)
#pragma unroll
    for (int j = 0; j < 3; ++j) acc[m][j] = f32x4{0.f, 0.f, 0.f, 0.f};
  bf16x8 fA[2][3];
  s16x4 wl[2][3], wh[2][3];
  auto rd1 = [&](auto KK, auto II) {
    constexpr int kk = KK, i = II, bf = kk & 1, sl = kk % 9;
    if constexpr (i >= 2 && i <= 4) {
      if constexpr (i - 2 < NM) fA[bf][i - 2] = lds_r128(((kk & 1) ? a1 : a0) + ((kk >> 1) & 3) * 10240 + (i - 2) * 2048);
    } else {
      constexpr int j = i < 2 ? 0 : (i - 3) / 2, hh = i < 2 ? i : (i - 3) % 2;
      lds_ptr bp = (sl >= 5 ? wb5 + (sl - 5) * K1_SLOT : wb + sl * K1_SLOT) + j * 4096 + hh * 1024;
      if constexpr (hh == 0) wl[bf][j] = lds_tr(bp); else wh[bf][j] = lds_tr(bp);
    }
  };
  lds_bar();
  STAMP_MARKW(0, 2, 512);
  for_seq<9>([&](auto I) { rd1(std::integral_constant<int, 0>{}, I); });
  SB();
  for_seq<18>([&](auto KK) {
    constexpr int kk = KK, bf = kk & 1;
    if constexpr (kk < 17) { raw_bar(); SB(); }
    for_seq<9>([&](auto II) {
      constexpr int i = II, j = i / 3, m = i % 3;
      const bf16x8 wv = cat8(wl[bf][j], wh[bf][j]);
      const bf16x8 av = (m < NM) ? fA[bf][m] : ones;
      acc[m][j] = ISV ? MFMA16(av, wv, acc[m][j]) : MFMA16(wv, av, acc[m][j]);
      SB();
      if constexpr (kk < 17) { rd1(std::integral_constant<int, kk + 1>{}, II); SB(); }
    });
  });
  STAMP_MARKW(0, 4, 512);
  lds_ptr cx = lds + K1_CS + cb * 3072 + lane * 16;
  if (HI) {
#pragma unroll
    for (int j = 0; j < 3; ++j) *(LDS_AS f32x4*)(cx + j * 1024) = acc[2][j];
  }
  lds_bar();
  STAMP_MARKW(0, 5, 512);
  f32x4 cs[3];
#pragma unroll
  for (int j = 0; j < 3; ++j) cs[j] = HI ? acc[2][j] : *(LDS_AS f32x4*)(cx + j * 1024);
  u32x2 o[NM * 3];
  if (ISV) {
#pragma unroll
    for (int m = 0; m < NM; ++m) {
      const f32x4 s0 = *(LDS_AS f32x4*)(lds + K1_ST + ((MB + m) * 16 + 4 * g) * 8), s1 = *(LDS_AS f32x4*)(lds + K1_ST + ((MB + m) * 16 + 4 * g) * 8 + 16);
#pragma unroll
      for (int j = 0; j < 3; ++j) {
        const float c0 = cs[j][0];
        o[3 * m + j] = u32x2{cvt_pk(fmaf(s0[0], c0, acc[m][j][0]) * s0[1], fmaf(s0[2], c0, acc[m][j][1]) * s0[3]),
                             cvt_pk(fmaf(s1[0], c0, acc[m][j][2]) * s1[1], fmaf(s1[2], c0, acc[m][j][3]) * s1[3])};
      }
    }
  } else {
    const float sc = ct < 3 ? QSCALE : 1.f;
#pragma unroll
    for (int m = 0; m < NM; ++m) {
      f32x2 st = *(LDS_AS f32x2*)(lds + K1_ST + ((MB + m) * 16 + r) * 8);
      st[1] *= sc;
#pragma unroll
      for (int j = 0; j < 3; ++j)
        o[3 * m + j] = u32x2{cvt_pk(fmaf(st[0], cs[j][0], acc[m][j][0]) * st[1], fmaf(st[0], cs[j][1], acc[m][j][1]) * st[1]),
                             cvt_pk(fmaf(st[0], cs[j][2], acc[m][j][2]) * st[1], fmaf(st[0], cs[j][3], acc[m][j][3]) * st[1])};
    }
  }
  char* base;
  int sm, sj, sel, half;
  if (ISV) { base = ws + WS_V + (size_t)(((m0 >> 4) + MB) * 2 + (g & 1)) * 9216 + ((ct - 6) * 192 + cb * 16 + r) * 16; sm = 18432; sj = 1024; sel = g >> 1; half = g >> 1; }
  else { base = ws + WS_Q + (size_t)(ct * 24 + cb * 2 + (g >> 1)) * 32768 + (m0 + MB * 16 + r) * 16; sm = 256; sj = 8 * 32768; sel = g & 1; half = g & 1; }
#pragma unroll
  for (int n = 0; n + 1 < NM * 3; n += 2) {
    u32x4 c;
#pragma unroll
    for (int e = 0; e < 2; ++e) {
      auto rr = ISV ? __builtin_amdgcn_permlane32_swap(o[n][e], o[n + 1][e], false, false) : __builtin_amdgcn_permlane16_swap(o[n][e], o[n + 1][e], false, false);
      c[e] = rr[0]; c[2 + e] = rr[1];
    }
    const int o0 = (n / 3) * sm + (n % 3) * sj, o1 = ((n + 1) / 3) * sm + ((n + 1) % 3) * sj;
    st16(base + (sel ? o1 : o0), c);
  }
  if (NM == 3) st8wt(base + 2 * sm + 2 * sj + half * 8, o[8]);
}

__global__ __launch_bounds__(1024) void k1_ln_qkv(const char* __restrict__ x, const char* __restrict__ wqkv, char* __restrict__ ws, const char* __restrict__ wo) {
  extern __shared__ __attribute__((aligned(16))) char smem[];
  STAMP_BEGIN
  lds_ptr lds = (lds_ptr)smem;
  const unsigned lds0 = (unsigned)(uintptr_t)lds;
  const int tid = threadIdx.x, lane = tid & 63, wid = __builtin_amdgcn_readfirstlane(tid >> 6);
  const int tile = xcd_remap(blockIdx.x, 234);
  const int cgp = tile / 78, rem = tile - cgp * 78;
  const int mt = rem / 3, ct = cgp * 3 + (rem - mt * 3);
  const int m0 = min(mt * 80, SEQ - 80);
  const int sm = wid == 3 ? 4 : wid - 4;
  lds_ptr sa0 = lds + K1_X + sm * 2048 + ((lane >> 3) & 1) * 1024 + (lane & 7) * 128 + (((lane >> 4) ^ ((lane & 15) >> 1)) * 16);
  lds_ptr sa1 = lds + K1_X + sm * 2048 + ((lane >> 3) & 1) * 1024 + (lane & 7) * 128 + (((4 + (lane >> 4)) ^ ((lane & 15) >> 1)) * 16);
  f32x4 ssum = {0.f, 0.f, 0.f, 0.f}, sgram = {0.f, 0.f, 0.f, 0.f};
  bf16x8 sf0, sf1;
  auto stat_rd = [&](int st) { sf0 = lds_r128(sa0 + (st & 3) * 10240); sf1 = lds_r128(sa1 + (st & 3) * 10240); };
  auto stat_mm = [&]() {
    const bf16x8 ones = __builtin_bit_cast(bf16x8, u32x4{0x3f803f80u, 0x3f803f80u, 0x3f803f80u, 0x3f803f80u});
    ssum = MFMA16(ones, sf0, ssum); sgram = MFMA16(sf0, sf0, sgram);
    ssum = MFMA16(ones, sf1, ssum); sgram = MFMA16(sf1, sf1, sgram);
    pin(ssum); pin(sgram);
  };
  auto stat_fin = [&]() {
    const float q01 = (lane & 1) ? sgram[1] : sgram[0], q23 = (lane & 1) ? sgram[3] : sgram[2];
    const float mean = ssum[0] * (1.f / 576.f);
    const float var = fmaxf(((lane & 2) ? q23 : q01) * (1.f / 576.f) - mean * mean, 0.f);
    if ((lane >> 4) == ((lane & 15) >> 2)) {
      *(LDS_AS f32x2*)(lds + K1_ST + (sm * 16 + (lane & 15)) * 8) = f32x2{-mean, __builtin_amdgcn_rsqf(var + LN_EPS)};
      if (ct == 0) *(float*)(ws + WS_XM + (m0 + sm * 16 + (lane & 15)) * 4) = mean;
    }
    lds_bar();
  };
  if (wid < 3) {
    unsigned wv[4];
#pragma unroll
    for (int i = 0; i < 4; ++i) {
      const int rho = 8 * i + (lane >> 3);
      const int gk = ((rho >> 4) & 1) * 2 + ((rho >> 2) & 1), tk = (rho >> 3) & 1, qk = rho & 3;
      wv[i] = (8 * gk + 4 * tk + qk) * 3456 + (((lane & 7) ^ (((rho >> 1) & 3) << 1)) * 16);
    }
    const char* wsrc = wqkv + ct * 384 + wid * 128;
    const unsigned wdst = lds0 + K1_W + wid * 4096;
    for_seq<18>([&](auto KK) {
      constexpr int kk = KK;
#pragma unroll
      for (int n = (kk == 0 ? 0 : k1_nw(kk - 1)); n < k1_nw(kk); ++n)
#pragma unroll
        for (int i = 0; i < 4; ++i) glds16(wsrc + (size_t)n * 32 * 3456, wv[i], wdst + (n % 9) * K1_SLOT + i * 1024);
      wait_bar<(k1_nw(kk) - kk - 1) * 4>();
    });
    return;
  }
  if (wid == 3) {
    const unsigned v0 = (lane >> 3) * 1152 + (((lane & 7) ^ (lane >> 4)) * 16);
    const unsigned v1 = (lane >> 3) * 1152 + (((lane & 7) ^ (4 + (lane >> 4))) * 16);
    const char* xs = x + (size_t)m0 * 1152;
    for_seq<18>([&](auto KK) {
      constexpr int kk = KK;
#pragma unroll
      for (int h = (kk == 0 ? 0 : k1_nx(kk - 1)); h < k1_nx(kk); ++h)
#pragma unroll
        for (int i = 0; i < 5; ++i) {
          const int pc = (h & 1) * 5 + i;
          glds16(xs + pc * 9216 + (h >> 1) * 128, (pc & 1) ? v1 : v0, lds0 + K1_X + ((h >> 1) & 3) * 10240 + pc * 1024);
        }
      if constexpr (kk & 1) stat_rd(kk >> 1);
      else if constexpr (kk > 0) stat_mm();
      SB();
      wait_bar<(k1_nx(kk) - 2 * (kk >> 1) - 2) * 5>();
    });
    stat_mm();
    stat_fin();
    return;
  }
  if (wid < 8) {
    const int j = wid - 4;
    if (j == 0) CODE_TOUCH(SYM_K1, LEN_K1, lds0 + K1_PF);
    if (j == 1) CODE_TOUCH(SYM_K4, LEN_K4, lds0 + K1_PF + 256);
    if (j == 1 && blockIdx.x == 0 && lane < 4) *(unsigned*)(ws + WS_ONES + lane * 4) = 0x3f803f80u;
    if (j >= 1 && blockIdx.x == 0) *(unsigned*)(ws + WS_CNT + ((j - 1) * 64 + lane) * 128) = 0u;
    {
      const int ln = blockIdx.x * 148 + j * 37 + lane;
      if (lane < 37) {
        if (ln < 15552) touch_lines(wqkv, ln * 128, lds0 + K1_PF + j * 256);
        else if (ln < 33984) touch_lines(x, (ln - 15552) * 128, lds0 + K1_PF + j * 256);
      }
    }
    const bool do_wb = blockIdx.x >= 100 && blockIdx.x < 118;
    const int wrow = do_wb ? ((blockIdx.x - 100) * 4 + j) * 8 + (lane >> 3) : 0;
    u32x4 wv[9];
    if (do_wb) {
#pragma unroll
      for (int i = 0; i < 9; ++i) wv[i] = *(const u32x4*)(wo + (size_t)wrow * 1152 + (lane & 7) * 16 + i * 128);
    }
    SB();
#pragma unroll 1
    for (int s = 0; s < 9; ++s) {
      raw_bar();
      stat_rd(s);
      SB();
      raw_bar();
      SB();
      stat_mm();
    }
    stat_fin();
    if (do_wb) {
      float t = 0.f;
#pragma unroll
      for (int i = 0; i < 9; ++i)
#pragma unroll
        for (int e = 0; e < 4; ++e) t = dot2(wv[i][e], 0x3f803f80u, t);
      t = sum8(t) * (1.f / 576.f);
      const unsigned h = cvt_pk(t, 0.f) & 0xffffu, l = cvt_pk(t - bf_lo(h), 0.f) & 0xffffu;
      if ((lane & 7) == 0) *(u32x4*)(ws + WS_WB + wrow * 16) = u32x4{h | (l << 16), 0u, 0u, 0u};
    }
    return;
  }
  if (ct >= 6) {
    if (wid < 12) k1_consumer<true, false>(lds, wid - 8, lane, ws, ct, m0);
    else k1_consumer<true, true>(lds, wid - 12, lane, ws, ct, m0);
  } else {
    if (wid < 12) k1_consumer<false, false>(lds, wid - 8, lane, ws, ct, m0);
    else k1_consumer<false, true>(lds, wid - 12, lane, ws, ct, m0);
  }
  STAMP_ENDW(0, 512);
}

constexpr int K2_KM = 0, K2_V = 24576, K2_W = 65536, K2_SG = 98304;

#define MFMA32K8(a, b, c) __builtin_amdgcn_mfma_f32_32x32x8bf16_1k(a, b, c, 0, 0, 0)
template <class F>
DI void k2_core(lds_ptr lds, char* ws, int hd, int t, int wid, int lane, u32x4 q0u, u32x2 q1u, bool first, F&& hook) {
  const int hi = lane >> 5, l31 = lane & 31;
  const int dlo = ((18 * hd) & 7) >> 1;
#pragma unroll
  for (int e = 0; e < 4; ++e) q0u[e] = (4 * hi + e >= dlo) ? q0u[e] : 0u;
#pragma unroll
  for (int e = 0; e < 2; ++e) q1u[e] = (8 + 2 * hi + e < dlo + 9) ? q1u[e] : 0u;
  const bf16x8 q0 = __builtin_bit_cast(bf16x8, q0u);
  const s16x4 q1 = __builtin_bit_cast(s16x4, q1u);
  f32x16 S[9];
  lds_ptr kmp = lds + K2_KM + hi * 8192 + (wid * 32 + l31) * 16;
  lds_ptr kxp = lds + K2_KM + 16384 + (wid * 32 + l31) * 16 + hi * 8;
  const float NEG = -1e30f;
  const int dq = l31 - 4 * hi;
  float mx0 = NEG, mx1 = NEG;
  bf16x8 kf[3];
  s16x4 kx[3];
  auto rdk = [&](auto NN) {
    constexpr int n = NN, i = n == 0 ? 8 : n - 1;
    kf[n % 3] = lds_r128(kmp + i * 512);
    kx[n % 3] = *(LDS_AS const s16x4*)(kxp + i * 512);
  };
  auto post = [&](auto NN) {
    constexpr int n = NN, i = n == 0 ? 8 : n - 1;
    if constexpr (i < 8) {
      if constexpr (i == 0) {
#pragma unroll
        for (int r = 0; r < 16; ++r) S[0][r] = (((r & 3) + 8 * (r >> 2)) <= dq) ? S[8][r] : S[0][r];
      }
#pragma unroll
      for (int r = 0; r < 16; r += 4) {
        mx0 = fmaxf(fmaxf(mx0, S[i][r]), S[i][r + 1]);
        mx1 = fmaxf(fmaxf(mx1, S[i][r + 2]), S[i][r + 3]);
      }
    }
  };
  rdk(std::integral_constant<int, 0>{});
  rdk(std::integral_constant<int, 1>{});
  SB();
  for_seq<9>([&](auto NN) {
    constexpr int n = NN, i = n == 0 ? 8 : n - 1;
    const f32x16 z = {};
    S[i] = MFMA32(kf[n % 3], q0, z);
    SB();
    if constexpr (n + 2 < 9) { rdk(std::integral_constant<int, n + 2>{}); SB(); }
    S[i] = MFMA32K8(kx[n % 3], q1, S[i]);
    SB();
    if constexpr (n >= 1) { post(std::integral_constant<int, n - 1>{}); SB(); }
    if constexpr (n % 2 == 0) { hook(std::integral_constant<int, n / 2>{}); SB(); }
  });
  post(std::integral_constant<int, 8>{});
  float mx = fmaxf(mx0, mx1);
  {
    auto rr = __builtin_amdgcn_permlane32_swap(__float_as_uint(mx), __float_as_uint(mx), false, false);
    mx = fmaxf(__uint_as_float(rr[0]), __uint_as_float(rr[1]));
  }
  s16x4 mq;
  {
    const float nm = -mx;
    const unsigned h1 = __float_as_uint(nm) & 0xffff0000u;
    const float r1 = nm - __uint_as_float(h1);
    const unsigned h2 = __float_as_uint(r1) & 0xffff0000u;
    const float r2 = r1 - __uint_as_float(h2);
    const u32x2 mu = {hi ? 0u : ((h1 >> 16) | h2), hi ? 0u : ((__float_as_uint(r2) >> 16) | 0xf1490000u)};
    mq = __builtin_bit_cast(s16x4, mu);
  }
  auto mk1 = [&](int i) { return __builtin_bit_cast(s16x4, u32x2{0x3f803f80u, (first && i > 0 && wid + i < 8) ? 0x3f803f80u : 0x00003f80u}); };
  S[1] = MFMA32K8(mk1(1), mq, S[1]);
  S[2] = MFMA32K8(mk1(2), mq, S[2]);
  SB();
  lds_ptr vp = lds + K2_V + wid * 2 * 592 + (l31 < 18 ? hi * 288 + l31 * 16 : 576);
  f32x16 O;
  const unsigned keep0 = first ? 0u : ~0u;
  bf16x8 vf[3];
  auto rdv = [&](auto EE) {
    constexpr int e = EE, blk = e < 14 ? e + 2 : ((e - 14) >> 1) + ((e & 1) ? 16 : 0);
    if constexpr (e < 18) vf[e % 3] = lds_r128(vp + blk * 592);
  };
  rdv(std::integral_constant<int, 0>{});
  rdv(std::integral_constant<int, 1>{});
  SB();
  for_seq<16>([&](auto HH) {
    constexpr int h = HH, i = ((h >> 1) + 1) & 7, s = h & 1;
    u32x4 P;
#pragma unroll
    for (int j = 0; j < 4; ++j) P[j] = cvt_pk(__builtin_amdgcn_exp2f(S[i][8 * s + 2 * j]), __builtin_amdgcn_exp2f(S[i][8 * s + 2 * j + 1]));
    if constexpr (i == 0) {
      constexpr int e = 14 + 2 * s;
      u32x4 P8, P0;
#pragma unroll
      for (int j = 0; j < 4; ++j) {
        const int r0 = 8 * s + 2 * j, r1 = r0 + 1;
        const unsigned mk = ((((r0 & 3) + 8 * (r0 >> 2)) <= dq) ? 0xffffu : 0u) | ((((r1 & 3) + 8 * (r1 >> 2)) <= dq) ? 0xffff0000u : 0u);
        P8[j] = P[j] & mk;
        P0[j] = P[j] & ~mk & keep0;
      }
      rdv(std::integral_constant<int, e + 2>{});
      SB();
      O = MFMA32(vf[e % 3], __builtin_bit_cast(bf16x8, P0), O);
      SB();
      rdv(std::integral_constant<int, e + 3>{});
      O = MFMA32(vf[(e + 1) % 3], __builtin_bit_cast(bf16x8, P8), O);
      SB();
    } else {
      rdv(std::integral_constant<int, h + 2>{});
      if constexpr (h < 7) hook(std::integral_constant<int, 5 + h>{});
      SB();
      if constexpr (h == 0) { const f32x16 z = {}; O = MFMA32(vf[0], __builtin_bit_cast(bf16x8, P), z); }
      else O = MFMA32(vf[h % 3], __builtin_bit_cast(bf16x8, P), O);
      if constexpr (s == 0 && i + 2 <= 8) S[(i + 2) & 7] = MFMA32K8(mk1((i + 2) & 7), mq, S[(i + 2) & 7]);
      SB();
    }
  });
  const float inv = __builtin_amdgcn_rcpf(O[10]);
  const int row = t * 32 + l31;
  char* ob = ws + WS_O + (size_t)(row >> 4) * 18 * 1024 + (row & 15) * 16;
  u32x4 o = {cvt_pk(O[0] * inv, O[1] * inv), cvt_pk(O[2] * inv, O[3] * inv), cvt_pk(O[4] * inv, O[5] * inv), cvt_pk(O[6] * inv, O[7] * inv)};
  st16(ob + (hd >> 1) * 1024 + (2 * (hd & 1) + hi) * 256, o);
  if (hi == 0) st4(ws + WS_OX + hd * 8192 + row * 4, cvt_pk(O[8] * inv, O[9] * inv));
}

DI void glds16v(const void* vaddr, unsigned lds_dst) {
  asm volatile("s_mov_b32 m0, %1\n\ts_nop 3\n\tglobal_load_lds_dwordx4 %0, off" ::"v"(vaddr), "s"(lds_dst) : "memory");
}
DI void poll_count(const unsigned* c, unsigned n) {
  for (int spin = 0; spin < (1 << 18); ++spin) {
    if (__hip_atomic_load(c, __ATOMIC_RELAXED, __HIP_MEMORY_SCOPE_AGENT) >= n) break;
    __builtin_amdgcn_s_sleep(6);
  }
}

template <class F>
DI void front_phase(lds_ptr lds, unsigned lds0, char* __restrict__ ws, const char* __restrict__ wo, const char* __restrict__ x, F&& prefill) {
  const int tid = threadIdx.x, lane = tid & 63, wid = __builtin_amdgcn_readfirstlane(tid >> 6);
  const int hd = (blockIdx.x & 7) * 4 + ((blockIdx.x >> 3) & 3), qb = blockIdx.x >> 5;
  const int p3 = blockIdx.x & 7, m0 = p3 * 256 + ((blockIdx.x >> 3) & 3) * 64, cb = blockIdx.x >> 5;
  const int t = qb * 8 + wid;
  u32x4 q0;
  u32x2 q1;
  const char* wa[12];
  {
    const int c0 = (18 * hd) >> 3;
    asm volatile("global_load_dwordx4 %0, %2, %4\n\tglobal_load_dwordx2 %1, %3, %4"
                 : "=&v"(q0), "=&v"(q1)
                 : "v"((lane >> 5) * 32768 + (lane & 31) * 16), "v"(65536 + (lane & 31) * 16 + (lane >> 5) * 8),
                   "s"(ws + WS_Q + (size_t)c0 * 32768 + t * 512)
                 : "memory");
    const int key0 = max(qb * 256 - 256 + wid * 64, 0);
#pragma unroll
    for (int i = 0; i < 3; ++i) glds16(ws + WS_K + (size_t)(c0 + i) * 32768 + key0 * 16, (qb == 0 && wid < 4) ? 0 : lane * 16, lds0 + K2_KM + i * 8192 + wid * 1024);
#pragma unroll
    for (int i = 0; i < 3; ++i) {
      const int pc = wid + 8 * i;
      if (pc < 19) {
        const int u = min(pc * 64 + lane, 32 * 37 - 1), bl = u / 37, w = u - bl * 37;
        const int blk = max(qb * 16 - 16 + bl, 0);
        const unsigned vo = w == 36 ? (unsigned)(WS_ONES - WS_V) : (blk * 2 + (w >= 18)) * 9216 + (hd * 18 + (w >= 18 ? w - 18 : w)) * 16;
        glds16(ws + WS_V, vo, lds0 + K2_V + pc * 1024);
      }
    }
#pragma unroll
    for (int i = 0; i < 12; ++i) {
      const int pc = min(wid + 8 * i, 89), ks = pc / 5;
      const int u = (pc - ks * 5) * 64 + lane, rho = u / 10, c = u - rho * 10;
      const int g_ = ((rho >> 4) & 1) * 2 + ((rho >> 2) & 1), t_ = (rho >> 3) & 1, q_ = rho & 3;
      const int row = ks < 16 ? 36 * ks + 18 * (g_ >> 1) + 8 * t_ + 4 * (g_ & 1) + q_ : 288 * (ks - 16) + 18 * (4 * g_ + 2 * t_ + (q_ >> 1)) + 16 + (q_ & 1);
      wa[i] = c < 9 ? wo + row * 1152 + cb * 144 + c * 16 : ws + WS_WB + row * 16;
      pin(wa[i]);
    }
  }
  asm volatile("s_waitcnt vmcnt(0)\n\ts_barrier" : "+v"(q0), "+v"(q1)::"memory");
  STAMP_MARK(1, 2);
  k2_core(lds, ws, hd, t, wid, lane, q0, q1, qb == 0, [&](auto I) { glds16v(wa[I], lds0 + K2_W + min(wid + 8 * I, 89) * 1024); });
  STAMP_MARK(1, 3);
  wait_bar<0>();
  unsigned* cnt = (unsigned*)(ws + WS_CNT);
  if (tid == 0) __hip_atomic_fetch_add(cnt + qb * 32, 1u, __ATOMIC_RELAXED, __HIP_MEMORY_SCOPE_AGENT);
  const int mi = wid >> 1, kg = wid & 1;
  const int g = lane >> 4, q = (lane & 15) >> 2, p = lane & 3, er = mi * 16 + (lane & 15);
  const bool ex = kg == 0 && g == 0;
  const u32x4 res = *(const u32x4*)(x + (size_t)(m0 + er) * 1152 + cb * 144 + (kg * 4 + g) * 16);
  const u32x4 res8 = *(const u32x4*)(x + (size_t)(m0 + er) * 1152 + cb * 144 + 128);
  const float xm = *(const float*)(ws + WS_XM + (m0 + er) * 4);
  lds_ptr wb = lds + K2_W + kg * 5120 + (16 * (g >> 1) + 4 * (g & 1) + q) * 160 + p * 8, wb2 = wb + 5 * 10240;
  pin(wb); pin(wb2);
  bf16x8 wf[9][5];
#pragma unroll
  for (int j = 0; j < 9; ++j)
#pragma unroll
    for (int n = 0; n < 5; ++n) {
      lds_ptr bp = (j < 5 ? wb + j * 10240 : wb2 + (j - 5) * 10240) + n * 32;
      wf[j][n] = cat8(lds_tr(bp), lds_tr(bp + 1280));
    }
  if (tid == 448) poll_count(cnt + p3 * 32, 32u);
  SB();
  lds_bar();
  SB();
  STAMP_MARK(1, 5);
  const __amdgpu_buffer_rsrc_t ors = __builtin_amdgcn_make_buffer_rsrc((void*)(ws + WS_O), 0, 128 * 18432 + 32 * 8192, 0x00020000);
  const int aoff = ((m0 >> 4) + mi) * 18432 + kg * 1024 + lane * 16;
  bf16x8 a[9];
#pragma unroll
  for (int i = 0; i < 8; ++i) a[i] = __builtin_bit_cast(bf16x8, __builtin_amdgcn_raw_buffer_load_b128(ors, aoff + i * 2048, 0, 16));
  {
    u32x4 ax;
    const int xo = 128 * 18432 + (kg * 16 + 4 * g) * 8192 + (m0 + er) * 4;
#pragma unroll
    for (int e = 0; e < 4; ++e) ax[e] = __builtin_amdgcn_raw_buffer_load_b32(ors, xo, e * 8192, 16);
    a[8] = __builtin_bit_cast(bf16x8, ax);
  }
  SB();
  f32x4 acc[5];
#pragma unroll
  for (int n = 0; n < 5; ++n) acc[n] = f32x4{0.f, 0.f, 0.f, 0.f};
#pragma unroll
  for (int j = 0; j < 9; ++j)
#pragma unroll
    for (int n = 0; n < 5; ++n) acc[n] = MFMA16(wf[j][n], a[j], acc[n]);
  SB();
  prefill();
  SB();
  STAMP_MARK(1, 6);
  {
    lds_ptr cw = lds + K2_SG + (kg * 64 + er) * 336 + 16 * g;
#pragma unroll
    for (int n = 0; n < 5; ++n) *(LDS_AS f32x4*)(cw + n * 64) = acc[n];
  }
  lds_bar();
  {
    lds_ptr cr = lds + K2_SG + er * 336;
    const f32x2 ma = *(LDS_AS f32x2*)(cr + 288), mb = *(LDS_AS f32x2*)(cr + 64 * 336 + 288);
    const float mean = xm + ((ma[0] + ma[1]) + (mb[0] + mb[1]));
    const int row = m0 + er;
    u32x4 o[2];
#pragma unroll
    for (int k = 0; k < 2; ++k) {
      const int c = k ? 8 : kg * 4 + g;
      const u32x4 rs = k ? res8 : res;
      f32x4 a0 = *(LDS_AS f32x4*)(cr + c * 32), a1 = *(LDS_AS f32x4*)(cr + c * 32 + 16);
      a0 += *(LDS_AS f32x4*)(cr + 64 * 336 + c * 32) - mean; a1 += *(LDS_AS f32x4*)(cr + 64 * 336 + c * 32 + 16) - mean;
      o[k] = u32x4{cvt_pk(a0[0] + bf_lo(rs[0]), a0[1] + bf_hi(rs[0])), cvt_pk(a0[2] + bf_lo(rs[1]), a0[3] + bf_hi(rs[1])),
                   cvt_pk(a1[0] + bf_lo(rs[2]), a1[1] + bf_hi(rs[2])), cvt_pk(a1[2] + bf_lo(rs[3]), a1[3] + bf_hi(rs[3]))};
    }
    char* hp = ws + WS_H + (size_t)(row >> 4) * 18432 + cb * 2304 + (row & 15) * 16;
    st16(hp + (kg * 4 + g) * 256, o[0]);
    if (ex) st16(hp + 2048, o[1]);
    float s1 = 0.f, s2 = 0.f, t1 = 0.f, t2 = 0.f;
#pragma unroll
    for (int j = 0; j < 4; ++j) { s1 = dot2(o[0][j], 0x3f803f80u, s1); s2 = dot2(o[0][j], o[0][j], s2); t1 = dot2(o[1][j], 0x3f803f80u, t1); t2 = dot2(o[1][j], o[1][j], t2); }
    s1 += ex ? t1 : 0.f; s2 += ex ? t2 : 0.f;
    s1 = sum_g(s1); s2 = sum_g(s2);
    if (g == 0 || (g == 1 && kg == 0 && cb == 0)) st8wt(ws + WS_ST + (size_t)(g ? 16 : 2 * cb + kg) * 16384 + row * 8, u32x2{__float_as_uint(g ? mean : s1), __float_as_uint(s2)});
  }
}

constexpr int K4_SLOT = 18432, K4_PF = K4_SLOT * 8, K4_SY = 160000, K4_LDS = 160 * 1024;

constexpr int k4_frag(int i) { return i < 16 ? ((i >> 2) * 2 + (i & 1) + ((i & 2) ? 9 : 0)) : (i == 16 ? 8 : 17); }
constexpr int k4_foff(int f) { return (f >= 9 ? 9216 + (f - 9) * 32 : f * 32); }
constexpr int K4_T0 = 13 * 18;
constexpr int k4_slab(int G) { return G < K4_T0 ? G / 18 : (G - K4_T0 < 80 ? 13 + ((G - K4_T0) % 20) / 4 : 13 + (G - K4_T0 - 80) / 2); }
constexpr int k4_pos(int G) { return G < K4_T0 ? G % 18 : (G - K4_T0 < 80 ? ((G - K4_T0) / 20) * 4 + ((G - K4_T0) & 3) : 16 + ((G - K4_T0) & 1)); }
constexpr int k4_op(int G) {
  const int s = G / 18, i = G % 18;
  if (s > 12) return 0;
  if (s < 3) {
    const int pos[10] = {0, 2, 4, 6, 7, 9, 11, 13, 15, 16};
    for (int k = 0; k < 10; ++k) if (pos[k] == i) return 1 + (2 * s + 2 + k / 5) * 8 + k % 5;
    return 0;
  }
  if (s == 3) {
    const int pos[8] = {0, 2, 4, 6, 8, 11, 14, 16};
    for (int k = 0; k < 8; ++k) if (pos[k] == i) return k < 5 ? 1 + 8 * 8 + k : 1 + 9 * 8 + (k - 3);
    return 0;
  }
  if (i == 1) return 1 + (s + 5) * 8;
  if (i == 4) return 1 + (s + 5) * 8 + 1;
  if (s + 6 > 17) return 0;
  if (i == 8) return 1 + (s + 6) * 8 + 2;
  if (i == 13) return 1 + (s + 6) * 8 + 3;
  if (i == 16) return 1 + (s + 6) * 8 + 4;
  return 0;
}
constexpr int k4_allow(int G, int need) {
  int n = (need == 0) ? 2 : 0;
  for (int g = 0; g < G; ++g) {
    const int op = k4_op(g);
    if (op == 0) continue;
    const int sl = (op - 1) >> 3, kind = (op - 1) & 7;
    if (sl <= need) n = 0;
    else if (kind != 4) ++n;
  }
  return n;
}
constexpr int k4_need(int G) {
  if (G + 5 >= 324 || G == 0) return 0;
  int mx = 0;
  for (int g = 0; g < G + 5; ++g) mx = k4_slab(g) > mx ? k4_slab(g) : mx;
  return k4_slab(G + 5) > mx ? k4_slab(G + 5) : 0;
}

constexpr int d_off(int i) { return i < 6 ? 36864 + i * 9216 : (i < 10 ? (i - 6) * 9216 : (i < 17 ? 92160 + (i - 10) * 9216 : 36864)); }
#define DT 8
constexpr int D_G0 = (18 - DT) * 9;
constexpr int d_i(int H) { return H < D_G0 ? H / 9 : (H - D_G0 < 8 * DT ? 18 - DT + ((H - D_G0) % (2 * DT)) / 2 : 18 - DT + (H - D_G0 - 8 * DT)); }
constexpr int d_f(int H) { return H < D_G0 ? H % 9 : (H - D_G0 < 8 * DT ? ((H - D_G0) / (2 * DT)) * 2 + ((H - D_G0) & 1) : 8); }
constexpr int d_first(int H) {
  for (int h = 0; h < H; ++h) if (d_i(h) == d_i(H)) return 0;
  return 1;
}
DI void ld2_sc1(bf16x8& d0, bf16x8& d1, unsigned o0, unsigned o1, const char* base) {
  asm volatile("global_load_dwordx4 %0, %2, %4 sc1\n\tglobal_load_dwordx4 %1, %3, %4 sc1" : "=&v"(d0), "=&v"(d1) : "v"(o0), "v"(o1), "s"(base) : "memory");
}
template <int N> DI void wait_vm2(bf16x8& d0, bf16x8& d1) { asm volatile("s_waitcnt vmcnt(%2)" : "+v"(d0), "+v"(d1) : "n"(N) : "memory"); }
DI void ld8(f32x2& d, unsigned o, const char* base) { asm volatile("global_load_dwordx2 %0, %1, %2 sc1" : "=v"(d) : "v"(o), "s"(base) : "memory"); }

__global__ __launch_bounds__(512, 2) void k4_gateup(const char* __restrict__ wgate, const char* __restrict__ wup, char* __restrict__ ws,
                                                    const char* __restrict__ wd, char* __restrict__ out, const char* __restrict__ wo, const char* __restrict__ x) {
  extern __shared__ __attribute__((aligned(16))) char smem[];
  STAMP_BEGIN
  lds_ptr lds = (lds_ptr)smem;
  const unsigned lds0 = (unsigned)(uintptr_t)lds;
  *(LDS_AS unsigned*)(lds + K4_SY + (threadIdx.x & 1) * 4) = 0u;
  const int wid = __builtin_amdgcn_readfirstlane(threadIdx.x >> 6);
  const int xcd = blockIdx.x & 7, idx = blockIdx.x >> 3;
  const int pn = 4 * (xcd >> 2) + (idx & 3), jt = 8 * (xcd & 3) + (idx >> 2);
  const int wm = wid & 3, wn = wid >> 2;
  const char* wsrc = (wn ? wup : wgate) + jt * 288;
  unsigned wvo[3];
  const unsigned wdst = lds0 + wn * 9216;
  auto issue_w = [&](int s, int i) {
    const unsigned so = (s & 7) * K4_SLOT;
    const char* wp = wsrc + (size_t)s * 32 * 9216;
    if (i < 2) glds16(wp, wvo[i], wdst + so + wm * 2048 + i * 1024);
    else glds16_if0(wm, wp, wvo[2], wdst + so + 8192);
  };
  front_phase(lds, lds0, ws, wo, x, [&]() {
    int ln = threadIdx.x;
    pin(ln);
    ln &= 63;
#pragma unroll
    for (int i = 0; i < 3; ++i) {
      const int pc = i < 2 ? 2 * wm + i : 8;
      const int o = pc * 1024 + ln * 16;
      const int rho = o / 288, u = (o - rho * 288) >> 4;
      const int gk = ((rho >> 4) & 1) * 2 + ((rho >> 2) & 1), tk = (rho >> 3) & 1, qk = rho & 3;
      wvo[i] = (8 * gk + 4 * tk + qk) * 9216 + u * 16;
    }
    for_seq<2>([&](auto S) { issue_w(S, 0); issue_w(S, 1); issue_w(S, 2); });
  });
  int tid = threadIdx.x;
  pin(tid);
  const int lane = tid & 63;
  const char* asrc = ws + WS_H + (size_t)(pn * 16 + 2 * wid) * 18432;
  const unsigned avo = lane * 16, avo2 = lane * 16 + 18432;
  bf16x8 A[6][2];
  auto issue_a = [&](int s, bf16x8& dst, int m) {
    asm volatile("global_load_dwordx4 %0, %1, %2 sc1" : "=v"(dst) : "v"(m ? avo2 : avo), "s"(asrc + s * 1024) : "memory");
  };
  wait_vm<0>();
  unsigned* cnt3 = (unsigned*)(ws + WS_CNT + 20480);
  if (lds_arrive(lds + K4_SY + 4, lane) == 7 && lane == 0) __hip_atomic_fetch_add(cnt3 + (blockIdx.x & 7) * 32, 1u, __ATOMIC_RELAXED, __HIP_MEMORY_SCOPE_AGENT);
  if (tid == 0) poll_count(cnt3 + pn * 32, 32u);
  SB();
  raw_bar();
  STAMP_MARK(1, 7);
  for_seq<2>([&](auto S) { issue_a(S, A[S][0], 0); issue_a(S, A[S][1], 1); });
  f32x2 sp[8];
  const int g = lane >> 4, q = (lane & 15) >> 2, p = lane & 3, r = lane & 15;
  lds_ptr wb = lds + (16 * (g >> 1) + 4 * (g & 1) + q) * 288 + p * 8;
  lds_ptr wbs[3] = {wb, wb + 3 * K4_SLOT, wb + 6 * K4_SLOT};
  asm volatile("" : "+v"(wbs[1]), "+v"(wbs[2]));
  f32x4 acc[2][18];
  asm volatile("s_waitcnt vmcnt(%2)\n\ts_barrier" : "+v"(A[0][0]), "+v"(A[0][1]) : "n"(k4_allow(0, 0)) : "memory");
  STAMP_MARK(2, 2);
  STAMP_CYC0
  s16x4 Bl[6], Bh[6];
  for_seq<5>([&](auto I) { Bl[I] = lds_tr(wb + k4_foff(k4_frag(I))); Bh[I] = lds_tr(wb + k4_foff(k4_frag(I)) + 2304); });
  SB();
  char* hb = ws + WS_HID + (size_t)(pn * 16 + wid * 2) * (144 * 1024) + (size_t)(jt >> 2) * 18432;
  float c1[2];
  float sd[2];
  auto glu = [&](float ag, float au, int m) -> float {
    return au * __builtin_amdgcn_rcpf(fmaf(__builtin_amdgcn_exp2f(ag * c1[m]), sd[m], sd[m]));
  };
  u32x4 own[4][2];
  float eg[2][2];
  auto micro = [&](auto QQ, auto KK, auto JJ) {
    constexpr int Q = QQ, k = KK, j = JJ, m = k >> 2, d = k & 3, h = d >> 1, e = (d & 1) * 2, b = k & 1;
#pragma unroll
    for (int x = 0; x < 2; ++x) {
      if constexpr (j == 0) eg[b][x] = acc[m][2 * Q + h][e + x] * c1[m];
      if constexpr (j == 1) eg[b][x] = __builtin_amdgcn_exp2f(eg[b][x]);
      if constexpr (j == 2) eg[b][x] = fmaf(eg[b][x], sd[m], sd[m]);
      if constexpr (j == 3) eg[b][x] = __builtin_amdgcn_rcpf(eg[b][x]);
      if constexpr (j == 4) eg[b][x] *= acc[m][9 + 2 * Q + h][e + x];
      if constexpr (j < 5) pin(eg[b][x]);
    }
    if constexpr (j == 6) {
      own[Q][m][d] = cvt_pk(eg[b][0], eg[b][1]);
      if constexpr (d == 3) st16(hb + (size_t)m * (144 * 1024) + (4 * (jt & 3) + Q) * 1024 + lane * 16, own[Q][m]);
      else pin(own[Q][m]);
    }
  };
  auto gapops = [&](auto QQ, auto GG) {
    constexpr int gg = GG;
    if constexpr (gg >= 4 && gg < 36) micro(QQ, std::integral_constant<int, gg / 4 - 1>{}, std::integral_constant<int, 4 + gg % 4>{});
    if constexpr (gg >= 0 && gg < 32) micro(QQ, std::integral_constant<int, gg / 4>{}, std::integral_constant<int, gg % 4>{});
    SB();
  };
  unsigned dvo[2], dvl[2];
  const char* dsrc = wd + (size_t)(jt >> 2) * 576 * 1152 + (jt & 3) * 288;
  auto dvoff = [&]() {
    int ln = tid;
    asm volatile("" : "+v"(ln));
    ln &= 63;
#pragma unroll
    for (int i = 0; i < 2; ++i) {
      const int o = (i ? 8 : wid) * 1024 + ln * 16;
      const int rho = o / 288, u = (o - rho * 288) >> 4;
      const int gk = ((rho >> 4) & 1) * 2 + ((rho >> 2) & 1), tk = (rho >> 3) & 1, qk = rho & 3;
      dvo[i] = (16 * tk + 4 * gk + qk) * 1152 + u * 16;
      dvl[i] = (144 * (gk >> 1) + 128 + 8 * (gk & 1) + 4 * tk + qk) * 1152 + u * 16;
    }
  };
  auto issue_d = [&](auto II, int pc) {
    constexpr int i = II;
    const int k = (4 * (jt & 3) + i) & 15;
    const char* src = dsrc + (size_t)(i < 16 ? 144 * (k >> 2) + 32 * (k & 3) : 288 * (i - 16)) * 1152;
    if (pc == 0) glds16(src, i < 16 ? dvo[0] : dvl[0], lds0 + d_off(i) + wid * 1024);
    else glds16_if0(wid ^ (i & 7), src, i < 16 ? dvo[1] : dvl[1], lds0 + d_off(i) + 8192);
  };
  for_seq<18>([&](auto GH) { for_seq<18>([&](auto GL) {
    constexpr int G = GH * 18 + GL, sl = k4_slab(G), f = k4_frag(k4_pos(G)), rb = G % 6, nb = (G + 5) % 6, NG = G + 5;
    const bf16x8 b = cat8(Bl[rb], Bh[rb]);
    constexpr int need = k4_need(G);
    if constexpr (need != 0) {
      asm volatile("s_waitcnt vmcnt(%2)\n\ts_barrier" : "+v"(A[need % 6][0]), "+v"(A[need % 6][1]) : "n"(k4_allow(G, need)) : "memory");
      SB();
    }
    if constexpr (sl == 0) mfma16_zero(acc[0][f], b, A[0][0]); else mfma16_acc(acc[0][f], b, A[sl % 6][0]);
    SB();
    if constexpr (NG < 324) { Bl[nb] = lds_tr(wbs[(k4_slab(NG) & 7) / 3] + ((k4_slab(NG) & 7) % 3) * K4_SLOT + k4_foff(k4_frag(k4_pos(NG)))); SB(); }
    if constexpr (G >= K4_T0 + 20 && G < K4_T0 + 80 && (G - K4_T0) % 20 >= 1 && (G - K4_T0) % 20 < 19)
      gapops(std::integral_constant<int, (G - K4_T0) / 20 - 1>{}, std::integral_constant<int, 2 * ((G - K4_T0) % 20 - 1)>{});
    if constexpr (G >= K4_T0 + 81) {
      gapops(std::integral_constant<int, 3>{}, std::integral_constant<int, 4 * (G - K4_T0 - 81)>{});
      gapops(std::integral_constant<int, 3>{}, std::integral_constant<int, 4 * (G - K4_T0 - 81) + 1>{});
    }
    if constexpr (sl == 0) mfma16_zero(acc[1][f], b, A[0][1]); else mfma16_acc(acc[1][f], b, A[sl % 6][1]);
    SB();
    if constexpr (NG < 324) { Bh[nb] = lds_tr(wbs[(k4_slab(NG) & 7) / 3] + ((k4_slab(NG) & 7) % 3) * K4_SLOT + k4_foff(k4_frag(k4_pos(NG))) + 2304); SB(); }
    constexpr int op = k4_op(G);
    if constexpr (op != 0) {
      constexpr int os = (op - 1) >> 3, kind = (op - 1) & 7;
      if constexpr (kind < 2) issue_a(os, A[os % 6][kind], kind);
      else issue_w(os, kind - 2);
      SB();
    }
    if constexpr (G % 18 == 10 && (G / 18 == 5 || G / 18 == 7 || G / 18 == 9)) {
      constexpr int k = (G / 18 - 5) / 2;
      touch_if0((int)(blockIdx.x / 216) + (wid ^ (k + 1)), wd + (size_t)(blockIdx.x * 3 + k) * 8192, avo * 8, lds0 + K4_PF + k * 256);
      SB();
    }
    if constexpr (G >= 222 && G < 230) { ld8(sp[G - 222], (lane & 31) * 8 + (lane >> 5) * 8 * 16384, ws + WS_ST + (size_t)(pn * 256 + wid * 32) * 8 + (G - 222) * 16384); SB(); }
    if constexpr (G == K4_T0 + 11) {
      for_seq<8>([&](auto I) { pin(sp[I]); });
      const f32x2 t4 = ((sp[0] + sp[1]) + (sp[2] + sp[3])) + ((sp[4] + sp[5]) + (sp[6] + sp[7]));
      auto r1 = __builtin_amdgcn_permlane32_swap(__float_as_uint(t4[0]), __float_as_uint(t4[0]), false, false);
      auto r2 = __builtin_amdgcn_permlane32_swap(__float_as_uint(t4[1]), __float_as_uint(t4[1]), false, false);
      const float mean = (__uint_as_float(r1[0]) + __uint_as_float(r1[1])) * (1.f / 576.f);
      const float var = fmaxf((__uint_as_float(r2[0]) + __uint_as_float(r2[1])) * (1.f / 576.f) - mean * mean, 0.f) + LN_EPS;
      auto rv = __builtin_amdgcn_permlane16_swap(__float_as_uint(var), __float_as_uint(var), false, false);
      const float v0 = __uint_as_float(rv[0]), v1 = __uint_as_float(rv[1]);
      c1[0] = -1.4426950408889634f * __builtin_amdgcn_rsqf(v0); sd[0] = __builtin_amdgcn_sqrtf(v0);
      c1[1] = -1.4426950408889634f * __builtin_amdgcn_rsqf(v1); sd[1] = __builtin_amdgcn_sqrtf(v1);
      SB();
    }
    if constexpr (G >= K4_T0) {
      constexpr int t = G - K4_T0, u = t % 20;
      if constexpr (t == 11) { dvoff(); SB(); }
      if constexpr (t >= 12 && t < 24) { issue_d(std::integral_constant<int, (t - 12) / 2>{}, t & 1); SB(); }
      if constexpr (t >= 20 && t < 80 && u >= 1 && u < 19) gapops(std::integral_constant<int, t / 20 - 1>{}, std::integral_constant<int, 2 * (u - 1) + 1>{});
      if constexpr (t >= 81) {
        gapops(std::integral_constant<int, 3>{}, std::integral_constant<int, 4 * (t - 81) + 2>{});
        gapops(std::integral_constant<int, 3>{}, std::integral_constant<int, 4 * (t - 81) + 3>{});
      }
    }
  }); });
  {
    asm volatile("s_nop 7\n\ts_nop 1" : "+v"(acc[0][8]), "+v"(acc[1][8]), "+v"(acc[0][17]), "+v"(acc[1][17]));
#pragma unroll
    for (int m = 0; m < 2; ++m) {
      const f32x4 ag = acc[m][8], au = acc[m][17];
      u32x2 o = {cvt_pk(glu(ag[0], au[0], m), glu(ag[1], au[1], m)), cvt_pk(glu(ag[2], au[2], m), glu(ag[3], au[3], m))};
      st8wt(hb + (size_t)m * (144 * 1024) + (16 + ((jt >> 1) & 1)) * 1024 + (2 * (jt & 1) + (g >> 1)) * 256 + r * 16 + (g & 1) * 8, o);
    }
  }
  STAMP_MARK(2, 5);
  const int sb = jt >> 2, nq = jt & 3;
  unsigned* cntA = (unsigned*)(ws + WS_CNT + 8192) + (pn * 8 + sb) * 32;
  unsigned* cntB = (unsigned*)(ws + WS_CNT + 16384) + (pn * 4 + nq) * 32;
  wait_bar<10>();
  auto dptr = [&](auto HH) -> lds_ptr {
    constexpr int H = HH, o = d_off(d_i(H)) + d_f(H) * 32;
    return wbs[o / (3 * K4_SLOT)] + o % (3 * K4_SLOT);
  };
  for_seq<5>([&](auto I) { Bl[I] = lds_tr(dptr(I)); Bh[I] = lds_tr(dptr(I) + 2304); });
  SB();
  f32x4 dacc[2][9];
  bf16x8 DA[14][2];
  char* pb = ws + WS_PART + (size_t)(((pn * 4 + nq) * 8 + wid) * 8 + sb) * 9216 + lane * 16;
  for_seq<18>([&](auto HH) { for_seq<9>([&](auto HL) {
    constexpr int H = HH * 9 + HL, i = d_i(H), f = d_f(H), rb = H % 6, nb = (H + 5) % 6, NH = H + 5;
    if constexpr (H == 36) {
      if (tid == 0) poll_count(cntA, 4u);
      SB();
      wait_bar<0>();
      STAMP_MARK(2, 6);
      issue_d(std::integral_constant<int, 17>{}, 0); issue_d(std::integral_constant<int, 17>{}, 1);
      const unsigned ho = lane * 16, ho2 = lane * 16 + 144 * 1024;
      for_seq<14>([&](auto JJ) {
        constexpr int j = JJ;
        const int k = j < 12 ? ((4 * nq + 4 + j) & 15) : 4 + j;
        ld2_sc1(DA[j][0], DA[j][1], ho, ho2, hb + k * 1024);
      });
      SB();
    }
    if constexpr (i >= 4 && d_first(H)) {
      wait_vm2<2 * (17 - i)>(DA[i - 4][0], DA[i - 4][1]);
      SB();
    }
    if constexpr (H == D_G0) { raw_bar(); SB(); }
    const bf16x8 b = cat8(Bl[rb], Bh[rb]);
    bf16x8 a0, a1;
    if constexpr (i < 4) { a0 = __builtin_bit_cast(bf16x8, own[i][0]); a1 = __builtin_bit_cast(bf16x8, own[i][1]); }
    else { a0 = DA[i - 4][0]; a1 = DA[i - 4][1]; }
    if constexpr (i == 0) mfma16_zero(dacc[0][f], b, a0); else mfma16_acc(dacc[0][f], b, a0);
    SB();
    if constexpr (NH < 162) { Bl[nb] = lds_tr(dptr(std::integral_constant<int, NH>{})); SB(); }
    if constexpr (i == 0) mfma16_zero(dacc[1][f], b, a1); else mfma16_acc(dacc[1][f], b, a1);
    SB();
    if constexpr (NH < 162) { Bh[nb] = lds_tr(dptr(std::integral_constant<int, NH>{}) + 2304); SB(); }
    if constexpr (H < 22) { issue_d(std::integral_constant<int, 6 + H / 2>{}, H & 1); SB(); }
    if constexpr (H == 24) {
      wait_vm<11>();
      if (lds_arrive(lds + K4_SY, lane) == 7 && lane == 0) __hip_atomic_fetch_add(cntA, 1u, __ATOMIC_RELAXED, __HIP_MEMORY_SCOPE_AGENT);
      SB();
    }
    if constexpr (H >= D_G0 + 2 * DT && (((H - D_G0) % (2 * DT) == 2 || (H - D_G0) % (2 * DT) == 3))) {
      constexpr int pr = (H - D_G0) / (2 * DT) - 1, m = ((H - D_G0) % (2 * DT) == 3);
      const f32x4 x0 = dacc[m][2 * pr], x1 = dacc[m][2 * pr + 1];
      st16(pb + (m * 4 + pr) * 1024, u32x4{cvt_pk(x0[0], x0[1]), cvt_pk(x0[2], x0[3]), cvt_pk(x1[0], x1[1]), cvt_pk(x1[2], x1[3])});
      SB();
    }
  }); });
  {
    asm volatile("s_nop 7\n\ts_nop 1" : "+v"(dacc[0][8]), "+v"(dacc[1][8]));
    const f32x4 x0 = dacc[0][8], x1 = dacc[1][8];
    st16(pb + 8192, u32x4{cvt_pk(x0[0], x0[1]), cvt_pk(x0[2], x0[3]), cvt_pk(x1[0], x1[1]), cvt_pk(x1[2], x1[3])});
  }
  wait_bar<0>();
  STAMP_MARK(2, 7);
  if (tid == 0) __hip_atomic_fetch_add(cntB, 1u, __ATOMIC_RELAXED, __HIP_MEMORY_SCOPE_AGENT);
  {
    const int nc = wid == 1 ? 2 : 1;
    const int rbase = pn * 256 + sb * 32;
    int row[2][2], col[2][2];
    row[0][0] = row[0][1] = rbase + (wid >> 2) * 16 + r; col[0][0] = nq * 144 + (wid & 3) * 32 + 4 * g; col[0][1] = col[0][0] + 16;
    row[1][0] = rbase + r; row[1][1] = rbase + 16 + r; col[1][0] = col[1][1] = nq * 144 + 128 + 4 * g;
    const __amdgpu_buffer_rsrc_t hrs = __builtin_amdgcn_make_buffer_rsrc((void*)(ws + WS_H), 0, (int)(WS_XM - WS_H), 0x00020000);
    u32x2 res[2][2];
    float hm[2][2];
#pragma unroll
    for (int c = 0; c < 2; ++c)
#pragma unroll
      for (int e = 0; e < 2; ++e)
      {
        res[c][e] = __builtin_amdgcn_raw_buffer_load_b64(hrs, (row[c][e] >> 4) * 18432 + (col[c][e] >> 3) * 256 + (row[c][e] & 15) * 16 + ((col[c][e] >> 2) & 1) * 8, 0, 16);
        hm[c][e] = __uint_as_float(__builtin_amdgcn_raw_buffer_load_b32(hrs, (int)(WS_HM - WS_H) + row[c][e] * 8, 0, 16));
      }
    if (tid == 0) poll_count(cntB, 8u);
    SB();
    raw_bar();
    SB();
    const __amdgpu_buffer_rsrc_t prs = __builtin_amdgcn_make_buffer_rsrc((void*)(ws + WS_PART + (size_t)((pn * 4 + nq) * 8 + sb) * 8 * 9216), 0, 8 * 9216, 0x00020000);
#pragma unroll
    for (int c = 0; c < 2; ++c) {
      if (c < nc) {
        u32x4 pv[8];
#pragma unroll
        for (int k = 0; k < 8; ++k) pv[k] = __builtin_amdgcn_raw_buffer_load_b128(prs, lane * 16 + (c ? 8192 : wid * 1024), k * 9216, 16);
        float sum[8];
#pragma unroll
        for (int e = 0; e < 4; ++e) { sum[2 * e] = bf_lo(res[c][e >> 1][e & 1]) + hm[c][e >> 1]; sum[2 * e + 1] = bf_hi(res[c][e >> 1][e & 1]) + hm[c][e >> 1]; }
        float acc2[8];
#pragma unroll
        for (int e = 0; e < 8; ++e) acc2[e] = 0.f;
#pragma unroll
        for (int k = 0; k < 8; ++k)
#pragma unroll
          for (int e = 0; e < 4; ++e) { acc2[2 * e] += bf_lo(pv[k][e]); acc2[2 * e + 1] += bf_hi(pv[k][e]); }
        unsigned pk[4];
#pragma unroll
        for (int e = 0; e < 4; ++e) pk[e] = cvt_pk(sum[2 * e] + acc2[2 * e], sum[2 * e + 1] + acc2[2 * e + 1]);
        auto x0 = __builtin_amdgcn_permlane16_swap(pk[0], pk[2], false, false);
        auto x1 = __builtin_amdgcn_permlane16_swap(pk[1], pk[3], false, false);
        const int sel = g & 1;
        st16(out + (size_t)(row[c][0] + (c ? 16 * sel : 0)) * 1152 + (col[c][0] + (c ? -4 : 12) * sel) * 2, (u32x4{x0[0], x1[0], x0[1], x1[1]}));
      }
    }
  }
  STAMP_END(2);
  STAMP_END(1);
}

extern "C" void kernel_launch(void* const* d_in, const int* in_sizes, int n_in, void* d_out, int out_size, void* d_ws, size_t ws_size,
                              hipStream_t stream) {
  const char* x = (const char*)d_in[0];
  const char* wqkv = (const char*)d_in[1];
  const char* wo = (const char*)d_in[2];
  const char* wg = (const char*)d_in[4];
  const char* wu = (const char*)d_in[5];
  const char* wd = (const char*)d_in[6];
  char* ws = (char*)d_ws + WS_BASE(ws_size);
  (void)hipFuncSetAttribute((const void*)k1_ln_qkv, hipFuncAttributeMaxDynamicSharedMemorySize, K1_LDS);
  (void)hipFuncSetAttribute((const void*)k4_gateup, hipFuncAttributeMaxDynamicSharedMemorySize, K4_LDS);
  k1_ln_qkv<<<234, 1024, K1_LDS, stream>>>(x, wqkv, ws, wo);
  k4_gateup<<<256, 512, K4_LDS, stream>>>(wg, wu, ws, wd, (char*)d_out, wo, x);
}
```

```cpp
#include <hip/hip_runtime.h>
#include <hip/hip_bf16.h>
#include <stdint.h>
#include <utility>
#include <type_traits>

typedef __attribute__((ext_vector_type(8))) short bf16x8;
typedef __attribute__((ext_vector_type(4))) short s16x4;
typedef __attribute__((ext_vector_type(4))) float f32x4;
typedef __attribute__((ext_vector_type(16))) float f32x16;
typedef __attribute__((ext_vector_type(4))) unsigned u32x4;
typedef __attribute__((ext_vector_type(2))) unsigned u32x2;
typedef __attribute__((ext_vector_type(2))) __bf16 bf16x2_t;
typedef __attribute__((ext_vector_type(2))) float f32x2;
#define LDS_AS __attribute__((address_space(3)))
typedef LDS_AS char* lds_ptr;
#define DI __device__ __forceinline__

constexpr int SEQ = 2048, DM = 576, NH = 32, FF = 4608;
constexpr float LN_EPS = 1e-5f;
constexpr float QSCALE = 0.23570226039551584f * 1.4426950408889634f;

constexpr size_t WS_Q = 0;
constexpr size_t WS_K = WS_Q + (size_t)72 * 32768;
constexpr size_t WS_V = WS_K + (size_t)72 * 32768;
constexpr size_t WS_ONES = WS_V + (size_t)256 * 9216;
constexpr size_t WS_O = WS_ONES + 1024;
constexpr size_t WS_OX = WS_O + (size_t)128 * 18 * 1024;
constexpr size_t WS_H = WS_OX + (size_t)32 * 8192;
constexpr size_t WS_ST = WS_H + (size_t)128 * 18 * 1024;
constexpr size_t WS_HM = WS_ST + (size_t)16 * 16384;
constexpr size_t WS_XM = WS_HM + 16384;
constexpr size_t WS_WB = WS_XM + 8192;
constexpr size_t WS_HID = WS_WB + 9216;
constexpr size_t WS_CNT = WS_HID + (size_t)128 * 144 * 1024;
constexpr size_t WS_PART = WS_CNT + 24576;
constexpr size_t WS_END = WS_PART + (size_t)2048 * 9216;
static_assert(WS_O % 1024 == 0 && WS_H % 1024 == 0 && WS_ST % 256 == 0 && WS_HID % 256 == 0, "align");

#define WS_BASE(sz) (((sz) - WS_END) & ~(size_t)0x1fffff)
#define STAMP_BEGIN
#define STAMP_MARK(k, i)
#define STAMP_MARKW(k, i, T)
#define STAMP_ENDW(k, T)
#define STAMP_CYC0
#define STAMP_CYC(k, i)
#define STAMP_END(k)

DI unsigned cvt_pk(float lo, float hi) {
  f32x2 v = {lo, hi};
  bf16x2_t r = __builtin_convertvector(v, bf16x2_t);
  return __builtin_bit_cast(unsigned, r);
}
DI float bf_lo(unsigned u) { return __builtin_bit_cast(float, u << 16); }
DI float bf_hi(unsigned u) { return __builtin_bit_cast(float, u & 0xffff0000u); }
DI float dot2(unsigned a, unsigned b, float c) {
  return __builtin_amdgcn_fdot2_f32_bf16(__builtin_bit_cast(bf16x2_t, a), __builtin_bit_cast(bf16x2_t, b), c, false);
}
template <int CTRL> DI float dpp_add(float v) {
  int t = __builtin_amdgcn_update_dpp(0, __builtin_bit_cast(int, v), CTRL, 0xf, 0xf, true);
  return v + __builtin_bit_cast(float, t);
}
DI float sum8(float v) {
  v = dpp_add<0xB1>(v);
  v = dpp_add<0x4E>(v);
  v = dpp_add<0x141>(v);
  return v;
}
#define GNOP "3"
DI float sum_g(float v) {
  auto a = __builtin_amdgcn_permlane16_swap(__float_as_uint(v), __float_as_uint(v), false, false);
  v = __uint_as_float(a[0]) + __uint_as_float(a[1]);
  auto b = __builtin_amdgcn_permlane32_swap(__float_as_uint(v), __float_as_uint(v), false, false);
  return __uint_as_float(b[0]) + __uint_as_float(b[1]);
}
DI void glds16(const void* sbase, unsigned voff, unsigned lds_dst) {
  asm volatile("s_mov_b32 m0, %2\n\ts_nop " GNOP "\n\tglobal_load_lds_dwordx4 %0, %1" ::"v"(voff), "s"(sbase), "s"(lds_dst) : "memory");
}
#define ST_AUX " sc1"
DI void st16(void* p, u32x4 v) { asm volatile("global_store_dwordx4 %0, %1, off" ST_AUX "\n\ts_nop 1" ::"v"(p), "v"(v) : "memory"); }
DI void st8(void* p, u32x2 v) { *(u32x2*)p = v; }
DI void st8wt(void* p, u32x2 v) { asm volatile("global_store_dwordx2 %0, %1, off sc1" ::"v"(p), "v"(v) : "memory"); }
DI void st4(void* p, unsigned v) { asm volatile("global_store_dword %0, %1, off sc1" ::"v"(p), "v"(v) : "memory"); }
DI void glds16_if0(int c, const void* sbase, unsigned voff, unsigned lds_dst) {
  asm volatile("s_cmp_lg_u32 %3, 0\n\ts_cbranch_scc1 1f\n\ts_mov_b32 m0, %2\n\ts_nop 1\n\tglobal_load_lds_dwordx4 %0, %1\n1:"
               ::"v"(voff), "s"(sbase), "s"(lds_dst), "s"(c) : "memory", "scc");
}
DI void touch_if0(int c, const void* sbase, unsigned voff, unsigned lds_dst) {
  asm volatile("s_cmp_lg_u32 %3, 0\n\ts_cbranch_scc1 1f\n\ts_mov_b32 m0, %2\n\ts_nop 1\n\tglobal_load_lds_dword %0, %1\n1:"
               ::"v"(voff), "s"(sbase), "s"(lds_dst), "s"(c) : "memory", "scc");
}
DI void touch_lines(const char* base, unsigned voff, unsigned lds_dummy) {
  asm volatile("s_mov_b32 m0, %2\n\ts_nop 3\n\tglobal_load_lds_dword %0, %1" ::"v"(voff), "s"(base), "s"(lds_dummy) : "memory");
}
#define CODE_TOUCH(SYM, BYTES, LDS_DUMMY)                                                                          \
  do {                                                                                                             \
    const char* cp_;                                                                                               \
    asm volatile("s_getpc_b64 s[20:21]\n\ts_add_u32 s20, s20, " SYM "@rel32@lo+4\n\ts_addc_u32 s21, s21, " SYM     \
                 "@rel32@hi+12\n\ts_mov_b64 %0, s[20:21]" : "=s"(cp_)::"s20", "s21", "scc");                      \
    constexpr int nl_ = (BYTES) / 128, per_ = (nl_ + 28) / 29;                                                      \
    const int ln_ = (blockIdx.x >> 3) * per_ + lane;                                                               \
    if (lane < per_ && ln_ < nl_) touch_lines(cp_, ln_ * 128, LDS_DUMMY);                                          \
  } while (0)
#define SYM_K1 "_Z9k1_ln_qkvPKcS0_PcS0_"
#define SYM_K4 "_Z9k4_gateupPKcS0_PcS0_S1_S0_S0_"
constexpr int LEN_K1 = 22600, LEN_K4 = 40400;
template <int N> DI void wait_bar() { asm volatile("s_waitcnt vmcnt(%0) lgkmcnt(0)\n\ts_barrier" ::"n"(N) : "memory"); }
template <int N> DI void wait_vm() { asm volatile("s_waitcnt vmcnt(%0)" ::"n"(N) : "memory"); }
DI unsigned lds_arrive(lds_ptr c, int lane) {
  unsigned old = 0;
  if (lane == 0) old = __hip_atomic_fetch_add((LDS_AS unsigned*)c, 1u, __ATOMIC_RELAXED, __HIP_MEMORY_SCOPE_WORKGROUP);
  return __builtin_amdgcn_readfirstlane(old);
}
DI void lds_sync(lds_ptr c, unsigned n, int lane) {
  asm volatile("" ::: "memory");
  if (lds_arrive(c, lane) + 1 != n) {
    for (int spin = 0; spin < (1 << 20); ++spin) {
      const unsigned v = __builtin_amdgcn_readfirstlane(__hip_atomic_load((LDS_AS unsigned*)c, __ATOMIC_RELAXED, __HIP_MEMORY_SCOPE_WORKGROUP));
      if (v >= n) break;
      __builtin_amdgcn_s_sleep(1);
    }
  }
  asm volatile("" ::: "memory");
}
DI void lds_post(lds_ptr c, int lane) {
  asm volatile("" ::: "memory");
  if (lane == 0) (void)__hip_atomic_fetch_add((LDS_AS unsigned*)c, 1u, __ATOMIC_RELAXED, __HIP_MEMORY_SCOPE_WORKGROUP);
}
DI void lds_wait(lds_ptr c, unsigned n) {
  for (int spin = 0; spin < (1 << 20); ++spin) {
    const unsigned v = __builtin_amdgcn_readfirstlane(__hip_atomic_load((LDS_AS unsigned*)c, __ATOMIC_RELAXED, __HIP_MEMORY_SCOPE_WORKGROUP));
    if (v >= n) break;
    __builtin_amdgcn_s_sleep(1);
  }
  asm volatile("" ::: "memory");
}
template <class T> DI void pin(T& v) { asm volatile("" : "+v"(v)); }
DI void raw_bar() { asm volatile("s_barrier" ::: "memory"); }
DI void lds_bar() { asm volatile("s_waitcnt lgkmcnt(0)\n\ts_barrier" ::: "memory"); }
DI bf16x8 lds_r128(lds_ptr p) { return *(LDS_AS const bf16x8*)p; }
typedef short v4i16_t __attribute__((ext_vector_type(4)));
DI s16x4 lds_tr(lds_ptr p) { return __builtin_amdgcn_ds_read_tr16_b64_v4i16((LDS_AS v4i16_t*)p); }
DI bf16x8 cat8(s16x4 a, s16x4 b) { return __builtin_shufflevector(a, b, 0, 1, 2, 3, 4, 5, 6, 7); }
#define SB() __builtin_amdgcn_sched_barrier(0)
#define MFMA16(a, b, c) __builtin_amdgcn_mfma_f32_16x16x32_bf16(a, b, c, 0, 0, 0)
#define MFMA32(a, b, c) __builtin_amdgcn_mfma_f32_32x32x16_bf16(a, b, c, 0, 0, 0)
DI void mfma16_acc(f32x4& c, bf16x8 a, bf16x8 b) { asm volatile("v_mfma_f32_16x16x32_bf16 %0, %1, %2, %0" : "+v"(c) : "v"(a), "v"(b)); }
DI void mfma16_zero(f32x4& c, bf16x8 a, bf16x8 b) { asm volatile("v_mfma_f32_16x16x32_bf16 %0, %1, %2, 0" : "=&v"(c) : "v"(a), "v"(b)); }
template <class F, int... I> DI void for_seq_impl(F&& f, std::integer_sequence<int, I...>) { (f(std::integral_constant<int, I>{}), ...); }
template <int N, class F> DI void for_seq(F&& f) { for_seq_impl(f, std::make_integer_sequence<int, N>{}); }
DI int xcd_remap(int b, int nwg) {
  int q = nwg >> 3, r = nwg & 7, x = b & 7, o = b >> 3;
  return (x < r ? x * (q + 1) : r * (q + 1) + (x - r) * q) + o;
}

constexpr int K1_X = 0, K1_W = 40960, K1_SLOT = 12288, K1_ST = K1_W + K1_SLOT * 9, K1_CS = K1_W, K1_PF = K1_ST + 1024, K1_LDS = K1_PF + 1024;
constexpr int cmin(int a, int b) { return a < b ? a : b; }
constexpr int k1_nw(int kk) { return cmin(18, cmin(3 + 2 * kk, kk + 7)); }
constexpr int k1_nx(int kk) { return cmin(18, cmin(3 + 2 * kk, kk + 5)); }

template <bool ISV, bool HI>
DI void k1_consumer(lds_ptr lds, int cb, int lane, char* ws, int ct, int m0) {
  const int g = lane >> 4, q = (lane & 15) >> 2, p = lane & 3, r = lane & 15;
  constexpr int NM = HI ? 2 : 3, MB = HI ? 3 : 0;
  lds_ptr a0 = lds + K1_X + MB * 2048 + (r >> 3) * 1024 + (r & 7) * 128 + ((g ^ (r >> 1)) * 16);
  lds_ptr a1 = lds + K1_X + MB * 2048 + (r >> 3) * 1024 + (r & 7) * 128 + (((4 + g) ^ (r >> 1)) * 16);
  const int rho0 = 16 * (g >> 1) + 4 * (g & 1) + q;
  lds_ptr wb = lds + K1_W + rho0 * 128 + ((cb ^ ((rho0 >> 1) & 3)) * 32) + p * 8;
  lds_ptr wb5 = wb + 5 * K1_SLOT;
  asm volatile("" : "+v"(wb), "+v"(wb5));
  const u32x4 onesu = {0x3f803f80u, 0x3f803f80u, 0x3f803f80u, 0x3f803f80u};
  const bf16x8 ones = __builtin_bit_cast(bf16x8, onesu);
  f32x4 acc[3][3];
#pragma unroll
  for (int m = 0; m < 3; ++m)
#pragma unroll
    for (int j = 0; j < 3; ++j) acc[m][j] = f32x4{0.f, 0.f, 0.f, 0.f};
  bf16x8 fA[2][3];
  s16x4 wl[2][3], wh[2][3];
  auto rd1 = [&](auto KK, auto II) {
    constexpr int kk = KK, i = II, bf = kk & 1, sl = kk % 9;
    if constexpr (i >= 2 && i <= 4) {
      if constexpr (i - 2 < NM) fA[bf][i - 2] = lds_r128(((kk & 1) ? a1 : a0) + ((kk >> 1) & 3) * 10240 + (i - 2) * 2048);
    } else {
      constexpr int j = i < 2 ? 0 : (i - 3) / 2, hh = i < 2 ? i : (i - 3) % 2;
      lds_ptr bp = (sl >= 5 ? wb5 + (sl - 5) * K1_SLOT : wb + sl * K1_SLOT) + j * 4096 + hh * 1024;
      if constexpr (hh == 0) wl[bf][j] = lds_tr(bp); else wh[bf][j] = lds_tr(bp);
    }
  };
  lds_bar();
  STAMP_MARKW(0, 2, 512);
  for_seq<9>([&](auto I) { rd1(std::integral_constant<int, 0>{}, I); });
  SB();
  for_seq<18>([&](auto KK) {
    constexpr int kk = KK, bf = kk & 1;
    if constexpr (kk < 17) { raw_bar(); SB(); }
    for_seq<9>([&](auto II) {
      constexpr int i = II, j = i / 3, m = i % 3;
      const bf16x8 wv = cat8(wl[bf][j], wh[bf][j]);
      const bf16x8 av = (m < NM) ? fA[bf][m] : ones;
      acc[m][j] = ISV ? MFMA16(av, wv, acc[m][j]) : MFMA16(wv, av, acc[m][j]);
      SB();
      if constexpr (kk < 17) { rd1(std::integral_constant<int, kk + 1>{}, II); SB(); }
    });
  });
  STAMP_MARKW(0, 4, 512);
  lds_ptr cx = lds + K1_CS + cb * 3072 + lane * 16;
  if (HI) {
#pragma unroll
    for (int j = 0; j < 3; ++j) *(LDS_AS f32x4*)(cx + j * 1024) = acc[2][j];
  }
  lds_bar();
  STAMP_MARKW(0, 5, 512);
  f32x4 cs[3];
#pragma unroll
  for (int j = 0; j < 3; ++j) cs[j] = HI ? acc[2][j] : *(LDS_AS f32x4*)(cx + j * 1024);
  u32x2 o[NM * 3];
  if (ISV) {
#pragma unroll
    for (int m = 0; m < NM; ++m) {
      const f32x4 s0 = *(LDS_AS f32x4*)(lds + K1_ST + ((MB + m) * 16 + 4 * g) * 8), s1 = *(LDS_AS f32x4*)(lds + K1_ST + ((MB + m) * 16 + 4 * g) * 8 + 16);
#pragma unroll
      for (int j = 0; j < 3; ++j) {
        const float c0 = cs[j][0];
        o[3 * m + j] = u32x2{cvt_pk(fmaf(s0[0], c0, acc[m][j][0]) * s0[1], fmaf(s0[2], c0, acc[m][j][1]) * s0[3]),
                             cvt_pk(fmaf(s1[0], c0, acc[m][j][2]) * s1[1], fmaf(s1[2], c0, acc[m][j][3]) * s1[3])};
      }
    }
  } else {
    const float sc = ct < 3 ? QSCALE : 1.f;
#pragma unroll
    for (int m = 0; m < NM; ++m) {
      f32x2 st = *(LDS_AS f32x2*)(lds + K1_ST + ((MB + m) * 16 + r) * 8);
      st[1] *= sc;
#pragma unroll
      for (int j = 0; j < 3; ++j)
        o[3 * m + j] = u32x2{cvt_pk(fmaf(st[0], cs[j][0], acc[m][j][0]) * st[1], fmaf(st[0], cs[j][1], acc[m][j][1]) * st[1]),
                             cvt_pk(fmaf(st[0], cs[j][2], acc[m][j][2]) * st[1], fmaf(st[0], cs[j][3], acc[m][j][3]) * st[1])};
    }
  }
  char* base;
  int sm, sj, sel, half;
  if (ISV) { base = ws + WS_V + (size_t)(((m0 >> 4) + MB) * 2 + (g & 1)) * 9216 + ((ct - 6) * 192 + cb * 16 + r) * 16; sm = 18432; sj = 1024; sel = g >> 1; half = g >> 1; }
  else { base = ws + WS_Q + (size_t)(ct * 24 + cb * 2 + (g >> 1)) * 32768 + (m0 + MB * 16 + r) * 16; sm = 256; sj = 8 * 32768; sel = g & 1; half = g & 1; }
#pragma unroll
  for (int n = 0; n + 1 < NM * 3; n += 2) {
    u32x4 c;
#pragma unroll
    for (int e = 0; e < 2; ++e) {
      auto rr = ISV ? __builtin_amdgcn_permlane32_swap(o[n][e], o[n + 1][e], false, false) : __builtin_amdgcn_permlane16_swap(o[n][e], o[n + 1][e], false, false);
      c[e] = rr[0]; c[2 + e] = rr[1];
    }
    const int o0 = (n / 3) * sm + (n % 3) * sj, o1 = ((n + 1) / 3) * sm + ((n + 1) % 3) * sj;
    st16(base + (sel ? o1 : o0), c);
  }
  if (NM == 3) st8wt(base + 2 * sm + 2 * sj + half * 8, o[8]);
}

__global__ __launch_bounds__(1024) void k1_ln_qkv(const char* __restrict__ x, const char* __restrict__ wqkv, char* __restrict__ ws, const char* __restrict__ wo) {
  extern __shared__ __attribute__((aligned(16))) char smem[];
  STAMP_BEGIN
  lds_ptr lds = (lds_ptr)smem;
  const unsigned lds0 = (unsigned)(uintptr_t)lds;
  const int tid = threadIdx.x, lane = tid & 63, wid = __builtin_amdgcn_readfirstlane(tid >> 6);
  const int tile = xcd_remap(blockIdx.x, 234);
  const int cgp = tile / 78, rem = tile - cgp * 78;
  const int mt = rem / 3, ct = cgp * 3 + (rem - mt * 3);
  const int m0 = min(mt * 80, SEQ - 80);
  const int sm = wid == 3 ? 4 : wid - 4;
  lds_ptr sa0 = lds + K1_X + sm * 2048 + ((lane >> 3) & 1) * 1024 + (lane & 7) * 128 + (((lane >> 4) ^ ((lane & 15) >> 1)) * 16);
  lds_ptr sa1 = lds + K1_X + sm * 2048 + ((lane >> 3) & 1) * 1024 + (lane & 7) * 128 + (((4 + (lane >> 4)) ^ ((lane & 15) >> 1)) * 16);
  f32x4 ssum = {0.f, 0.f, 0.f, 0.f}, sgram = {0.f, 0.f, 0.f, 0.f};
  bf16x8 sf0, sf1;
  auto stat_rd = [&](int st) { sf0 = lds_r128(sa0 + (st & 3) * 10240); sf1 = lds_r128(sa1 + (st & 3) * 10240); };
  auto stat_mm = [&]() {
    const bf16x8 ones = __builtin_bit_cast(bf16x8, u32x4{0x3f803f80u, 0x3f803f80u, 0x3f803f80u, 0x3f803f80u});
    ssum = MFMA16(ones, sf0, ssum); sgram = MFMA16(sf0, sf0, sgram);
    ssum = MFMA16(ones, sf1, ssum); sgram = MFMA16(sf1, sf1, sgram);
    pin(ssum); pin(sgram);
  };
  auto stat_fin = [&]() {
    const float q01 = (lane & 1) ? sgram[1] : sgram[0], q23 = (lane & 1) ? sgram[3] : sgram[2];
    const float mean = ssum[0] * (1.f / 576.f);
    const float var = fmaxf(((lane & 2) ? q23 : q01) * (1.f / 576.f) - mean * mean, 0.f);
    if ((lane >> 4) == ((lane & 15) >> 2)) {
      *(LDS_AS f32x2*)(lds + K1_ST + (sm * 16 + (lane & 15)) * 8) = f32x2{-mean, __builtin_amdgcn_rsqf(var + LN_EPS)};
      if (ct == 0) *(float*)(ws + WS_XM + (m0 + sm * 16 + (lane & 15)) * 4) = mean;
    }
    lds_bar();
  };
  if (wid < 3) {
    unsigned wv[4];
#pragma unroll
    for (int i = 0; i < 4; ++i) {
      const int rho = 8 * i + (lane >> 3);
      const int gk = ((rho >> 4) & 1) * 2 + ((rho >> 2) & 1), tk = (rho >> 3) & 1, qk = rho & 3;
      wv[i] = (8 * gk + 4 * tk + qk) * 3456 + (((lane & 7) ^ (((rho >> 1) & 3) << 1)) * 16);
    }
    const char* wsrc = wqkv + ct * 384 + wid * 128;
    const unsigned wdst = lds0 + K1_W + wid * 4096;
    for_seq<18>([&](auto KK) {
      constexpr int kk = KK;
#pragma unroll
      for (int n = (kk == 0 ? 0 : k1_nw(kk - 1)); n < k1_nw(kk); ++n)
#pragma unroll
        for (int i = 0; i < 4; ++i) glds16(wsrc + (size_t)n * 32 * 3456, wv[i], wdst + (n % 9) * K1_SLOT + i * 1024);
      wait_bar<(k1_nw(kk) - kk - 1) * 4>();
    });
    return;
  }
  if (wid == 3) {
    const unsigned v0 = (lane >> 3) * 1152 + (((lane & 7) ^ (lane >> 4)) * 16);
    const unsigned v1 = (lane >> 3) * 1152 + (((lane & 7) ^ (4 + (lane >> 4))) * 16);
    const char* xs = x + (size_t)m0 * 1152;
    for_seq<18>([&](auto KK) {
      constexpr int kk = KK;
#pragma unroll
      for (int h = (kk == 0 ? 0 : k1_nx(kk - 1)); h < k1_nx(kk); ++h)
#pragma unroll
        for (int i = 0; i < 5; ++i) {
          const int pc = (h & 1) * 5 + i;
          glds16(xs + pc * 9216 + (h >> 1) * 128, (pc & 1) ? v1 : v0, lds0 + K1_X + ((h >> 1) & 3) * 10240 + pc * 1024);
        }
      if constexpr (kk & 1) stat_rd(kk >> 1);
      else if constexpr (kk > 0) stat_mm();
      SB();
      wait_bar<(k1_nx(kk) - 2 * (kk >> 1) - 2) * 5>();
    });
    stat_mm();
    stat_fin();
    return;
  }
  if (wid < 8) {
    const int j = wid - 4;
    if (j == 0) CODE_TOUCH(SYM_K1, LEN_K1, lds0 + K1_PF);
    if (j == 1) CODE_TOUCH(SYM_K4, LEN_K4, lds0 + K1_PF + 256);
    if (j == 1 && blockIdx.x == 0 && lane < 4) *(unsigned*)(ws + WS_ONES + lane * 4) = 0x3f803f80u;
    if (j >= 1 && blockIdx.x == 0) *(unsigned*)(ws + WS_CNT + ((j - 1) * 64 + lane) * 128) = 0u;
    {
      const int ln = blockIdx.x * 148 + j * 37 + lane;
      if (lane < 37) {
        if (ln < 15552) touch_lines(wqkv, ln * 128, lds0 + K1_PF + j * 256);
        else if (ln < 33984) touch_lines(x, (ln - 15552) * 128, lds0 + K1_PF + j * 256);
      }
    }
    const bool do_wb = blockIdx.x >= 100 && blockIdx.x < 118;
    const int wrow = do_wb ? ((blockIdx.x - 100) * 4 + j) * 8 + (lane >> 3) : 0;
    u32x4 wv[9];
    if (do_wb) {
#pragma unroll
      for (int i = 0; i < 9; ++i) wv[i] = *(const u32x4*)(wo + (size_t)wrow * 1152 + (lane & 7) * 16 + i * 128);
    }
    SB();
#pragma unroll 1
    for (int s = 0; s < 9; ++s) {
      raw_bar();
      stat_rd(s);
      SB();
      raw_bar();
      SB();
      stat_mm();
    }
    stat_fin();
    if (do_wb) {
      float t = 0.f;
#pragma unroll
      for (int i = 0; i < 9; ++i)
#pragma unroll
        for (int e = 0; e < 4; ++e) t = dot2(wv[i][e], 0x3f803f80u, t);
      t = sum8(t) * (1.f / 576.f);
      const unsigned h = cvt_pk(t, 0.f) & 0xffffu, l = cvt_pk(t - bf_lo(h), 0.f) & 0xffffu;
      if ((lane & 7) == 0) *(u32x4*)(ws + WS_WB + wrow * 16) = u32x4{h | (l << 16), 0u, 0u, 0u};
    }
    return;
  }
  if (ct >= 6) {
    if (wid < 12) k1_consumer<true, false>(lds, wid - 8, lane, ws, ct, m0);
    else k1_consumer<true, true>(lds, wid - 12, lane, ws, ct, m0);
  } else {
    if (wid < 12) k1_consumer<false, false>(lds, wid - 8, lane, ws, ct, m0);
    else k1_consumer<false, true>(lds, wid - 12, lane, ws, ct, m0);
  }
  STAMP_ENDW(0, 512);
}

#define GU_LINES 27
constexpr int K2_KM = 0, K2_V = 24576, K2_PF = 45056, K2_W = 65536, K2_SG = 98304;

#define MFMA32K8(a, b, c) __builtin_amdgcn_mfma_f32_32x32x8bf16_1k(a, b, c, 0, 0, 0)
template <class F>
DI void k2_core(lds_ptr lds, char* ws, int hd, int t, int wid, int lane, u32x4 q0u, u32x2 q1u, bool first, F&& hook) {
  const int hi = lane >> 5, l31 = lane & 31;
  const int dlo = ((18 * hd) & 7) >> 1;
#pragma unroll
  for (int e = 0; e < 4; ++e) q0u[e] = (4 * hi + e >= dlo) ? q0u[e] : 0u;
#pragma unroll
  for (int e = 0; e < 2; ++e) q1u[e] = (8 + 2 * hi + e < dlo + 9) ? q1u[e] : 0u;
  const bf16x8 q0 = __builtin_bit_cast(bf16x8, q0u);
  const s16x4 q1 = __builtin_bit_cast(s16x4, q1u);
  f32x16 S[9];
  lds_ptr kmp = lds + K2_KM + hi * 8192 + (wid * 32 + l31) * 16;
  lds_ptr kxp = lds + K2_KM + 16384 + (wid * 32 + l31) * 16 + hi * 8;
  const float NEG = -1e30f;
  const int dq = l31 - 4 * hi;
  float mx0 = NEG, mx1 = NEG;
  bf16x8 kf[3];
  s16x4 kx[3];
  auto rdk = [&](auto NN) {
    constexpr int n = NN, i = n == 0 ? 8 : n - 1;
    kf[n % 3] = lds_r128(kmp + i * 512);
    kx[n % 3] = *(LDS_AS const s16x4*)(kxp + i * 512);
  };
  auto post = [&](auto NN) {
    constexpr int n = NN, i = n == 0 ? 8 : n - 1;
    if constexpr (i < 8) {
      if constexpr (i == 0) {
#pragma unroll
        for (int r = 0; r < 16; ++r) S[0][r] = (((r & 3) + 8 * (r >> 2)) <= dq) ? S[8][r] : S[0][r];
      }
#pragma unroll
      for (int r = 0; r < 16; r += 4) {
        mx0 = fmaxf(fmaxf(mx0, S[i][r]), S[i][r + 1]);
        mx1 = fmaxf(fmaxf(mx1, S[i][r + 2]), S[i][r + 3]);
      }
    }
  };
  rdk(std::integral_constant<int, 0>{});
  rdk(std::integral_constant<int, 1>{});
  SB();
  for_seq<9>([&](auto NN) {
    constexpr int n = NN, i = n == 0 ? 8 : n - 1;
    const f32x16 z = {};
    S[i] = MFMA32(kf[n % 3], q0, z);
    SB();
    if constexpr (n + 2 < 9) { rdk(std::integral_constant<int, n + 2>{}); SB(); }
    S[i] = MFMA32K8(kx[n % 3], q1, S[i]);
    SB();
    if constexpr (n >= 1) { post(std::integral_constant<int, n - 1>{}); SB(); }
    hook(std::integral_constant<int, n>{}); SB();
  });
  post(std::integral_constant<int, 8>{});
  float mx = fmaxf(mx0, mx1);
  {
    auto rr = __builtin_amdgcn_permlane32_swap(__float_as_uint(mx), __float_as_uint(mx), false, false);
    mx = fmaxf(__uint_as_float(rr[0]), __uint_as_float(rr[1]));
  }
  s16x4 mq;
  {
    const float nm = -mx;
    const unsigned h1 = __float_as_uint(nm) & 0xffff0000u;
    const float r1 = nm - __uint_as_float(h1);
    const unsigned h2 = __float_as_uint(r1) & 0xffff0000u;
    const float r2 = r1 - __uint_as_float(h2);
    const u32x2 mu = {hi ? 0u : ((h1 >> 16) | h2), hi ? 0u : ((__float_as_uint(r2) >> 16) | 0xf1490000u)};
    mq = __builtin_bit_cast(s16x4, mu);
  }
  auto mk1 = [&](int i) { return __builtin_bit_cast(s16x4, u32x2{0x3f803f80u, (first && i > 0 && wid + i < 8) ? 0x3f803f80u : 0x00003f80u}); };
  S[1] = MFMA32K8(mk1(1), mq, S[1]);
  S[2] = MFMA32K8(mk1(2), mq, S[2]);
  SB();
  lds_ptr vp = lds + K2_V + wid * 2 * 592 + (l31 < 18 ? hi * 288 + l31 * 16 : 576);
  f32x16 O;
  const unsigned keep0 = first ? 0u : ~0u;
  bf16x8 vf[3];
  auto rdv = [&](auto EE) {
    constexpr int e = EE, blk = e < 14 ? e + 2 : ((e - 14) >> 1) + ((e & 1) ? 16 : 0);
    if constexpr (e < 18) vf[e % 3] = lds_r128(vp + blk * 592);
  };
  rdv(std::integral_constant<int, 0>{});
  rdv(std::integral_constant<int, 1>{});
  SB();
  for_seq<16>([&](auto HH) {
    constexpr int h = HH, i = ((h >> 1) + 1) & 7, s = h & 1;
    u32x4 P;
#pragma unroll
    for (int j = 0; j < 4; ++j) P[j] = cvt_pk(__builtin_amdgcn_exp2f(S[i][8 * s + 2 * j]), __builtin_amdgcn_exp2f(S[i][8 * s + 2 * j + 1]));
    if constexpr (i == 0) {
      constexpr int e = 14 + 2 * s;
      u32x4 P8, P0;
#pragma unroll
      for (int j = 0; j < 4; ++j) {
        const int r0 = 8 * s + 2 * j, r1 = r0 + 1;
        const unsigned mk = ((((r0 & 3) + 8 * (r0 >> 2)) <= dq) ? 0xffffu : 0u) | ((((r1 & 3) + 8 * (r1 >> 2)) <= dq) ? 0xffff0000u : 0u);
        P8[j] = P[j] & mk;
        P0[j] = P[j] & ~mk & keep0;
      }
      rdv(std::integral_constant<int, e + 2>{});
      SB();
      O = MFMA32(vf[e % 3], __builtin_bit_cast(bf16x8, P0), O);
      SB();
      rdv(std::integral_constant<int, e + 3>{});
      O = MFMA32(vf[(e + 1) % 3], __builtin_bit_cast(bf16x8, P8), O);
      SB();
    } else {
      rdv(std::integral_constant<int, h + 2>{});
      if constexpr (h < 4) hook(std::integral_constant<int, 9 + h>{});
      SB();
      if constexpr (h == 0) { const f32x16 z = {}; O = MFMA32(vf[0], __builtin_bit_cast(bf16x8, P), z); }
      else O = MFMA32(vf[h % 3], __builtin_bit_cast(bf16x8, P), O);
      if constexpr (s == 0 && i + 2 <= 8) S[(i + 2) & 7] = MFMA32K8(mk1((i + 2) & 7), mq, S[(i + 2) & 7]);
      SB();
    }
  });
  const float inv = __builtin_amdgcn_rcpf(O[10]);
  const int row = t * 32 + l31;
  char* ob = ws + WS_O + (size_t)(row >> 4) * 18 * 1024 + (row & 15) * 16;
  u32x4 o = {cvt_pk(O[0] * inv, O[1] * inv), cvt_pk(O[2] * inv, O[3] * inv), cvt_pk(O[4] * inv, O[5] * inv), cvt_pk(O[6] * inv, O[7] * inv)};
  st16(ob + (hd >> 1) * 1024 + (2 * (hd & 1) + hi) * 256, o);
  if (hi == 0) st4(ws + WS_OX + hd * 8192 + row * 4, cvt_pk(O[8] * inv, O[9] * inv));
}

DI void glds16v(const void* vaddr, unsigned lds_dst) {
  asm volatile("s_mov_b32 m0, %1\n\ts_nop 3\n\tglobal_load_lds_dwordx4 %0, off" ::"v"(vaddr), "s"(lds_dst) : "memory");
}
DI void poll_count(const unsigned* c, unsigned n) {
  for (int spin = 0; spin < (1 << 18); ++spin) {
    if (__hip_atomic_load(c, __ATOMIC_RELAXED, __HIP_MEMORY_SCOPE_AGENT) >= n) break;
    __builtin_amdgcn_s_sleep(1);
  }
}

template <class F>
DI void front_phase(lds_ptr lds, unsigned lds0, char* __restrict__ ws, const char* __restrict__ wo, const char* __restrict__ x,
                    const char* __restrict__ wg, const char* __restrict__ wu, F&& prefill) {
  const int tid = threadIdx.x, lane = tid & 63, wid = __builtin_amdgcn_readfirstlane(tid >> 6);
  const int hd = (blockIdx.x & 7) * 4 + ((blockIdx.x >> 3) & 3), qb = blockIdx.x >> 5;
  const int p3 = blockIdx.x & 7, m0 = p3 * 256 + ((blockIdx.x >> 3) & 3) * 64, cb = blockIdx.x >> 5;
  const int t = qb * 8 + wid;
  u32x4 q0;
  u32x2 q1;
  const char* wa[12];
  {
    const int c0 = (18 * hd) >> 3;
    asm volatile("global_load_dwordx4 %0, %2, %4\n\tglobal_load_dwordx2 %1, %3, %4"
                 : "=&v"(q0), "=&v"(q1)
                 : "v"((lane >> 5) * 32768 + (lane & 31) * 16), "v"(65536 + (lane & 31) * 16 + (lane >> 5) * 8),
                   "s"(ws + WS_Q + (size_t)c0 * 32768 + t * 512)
                 : "memory");
    const int key0 = max(qb * 256 - 256 + wid * 64, 0);
#pragma unroll
    for (int i = 0; i < 3; ++i) glds16(ws + WS_K + (size_t)(c0 + i) * 32768 + key0 * 16, (qb == 0 && wid < 4) ? 0 : lane * 16, lds0 + K2_KM + i * 8192 + wid * 1024);
#pragma unroll
    for (int i = 0; i < 3; ++i) {
      const int pc = wid + 8 * i;
      if (pc < 19) {
        const int u = min(pc * 64 + lane, 32 * 37 - 1), bl = u / 37, w = u - bl * 37;
        const int blk = max(qb * 16 - 16 + bl, 0);
        const unsigned vo = w == 36 ? (unsigned)(WS_ONES - WS_V) : (blk * 2 + (w >= 18)) * 9216 + (hd * 18 + (w >= 18 ? w - 18 : w)) * 16;
        glds16(ws + WS_V, vo, lds0 + K2_V + pc * 1024);
      }
    }
#pragma unroll
    for (int i = 0; i < 12; ++i) {
      const int pc = min(wid + 8 * i, 89), ks = pc / 5;
      const int u = (pc - ks * 5) * 64 + lane, rho = u / 10, c = u - rho * 10;
      const int g_ = ((rho >> 4) & 1) * 2 + ((rho >> 2) & 1), t_ = (rho >> 3) & 1, q_ = rho & 3;
      const int row = ks < 16 ? 36 * ks + 18 * (g_ >> 1) + 8 * t_ + 4 * (g_ & 1) + q_ : 288 * (ks - 16) + 18 * (4 * g_ + 2 * t_ + (q_ >> 1)) + 16 + (q_ & 1);
      wa[i] = c < 9 ? wo + row * 1152 + cb * 144 + c * 16 : ws + WS_WB + row * 16;
      pin(wa[i]);
    }
  }
  asm volatile("s_waitcnt vmcnt(0)\n\ts_barrier" : "+v"(q0), "+v"(q1)::"memory");
  STAMP_MARK(1, 2);
  k2_core(lds, ws, hd, t, wid, lane, q0, q1, qb == 0, [&](auto I) {
    if constexpr (I < 12) glds16v(wa[I], lds0 + K2_W + min(wid + 8 * I, 89) * 1024);
    else if constexpr (GU_LINES > 0)
      touch_lines((wid & 1) ? wu : wg, ((blockIdx.x * 4 + (wid >> 1)) * GU_LINES + min(lane, GU_LINES - 1)) * 128, lds0 + K2_PF + wid * 256);
  });
  STAMP_MARK(1, 3);
  wait_bar<0>();
  unsigned* cnt = (unsigned*)(ws + WS_CNT);
  if (tid == 0) __hip_atomic_fetch_add(cnt + qb * 32, 1u, __ATOMIC_RELAXED, __HIP_MEMORY_SCOPE_AGENT);
  const int mi = wid >> 1, kg = wid & 1;
  const int g = lane >> 4, q = (lane & 15) >> 2, p = lane & 3, er = mi * 16 + (lane & 15);
  const bool ex = kg == 0 && g == 0;
  const u32x4 res = *(const u32x4*)(x + (size_t)(m0 + er) * 1152 + cb * 144 + (kg * 4 + g) * 16);
  const u32x4 res8 = *(const u32x4*)(x + (size_t)(m0 + er) * 1152 + cb * 144 + 128);
  const float xm = *(const float*)(ws + WS_XM + (m0 + er) * 4);
  lds_ptr wb = lds + K2_W + kg * 5120 + (16 * (g >> 1) + 4 * (g & 1) + q) * 160 + p * 8, wb2 = wb + 5 * 10240;
  pin(wb); pin(wb2);
  bf16x8 wf[9][5];
#pragma unroll
  for (int j = 0; j < 9; ++j)
#pragma unroll
    for (int n = 0; n < 5; ++n) {
      lds_ptr bp = (j < 5 ? wb + j * 10240 : wb2 + (j - 5) * 10240) + n * 32;
      wf[j][n] = cat8(lds_tr(bp), lds_tr(bp + 1280));
    }
  if (tid == 448) poll_count(cnt + p3 * 32, 32u);
  SB();
  lds_bar();
  SB();
  STAMP_MARK(1, 5);
  const __amdgpu_buffer_rsrc_t ors = __builtin_amdgcn_make_buffer_rsrc((void*)(ws + WS_O), 0, 128 * 18432 + 32 * 8192, 0x00020000);
  const int aoff = ((m0 >> 4) + mi) * 18432 + kg * 1024 + lane * 16;
  bf16x8 a[9];
#pragma unroll
  for (int i = 0; i < 8; ++i) a[i] = __builtin_bit_cast(bf16x8, __builtin_amdgcn_raw_buffer_load_b128(ors, aoff + i * 2048, 0, 16));
  {
    u32x4 ax;
    const int xo = 128 * 18432 + (kg * 16 + 4 * g) * 8192 + (m0 + er) * 4;
#pragma unroll
    for (int e = 0; e < 4; ++e) ax[e] = __builtin_amdgcn_raw_buffer_load_b32(ors, xo, e * 8192, 16);
    a[8] = __builtin_bit_cast(bf16x8, ax);
  }
  SB();
  f32x4 acc[5];
#pragma unroll
  for (int n = 0; n < 5; ++n) acc[n] = f32x4{0.f, 0.f, 0.f, 0.f};
#pragma unroll
  for (int j = 0; j < 9; ++j)
#pragma unroll
    for (int n = 0; n < 5; ++n) acc[n] = MFMA16(wf[j][n], a[j], acc[n]);
  SB();
  prefill();
  SB();
  STAMP_MARK(1, 6);
  {
    lds_ptr cw = lds + K2_SG + (kg * 64 + er) * 336 + 16 * g;
#pragma unroll
    for (int n = 0; n < 5; ++n) *(LDS_AS f32x4*)(cw + n * 64) = acc[n];
  }
  lds_bar();
  {
    lds_ptr cr = lds + K2_SG + er * 336;
    const f32x2 ma = *(LDS_AS f32x2*)(cr + 288), mb = *(LDS_AS f32x2*)(cr + 64 * 336 + 288);
    const float mean = xm + ((ma[0] + ma[1]) + (mb[0] + mb[1]));
    const int row = m0 + er;
    u32x4 o[2];
#pragma unroll
    for (int k = 0; k < 2; ++k) {
      const int c = k ? 8 : kg * 4 + g;
      const u32x4 rs = k ? res8 : res;
      f32x4 a0 = *(LDS_AS f32x4*)(cr + c * 32), a1 = *(LDS_AS f32x4*)(cr + c * 32 + 16);
      a0 += *(LDS_AS f32x4*)(cr + 64 * 336 + c * 32) - mean; a1 += *(LDS_AS f32x4*)(cr + 64 * 336 + c * 32 + 16) - mean;
      o[k] = u32x4{cvt_pk(a0[0] + bf_lo(rs[0]), a0[1] + bf_hi(rs[0])), cvt_pk(a0[2] + bf_lo(rs[1]), a0[3] + bf_hi(rs[1])),
                   cvt_pk(a1[0] + bf_lo(rs[2]), a1[1] + bf_hi(rs[2])), cvt_pk(a1[2] + bf_lo(rs[3]), a1[3] + bf_hi(rs[3]))};
    }
    char* hp = ws + WS_H + (size_t)(row >> 4) * 18432 + cb * 2304 + (row & 15) * 16;
    st16(hp + (kg * 4 + g) * 256, o[0]);
    if (ex) st16(hp + 2048, o[1]);
    float s1 = 0.f, s2 = 0.f, t1 = 0.f, t2 = 0.f;
#pragma unroll
    for (int j = 0; j < 4; ++j) { s1 = dot2(o[0][j], 0x3f803f80u, s1); s2 = dot2(o[0][j], o[0][j], s2); t1 = dot2(o[1][j], 0x3f803f80u, t1); t2 = dot2(o[1][j], o[1][j], t2); }
    s1 += ex ? t1 : 0.f; s2 += ex ? t2 : 0.f;
    s1 = sum_g(s1); s2 = sum_g(s2);
    if (g == 0 || (g == 1 && kg == 0 && cb == 0)) st8wt(ws + WS_ST + (size_t)(g ? 16 : 2 * cb + kg) * 16384 + row * 8, u32x2{__float_as_uint(g ? mean : s1), __float_as_uint(s2)});
  }
}

constexpr int K4_SLOT = 18432, K4_PF = K4_SLOT * 8, K4_SY = 160000, K4_LDS = 160 * 1024;

constexpr int k4_frag(int i) { return i < 16 ? ((i >> 2) * 2 + (i & 1) + ((i & 2) ? 9 : 0)) : (i == 16 ? 8 : 17); }
constexpr int k4_foff(int f) { return (f >= 9 ? 9216 + (f - 9) * 32 : f * 32); }
constexpr int K4_T0 = 13 * 18;
constexpr int k4_slab(int G) { return G < K4_T0 ? G / 18 : (G - K4_T0 < 80 ? 13 + ((G - K4_T0) % 20) / 4 : 13 + (G - K4_T0 - 80) / 2); }
constexpr int k4_pos(int G) { return G < K4_T0 ? G % 18 : (G - K4_T0 < 80 ? ((G - K4_T0) / 20) * 4 + ((G - K4_T0) & 3) : 16 + ((G - K4_T0) & 1)); }
constexpr int k4_op(int G) {
  const int s = G / 18, i = G % 18;
  if (s > 12) return 0;
  if (s < 3) {
    const int pos[10] = {0, 2, 4, 6, 7, 9, 11, 13, 15, 16};
    for (int k = 0; k < 10; ++k) if (pos[k] == i) return 1 + (2 * s + 2 + k / 5) * 8 + k % 5;
    return 0;
  }
  if (s == 3) {
    const int pos[8] = {0, 2, 4, 6, 8, 11, 14, 16};
    for (int k = 0; k < 8; ++k) if (pos[k] == i) return k < 5 ? 1 + 8 * 8 + k : 1 + 9 * 8 + (k - 3);
    return 0;
  }
  if (i == 1) return 1 + (s + 5) * 8;
  if (i == 4) return 1 + (s + 5) * 8 + 1;
  if (s + 6 > 17) return 0;
  if (i == 8) return 1 + (s + 6) * 8 + 2;
  if (i == 13) return 1 + (s + 6) * 8 + 3;
  if (i == 16) return 1 + (s + 6) * 8 + 4;
  return 0;
}
constexpr int k4_allow(int G, int need) {
  int n = (need == 0) ? 2 : 0;
  for (int g = 0; g < G; ++g) {
    const int op = k4_op(g);
    if (op == 0) continue;
    const int sl = (op - 1) >> 3, kind = (op - 1) & 7;
    if (sl <= need) n = 0;
    else if (kind != 4) ++n;
  }
  return n;
}
constexpr int k4_need(int G) {
  if (G + 5 >= 324 || G == 0) return 0;
  int mx = 0;
  for (int g = 0; g < G + 5; ++g) mx = k4_slab(g) > mx ? k4_slab(g) : mx;
  return k4_slab(G + 5) > mx ? k4_slab(G + 5) : 0;
}

constexpr int d_off(int i) { return i < 6 ? 36864 + i * 9216 : (i < 10 ? (i - 6) * 9216 : (i < 17 ? 92160 + (i - 10) * 9216 : 36864)); }
#define DT 8
constexpr int D_G0 = (18 - DT) * 9;
constexpr int d_i(int H) { return H < D_G0 ? H / 9 : (H - D_G0 < 8 * DT ? 18 - DT + ((H - D_G0) % (2 * DT)) / 2 : 18 - DT + (H - D_G0 - 8 * DT)); }
constexpr int d_f(int H) { return H < D_G0 ? H % 9 : (H - D_G0 < 8 * DT ? ((H - D_G0) / (2 * DT)) * 2 + ((H - D_G0) & 1) : 8); }
constexpr int d_first(int H) {
  for (int h = 0; h < H; ++h) if (d_i(h) == d_i(H)) return 0;
  return 1;
}
DI void ld2_sc1(bf16x8& d0, bf16x8& d1, unsigned o0, unsigned o1, const char* base) {
  asm volatile("global_load_dwordx4 %0, %2, %4 sc1\n\tglobal_load_dwordx4 %1, %3, %4 sc1" : "=&v"(d0), "=&v"(d1) : "v"(o0), "v"(o1), "s"(base) : "memory");
}
template <int N> DI void wait_vm2(bf16x8& d0, bf16x8& d1) { asm volatile("s_waitcnt vmcnt(%2)" : "+v"(d0), "+v"(d1) : "n"(N) : "memory"); }
DI void ld8(f32x2& d, unsigned o, const char* base) { asm volatile("global_load_dwordx2 %0, %1, %2 sc1" : "=v"(d) : "v"(o), "s"(base) : "memory"); }

__global__ __launch_bounds__(512, 2) void k4_gateup(const char* __restrict__ wgate, const char* __restrict__ wup, char* __restrict__ ws,
                                                    const char* __restrict__ wd, char* __restrict__ out, const char* __restrict__ wo, const char* __restrict__ x) {
  extern __shared__ __attribute__((aligned(16))) char smem[];
  STAMP_BEGIN
  lds_ptr lds = (lds_ptr)smem;
  const unsigned lds0 = (unsigned)(uintptr_t)lds;
  *(LDS_AS unsigned*)(lds + K4_SY + (threadIdx.x & 1) * 4) = 0u;
  const int wid = __builtin_amdgcn_readfirstlane(threadIdx.x >> 6);
  const int xcd = blockIdx.x & 7, idx = blockIdx.x >> 3;
  const int pn = 4 * (xcd >> 2) + (idx & 3), jt = 8 * (xcd & 3) + (idx >> 2);
  const int wm = wid & 3, wn = wid >> 2;
  const char* wsrc = (wn ? wup : wgate) + jt * 288;
  unsigned wvo[3];
  const unsigned wdst = lds0 + wn * 9216;
  auto issue_w = [&](int s, int i) {
    const unsigned so = (s & 7) * K4_SLOT;
    const char* wp = wsrc + (size_t)s * 32 * 9216;
    if (i < 2) glds16(wp, wvo[i], wdst + so + wm * 2048 + i * 1024);
    else glds16_if0(wm, wp, wvo[2], wdst + so + 8192);
  };
  front_phase(lds, lds0, ws, wo, x, wgate, wup, [&]() {
    int ln = threadIdx.x;
    pin(ln);
    ln &= 63;
#pragma unroll
    for (int i = 0; i < 3; ++i) {
      const int pc = i < 2 ? 2 * wm + i : 8;
      const int o = pc * 1024 + ln * 16;
      const int rho = o / 288, u = (o - rho * 288) >> 4;
      const int gk = ((rho >> 4) & 1) * 2 + ((rho >> 2) & 1), tk = (rho >> 3) & 1, qk = rho & 3;
      wvo[i] = (8 * gk + 4 * tk + qk) * 9216 + u * 16;
    }
    for_seq<2>([&](auto S) { issue_w(S, 0); issue_w(S, 1); issue_w(S, 2); });
  });
  int tid = threadIdx.x;
  pin(tid);
  const int lane = tid & 63;
  const char* asrc = ws + WS_H + (size_t)(pn * 16 + 2 * wid) * 18432;
  const unsigned avo = lane * 16, avo2 = lane * 16 + 18432;
  bf16x8 A[6][2];
  auto issue_a = [&](int s, bf16x8& dst, int m) {
    asm volatile("global_load_dwordx4 %0, %1, %2 sc1" : "=v"(dst) : "v"(m ? avo2 : avo), "s"(asrc + s * 1024) : "memory");
  };
  wait_vm<0>();
  unsigned* cnt3 = (unsigned*)(ws + WS_CNT + 20480);
  if (lds_arrive(lds + K4_SY + 4, lane) == 7 && lane == 0) __hip_atomic_fetch_add(cnt3 + (blockIdx.x & 7) * 32, 1u, __ATOMIC_RELAXED, __HIP_MEMORY_SCOPE_AGENT);
  if (tid == 0) poll_count(cnt3 + pn * 32, 32u);
  SB();
  raw_bar();
  STAMP_MARK(1, 7);
  for_seq<2>([&](auto S) { issue_a(S, A[S][0], 0); issue_a(S, A[S][1], 1); });
  f32x2 sp[8];
  const int g = lane >> 4, q = (lane & 15) >> 2, p = lane & 3, r = lane & 15;
  lds_ptr wb = lds + (16 * (g >> 1) + 4 * (g & 1) + q) * 288 + p * 8;
  lds_ptr wbs[3] = {wb, wb + 3 * K4_SLOT, wb + 6 * K4_SLOT};
  asm volatile("" : "+v"(wbs[1]), "+v"(wbs[2]));
  f32x4 acc[2][18];
  asm volatile("s_waitcnt vmcnt(%2)\n\ts_barrier" : "+v"(A[0][0]), "+v"(A[0][1]) : "n"(k4_allow(0, 0)) : "memory");
  STAMP_MARK(2, 2);
  STAMP_CYC0
  s16x4 Bl[6], Bh[6];
  for_seq<5>([&](auto I) { Bl[I] = lds_tr(wb + k4_foff(k4_frag(I))); Bh[I] = lds_tr(wb + k4_foff(k4_frag(I)) + 2304); });
  SB();
  char* hb = ws + WS_HID + (size_t)(pn * 16 + wid * 2) * (144 * 1024) + (size_t)(jt >> 2) * 18432;
  float c1[2];
  float sd[2];
  auto glu = [&](float ag, float au, int m) -> float {
    return au * __builtin_amdgcn_rcpf(fmaf(__builtin_amdgcn_exp2f(ag * c1[m]), sd[m], sd[m]));
  };
  u32x4 own[4][2];
  float eg[2][2];
  auto micro = [&](auto QQ, auto KK, auto JJ) {
    constexpr int Q = QQ, k = KK, j = JJ, m = k >> 2, d = k & 3, h = d >> 1, e = (d & 1) * 2, b = k & 1;
#pragma unroll
    for (int x = 0; x < 2; ++x) {
      if constexpr (j == 0) eg[b][x] = acc[m][2 * Q + h][e + x] * c1[m];
      if constexpr (j == 1) eg[b][x] = __builtin_amdgcn_exp2f(eg[b][x]);
      if constexpr (j == 2) eg[b][x] = fmaf(eg[b][x], sd[m], sd[m]);
      if constexpr (j == 3) eg[b][x] = __builtin_amdgcn_rcpf(eg[b][x]);
      if constexpr (j == 4) eg[b][x] *= acc[m][9 + 2 * Q + h][e + x];
      if constexpr (j < 5) pin(eg[b][x]);
    }
    if constexpr (j == 6) {
      own[Q][m][d] = cvt_pk(eg[b][0], eg[b][1]);
      if constexpr (d == 3) st16(hb + (size_t)m * (144 * 1024) + (4 * (jt & 3) + Q) * 1024 + lane * 16, own[Q][m]);
      else pin(own[Q][m]);
    }
  };
  auto gapops = [&](auto QQ, auto GG) {
    constexpr int gg = GG;
    if constexpr (gg >= 4 && gg < 36) micro(QQ, std::integral_constant<int, gg / 4 - 1>{}, std::integral_constant<int, 4 + gg % 4>{});
    if constexpr (gg >= 0 && gg < 32) micro(QQ, std::integral_constant<int, gg / 4>{}, std::integral_constant<int, gg % 4>{});
    SB();
  };
  unsigned dvo[2], dvl[2];
  const char* dsrc = wd + (size_t)(jt >> 2) * 576 * 1152 + (jt & 3) * 288;
  auto dvoff = [&]() {
    int ln = tid;
    asm volatile("" : "+v"(ln));
    ln &= 63;
#pragma unroll
    for (int i = 0; i < 2; ++i) {
      const int o = (i ? 8 : wid) * 1024 + ln * 16;
      const int rho = o / 288, u = (o - rho * 288) >> 4;
      const int gk = ((rho >> 4) & 1) * 2 + ((rho >> 2) & 1), tk = (rho >> 3) & 1, qk = rho & 3;
      dvo[i] = (16 * tk + 4 * gk + qk) * 1152 + u * 16;
      dvl[i] = (144 * (gk >> 1) + 128 + 8 * (gk & 1) + 4 * tk + qk) * 1152 + u * 16;
    }
  };
  auto issue_d = [&](auto II, int pc) {
    constexpr int i = II;
    const int k = (4 * (jt & 3) + i) & 15;
    const char* src = dsrc + (size_t)(i < 16 ? 144 * (k >> 2) + 32 * (k & 3) : 288 * (i - 16)) * 1152;
    if (pc == 0) glds16(src, i < 16 ? dvo[0] : dvl[0], lds0 + d_off(i) + wid * 1024);
    else glds16_if0(wid ^ (i & 7), src, i < 16 ? dvo[1] : dvl[1], lds0 + d_off(i) + 8192);
  };
  for_seq<18>([&](auto GH) { for_seq<18>([&](auto GL) {
    constexpr int G = GH * 18 + GL, sl = k4_slab(G), f = k4_frag(k4_pos(G)), rb = G % 6, nb = (G + 5) % 6, NG = G + 5;
    const bf16x8 b = cat8(Bl[rb], Bh[rb]);
    constexpr int need = k4_need(G);
    if constexpr (need != 0) {
      asm volatile("s_waitcnt vmcnt(%2)\n\ts_barrier" : "+v"(A[need % 6][0]), "+v"(A[need % 6][1]) : "n"(k4_allow(G, need)) : "memory");
      SB();
    }
    if constexpr (sl == 0) mfma16_zero(acc[0][f], b, A[0][0]); else mfma16_acc(acc[0][f], b, A[sl % 6][0]);
    SB();
    if constexpr (NG < 324) { Bl[nb] = lds_tr(wbs[(k4_slab(NG) & 7) / 3] + ((k4_slab(NG) & 7) % 3) * K4_SLOT + k4_foff(k4_frag(k4_pos(NG)))); SB(); }
    if constexpr (G >= K4_T0 + 20 && G < K4_T0 + 80 && (G - K4_T0) % 20 >= 1 && (G - K4_T0) % 20 < 19)
      gapops(std::integral_constant<int, (G - K4_T0) / 20 - 1>{}, std::integral_constant<int, 2 * ((G - K4_T0) % 20 - 1)>{});
    if constexpr (G >= K4_T0 + 81) {
      gapops(std::integral_constant<int, 3>{}, std::integral_constant<int, 4 * (G - K4_T0 - 81)>{});
      gapops(std::integral_constant<int, 3>{}, std::integral_constant<int, 4 * (G - K4_T0 - 81) + 1>{});
    }
    if constexpr (sl == 0) mfma16_zero(acc[1][f], b, A[0][1]); else mfma16_acc(acc[1][f], b, A[sl % 6][1]);
    SB();
    if constexpr (NG < 324) { Bh[nb] = lds_tr(wbs[(k4_slab(NG) & 7) / 3] + ((k4_slab(NG) & 7) % 3) * K4_SLOT + k4_foff(k4_frag(k4_pos(NG))) + 2304); SB(); }
    constexpr int op = k4_op(G);
    if constexpr (op != 0) {
      constexpr int os = (op - 1) >> 3, kind = (op - 1) & 7;
      if constexpr (kind < 2) issue_a(os, A[os % 6][kind], kind);
      else issue_w(os, kind - 2);
      SB();
    }
    if constexpr (G % 18 == 10 && (G / 18 == 5 || G / 18 == 7 || G / 18 == 9)) {
      constexpr int k = (G / 18 - 5) / 2;
      touch_if0((int)(blockIdx.x / 216) + (wid ^ (k + 1)), wd + (size_t)(blockIdx.x * 3 + k) * 8192, avo * 8, lds0 + K4_PF + k * 256);
      SB();
    }
    if constexpr (G >= 222 && G < 230) { ld8(sp[G - 222], (lane & 31) * 8 + (lane >> 5) * 8 * 16384, ws + WS_ST + (size_t)(pn * 256 + wid * 32) * 8 + (G - 222) * 16384); SB(); }
    if constexpr (G == K4_T0 + 11) {
      for_seq<8>([&](auto I) { pin(sp[I]); });
      const f32x2 t4 = ((sp[0] + sp[1]) + (sp[2] + sp[3])) + ((sp[4] + sp[5]) + (sp[6] + sp[7]));
      auto r1 = __builtin_amdgcn_permlane32_swap(__float_as_uint(t4[0]), __float_as_uint(t4[0]), false, false);
      auto r2 = __builtin_amdgcn_permlane32_swap(__float_as_uint(t4[1]), __float_as_uint(t4[1]), false, false);
      const float mean = (__uint_as_float(r1[0]) + __uint_as_float(r1[1])) * (1.f / 576.f);
      const float var = fmaxf((__uint_as_float(r2[0]) + __uint_as_float(r2[1])) * (1.f / 576.f) - mean * mean, 0.f) + LN_EPS;
      auto rv = __builtin_amdgcn_permlane16_swap(__float_as_uint(var), __float_as_uint(var), false, false);
      const float v0 = __uint_as_float(rv[0]), v1 = __uint_as_float(rv[1]);
      c1[0] = -1.4426950408889634f * __builtin_amdgcn_rsqf(v0); sd[0] = __builtin_amdgcn_sqrtf(v0);
      c1[1] = -1.4426950408889634f * __builtin_amdgcn_rsqf(v1); sd[1] = __builtin_amdgcn_sqrtf(v1);
      SB();
    }
    if constexpr (G >= K4_T0) {
      constexpr int t = G - K4_T0, u = t % 20;
      if constexpr (t == 11) { dvoff(); SB(); }
      if constexpr (t >= 12 && t < 24) { issue_d(std::integral_constant<int, (t - 12) / 2>{}, t & 1); SB(); }
      if constexpr (t >= 20 && t < 80 && u >= 1 && u < 19) gapops(std::integral_constant<int, t / 20 - 1>{}, std::integral_constant<int, 2 * (u - 1) + 1>{});
      if constexpr (t >= 81) {
        gapops(std::integral_constant<int, 3>{}, std::integral_constant<int, 4 * (t - 81) + 2>{});
        gapops(std::integral_constant<int, 3>{}, std::integral_constant<int, 4 * (t - 81) + 3>{});
      }
    }
  }); });
  {
    asm volatile("s_nop 7\n\ts_nop 1" : "+v"(acc[0][8]), "+v"(acc[1][8]), "+v"(acc[0][17]), "+v"(acc[1][17]));
#pragma unroll
    for (int m = 0; m < 2; ++m) {
      const f32x4 ag = acc[m][8], au = acc[m][17];
      u32x2 o = {cvt_pk(glu(ag[0], au[0], m), glu(ag[1], au[1], m)), cvt_pk(glu(ag[2], au[2], m), glu(ag[3], au[3], m))};
      st8wt(hb + (size_t)m * (144 * 1024) + (16 + ((jt >> 1) & 1)) * 1024 + (2 * (jt & 1) + (g >> 1)) * 256 + r * 16 + (g & 1) * 8, o);
    }
  }
  STAMP_MARK(2, 5);
  const int sb = jt >> 2, nq = jt & 3;
  unsigned* cntA = (unsigned*)(ws + WS_CNT + 8192) + (pn * 8 + sb) * 32;
  unsigned* cntB = (unsigned*)(ws + WS_CNT + 16384) + (pn * 4 + nq) * 32;
  wait_bar<10>();
  auto dptr = [&](auto HH) -> lds_ptr {
    constexpr int H = HH, o = d_off(d_i(H)) + d_f(H) * 32;
    return wbs[o / (3 * K4_SLOT)] + o % (3 * K4_SLOT);
  };
  for_seq<5>([&](auto I) { Bl[I] = lds_tr(dptr(I)); Bh[I] = lds_tr(dptr(I) + 2304); });
  SB();
  f32x4 dacc[2][9];
  bf16x8 DA[14][2];
  char* pb = ws + WS_PART + (size_t)(((pn * 4 + nq) * 8 + wid) * 8 + sb) * 9216 + lane * 16;
  for_seq<18>([&](auto HH) { for_seq<9>([&](auto HL) {
    constexpr int H = HH * 9 + HL, i = d_i(H), f = d_f(H), rb = H % 6, nb = (H + 5) % 6, NH = H + 5;
    if constexpr (H == 36) {
      if (tid == 0) poll_count(cntA, 4u);
      SB();
      wait_bar<0>();
      STAMP_MARK(2, 6);
      issue_d(std::integral_constant<int, 17>{}, 0); issue_d(std::integral_constant<int, 17>{}, 1);
      const unsigned ho = lane * 16, ho2 = lane * 16 + 144 * 1024;
      for_seq<14>([&](auto JJ) {
        constexpr int j = JJ;
        const int k = j < 12 ? ((4 * nq + 4 + j) & 15) : 4 + j;
        ld2_sc1(DA[j][0], DA[j][1], ho, ho2, hb + k * 1024);
      });
      SB();
    }
    if constexpr (i >= 4 && d_first(H)) {
      wait_vm2<2 * (17 - i)>(DA[i - 4][0], DA[i - 4][1]);
      SB();
    }
    if constexpr (H == D_G0) { raw_bar(); SB(); }
    const bf16x8 b = cat8(Bl[rb], Bh[rb]);
    bf16x8 a0, a1;
    if constexpr (i < 4) { a0 = __builtin_bit_cast(bf16x8, own[i][0]); a1 = __builtin_bit_cast(bf16x8, own[i][1]); }
    else { a0 = DA[i - 4][0]; a1 = DA[i - 4][1]; }
    if constexpr (i == 0) mfma16_zero(dacc[0][f], b, a0); else mfma16_acc(dacc[0][f], b, a0);
    SB();
    if constexpr (NH < 162) { Bl[nb] = lds_tr(dptr(std::integral_constant<int, NH>{})); SB(); }
    if constexpr (i == 0) mfma16_zero(dacc[1][f], b, a1); else mfma16_acc(dacc[1][f], b, a1);
    SB();
    if constexpr (NH < 162) { Bh[nb] = lds_tr(dptr(std::integral_constant<int, NH>{}) + 2304); SB(); }
    if constexpr (H < 22) { issue_d(std::integral_constant<int, 6 + H / 2>{}, H & 1); SB(); }
    if constexpr (H == 24) {
      wait_vm<11>();
      if (lds_arrive(lds + K4_SY, lane) == 7 && lane == 0) __hip_atomic_fetch_add(cntA, 1u, __ATOMIC_RELAXED, __HIP_MEMORY_SCOPE_AGENT);
      SB();
    }
    if constexpr (H >= D_G0 + 2 * DT && (((H - D_G0) % (2 * DT) == 2 || (H - D_G0) % (2 * DT) == 3))) {
      constexpr int pr = (H - D_G0) / (2 * DT) - 1, m = ((H - D_G0) % (2 * DT) == 3);
      const f32x4 x0 = dacc[m][2 * pr], x1 = dacc[m][2 * pr + 1];
      st16(pb + (m * 4 + pr) * 1024, u32x4{cvt_pk(x0[0], x0[1]), cvt_pk(x0[2], x0[3]), cvt_pk(x1[0], x1[1]), cvt_pk(x1[2], x1[3])});
      SB();
    }
  }); });
  {
    asm volatile("s_nop 7\n\ts_nop 1" : "+v"(dacc[0][8]), "+v"(dacc[1][8]));
    const f32x4 x0 = dacc[0][8], x1 = dacc[1][8];
    st16(pb + 8192, u32x4{cvt_pk(x0[0], x0[1]), cvt_pk(x0[2], x0[3]), cvt_pk(x1[0], x1[1]), cvt_pk(x1[2], x1[3])});
  }
  wait_bar<0>();
  STAMP_MARK(2, 7);
  if (tid == 0) __hip_atomic_fetch_add(cntB, 1u, __ATOMIC_RELAXED, __HIP_MEMORY_SCOPE_AGENT);
  {
    const int nc = wid == 1 ? 2 : 1;
    const int rbase = pn * 256 + sb * 32;
    int row[2][2], col[2][2];
    row[0][0] = row[0][1] = rbase + (wid >> 2) * 16 + r; col[0][0] = nq * 144 + (wid & 3) * 32 + 4 * g; col[0][1] = col[0][0] + 16;
    row[1][0] = rbase + r; row[1][1] = rbase + 16 + r; col[1][0] = col[1][1] = nq * 144 + 128 + 4 * g;
    const __amdgpu_buffer_rsrc_t hrs = __builtin_amdgcn_make_buffer_rsrc((void*)(ws + WS_H), 0, (int)(WS_XM - WS_H), 0x00020000);
    u32x2 res[2][2];
    float hm[2][2];
#pragma unroll
    for (int c = 0; c < 2; ++c)
#pragma unroll
      for (int e = 0; e < 2; ++e)
      {
        res[c][e] = __builtin_amdgcn_raw_buffer_load_b64(hrs, (row[c][e] >> 4) * 18432 + (col[c][e] >> 3) * 256 + (row[c][e] & 15) * 16 + ((col[c][e] >> 2) & 1) * 8, 0, 16);
        hm[c][e] = __uint_as_float(__builtin_amdgcn_raw_buffer_load_b32(hrs, (int)(WS_HM - WS_H) + row[c][e] * 8, 0, 16));
      }
    if (tid == 0) poll_count(cntB, 8u);
    SB();
    raw_bar();
    SB();
    const __amdgpu_buffer_rsrc_t prs = __builtin_amdgcn_make_buffer_rsrc((void*)(ws + WS_PART + (size_t)((pn * 4 + nq) * 8 + sb) * 8 * 9216), 0, 8 * 9216, 0x00020000);
#pragma unroll
    for (int c = 0; c < 2; ++c) {
      if (c < nc) {
        u32x4 pv[8];
#pragma unroll
        for (int k = 0; k < 8; ++k) pv[k] = __builtin_amdgcn_raw_buffer_load_b128(prs, lane * 16 + (c ? 8192 : wid * 1024), k * 9216, 16);
        float sum[8];
#pragma unroll
        for (int e = 0; e < 4; ++e) { sum[2 * e] = bf_lo(res[c][e >> 1][e & 1]) + hm[c][e >> 1]; sum[2 * e + 1] = bf_hi(res[c][e >> 1][e & 1]) + hm[c][e >> 1]; }
        float acc2[8];
#pragma unroll
        for (int e = 0; e < 8; ++e) acc2[e] = 0.f;
#pragma unroll
        for (int k = 0; k < 8; ++k)
#pragma unroll
          for (int e = 0; e < 4; ++e) { acc2[2 * e] += bf_lo(pv[k][e]); acc2[2 * e + 1] += bf_hi(pv[k][e]); }
        unsigned pk[4];
#pragma unroll
        for (int e = 0; e < 4; ++e) pk[e] = cvt_pk(sum[2 * e] + acc2[2 * e], sum[2 * e + 1] + acc2[2 * e + 1]);
        auto x0 = __builtin_amdgcn_permlane16_swap(pk[0], pk[2], false, false);
        auto x1 = __builtin_amdgcn_permlane16_swap(pk[1], pk[3], false, false);
        const int sel = g & 1;
        st16(out + (size_t)(row[c][0] + (c ? 16 * sel : 0)) * 1152 + (col[c][0] + (c ? -4 : 12) * sel) * 2, (u32x4{x0[0], x1[0], x0[1], x1[1]}));
      }
    }
  }
  STAMP_END(2);
  STAMP_END(1);
}

extern "C" void kernel_launch(void* const* d_in, const int* in_sizes, int n_in, void* d_out, int out_size, void* d_ws, size_t ws_size,
                              hipStream_t stream) {
  const char* x = (const char*)d_in[0];
  const char* wqkv = (const char*)d_in[1];
  const char* wo = (const char*)d_in[2];
  const char* wg = (const char*)d_in[4];
  const char* wu = (const char*)d_in[5];
  const char* wd = (const char*)d_in[6];
  char* ws = (char*)d_ws + WS_BASE(ws_size);
  (void)hipFuncSetAttribute((const void*)k1_ln_qkv, hipFuncAttributeMaxDynamicSharedMemorySize, K1_LDS);
  (void)hipFuncSetAttribute((const void*)k4_gateup, hipFuncAttributeMaxDynamicSharedMemorySize, K4_LDS);
  k1_ln_qkv<<<234, 1024, K1_LDS, stream>>>(x, wqkv, ws, wo);
  k4_gateup<<<256, 512, K4_LDS, stream>>>(wg, wu, ws, wd, (char*)d_out, wo, x);
}
```

```cpp
#include <hip/hip_runtime.h>
#include <hip/hip_bf16.h>
#include <stdint.h>
#include <utility>
#include <type_traits>

typedef __attribute__((ext_vector_type(8))) short bf16x8;
typedef __attribute__((ext_vector_type(4))) short s16x4;
typedef __attribute__((ext_vector_type(4))) float f32x4;
typedef __attribute__((ext_vector_type(16))) float f32x16;
typedef __attribute__((ext_vector_type(4))) unsigned u32x4;
typedef __attribute__((ext_vector_type(2))) unsigned u32x2;
typedef __attribute__((ext_vector_type(2))) __bf16 bf16x2_t;
typedef __attribute__((ext_vector_type(2))) float f32x2;
#define LDS_AS __attribute__((address_space(3)))
typedef LDS_AS char* lds_ptr;
#define DI __device__ __forceinline__

constexpr int SEQ = 2048;
constexpr float LN_EPS = 1e-5f;
constexpr float QSCALE = 0.23570226039551584f * 1.4426950408889634f;

constexpr size_t WS_Q = 0;
constexpr size_t WS_K = WS_Q + (size_t)72 * 32768;
constexpr size_t WS_V = WS_K + (size_t)72 * 32768;
constexpr size_t WS_ONES = WS_V + (size_t)256 * 9216;
constexpr size_t WS_O = WS_ONES + 1024;
constexpr size_t WS_OX = WS_O + (size_t)128 * 18 * 1024;
constexpr size_t WS_H = WS_OX + (size_t)32 * 8192;
constexpr size_t WS_ST = WS_H + (size_t)128 * 18 * 1024;
constexpr size_t WS_HM = WS_ST + (size_t)16 * 16384;
constexpr size_t WS_XM = WS_HM + 16384;
constexpr size_t WS_WB = WS_XM + 8192;
constexpr size_t WS_HID = WS_WB + 9216;
constexpr size_t WS_CNT = WS_HID + (size_t)128 * 144 * 1024;
constexpr size_t WS_PART = WS_CNT + 24576;
constexpr size_t WS_END = WS_PART + (size_t)2048 * 9216;
static_assert(WS_O % 1024 == 0 && WS_H % 1024 == 0 && WS_ST % 256 == 0 && WS_HID % 256 == 0, "align");

#define WS_BASE(sz) (((sz) - WS_END) & ~(size_t)0x1fffff)
#define STAMP_BEGIN
#define STAMP_MARK(k, i)
#define STAMP_MARKW(k, i, T)
#define STAMP_ENDW(k, T)
#define STAMP_CYC0
#define STAMP_CYC(k, i)
#define STAMP_END(k)

DI unsigned cvt_pk(float lo, float hi) {
  f32x2 v = {lo, hi};
  bf16x2_t r = __builtin_convertvector(v, bf16x2_t);
  return __builtin_bit_cast(unsigned, r);
}
DI float bf_lo(unsigned u) { return __builtin_bit_cast(float, u << 16); }
DI float bf_hi(unsigned u) { return __builtin_bit_cast(float, u & 0xffff0000u); }
DI float dot2(unsigned a, unsigned b, float c) {
  return __builtin_amdgcn_fdot2_f32_bf16(__builtin_bit_cast(bf16x2_t, a), __builtin_bit_cast(bf16x2_t, b), c, false);
}
template <int CTRL> DI float dpp_add(float v) {
  int t = __builtin_amdgcn_update_dpp(0, __builtin_bit_cast(int, v), CTRL, 0xf, 0xf, true);
  return v + __builtin_bit_cast(float, t);
}
DI float sum8(float v) {
  v = dpp_add<0xB1>(v);
  v = dpp_add<0x4E>(v);
  v = dpp_add<0x141>(v);
  return v;
}
DI float sum_g(float v) {
  auto a = __builtin_amdgcn_permlane16_swap(__float_as_uint(v), __float_as_uint(v), false, false);
  v = __uint_as_float(a[0]) + __uint_as_float(a[1]);
  auto b = __builtin_amdgcn_permlane32_swap(__float_as_uint(v), __float_as_uint(v), false, false);
  return __uint_as_float(b[0]) + __uint_as_float(b[1]);
}
DI void glds16(const void* sbase, unsigned voff, unsigned lds_dst) {
  asm volatile("s_mov_b32 m0, %2\n\ts_nop 3\n\tglobal_load_lds_dwordx4 %0, %1" ::"v"(voff), "s"(sbase), "s"(lds_dst) : "memory");
}
DI void st16(void* p, u32x4 v) { asm volatile("global_store_dwordx4 %0, %1, off sc1\n\ts_nop 1" ::"v"(p), "v"(v) : "memory"); }
DI void st8wt(void* p, u32x2 v) { asm volatile("global_store_dwordx2 %0, %1, off sc1" ::"v"(p), "v"(v) : "memory"); }
DI void st4(void* p, unsigned v) { asm volatile("global_store_dword %0, %1, off sc1" ::"v"(p), "v"(v) : "memory"); }
DI void glds16_if0(int c, const void* sbase, unsigned voff, unsigned lds_dst) {
  asm volatile("s_cmp_lg_u32 %3, 0\n\ts_cbranch_scc1 1f\n\ts_mov_b32 m0, %2\n\ts_nop 1\n\tglobal_load_lds_dwordx4 %0, %1\n1:"
               ::"v"(voff), "s"(sbase), "s"(lds_dst), "s"(c) : "memory", "scc");
}
DI void touch_if0(int c, const void* sbase, unsigned voff, unsigned lds_dst) {
  asm volatile("s_cmp_lg_u32 %3, 0\n\ts_cbranch_scc1 1f\n\ts_mov_b32 m0, %2\n\ts_nop 1\n\tglobal_load_lds_dword %0, %1\n1:"
               ::"v"(voff), "s"(sbase), "s"(lds_dst), "s"(c) : "memory", "scc");
}
DI void touch_lines(const char* base, unsigned voff, unsigned lds_dummy) {
  asm volatile("s_mov_b32 m0, %2\n\ts_nop 3\n\tglobal_load_lds_dword %0, %1" ::"v"(voff), "s"(base), "s"(lds_dummy) : "memory");
}
#define CODE_TOUCH(SYM, BYTES, LDS_DUMMY)                                                                          \
  do {                                                                                                             \
    const char* cp_;                                                                                               \
    asm volatile("s_getpc_b64 s[20:21]\n\ts_add_u32 s20, s20, " SYM "@rel32@lo+4\n\ts_addc_u32 s21, s21, " SYM     \
                 "@rel32@hi+12\n\ts_mov_b64 %0, s[20:21]" : "=s"(cp_)::"s20", "s21", "scc");                      \
    constexpr int nl_ = (BYTES) / 128, per_ = (nl_ + 28) / 29;                                                      \
    const int ln_ = (blockIdx.x >> 3) * per_ + lane;                                                               \
    if (lane < per_ && ln_ < nl_) touch_lines(cp_, ln_ * 128, LDS_DUMMY);                                          \
  } while (0)
#define SYM_K1 "_Z9k1_ln_qkvPKcS0_PcS0_"
#define SYM_K4 "_Z9k4_gateupPKcS0_PcS0_S1_S0_S0_"
constexpr int LEN_K1 = 20000, LEN_K4 = 38000;
template <int N> DI void wait_bar() { asm volatile("s_waitcnt vmcnt(%0) lgkmcnt(0)\n\ts_barrier" ::"n"(N) : "memory"); }
template <int N> DI void wait_vm() { asm volatile("s_waitcnt vmcnt(%0)" ::"n"(N) : "memory"); }
DI unsigned lds_arrive(lds_ptr c, int lane) {
  unsigned old = 0;
  if (lane == 0) old = __hip_atomic_fetch_add((LDS_AS unsigned*)c, 1u, __ATOMIC_RELAXED, __HIP_MEMORY_SCOPE_WORKGROUP);
  return __builtin_amdgcn_readfirstlane(old);
}
template <class T> DI void pin(T& v) { asm volatile("" : "+v"(v)); }
DI void raw_bar() { asm volatile("s_barrier" ::: "memory"); }
DI void lds_bar() { asm volatile("s_waitcnt lgkmcnt(0)\n\ts_barrier" ::: "memory"); }
DI bf16x8 lds_r128(lds_ptr p) { return *(LDS_AS const bf16x8*)p; }
typedef short v4i16_t __attribute__((ext_vector_type(4)));
DI s16x4 lds_tr(lds_ptr p) { return __builtin_amdgcn_ds_read_tr16_b64_v4i16((LDS_AS v4i16_t*)p); }
DI bf16x8 cat8(s16x4 a, s16x4 b) { return __builtin_shufflevector(a, b, 0, 1, 2, 3, 4, 5, 6, 7); }
#define SB() __builtin_amdgcn_sched_barrier(0)
#define MFMA16(a, b, c) __builtin_amdgcn_mfma_f32_16x16x32_bf16(a, b, c, 0, 0, 0)
#define MFMA32(a, b, c) __builtin_amdgcn_mfma_f32_32x32x16_bf16(a, b, c, 0, 0, 0)
DI void mfma16_acc(f32x4& c, bf16x8 a, bf16x8 b) { asm volatile("v_mfma_f32_16x16x32_bf16 %0, %1, %2, %0" : "+v"(c) : "v"(a), "v"(b)); }
DI void mfma16_zero(f32x4& c, bf16x8 a, bf16x8 b) { asm volatile("v_mfma_f32_16x16x32_bf16 %0, %1, %2, 0" : "=&v"(c) : "v"(a), "v"(b)); }
template <class F, int... I> DI void for_seq_impl(F&& f, std::integer_sequence<int, I...>) { (f(std::integral_constant<int, I>{}), ...); }
template <int N, class F> DI void for_seq(F&& f) { for_seq_impl(f, std::make_integer_sequence<int, N>{}); }
DI int xcd_remap(int b, int nwg) {
  int q = nwg >> 3, r = nwg & 7, x = b & 7, o = b >> 3;
  return (x < r ? x * (q + 1) : r * (q + 1) + (x - r) * q) + o;
}

constexpr int K1_X = 0, K1_W = 40960, K1_SLOT = 12288, K1_ST = K1_W + K1_SLOT * 9, K1_CS = K1_W, K1_PF = K1_ST + 1024, K1_LDS = K1_PF + 1024;
constexpr int cmin(int a, int b) { return a < b ? a : b; }
constexpr int k1_nw(int kk) { return cmin(18, cmin(3 + 2 * kk, kk + 7)); }
constexpr int k1_nx(int kk) { return cmin(18, cmin(3 + 2 * kk, kk + 5)); }

template <bool ISV, bool HI>
DI void k1_consumer(lds_ptr lds, int cb, int lane, char* ws, int ct, int m0) {
  const int g = lane >> 4, q = (lane & 15) >> 2, p = lane & 3, r = lane & 15;
  constexpr int NM = HI ? 2 : 3, MB = HI ? 3 : 0;
  lds_ptr a0 = lds + K1_X + MB * 2048 + (r >> 3) * 1024 + (r & 7) * 128 + ((g ^ (r >> 1)) * 16);
  lds_ptr a1 = lds + K1_X + MB * 2048 + (r >> 3) * 1024 + (r & 7) * 128 + (((4 + g) ^ (r >> 1)) * 16);
  const int rho0 = 16 * (g >> 1) + 4 * (g & 1) + q;
  lds_ptr wb = lds + K1_W + rho0 * 128 + ((cb ^ ((rho0 >> 1) & 3)) * 32) + p * 8;
  lds_ptr wb5 = wb + 5 * K1_SLOT;
  asm volatile("" : "+v"(wb), "+v"(wb5));
  const u32x4 onesu = {0x3f803f80u, 0x3f803f80u, 0x3f803f80u, 0x3f803f80u};
  const bf16x8 ones = __builtin_bit_cast(bf16x8, onesu);
  f32x4 acc[3][3];
#pragma unroll
  for (int m = 0; m < 3; ++m)
#pragma unroll
    for (int j = 0; j < 3; ++j) acc[m][j] = f32x4{0.f, 0.f, 0.f, 0.f};
  bf16x8 fA[2][3];
  s16x4 wl[2][3], wh[2][3];
  auto rd1 = [&](auto KK, auto II) {
    constexpr int kk = KK, i = II, bf = kk & 1, sl = kk % 9;
    if constexpr (i >= 2 && i <= 4) {
      if constexpr (i - 2 < NM) fA[bf][i - 2] = lds_r128(((kk & 1) ? a1 : a0) + ((kk >> 1) & 3) * 10240 + (i - 2) * 2048);
    } else {
      constexpr int j = i < 2 ? 0 : (i - 3) / 2, hh = i < 2 ? i : (i - 3) % 2;
      lds_ptr bp = (sl >= 5 ? wb5 + (sl - 5) * K1_SLOT : wb + sl * K1_SLOT) + j * 4096 + hh * 1024;
      if constexpr (hh == 0) wl[bf][j] = lds_tr(bp); else wh[bf][j] = lds_tr(bp);
    }
  };
  lds_bar();
  STAMP_MARKW(0, 2, 512);
  for_seq<9>([&](auto I) { rd1(std::integral_constant<int, 0>{}, I); });
  SB();
  for_seq<18>([&](auto KK) {
    constexpr int kk = KK, bf = kk & 1;
    if constexpr (kk < 17) { raw_bar(); SB(); }
    for_seq<9>([&](auto II) {
      constexpr int i = II, j = i / 3, m = i % 3;
      const bf16x8 wv = cat8(wl[bf][j], wh[bf][j]);
      const bf16x8 av = (m < NM) ? fA[bf][m] : ones;
      acc[m][j] = ISV ? MFMA16(av, wv, acc[m][j]) : MFMA16(wv, av, acc[m][j]);
      SB();
      if constexpr (kk < 17) { rd1(std::integral_constant<int, kk + 1>{}, II); SB(); }
    });
  });
  STAMP_MARKW(0, 4, 512);
  lds_ptr cx = lds + K1_CS + cb * 3072 + lane * 16;
  if (HI) {
#pragma unroll
    for (int j = 0; j < 3; ++j) *(LDS_AS f32x4*)(cx + j * 1024) = acc[2][j];
  }
  lds_bar();
  STAMP_MARKW(0, 5, 512);
  f32x4 cs[3];
#pragma unroll
  for (int j = 0; j < 3; ++j) cs[j] = HI ? acc[2][j] : *(LDS_AS f32x4*)(cx + j * 1024);
  u32x2 o[NM * 3];
  if (ISV) {
#pragma unroll
    for (int m = 0; m < NM; ++m) {
      const f32x4 s0 = *(LDS_AS f32x4*)(lds + K1_ST + ((MB + m) * 16 + 4 * g) * 8), s1 = *(LDS_AS f32x4*)(lds + K1_ST + ((MB + m) * 16 + 4 * g) * 8 + 16);
#pragma unroll
      for (int j = 0; j < 3; ++j) {
        const float c0 = cs[j][0];
        o[3 * m + j] = u32x2{cvt_pk(fmaf(s0[0], c0, acc[m][j][0]) * s0[1], fmaf(s0[2], c0, acc[m][j][1]) * s0[3]),
                             cvt_pk(fmaf(s1[0], c0, acc[m][j][2]) * s1[1], fmaf(s1[2], c0, acc[m][j][3]) * s1[3])};
      }
    }
  } else {
    const float sc = ct < 3 ? QSCALE : 1.f;
#pragma unroll
    for (int m = 0; m < NM; ++m) {
      f32x2 st = *(LDS_AS f32x2*)(lds + K1_ST + ((MB + m) * 16 + r) * 8);
      st[1] *= sc;
#pragma unroll
      for (int j = 0; j < 3; ++j)
        o[3 * m + j] = u32x2{cvt_pk(fmaf(st[0], cs[j][0], acc[m][j][0]) * st[1], fmaf(st[0], cs[j][1], acc[m][j][1]) * st[1]),
                             cvt_pk(fmaf(st[0], cs[j][2], acc[m][j][2]) * st[1], fmaf(st[0], cs[j][3], acc[m][j][3]) * st[1])};
    }
  }
  char* base;
  int sm, sj, sel, half;
  if (ISV) { base = ws + WS_V + (size_t)(((m0 >> 4) + MB) * 2 + (g & 1)) * 9216 + ((ct - 6) * 192 + cb * 16 + r) * 16; sm = 18432; sj = 1024; sel = g >> 1; half = g >> 1; }
  else { base = ws + WS_Q + (size_t)(ct * 24 + cb * 2 + (g >> 1)) * 32768 + (m0 + MB * 16 + r) * 16; sm = 256; sj = 8 * 32768; sel = g & 1; half = g & 1; }
#pragma unroll
  for (int n = 0; n + 1 < NM * 3; n += 2) {
    u32x4 c;
#pragma unroll
    for (int e = 0; e < 2; ++e) {
      auto rr = ISV ? __builtin_amdgcn_permlane32_swap(o[n][e], o[n + 1][e], false, false) : __builtin_amdgcn_permlane16_swap(o[n][e], o[n + 1][e], false, false);
      c[e] = rr[0]; c[2 + e] = rr[1];
    }
    const int o0 = (n / 3) * sm + (n % 3) * sj, o1 = ((n + 1) / 3) * sm + ((n + 1) % 3) * sj;
    st16(base + (sel ? o1 : o0), c);
  }
  if (NM == 3) st8wt(base + 2 * sm + 2 * sj + half * 8, o[8]);
}

__global__ __launch_bounds__(1024) void k1_ln_qkv(const char* __restrict__ x, const char* __restrict__ wqkv, char* __restrict__ ws, const char* __restrict__ wo) {
  extern __shared__ __attribute__((aligned(16))) char smem[];
  STAMP_BEGIN
  lds_ptr lds = (lds_ptr)smem;
  const unsigned lds0 = (unsigned)(uintptr_t)lds;
  const int tid = threadIdx.x, lane = tid & 63, wid = __builtin_amdgcn_readfirstlane(tid >> 6);
  if (blockIdx.x >= 234) {
    const int sb = blockIdx.x - 234;
    if (sb == 18) {
      if (tid < 192) *(unsigned*)(ws + WS_CNT + tid * 128) = 0u;
      if (tid < 4) *(unsigned*)(ws + WS_ONES + tid * 4) = 0x3f803f80u;
    } else if (wid < 4) {
      const int wrow = (sb * 4 + wid) * 8 + (lane >> 3);
      float t = 0.f;
      u32x4 wv[9];
#pragma unroll
      for (int i = 0; i < 9; ++i) wv[i] = *(const u32x4*)(wo + (size_t)wrow * 1152 + (lane & 7) * 16 + i * 128);
#pragma unroll
      for (int i = 0; i < 9; ++i)
#pragma unroll
        for (int e = 0; e < 4; ++e) t = dot2(wv[i][e], 0x3f803f80u, t);
      t = sum8(t) * (1.f / 576.f);
      const unsigned h = cvt_pk(t, 0.f) & 0xffffu, l = cvt_pk(t - bf_lo(h), 0.f) & 0xffffu;
      if ((lane & 7) == 0) *(u32x4*)(ws + WS_WB + wrow * 16) = u32x4{h | (l << 16), 0u, 0u, 0u};
    }
    return;
  }
  const int tile = xcd_remap(blockIdx.x, 234);
  const int cgp = tile / 78, rem = tile - cgp * 78;
  const int mt = rem / 3, ct = cgp * 3 + (rem - mt * 3);
  const int m0 = min(mt * 80, SEQ - 80);
  const int sm = wid - 3;
  lds_ptr sa0 = lds + K1_X + sm * 2048 + ((lane >> 3) & 1) * 1024 + (lane & 7) * 128 + (((lane >> 4) ^ ((lane & 15) >> 1)) * 16);
  lds_ptr sa1 = lds + K1_X + sm * 2048 + ((lane >> 3) & 1) * 1024 + (lane & 7) * 128 + (((4 + (lane >> 4)) ^ ((lane & 15) >> 1)) * 16);
  f32x4 ssum = {0.f, 0.f, 0.f, 0.f}, sgram = {0.f, 0.f, 0.f, 0.f};
  bf16x8 sf0, sf1;
  auto stat_rd = [&](int st) { sf0 = lds_r128(sa0 + (st & 3) * 10240); sf1 = lds_r128(sa1 + (st & 3) * 10240); };
  auto stat_mm = [&]() {
    const bf16x8 ones = __builtin_bit_cast(bf16x8, u32x4{0x3f803f80u, 0x3f803f80u, 0x3f803f80u, 0x3f803f80u});
    ssum = MFMA16(ones, sf0, ssum); sgram = MFMA16(sf0, sf0, sgram);
    ssum = MFMA16(ones, sf1, ssum); sgram = MFMA16(sf1, sf1, sgram);
    pin(ssum); pin(sgram);
  };
  auto stat_fin = [&]() {
    const float q01 = (lane & 1) ? sgram[1] : sgram[0], q23 = (lane & 1) ? sgram[3] : sgram[2];
    const float mean = ssum[0] * (1.f / 576.f);
    const float var = fmaxf(((lane & 2) ? q23 : q01) * (1.f / 576.f) - mean * mean, 0.f);
    if ((lane >> 4) == ((lane & 15) >> 2)) {
      *(LDS_AS f32x2*)(lds + K1_ST + (sm * 16 + (lane & 15)) * 8) = f32x2{-mean, __builtin_amdgcn_rsqf(var + LN_EPS)};
      if (ct == 0) *(float*)(ws + WS_XM + (m0 + sm * 16 + (lane & 15)) * 4) = mean;
    }
    lds_bar();
  };
  if (wid < 3) {
    unsigned wv[4];
#pragma unroll
    for (int i = 0; i < 4; ++i) {
      const int rho = 8 * i + (lane >> 3);
      const int gk = ((rho >> 4) & 1) * 2 + ((rho >> 2) & 1), tk = (rho >> 3) & 1, qk = rho & 3;
      wv[i] = (8 * gk + 4 * tk + qk) * 3456 + (((lane & 7) ^ (((rho >> 1) & 3) << 1)) * 16);
    }
    const char* wsrc = wqkv + ct * 384 + wid * 128;
    const unsigned wdst = lds0 + K1_W + wid * 4096;
    for_seq<18>([&](auto KK) {
      constexpr int kk = KK;
#pragma unroll
      for (int n = (kk == 0 ? 0 : k1_nw(kk - 1)); n < k1_nw(kk); ++n)
#pragma unroll
        for (int i = 0; i < 4; ++i) glds16(wsrc + (size_t)n * 32 * 3456, wv[i], wdst + (n % 9) * K1_SLOT + i * 1024);
      wait_bar<(k1_nw(kk) - kk - 1) * 4>();
    });
    return;
  }
  if (wid < 8) {
    const unsigned v0 = (lane >> 3) * 1152 + (((lane & 7) ^ (lane >> 4)) * 16);
    const unsigned v1 = 9216 + (lane >> 3) * 1152 + (((lane & 7) ^ (4 + (lane >> 4))) * 16);
    const char* xs = x + (size_t)(m0 + sm * 16) * 1152;
    for_seq<18>([&](auto KK) {
      constexpr int kk = KK;
#pragma unroll
      for (int h = (kk == 0 ? 0 : k1_nx(kk - 1)); h < k1_nx(kk); ++h)
        glds16(xs + (h >> 1) * 128, (h & 1) ? v1 : v0, lds0 + K1_X + ((h >> 1) & 3) * 10240 + (sm * 2 + (h & 1)) * 1024);
      if constexpr (kk & 1) stat_rd(kk >> 1);
      else if constexpr (kk > 0) stat_mm();
      SB();
      wait_bar<k1_nx(kk) - 2 * (kk >> 1) - 2>();
    });
    stat_mm();
    stat_fin();
    return;
  }
  if (wid == 8) CODE_TOUCH(SYM_K1, LEN_K1, lds0 + K1_PF);
  if (wid == 9) CODE_TOUCH(SYM_K4, LEN_K4, lds0 + K1_PF + 256);
  if (wid >= 12) {
    const int ln = blockIdx.x * 148 + (wid - 12) * 37 + lane;
    if (lane < 37) {
      if (ln < 15552) touch_lines(wqkv, ln * 128, lds0 + K1_PF + 512);
      else if (ln < 33984) touch_lines(x, (ln - 15552) * 128, lds0 + K1_PF + 512);
    }
  }
  if (ct >= 6) {
    if (wid < 12) k1_consumer<true, false>(lds, wid - 8, lane, ws, ct, m0);
    else k1_consumer<true, true>(lds, wid - 12, lane, ws, ct, m0);
  } else {
    if (wid < 12) k1_consumer<false, false>(lds, wid - 8, lane, ws, ct, m0);
    else k1_consumer<false, true>(lds, wid - 12, lane, ws, ct, m0);
  }
  STAMP_ENDW(0, 512);
}

constexpr int K2_KM = 0, K2_V = 24576, K2_PF = 45056, K2_W = 65536, K2_SG = 98304;

#define MFMA32K8(a, b, c) __builtin_amdgcn_mfma_f32_32x32x8bf16_1k(a, b, c, 0, 0, 0)
template <class F>
DI void k2_core(lds_ptr lds, char* ws, int hd, int t, int wid, int lane, u32x4 q0u, u32x2 q1u, bool first, F&& hook) {
  const int hi = lane >> 5, l31 = lane & 31;
  const int dlo = ((18 * hd) & 7) >> 1;
#pragma unroll
  for (int e = 0; e < 4; ++e) q0u[e] = (4 * hi + e >= dlo) ? q0u[e] : 0u;
#pragma unroll
  for (int e = 0; e < 2; ++e) q1u[e] = (8 + 2 * hi + e < dlo + 9) ? q1u[e] : 0u;
  const bf16x8 q0 = __builtin_bit_cast(bf16x8, q0u);
  const s16x4 q1 = __builtin_bit_cast(s16x4, q1u);
  f32x16 S[9];
  lds_ptr kmp = lds + K2_KM + hi * 8192 + (wid * 32 + l31) * 16;
  lds_ptr kxp = lds + K2_KM + 16384 + (wid * 32 + l31) * 16 + hi * 8;
  const float NEG = -1e30f;
  const int dq = l31 - 4 * hi;
  float mx0 = NEG, mx1 = NEG;
  bf16x8 kf[3];
  s16x4 kx[3];
  auto rdk = [&](auto NN) {
    constexpr int n = NN, i = n == 0 ? 8 : n - 1;
    kf[n % 3] = lds_r128(kmp + i * 512);
    kx[n % 3] = *(LDS_AS const s16x4*)(kxp + i * 512);
  };
  auto post = [&](auto NN) {
    constexpr int n = NN, i = n == 0 ? 8 : n - 1;
    if constexpr (i < 8) {
      if constexpr (i == 0) {
#pragma unroll
        for (int r = 0; r < 16; ++r) S[0][r] = (((r & 3) + 8 * (r >> 2)) <= dq) ? S[8][r] : S[0][r];
      }
#pragma unroll
      for (int r = 0; r < 16; r += 4) {
        mx0 = fmaxf(fmaxf(mx0, S[i][r]), S[i][r + 1]);
        mx1 = fmaxf(fmaxf(mx1, S[i][r + 2]), S[i][r + 3]);
      }
    }
  };
  rdk(std::integral_constant<int, 0>{});
  rdk(std::integral_constant<int, 1>{});
  SB();
  for_seq<9>([&](auto NN) {
    constexpr int n = NN, i = n == 0 ? 8 : n - 1;
    const f32x16 z = {};
    S[i] = MFMA32(kf[n % 3], q0, z);
    SB();
    if constexpr (n + 2 < 9) { rdk(std::integral_constant<int, n + 2>{}); SB(); }
    S[i] = MFMA32K8(kx[n % 3], q1, S[i]);
    SB();
    if constexpr (n >= 1) { post(std::integral_constant<int, n - 1>{}); SB(); }
    hook(std::integral_constant<int, n>{}); SB();
  });
  post(std::integral_constant<int, 8>{});
  float mx = fmaxf(mx0, mx1);
  {
    auto rr = __builtin_amdgcn_permlane32_swap(__float_as_uint(mx), __float_as_uint(mx), false, false);
    mx = fmaxf(__uint_as_float(rr[0]), __uint_as_float(rr[1]));
  }
  s16x4 mq;
  {
    const float nm = -mx;
    const unsigned h1 = __float_as_uint(nm) & 0xffff0000u;
    const float r1 = nm - __uint_as_float(h1);
    const unsigned h2 = __float_as_uint(r1) & 0xffff0000u;
    const float r2 = r1 - __uint_as_float(h2);
    const u32x2 mu = {hi ? 0u : ((h1 >> 16) | h2), hi ? 0u : ((__float_as_uint(r2) >> 16) | 0xf1490000u)};
    mq = __builtin_bit_cast(s16x4, mu);
  }
  auto mk1 = [&](int i) { return __builtin_bit_cast(s16x4, u32x2{0x3f803f80u, (first && i > 0 && wid + i < 8) ? 0x3f803f80u : 0x00003f80u}); };
  S[1] = MFMA32K8(mk1(1), mq, S[1]);
  S[2] = MFMA32K8(mk1(2), mq, S[2]);
  SB();
  lds_ptr vp = lds + K2_V + wid * 2 * 592 + (l31 < 18 ? hi * 288 + l31 * 16 : 576);
  f32x16 O;
  const unsigned keep0 = first ? 0u : ~0u;
  bf16x8 vf[3];
  auto rdv = [&](auto EE) {
    constexpr int e = EE, blk = e < 14 ? e + 2 : ((e - 14) >> 1) + ((e & 1) ? 16 : 0);
    if constexpr (e < 18) vf[e % 3] = lds_r128(vp + blk * 592);
  };
  rdv(std::integral_constant<int, 0>{});
  rdv(std::integral_constant<int, 1>{});
  SB();
  for_seq<16>([&](auto HH) {
    constexpr int h = HH, i = ((h >> 1) + 1) & 7, s = h & 1;
    u32x4 P;
#pragma unroll
    for (int j = 0; j < 4; ++j) P[j] = cvt_pk(__builtin_amdgcn_exp2f(S[i][8 * s + 2 * j]), __builtin_amdgcn_exp2f(S[i][8 * s + 2 * j + 1]));
    if constexpr (i == 0) {
      constexpr int e = 14 + 2 * s;
      u32x4 P8, P0;
#pragma unroll
      for (int j = 0; j < 4; ++j) {
        const int r0 = 8 * s + 2 * j, r1 = r0 + 1;
        const unsigned mk = ((((r0 & 3) + 8 * (r0 >> 2)) <= dq) ? 0xffffu : 0u) | ((((r1 & 3) + 8 * (r1 >> 2)) <= dq) ? 0xffff0000u : 0u);
        P8[j] = P[j] & mk;
        P0[j] = P[j] & ~mk & keep0;
      }
      rdv(std::integral_constant<int, e + 2>{});
      SB();
      O = MFMA32(vf[e % 3], __builtin_bit_cast(bf16x8, P0), O);
      SB();
      rdv(std::integral_constant<int, e + 3>{});
      O = MFMA32(vf[(e + 1) % 3], __builtin_bit_cast(bf16x8, P8), O);
      SB();
    } else {
      rdv(std::integral_constant<int, h + 2>{});
      if constexpr (h < 9) hook(std::integral_constant<int, 9 + h>{});
      SB();
      if constexpr (h == 0) { const f32x16 z = {}; O = MFMA32(vf[0], __builtin_bit_cast(bf16x8, P), z); }
      else O = MFMA32(vf[h % 3], __builtin_bit_cast(bf16x8, P), O);
      if constexpr (s == 0 && i + 2 <= 8) S[(i + 2) & 7] = MFMA32K8(mk1((i + 2) & 7), mq, S[(i + 2) & 7]);
      SB();
    }
  });
  const float inv = __builtin_amdgcn_rcpf(O[10]);
  const int row = t * 32 + l31;
  char* ob = ws + WS_O + (size_t)(row >> 4) * 18 * 1024 + (row & 15) * 16;
  u32x4 o = {cvt_pk(O[0] * inv, O[1] * inv), cvt_pk(O[2] * inv, O[3] * inv), cvt_pk(O[4] * inv, O[5] * inv), cvt_pk(O[6] * inv, O[7] * inv)};
  st16(ob + (hd >> 1) * 1024 + (2 * (hd & 1) + hi) * 256, o);
  if (hi == 0) st4(ws + WS_OX + hd * 8192 + row * 4, cvt_pk(O[8] * inv, O[9] * inv));
}

DI void glds16v_if0(int c, const void* vaddr, unsigned lds_dst) {
  asm volatile("s_cmp_lg_u32 %2, 0\n\ts_cbranch_scc1 1f\n\ts_mov_b32 m0, %1\n\ts_nop 1\n\tglobal_load_lds_dwordx4 %0, off\n1:"
               ::"v"(vaddr), "s"(lds_dst), "s"(c) : "memory", "scc");
}
DI void poll_count(const unsigned* c, unsigned n) {
  for (int spin = 0; spin < (1 << 18); ++spin) {
    if (__hip_atomic_load(c, __ATOMIC_RELAXED, __HIP_MEMORY_SCOPE_AGENT) >= n) break;
    __builtin_amdgcn_s_sleep(1);
  }
}

template <class F>
DI void front_phase(lds_ptr lds, unsigned lds0, char* __restrict__ ws, const char* __restrict__ wo, const char* __restrict__ x,
                    const char* __restrict__ wg, const char* __restrict__ wu, F&& prefill) {
  const int tid = threadIdx.x, lane = tid & 63, wid = __builtin_amdgcn_readfirstlane(tid >> 6);
  const int hd = (blockIdx.x & 7) * 4 + ((blockIdx.x >> 3) & 3), qb = blockIdx.x >> 5;
  const int p3 = blockIdx.x & 7, m0 = p3 * 256 + ((blockIdx.x >> 3) & 3) * 64, cb = blockIdx.x >> 5;
  const int t = qb * 8 + wid;
  u32x4 q0;
  u32x2 q1;
  const char *wa, *wx;
  unsigned wst;
  {
    const int c0 = (18 * hd) >> 3;
    asm volatile("global_load_dwordx4 %0, %2, %4\n\tglobal_load_dwordx2 %1, %3, %4"
                 : "=&v"(q0), "=&v"(q1)
                 : "v"((lane >> 5) * 32768 + (lane & 31) * 16), "v"(65536 + (lane & 31) * 16 + (lane >> 5) * 8),
                   "s"(ws + WS_Q + (size_t)c0 * 32768 + t * 512)
                 : "memory");
    const int key0 = max(qb * 256 - 256 + wid * 64, 0);
#pragma unroll
    for (int i = 0; i < 3; ++i) glds16(ws + WS_K + (size_t)(c0 + i) * 32768 + key0 * 16, (qb == 0 && wid < 4) ? 0 : lane * 16, lds0 + K2_KM + i * 8192 + wid * 1024);
#pragma unroll
    for (int i = 0; i < 3; ++i) {
      const int pc = wid + 8 * i;
      if (pc < 19) {
        const int u = min(pc * 64 + lane, 32 * 37 - 1), bl = u / 37, w = u - bl * 37;
        const int blk = max(qb * 16 - 16 + bl, 0);
        const unsigned vo = w == 36 ? (unsigned)(WS_ONES - WS_V) : (blk * 2 + (w >= 18)) * 9216 + (hd * 18 + (w >= 18 ? w - 18 : w)) * 16;
        glds16(ws + WS_V, vo, lds0 + K2_V + pc * 1024);
      }
    }
    const int u = min(wid, 4) * 64 + lane, rho = u / 10, c = u - rho * 10;
    const int g_ = ((rho >> 4) & 1) * 2 + ((rho >> 2) & 1), t_ = (rho >> 3) & 1, q_ = rho & 3;
    const char* wbase = c < 9 ? wo + cb * 144 + c * 16 : ws + WS_WB;
    wst = c < 9 ? 1152 : 16;
    wa = wbase + (18 * (g_ >> 1) + 8 * t_ + 4 * (g_ & 1) + q_) * wst;
    wx = wbase + (18 * (4 * g_ + 2 * t_ + (q_ >> 1)) + 16 + (q_ & 1)) * wst;
    wst *= 36;
    pin(wa); pin(wx); pin(wst);
  }
  asm volatile("s_waitcnt vmcnt(0)\n\ts_barrier" : "+v"(q0), "+v"(q1)::"memory");
  STAMP_MARK(1, 2);
  k2_core(lds, ws, hd, t, wid, lane, q0, q1, qb == 0, [&](auto I) {
    if constexpr (I < 18) {
      glds16v_if0(wid / 5, I < 16 ? wa : wx, lds0 + K2_W + (I * 5 + wid) * 1024);
      if constexpr (I < 15) wa += wst;
      if constexpr (I == 16) wx += 8 * wst;
    }
    if constexpr (I == 17) {
      const int un = blockIdx.x * 3 + max(wid - 5, 0);
      touch_if0(1 - wid / 5, ((un & 1) ? wu : wg) + (size_t)(un >> 1) * 8192, lane * 128, lds0 + K2_PF + wid * 256);
    }
  });
  STAMP_MARK(1, 3);
  wait_bar<0>();
  unsigned* cnt = (unsigned*)(ws + WS_CNT);
  if (tid == 0) __hip_atomic_fetch_add(cnt + qb * 32, 1u, __ATOMIC_RELAXED, __HIP_MEMORY_SCOPE_AGENT);
  const int mi = wid >> 1, kg = wid & 1;
  const int g = lane >> 4, q = (lane & 15) >> 2, p = lane & 3, er = mi * 16 + (lane & 15);
  const bool ex = kg == 0 && g == 0;
  const u32x4 res = *(const u32x4*)(x + (size_t)(m0 + er) * 1152 + cb * 144 + (kg * 4 + g) * 16);
  const u32x4 res8 = *(const u32x4*)(x + (size_t)(m0 + er) * 1152 + cb * 144 + 128);
  const float xm = *(const float*)(ws + WS_XM + (m0 + er) * 4);
  lds_ptr wb = lds + K2_W + kg * 5120 + (16 * (g >> 1) + 4 * (g & 1) + q) * 160 + p * 8, wb2 = wb + 5 * 10240;
  pin(wb); pin(wb2);
  bf16x8 wf[9][5];
#pragma unroll
  for (int j = 0; j < 9; ++j)
#pragma unroll
    for (int n = 0; n < 5; ++n) {
      lds_ptr bp = (j < 5 ? wb + j * 10240 : wb2 + (j - 5) * 10240) + n * 32;
      wf[j][n] = cat8(lds_tr(bp), lds_tr(bp + 1280));
    }
  if (tid == 448) poll_count(cnt + p3 * 32, 32u);
  SB();
  lds_bar();
  SB();
  STAMP_MARK(1, 5);
  const __amdgpu_buffer_rsrc_t ors = __builtin_amdgcn_make_buffer_rsrc((void*)(ws + WS_O), 0, 128 * 18432 + 32 * 8192, 0x00020000);
  const int aoff = ((m0 >> 4) + mi) * 18432 + kg * 1024 + lane * 16;
  bf16x8 a[9];
#pragma unroll
  for (int i = 0; i < 8; ++i) a[i] = __builtin_bit_cast(bf16x8, __builtin_amdgcn_raw_buffer_load_b128(ors, aoff + i * 2048, 0, 16));
  {
    u32x4 ax;
    const int xo = 128 * 18432 + (kg * 16 + 4 * g) * 8192 + (m0 + er) * 4;
#pragma unroll
    for (int e = 0; e < 4; ++e) ax[e] = __builtin_amdgcn_raw_buffer_load_b32(ors, xo, e * 8192, 16);
    a[8] = __builtin_bit_cast(bf16x8, ax);
  }
  SB();
  f32x4 acc[5];
#pragma unroll
  for (int n = 0; n < 5; ++n) acc[n] = f32x4{0.f, 0.f, 0.f, 0.f};
#pragma unroll
  for (int j = 0; j < 9; ++j)
#pragma unroll
    for (int n = 0; n < 5; ++n) acc[n] = MFMA16(wf[j][n], a[j], acc[n]);
  SB();
  prefill();
  SB();
  STAMP_MARK(1, 6);
  {
    lds_ptr cw = lds + K2_SG + (kg * 64 + er) * 336 + 16 * g;
#pragma unroll
    for (int n = 0; n < 5; ++n) *(LDS_AS f32x4*)(cw + n * 64) = acc[n];
  }
  lds_bar();
  {
    lds_ptr cr = lds + K2_SG + er * 336;
    const f32x2 ma = *(LDS_AS f32x2*)(cr + 288), mb = *(LDS_AS f32x2*)(cr + 64 * 336 + 288);
    const float mean = xm + ((ma[0] + ma[1]) + (mb[0] + mb[1]));
    const int row = m0 + er;
    u32x4 o[2];
#pragma unroll
    for (int k = 0; k < 2; ++k) {
      const int c = k ? 8 : kg * 4 + g;
      const u32x4 rs = k ? res8 : res;
      f32x4 a0 = *(LDS_AS f32x4*)(cr + c * 32), a1 = *(LDS_AS f32x4*)(cr + c * 32 + 16);
      a0 += *(LDS_AS f32x4*)(cr + 64 * 336 + c * 32) - mean; a1 += *(LDS_AS f32x4*)(cr + 64 * 336 + c * 32 + 16) - mean;
      o[k] = u32x4{cvt_pk(a0[0] + bf_lo(rs[0]), a0[1] + bf_hi(rs[0])), cvt_pk(a0[2] + bf_lo(rs[1]), a0[3] + bf_hi(rs[1])),
                   cvt_pk(a1[0] + bf_lo(rs[2]), a1[1] + bf_hi(rs[2])), cvt_pk(a1[2] + bf_lo(rs[3]), a1[3] + bf_hi(rs[3]))};
    }
    char* hp = ws + WS_H + (size_t)(row >> 4) * 18432 + cb * 2304 + (row & 15) * 16;
    st16(hp + (kg * 4 + g) * 256, o[0]);
    if (ex) st16(hp + 2048, o[1]);
    float s1 = 0.f, s2 = 0.f, t1 = 0.f, t2 = 0.f;
#pragma unroll
    for (int j = 0; j < 4; ++j) { s1 = dot2(o[0][j], 0x3f803f80u, s1); s2 = dot2(o[0][j], o[0][j], s2); t1 = dot2(o[1][j], 0x3f803f80u, t1); t2 = dot2(o[1][j], o[1][j], t2); }
    s1 += ex ? t1 : 0.f; s2 += ex ? t2 : 0.f;
    s1 = sum_g(s1); s2 = sum_g(s2);
    if (g == 0 || (g == 1 && kg == 0 && cb == 0)) st8wt(ws + WS_ST + (size_t)(g ? 16 : 2 * cb + kg) * 16384 + row * 8, u32x2{__float_as_uint(g ? mean : s1), __float_as_uint(s2)});
  }
}

constexpr int K4_SLOT = 18432, K4_PF = K4_SLOT * 8, K4_SY = 160000, K4_LDS = 160 * 1024;

constexpr int k4_frag(int i) { return i < 16 ? ((i >> 2) * 2 + (i & 1) + ((i & 2) ? 9 : 0)) : (i == 16 ? 8 : 17); }
constexpr int k4_foff(int f) { return (f >= 9 ? 9216 + (f - 9) * 32 : f * 32); }
constexpr int K4_T0 = 13 * 18;
constexpr int k4_slab(int G) { return G < K4_T0 ? G / 18 : (G - K4_T0 < 80 ? 13 + ((G - K4_T0) % 20) / 4 : 13 + (G - K4_T0 - 80) / 2); }
constexpr int k4_pos(int G) { return G < K4_T0 ? G % 18 : (G - K4_T0 < 80 ? ((G - K4_T0) / 20) * 4 + ((G - K4_T0) & 3) : 16 + ((G - K4_T0) & 1)); }
constexpr int k4_op(int G) {
  const int s = G / 18, i = G % 18;
  if (s > 12) return 0;
  if (s < 3) {
    const int pos[10] = {0, 2, 4, 6, 7, 9, 11, 13, 15, 16};
    for (int k = 0; k < 10; ++k) if (pos[k] == i) return 1 + (2 * s + 2 + k / 5) * 8 + k % 5;
    return 0;
  }
  if (s == 3) {
    const int pos[8] = {0, 2, 4, 6, 8, 11, 14, 16};
    for (int k = 0; k < 8; ++k) if (pos[k] == i) return k < 5 ? 1 + 8 * 8 + k : 1 + 9 * 8 + (k - 3);
    return 0;
  }
  if (i == 1) return 1 + (s + 5) * 8;
  if (i == 4) return 1 + (s + 5) * 8 + 1;
  if (s + 6 > 17) return 0;
  if (i == 8) return 1 + (s + 6) * 8 + 2;
  if (i == 13) return 1 + (s + 6) * 8 + 3;
  if (i == 16) return 1 + (s + 6) * 8 + 4;
  return 0;
}
constexpr int k4_allow(int G, int need) {
  int n = (need == 0) ? 2 : 0;
  for (int g = 0; g < G; ++g) {
    const int op = k4_op(g);
    if (op == 0) continue;
    const int sl = (op - 1) >> 3, kind = (op - 1) & 7;
    if (sl <= need) n = 0;
    else if (kind != 4) ++n;
  }
  return n;
}
constexpr int k4_need(int G) {
  if (G + 5 >= 324 || G == 0) return 0;
  int mx = 0;
  for (int g = 0; g < G + 5; ++g) mx = k4_slab(g) > mx ? k4_slab(g) : mx;
  return k4_slab(G + 5) > mx ? k4_slab(G + 5) : 0;
}

constexpr int d_off(int i) { return i < 6 ? 36864 + i * 9216 : (i < 10 ? (i - 6) * 9216 : (i < 17 ? 92160 + (i - 10) * 9216 : 36864)); }
constexpr int DT = 8;
constexpr int D_G0 = (18 - DT) * 9;
constexpr int d_i(int H) { return H < D_G0 ? H / 9 : (H - D_G0 < 8 * DT ? 18 - DT + ((H - D_G0) % (2 * DT)) / 2 : 18 - DT + (H - D_G0 - 8 * DT)); }
constexpr int d_f(int H) { return H < D_G0 ? H % 9 : (H - D_G0 < 8 * DT ? ((H - D_G0) / (2 * DT)) * 2 + ((H - D_G0) & 1) : 8); }
constexpr int d_first(int H) {
  for (int h = 0; h < H; ++h) if (d_i(h) == d_i(H)) return 0;
  return 1;
}
DI void ld2_sc1(bf16x8& d0, bf16x8& d1, unsigned o0, unsigned o1, const char* base) {
  asm volatile("global_load_dwordx4 %0, %2, %4 sc1\n\tglobal_load_dwordx4 %1, %3, %4 sc1" : "=&v"(d0), "=&v"(d1) : "v"(o0), "v"(o1), "s"(base) : "memory");
}
template <int N> DI void wait_vm2(bf16x8& d0, bf16x8& d1) { asm volatile("s_waitcnt vmcnt(%2)" : "+v"(d0), "+v"(d1) : "n"(N) : "memory"); }
DI void ld8(f32x2& d, unsigned o, const char* base) { asm volatile("global_load_dwordx2 %0, %1, %2 sc1" : "=v"(d) : "v"(o), "s"(base) : "memory"); }

__global__ __launch_bounds__(512, 2) void k4_gateup(const char* __restrict__ wgate, const char* __restrict__ wup, char* __restrict__ ws,
                                                    const char* __restrict__ wd, char* __restrict__ out, const char* __restrict__ wo, const char* __restrict__ x) {
  extern __shared__ __attribute__((aligned(16))) char smem[];
  STAMP_BEGIN
  lds_ptr lds = (lds_ptr)smem;
  const unsigned lds0 = (unsigned)(uintptr_t)lds;
  *(LDS_AS unsigned*)(lds + K4_SY + (threadIdx.x & 1) * 4) = 0u;
  const int wid = __builtin_amdgcn_readfirstlane(threadIdx.x >> 6);
  const int xcd = blockIdx.x & 7, idx = blockIdx.x >> 3;
  const int pn = 4 * (xcd >> 2) + (idx & 3), jt = 8 * (xcd & 3) + (idx >> 2);
  const int wm = wid & 3, wn = wid >> 2;
  const char* wsrc = (wn ? wup : wgate) + jt * 288;
  unsigned wvo[3];
  const unsigned wdst = lds0 + wn * 9216;
  auto issue_w = [&](int s, int i) {
    const unsigned so = (s & 7) * K4_SLOT;
    const char* wp = wsrc + (size_t)s * 32 * 9216;
    if (i < 2) glds16(wp, wvo[i], wdst + so + wm * 2048 + i * 1024);
    else glds16_if0(wm, wp, wvo[2], wdst + so + 8192);
  };
  front_phase(lds, lds0, ws, wo, x, wgate, wup, [&]() {
    int ln = threadIdx.x;
    pin(ln);
    ln &= 63;
#pragma unroll
    for (int i = 0; i < 3; ++i) {
      const int pc = i < 2 ? 2 * wm + i : 8;
      const int o = pc * 1024 + ln * 16;
      const int rho = o / 288, u = (o - rho * 288) >> 4;
      const int gk = ((rho >> 4) & 1) * 2 + ((rho >> 2) & 1), tk = (rho >> 3) & 1, qk = rho & 3;
      wvo[i] = (8 * gk + 4 * tk + qk) * 9216 + u * 16;
    }
    for_seq<2>([&](auto S) { issue_w(S, 0); issue_w(S, 1); issue_w(S, 2); });
  });
  int tid = threadIdx.x;
  pin(tid);
  const int lane = tid & 63;
  const char* asrc = ws + WS_H + (size_t)(pn * 16 + 2 * wid) * 18432;
  const unsigned avo = lane * 16, avo2 = lane * 16 + 18432;
  bf16x8 A[6][2];
  auto issue_a = [&](int s, bf16x8& dst, int m) {
    asm volatile("global_load_dwordx4 %0, %1, %2 sc1" : "=v"(dst) : "v"(m ? avo2 : avo), "s"(asrc + s * 1024) : "memory");
  };
  wait_vm<0>();
  unsigned* cnt3 = (unsigned*)(ws + WS_CNT + 20480);
  if (lds_arrive(lds + K4_SY + 4, lane) == 7 && lane == 0) __hip_atomic_fetch_add(cnt3 + (blockIdx.x & 7) * 32, 1u, __ATOMIC_RELAXED, __HIP_MEMORY_SCOPE_AGENT);
  if (tid == 0) poll_count(cnt3 + pn * 32, 32u);
  SB();
  raw_bar();
  STAMP_MARK(1, 7);
  for_seq<2>([&](auto S) { issue_a(S, A[S][0], 0); issue_a(S, A[S][1], 1); });
  f32x2 sp[8];
  const int g = lane >> 4, q = (lane & 15) >> 2, p = lane & 3, r = lane & 15;
  lds_ptr wb = lds + (16 * (g >> 1) + 4 * (g & 1) + q) * 288 + p * 8;
  lds_ptr wbs[3] = {wb, wb + 3 * K4_SLOT, wb + 6 * K4_SLOT};
  asm volatile("" : "+v"(wbs[1]), "+v"(wbs[2]));
  f32x4 acc[2][18];
  asm volatile("s_waitcnt vmcnt(%2)\n\ts_barrier" : "+v"(A[0][0]), "+v"(A[0][1]) : "n"(k4_allow(0, 0)) : "memory");
  STAMP_MARK(2, 2);
  STAMP_CYC0
  s16x4 Bl[6], Bh[6];
  for_seq<5>([&](auto I) { Bl[I] = lds_tr(wb + k4_foff(k4_frag(I))); Bh[I] = lds_tr(wb + k4_foff(k4_frag(I)) + 2304); });
  SB();
  char* hb = ws + WS_HID + (size_t)(pn * 16 + wid * 2) * (144 * 1024) + (size_t)(jt >> 2) * 18432;
  float c1[2];
  float sd[2];
  auto glu = [&](float ag, float au, int m) -> float {
    return au * __builtin_amdgcn_rcpf(fmaf(__builtin_amdgcn_exp2f(ag * c1[m]), sd[m], sd[m]));
  };
  u32x4 own[4][2];
  float eg[2][2];
  auto micro = [&](auto QQ, auto KK, auto JJ) {
    constexpr int Q = QQ, k = KK, j = JJ, m = k >> 2, d = k & 3, h = d >> 1, e = (d & 1) * 2, b = k & 1;
#pragma unroll
    for (int x = 0; x < 2; ++x) {
      if constexpr (j == 0) eg[b][x] = acc[m][2 * Q + h][e + x] * c1[m];
      if constexpr (j == 1) eg[b][x] = __builtin_amdgcn_exp2f(eg[b][x]);
      if constexpr (j == 2) eg[b][x] = fmaf(eg[b][x], sd[m], sd[m]);
      if constexpr (j == 3) eg[b][x] = __builtin_amdgcn_rcpf(eg[b][x]);
      if constexpr (j == 4) eg[b][x] *= acc[m][9 + 2 * Q + h][e + x];
      if constexpr (j < 5) pin(eg[b][x]);
    }
    if constexpr (j == 6) {
      own[Q][m][d] = cvt_pk(eg[b][0], eg[b][1]);
      if constexpr (d == 3) st16(hb + (size_t)m * (144 * 1024) + (4 * (jt & 3) + Q) * 1024 + lane * 16, own[Q][m]);
      else pin(own[Q][m]);
    }
  };
  auto gapops = [&](auto QQ, auto GG) {
    constexpr int gg = GG;
    if constexpr (gg >= 4 && gg < 36) micro(QQ, std::integral_constant<int, gg / 4 - 1>{}, std::integral_constant<int, 4 + gg % 4>{});
    if constexpr (gg >= 0 && gg < 32) micro(QQ, std::integral_constant<int, gg / 4>{}, std::integral_constant<int, gg % 4>{});
    SB();
  };
  unsigned dvo[2], dvl[2];
  const char* dsrc = wd + (size_t)(jt >> 2) * 576 * 1152 + (jt & 3) * 288;
  auto dvoff = [&]() {
    int ln = tid;
    asm volatile("" : "+v"(ln));
    ln &= 63;
#pragma unroll
    for (int i = 0; i < 2; ++i) {
      const int o = (i ? 8 : wid) * 1024 + ln * 16;
      const int rho = o / 288, u = (o - rho * 288) >> 4;
      const int gk = ((rho >> 4) & 1) * 2 + ((rho >> 2) & 1), tk = (rho >> 3) & 1, qk = rho & 3;
      dvo[i] = (16 * tk + 4 * gk + qk) * 1152 + u * 16;
      dvl[i] = (144 * (gk >> 1) + 128 + 8 * (gk & 1) + 4 * tk + qk) * 1152 + u * 16;
    }
  };
  auto issue_d = [&](auto II, int pc) {
    constexpr int i = II;
    const int k = (4 * (jt & 3) + i) & 15;
    const char* src = dsrc + (size_t)(i < 16 ? 144 * (k >> 2) + 32 * (k & 3) : 288 * (i - 16)) * 1152;
    if (pc == 0) glds16(src, i < 16 ? dvo[0] : dvl[0], lds0 + d_off(i) + wid * 1024);
    else glds16_if0(wid ^ (i & 7), src, i < 16 ? dvo[1] : dvl[1], lds0 + d_off(i) + 8192);
  };
  for_seq<18>([&](auto GH) { for_seq<18>([&](auto GL) {
    constexpr int G = GH * 18 + GL, sl = k4_slab(G), f = k4_frag(k4_pos(G)), rb = G % 6, nb = (G + 5) % 6, NG = G + 5;
    const bf16x8 b = cat8(Bl[rb], Bh[rb]);
    constexpr int need = k4_need(G);
    if constexpr (need != 0) {
      asm volatile("s_waitcnt vmcnt(%2)\n\ts_barrier" : "+v"(A[need % 6][0]), "+v"(A[need % 6][1]) : "n"(k4_allow(G, need)) : "memory");
      SB();
    }
    if constexpr (sl == 0) mfma16_zero(acc[0][f], b, A[0][0]); else mfma16_acc(acc[0][f], b, A[sl % 6][0]);
    SB();
    if constexpr (NG < 324) { Bl[nb] = lds_tr(wbs[(k4_slab(NG) & 7) / 3] + ((k4_slab(NG) & 7) % 3) * K4_SLOT + k4_foff(k4_frag(k4_pos(NG)))); SB(); }
    if constexpr (G >= K4_T0 + 20 && G < K4_T0 + 80 && (G - K4_T0) % 20 >= 1 && (G - K4_T0) % 20 < 19)
      gapops(std::integral_constant<int, (G - K4_T0) / 20 - 1>{}, std::integral_constant<int, 2 * ((G - K4_T0) % 20 - 1)>{});
    if constexpr (G >= K4_T0 + 81) {
      gapops(std::integral_constant<int, 3>{}, std::integral_constant<int, 4 * (G - K4_T0 - 81)>{});
      gapops(std::integral_constant<int, 3>{}, std::integral_constant<int, 4 * (G - K4_T0 - 81) + 1>{});
    }
    if constexpr (sl == 0) mfma16_zero(acc[1][f], b, A[0][1]); else mfma16_acc(acc[1][f], b, A[sl % 6][1]);
    SB();
    if constexpr (NG < 324) { Bh[nb] = lds_tr(wbs[(k4_slab(NG) & 7) / 3] + ((k4_slab(NG) & 7) % 3) * K4_SLOT + k4_foff(k4_frag(k4_pos(NG))) + 2304); SB(); }
    constexpr int op = k4_op(G);
    if constexpr (op != 0) {
      constexpr int os = (op - 1) >> 3, kind = (op - 1) & 7;
      if constexpr (kind < 2) issue_a(os, A[os % 6][kind], kind);
      else issue_w(os, kind - 2);
      SB();
    }
    if constexpr (G % 18 == 10 && (G / 18 == 5 || G / 18 == 7 || G / 18 == 9)) {
      constexpr int k = (G / 18 - 5) / 2;
      touch_if0((int)(blockIdx.x / 216) + (wid ^ (k + 1)), wd + (size_t)(blockIdx.x * 3 + k) * 8192, avo * 8, lds0 + K4_PF + k * 256);
      SB();
    }
    if constexpr (G >= 222 && G < 230) { ld8(sp[G - 222], (lane & 31) * 8 + (lane >> 5) * 8 * 16384, ws + WS_ST + (size_t)(pn * 256 + wid * 32) * 8 + (G - 222) * 16384); SB(); }
    if constexpr (G == K4_T0 + 11) {
      for_seq<8>([&](auto I) { pin(sp[I]); });
      const f32x2 t4 = ((sp[0] + sp[1]) + (sp[2] + sp[3])) + ((sp[4] + sp[5]) + (sp[6] + sp[7]));
      auto r1 = __builtin_amdgcn_permlane32_swap(__float_as_uint(t4[0]), __float_as_uint(t4[0]), false, false);
      auto r2 = __builtin_amdgcn_permlane32_swap(__float_as_uint(t4[1]), __float_as_uint(t4[1]), false, false);
      const float mean = (__uint_as_float(r1[0]) + __uint_as_float(r1[1])) * (1.f / 576.f);
      const float var = fmaxf((__uint_as_float(r2[0]) + __uint_as_float(r2[1])) * (1.f / 576.f) - mean * mean, 0.f) + LN_EPS;
      auto rv = __builtin_amdgcn_permlane16_swap(__float_as_uint(var), __float_as_uint(var), false, false);
      const float v0 = __uint_as_float(rv[0]), v1 = __uint_as_float(rv[1]);
      c1[0] = -1.4426950408889634f * __builtin_amdgcn_rsqf(v0); sd[0] = __builtin_amdgcn_sqrtf(v0);
      c1[1] = -1.4426950408889634f * __builtin_amdgcn_rsqf(v1); sd[1] = __builtin_amdgcn_sqrtf(v1);
      SB();
    }
    if constexpr (G >= K4_T0) {
      constexpr int t = G - K4_T0, u = t % 20;
      if constexpr (t == 11) { dvoff(); SB(); }
      if constexpr (t >= 12 && t < 24) { issue_d(std::integral_constant<int, (t - 12) / 2>{}, t & 1); SB(); }
      if constexpr (t >= 20 && t < 80 && u >= 1 && u < 19) gapops(std::integral_constant<int, t / 20 - 1>{}, std::integral_constant<int, 2 * (u - 1) + 1>{});
      if constexpr (t >= 81) {
        gapops(std::integral_constant<int, 3>{}, std::integral_constant<int, 4 * (t - 81) + 2>{});
        gapops(std::integral_constant<int, 3>{}, std::integral_constant<int, 4 * (t - 81) + 3>{});
      }
    }
  }); });
  {
    asm volatile("s_nop 7\n\ts_nop 1" : "+v"(acc[0][8]), "+v"(acc[1][8]), "+v"(acc[0][17]), "+v"(acc[1][17]));
#pragma unroll
    for (int m = 0; m < 2; ++m) {
      const f32x4 ag = acc[m][8], au = acc[m][17];
      u32x2 o = {cvt_pk(glu(ag[0], au[0], m), glu(ag[1], au[1], m)), cvt_pk(glu(ag[2], au[2], m), glu(ag[3], au[3], m))};
      st8wt(hb + (size_t)m * (144 * 1024) + (16 + ((jt >> 1) & 1)) * 1024 + (2 * (jt & 1) + (g >> 1)) * 256 + r * 16 + (g & 1) * 8, o);
    }
  }
  STAMP_MARK(2, 5);
  const int sb = jt >> 2, nq = jt & 3;
  unsigned* cntA = (unsigned*)(ws + WS_CNT + 8192) + (pn * 8 + sb) * 32;
  unsigned* cntB = (unsigned*)(ws + WS_CNT + 16384) + (pn * 4 + nq) * 32;
  wait_bar<10>();
  auto dptr = [&](auto HH) -> lds_ptr {
    constexpr int H = HH, o = d_off(d_i(H)) + d_f(H) * 32;
    return wbs[o / (3 * K4_SLOT)] + o % (3 * K4_SLOT);
  };
  for_seq<5>([&](auto I) { Bl[I] = lds_tr(dptr(I)); Bh[I] = lds_tr(dptr(I) + 2304); });
  SB();
  f32x4 dacc[2][9];
  bf16x8 DA[14][2];
  char* pb = ws + WS_PART + (size_t)(((pn * 4 + nq) * 8 + wid) * 8 + sb) * 9216 + lane * 16;
  for_seq<18>([&](auto HH) { for_seq<9>([&](auto HL) {
    constexpr int H = HH * 9 + HL, i = d_i(H), f = d_f(H), rb = H % 6, nb = (H + 5) % 6, NH = H + 5;
    if constexpr (H == 36) {
      if (tid == 0) poll_count(cntA, 4u);
      SB();
      wait_bar<0>();
      STAMP_MARK(2, 6);
      issue_d(std::integral_constant<int, 17>{}, 0); issue_d(std::integral_constant<int, 17>{}, 1);
      const unsigned ho = lane * 16, ho2 = lane * 16 + 144 * 1024;
      for_seq<14>([&](auto JJ) {
        constexpr int j = JJ;
        const int k = j < 12 ? ((4 * nq + 4 + j) & 15) : 4 + j;
        ld2_sc1(DA[j][0], DA[j][1], ho, ho2, hb + k * 1024);
      });
      SB();
    }
    if constexpr (i >= 4 && d_first(H)) {
      wait_vm2<2 * (17 - i)>(DA[i - 4][0], DA[i - 4][1]);
      SB();
    }
    if constexpr (H == D_G0) { raw_bar(); SB(); }
    const bf16x8 b = cat8(Bl[rb], Bh[rb]);
    bf16x8 a0, a1;
    if constexpr (i < 4) { a0 = __builtin_bit_cast(bf16x8, own[i][0]); a1 = __builtin_bit_cast(bf16x8, own[i][1]); }
    else { a0 = DA[i - 4][0]; a1 = DA[i - 4][1]; }
    if constexpr (i == 0) mfma16_zero(dacc[0][f], b, a0); else mfma16_acc(dacc[0][f], b, a0);
    SB();
    if constexpr (NH < 162) { Bl[nb] = lds_tr(dptr(std::integral_constant<int, NH>{})); SB(); }
    if constexpr (i == 0) mfma16_zero(dacc[1][f], b, a1); else mfma16_acc(dacc[1][f], b, a1);
    SB();
    if constexpr (NH < 162) { Bh[nb] = lds_tr(dptr(std::integral_constant<int, NH>{}) + 2304); SB(); }
    if constexpr (H < 22) { issue_d(std::integral_constant<int, 6 + H / 2>{}, H & 1); SB(); }
    if constexpr (H == 24) {
      wait_vm<11>();
      if (lds_arrive(lds + K4_SY, lane) == 7 && lane == 0) __hip_atomic_fetch_add(cntA, 1u, __ATOMIC_RELAXED, __HIP_MEMORY_SCOPE_AGENT);
      SB();
    }
    if constexpr (H >= D_G0 + 2 * DT && (((H - D_G0) % (2 * DT) == 2 || (H - D_G0) % (2 * DT) == 3))) {
      constexpr int pr = (H - D_G0) / (2 * DT) - 1, m = ((H - D_G0) % (2 * DT) == 3);
      const f32x4 x0 = dacc[m][2 * pr], x1 = dacc[m][2 * pr + 1];
      st16(pb + (m * 4 + pr) * 1024, u32x4{cvt_pk(x0[0], x0[1]), cvt_pk(x0[2], x0[3]), cvt_pk(x1[0], x1[1]), cvt_pk(x1[2], x1[3])});
      SB();
    }
  }); });
  {
    asm volatile("s_nop 7\n\ts_nop 1" : "+v"(dacc[0][8]), "+v"(dacc[1][8]));
    const f32x4 x0 = dacc[0][8], x1 = dacc[1][8];
    st16(pb + 8192, u32x4{cvt_pk(x0[0], x0[1]), cvt_pk(x0[2], x0[3]), cvt_pk(x1[0], x1[1]), cvt_pk(x1[2], x1[3])});
  }
  wait_bar<0>();
  STAMP_MARK(2, 7);
  if (tid == 0) __hip_atomic_fetch_add(cntB, 1u, __ATOMIC_RELAXED, __HIP_MEMORY_SCOPE_AGENT);
  {
    const int nc = wid == 1 ? 2 : 1;
    const int rbase = pn * 256 + sb * 32;
    int row[2][2], col[2][2];
    row[0][0] = row[0][1] = rbase + (wid >> 2) * 16 + r; col[0][0] = nq * 144 + (wid & 3) * 32 + 4 * g; col[0][1] = col[0][0] + 16;
    row[1][0] = rbase + r; row[1][1] = rbase + 16 + r; col[1][0] = col[1][1] = nq * 144 + 128 + 4 * g;
    const __amdgpu_buffer_rsrc_t hrs = __builtin_amdgcn_make_buffer_rsrc((void*)(ws + WS_H), 0, (int)(WS_XM - WS_H), 0x00020000);
    u32x2 res[2][2];
    float hm[2][2];
#pragma unroll
    for (int c = 0; c < 2; ++c)
#pragma unroll
      for (int e = 0; e < 2; ++e)
      {
        res[c][e] = __builtin_amdgcn_raw_buffer_load_b64(hrs, (row[c][e] >> 4) * 18432 + (col[c][e] >> 3) * 256 + (row[c][e] & 15) * 16 + ((col[c][e] >> 2) & 1) * 8, 0, 16);
        hm[c][e] = __uint_as_float(__builtin_amdgcn_raw_buffer_load_b32(hrs, (int)(WS_HM - WS_H) + row[c][e] * 8, 0, 16));
      }
    if (tid == 0) poll_count(cntB, 8u);
    SB();
    raw_bar();
    SB();
    const __amdgpu_buffer_rsrc_t prs = __builtin_amdgcn_make_buffer_rsrc((void*)(ws + WS_PART + (size_t)((pn * 4 + nq) * 8 + sb) * 8 * 9216), 0, 8 * 9216, 0x00020000);
#pragma unroll
    for (int c = 0; c < 2; ++c) {
      if (c < nc) {
        u32x4 pv[8];
#pragma unroll
        for (int k = 0; k < 8; ++k) pv[k] = __builtin_amdgcn_raw_buffer_load_b128(prs, lane * 16 + (c ? 8192 : wid * 1024), k * 9216, 16);
        float sum[8];
#pragma unroll
        for (int e = 0; e < 4; ++e) { sum[2 * e] = bf_lo(res[c][e >> 1][e & 1]) + hm[c][e >> 1]; sum[2 * e + 1] = bf_hi(res[c][e >> 1][e & 1]) + hm[c][e >> 1]; }
        float acc2[8];
#pragma unroll
        for (int e = 0; e < 8; ++e) acc2[e] = 0.f;
#pragma unroll
        for (int k = 0; k < 8; ++k)
#pragma unroll
          for (int e = 0; e < 4; ++e) { acc2[2 * e] += bf_lo(pv[k][e]); acc2[2 * e + 1] += bf_hi(pv[k][e]); }
        unsigned pk[4];
#pragma unroll
        for (int e = 0; e < 4; ++e) pk[e] = cvt_pk(sum[2 * e] + acc2[2 * e], sum[2 * e + 1] + acc2[2 * e + 1]);
        auto x0 = __builtin_amdgcn_permlane16_swap(pk[0], pk[2], false, false);
        auto x1 = __builtin_amdgcn_permlane16_swap(pk[1], pk[3], false, false);
        const int sel = g & 1;
        st16(out + (size_t)(row[c][0] + (c ? 16 * sel : 0)) * 1152 + (col[c][0] + (c ? -4 : 12) * sel) * 2, (u32x4{x0[0], x1[0], x0[1], x1[1]}));
      }
    }
  }
  STAMP_END(2);
  STAMP_END(1);
}

extern "C" void kernel_launch(void* const* d_in, const int* in_sizes, int n_in, void* d_out, int out_size, void* d_ws, size_t ws_size,
                              hipStream_t stream) {
  const char* x = (const char*)d_in[0];
  const char* wqkv = (const char*)d_in[1];
  const char* wo = (const char*)d_in[2];
  const char* wg = (const char*)d_in[4];
  const char* wu = (const char*)d_in[5];
  const char* wd = (const char*)d_in[6];
  char* ws = (char*)d_ws + WS_BASE(ws_size);
  (void)hipFuncSetAttribute((const void*)k1_ln_qkv, hipFuncAttributeMaxDynamicSharedMemorySize, K1_LDS);
  (void)hipFuncSetAttribute((const void*)k4_gateup, hipFuncAttributeMaxDynamicSharedMemorySize, K4_LDS);
  k1_ln_qkv<<<253, 1024, K1_LDS, stream>>>(x, wqkv, ws, wo);
  k4_gateup<<<256, 512, K4_LDS, stream>>>(wg, wu, ws, wd, (char*)d_out, wo, x);
}
```

```cpp
#include <hip/hip_runtime.h>
#include <hip/hip_bf16.h>
#include <stdint.h>
#include <utility>
#include <type_traits>

typedef __attribute__((ext_vector_type(8))) short bf16x8;
typedef __attribute__((ext_vector_type(4))) short s16x4;
typedef __attribute__((ext_vector_type(4))) float f32x4;
typedef __attribute__((ext_vector_type(16))) float f32x16;
typedef __attribute__((ext_vector_type(4))) unsigned u32x4;
typedef __attribute__((ext_vector_type(2))) unsigned u32x2;
typedef __attribute__((ext_vector_type(2))) __bf16 bf16x2_t;
typedef __attribute__((ext_vector_type(2))) float f32x2;
#define LDS_AS __attribute__((address_space(3)))
typedef LDS_AS char* lds_ptr;
#define DI __device__ __forceinline__

constexpr int SEQ = 2048;
constexpr float LN_EPS = 1e-5f;
constexpr float QSCALE = 0.23570226039551584f * 1.4426950408889634f;

constexpr size_t WS_Q = 0;
constexpr size_t WS_K = WS_Q + (size_t)72 * 32768;
constexpr size_t WS_V = WS_K + (size_t)72 * 32768;
constexpr size_t WS_ONES = WS_V + (size_t)256 * 9216;
constexpr size_t WS_O = WS_ONES + 1024;
constexpr size_t WS_OX = WS_O + (size_t)128 * 18 * 1024;
constexpr size_t WS_H = WS_OX + (size_t)32 * 8192;
constexpr size_t WS_ST = WS_H + (size_t)128 * 18 * 1024;
constexpr size_t WS_HM = WS_ST + (size_t)16 * 16384;
constexpr size_t WS_XM = WS_HM + 16384;
constexpr size_t WS_WB = WS_XM + 8192;
constexpr size_t WS_HID = WS_WB + 9216;
constexpr size_t WS_CNT = WS_HID + (size_t)128 * 144 * 1024;
constexpr size_t WS_PART = WS_CNT + 24576;
constexpr size_t WS_END = WS_PART + (size_t)2048 * 9216;
static_assert(WS_O % 1024 == 0 && WS_H % 1024 == 0 && WS_ST % 256 == 0 && WS_HID % 256 == 0, "align");

#define WS_BASE(sz) (((sz) - WS_END) & ~(size_t)0x1fffff)
#define STAMP_BEGIN
#define STAMP_MARK(k, i)
#define STAMP_MARKW(k, i, T)
#define STAMP_ENDW(k, T)
#define STAMP_CYC0
#define STAMP_CYC(k, i)
#define STAMP_END(k)

DI unsigned cvt_pk(float lo, float hi) {
  f32x2 v = {lo, hi};
  bf16x2_t r = __builtin_convertvector(v, bf16x2_t);
  return __builtin_bit_cast(unsigned, r);
}
DI float bf_lo(unsigned u) { return __builtin_bit_cast(float, u << 16); }
DI float bf_hi(unsigned u) { return __builtin_bit_cast(float, u & 0xffff0000u); }
DI float dot2(unsigned a, unsigned b, float c) {
  return __builtin_amdgcn_fdot2_f32_bf16(__builtin_bit_cast(bf16x2_t, a), __builtin_bit_cast(bf16x2_t, b), c, false);
}
template <int CTRL> DI float dpp_add(float v) {
  int t = __builtin_amdgcn_update_dpp(0, __builtin_bit_cast(int, v), CTRL, 0xf, 0xf, true);
  return v + __builtin_bit_cast(float, t);
}
DI float sum8(float v) {
  v = dpp_add<0xB1>(v);
  v = dpp_add<0x4E>(v);
  v = dpp_add<0x141>(v);
  return v;
}
DI float sum_g(float v) {
  auto a = __builtin_amdgcn_permlane16_swap(__float_as_uint(v), __float_as_uint(v), false, false);
  v = __uint_as_float(a[0]) + __uint_as_float(a[1]);
  auto b = __builtin_amdgcn_permlane32_swap(__float_as_uint(v), __float_as_uint(v), false, false);
  return __uint_as_float(b[0]) + __uint_as_float(b[1]);
}
DI void glds16(const void* sbase, unsigned voff, unsigned lds_dst) {
  asm volatile("s_mov_b32 m0, %2\n\ts_nop 3\n\tglobal_load_lds_dwordx4 %0, %1" ::"v"(voff), "s"(sbase), "s"(lds_dst) : "memory");
}
DI void st16(void* p, u32x4 v) { asm volatile("global_store_dwordx4 %0, %1, off sc1\n\ts_nop 1" ::"v"(p), "v"(v) : "memory"); }
DI void st8wt(void* p, u32x2 v) { asm volatile("global_store_dwordx2 %0, %1, off sc1" ::"v"(p), "v"(v) : "memory"); }
DI void st4(void* p, unsigned v) { asm volatile("global_store_dword %0, %1, off sc1" ::"v"(p), "v"(v) : "memory"); }
DI void glds16_if0(int c, const void* sbase, unsigned voff, unsigned lds_dst) {
  asm volatile("s_cmp_lg_u32 %3, 0\n\ts_cbranch_scc1 1f\n\ts_mov_b32 m0, %2\n\ts_nop 1\n\tglobal_load_lds_dwordx4 %0, %1\n1:"
               ::"v"(voff), "s"(sbase), "s"(lds_dst), "s"(c) : "memory", "scc");
}
DI void touch_if0(int c, const void* sbase, unsigned voff, unsigned lds_dst) {
  asm volatile("s_cmp_lg_u32 %3, 0\n\ts_cbranch_scc1 1f\n\ts_mov_b32 m0, %2\n\ts_nop 1\n\tglobal_load_lds_dword %0, %1\n1:"
               ::"v"(voff), "s"(sbase), "s"(lds_dst), "s"(c) : "memory", "scc");
}
DI void touch_lines(const char* base, unsigned voff, unsigned lds_dummy) {
  asm volatile("s_mov_b32 m0, %2\n\ts_nop 3\n\tglobal_load_lds_dword %0, %1" ::"v"(voff), "s"(base), "s"(lds_dummy) : "memory");
}
#define CODE_TOUCH(SYM, BYTES, LDS_DUMMY)                                                                          \
  do {                                                                                                             \
    const char* cp_;                                                                                               \
    asm volatile("s_getpc_b64 s[20:21]\n\ts_add_u32 s20, s20, " SYM "@rel32@lo+4\n\ts_addc_u32 s21, s21, " SYM     \
                 "@rel32@hi+12\n\ts_mov_b64 %0, s[20:21]" : "=s"(cp_)::"s20", "s21", "scc");                      \
    constexpr int nl_ = (BYTES) / 128, per_ = (nl_ + 28) / 29;                                                      \
    const int ln_ = (blockIdx.x >> 3) * per_ + lane;                                                               \
    if (lane < per_ && ln_ < nl_) touch_lines(cp_, ln_ * 128, LDS_DUMMY);                                          \
  } while (0)
#define SYM_K1 "_Z9k1_ln_qkvPKcS0_PcS0_"
#define SYM_K4 "_Z9k4_gateupPKcS0_PcS0_S1_S0_S0_"
constexpr int LEN_K1 = 20000, LEN_K4 = 38000;
template <int N> DI void wait_bar() { asm volatile("s_waitcnt vmcnt(%0) lgkmcnt(0)\n\ts_barrier" ::"n"(N) : "memory"); }
template <int N> DI void wait_vm() { asm volatile("s_waitcnt vmcnt(%0)" ::"n"(N) : "memory"); }
DI unsigned lds_arrive(lds_ptr c, int lane) {
  unsigned old = 0;
  if (lane == 0) old = __hip_atomic_fetch_add((LDS_AS unsigned*)c, 1u, __ATOMIC_RELAXED, __HIP_MEMORY_SCOPE_WORKGROUP);
  return __builtin_amdgcn_readfirstlane(old);
}
template <class T> DI void pin(T& v) { asm volatile("" : "+v"(v)); }
DI void raw_bar() { asm volatile("s_barrier" ::: "memory"); }
DI void lds_bar() { asm volatile("s_waitcnt lgkmcnt(0)\n\ts_barrier" ::: "memory"); }
DI bf16x8 lds_r128(lds_ptr p) { return *(LDS_AS const bf16x8*)p; }
typedef short v4i16_t __attribute__((ext_vector_type(4)));
DI s16x4 lds_tr(lds_ptr p) { return __builtin_amdgcn_ds_read_tr16_b64_v4i16((LDS_AS v4i16_t*)p); }
DI bf16x8 cat8(s16x4 a, s16x4 b) { return __builtin_shufflevector(a, b, 0, 1, 2, 3, 4, 5, 6, 7); }
#define SB() __builtin_amdgcn_sched_barrier(0)
#define MFMA16(a, b, c) __builtin_amdgcn_mfma_f32_16x16x32_bf16(a, b, c, 0, 0, 0)
#define MFMA32(a, b, c) __builtin_amdgcn_mfma_f32_32x32x16_bf16(a, b, c, 0, 0, 0)
DI void mfma16_acc(f32x4& c, bf16x8 a, bf16x8 b) { asm volatile("v_mfma_f32_16x16x32_bf16 %0, %1, %2, %0" : "+v"(c) : "v"(a), "v"(b)); }
DI void mfma16_zero(f32x4& c, bf16x8 a, bf16x8 b) { asm volatile("v_mfma_f32_16x16x32_bf16 %0, %1, %2, 0" : "=&v"(c) : "v"(a), "v"(b)); }
template <class F, int... I> DI void for_seq_impl(F&& f, std::integer_sequence<int, I...>) { (f(std::integral_constant<int, I>{}), ...); }
template <int N, class F> DI void for_seq(F&& f) { for_seq_impl(f, std::make_integer_sequence<int, N>{}); }
DI int xcd_remap(int b, int nwg) {
  int q = nwg >> 3, r = nwg & 7, x = b & 7, o = b >> 3;
  return (x < r ? x * (q + 1) : r * (q + 1) + (x - r) * q) + o;
}

constexpr int K1_X = 0, K1_W = 40960, K1_SLOT = 12288, K1_ST = K1_W + K1_SLOT * 9, K1_CS = K1_W, K1_PF = K1_ST + 1024, K1_LDS = K1_PF + 1024;
constexpr int cmin(int a, int b) { return a < b ? a : b; }
constexpr int k1_nw(int kk) { return cmin(18, cmin(3 + 2 * kk, kk + 7)); }
constexpr int k1_nx(int kk) { return cmin(18, cmin(3 + 2 * kk, kk + 5)); }

template <bool ISV, bool HI>
DI void k1_consumer(lds_ptr lds, int cb, int lane, char* ws, int ct, int m0) {
  const int g = lane >> 4, q = (lane & 15) >> 2, p = lane & 3, r = lane & 15;
  constexpr int NM = HI ? 2 : 3, MB = HI ? 3 : 0;
  lds_ptr a0 = lds + K1_X + MB * 2048 + (r >> 3) * 1024 + (r & 7) * 128 + ((g ^ (r >> 1)) * 16);
  lds_ptr a1 = lds + K1_X + MB * 2048 + (r >> 3) * 1024 + (r & 7) * 128 + (((4 + g) ^ (r >> 1)) * 16);
  const int rho0 = 16 * (g >> 1) + 4 * (g & 1) + q;
  lds_ptr wb = lds + K1_W + rho0 * 128 + ((cb ^ ((rho0 >> 1) & 3)) * 32) + p * 8;
  lds_ptr wb5 = wb + 5 * K1_SLOT;
  asm volatile("" : "+v"(wb), "+v"(wb5));
  const u32x4 onesu = {0x3f803f80u, 0x3f803f80u, 0x3f803f80u, 0x3f803f80u};
  const bf16x8 ones = __builtin_bit_cast(bf16x8, onesu);
  f32x4 acc[3][3];
#pragma unroll
  for (int m = 0; m < 3; ++m)
#pragma unroll
    for (int j = 0; j < 3; ++j) acc[m][j] = f32x4{0.f, 0.f, 0.f, 0.f};
  bf16x8 fA[2][3];
  s16x4 wl[2][3], wh[2][3];
  auto rd1 = [&](auto KK, auto II) {
    constexpr int kk = KK, i = II, bf = kk & 1, sl = kk % 9;
    if constexpr (i >= 2 && i <= 4) {
      if constexpr (i - 2 < NM) fA[bf][i - 2] = lds_r128(((kk & 1) ? a1 : a0) + ((kk >> 1) & 3) * 10240 + (i - 2) * 2048);
    } else {
      constexpr int j = i < 2 ? 0 : (i - 3) / 2, hh = i < 2 ? i : (i - 3) % 2;
      lds_ptr bp = (sl >= 5 ? wb5 + (sl - 5) * K1_SLOT : wb + sl * K1_SLOT) + j * 4096 + hh * 1024;
      if constexpr (hh == 0) wl[bf][j] = lds_tr(bp); else wh[bf][j] = lds_tr(bp);
    }
  };
  lds_bar();
  STAMP_MARKW(0, 2, 512);
  for_seq<9>([&](auto I) { rd1(std::integral_constant<int, 0>{}, I); });
  SB();
  for_seq<18>([&](auto KK) {
    constexpr int kk = KK, bf = kk & 1;
    if constexpr (kk < 17) { raw_bar(); SB(); }
    for_seq<9>([&](auto II) {
      constexpr int i = II, j = i / 3, m = i % 3;
      const bf16x8 wv = cat8(wl[bf][j], wh[bf][j]);
      const bf16x8 av = (m < NM) ? fA[bf][m] : ones;
      acc[m][j] = ISV ? MFMA16(av, wv, acc[m][j]) : MFMA16(wv, av, acc[m][j]);
      SB();
      if constexpr (kk < 17) { rd1(std::integral_constant<int, kk + 1>{}, II); SB(); }
    });
  });
  STAMP_MARKW(0, 4, 512);
  lds_ptr cx = lds + K1_CS + cb * 3072 + lane * 16;
  if (HI) {
#pragma unroll
    for (int j = 0; j < 3; ++j) *(LDS_AS f32x4*)(cx + j * 1024) = acc[2][j];
  }
  lds_bar();
  STAMP_MARKW(0, 5, 512);
  f32x4 cs[3];
#pragma unroll
  for (int j = 0; j < 3; ++j) cs[j] = HI ? acc[2][j] : *(LDS_AS f32x4*)(cx + j * 1024);
  u32x2 o[NM * 3];
  if (ISV) {
#pragma unroll
    for (int m = 0; m < NM; ++m) {
      const f32x4 s0 = *(LDS_AS f32x4*)(lds + K1_ST + ((MB + m) * 16 + 4 * g) * 8), s1 = *(LDS_AS f32x4*)(lds + K1_ST + ((MB + m) * 16 + 4 * g) * 8 + 16);
#pragma unroll
      for (int j = 0; j < 3; ++j) {
        const float c0 = cs[j][0];
        o[3 * m + j] = u32x2{cvt_pk(fmaf(s0[0], c0, acc[m][j][0]) * s0[1], fmaf(s0[2], c0, acc[m][j][1]) * s0[3]),
                             cvt_pk(fmaf(s1[0], c0, acc[m][j][2]) * s1[1], fmaf(s1[2], c0, acc[m][j][3]) * s1[3])};
      }
    }
  } else {
    const float sc = ct < 3 ? QSCALE : 1.f;
#pragma unroll
    for (int m = 0; m < NM; ++m) {
      f32x2 st = *(LDS_AS f32x2*)(lds + K1_ST + ((MB + m) * 16 + r) * 8);
      st[1] *= sc;
#pragma unroll
      for (int j = 0; j < 3; ++j)
        o[3 * m + j] = u32x2{cvt_pk(fmaf(st[0], cs[j][0], acc[m][j][0]) * st[1], fmaf(st[0], cs[j][1], acc[m][j][1]) * st[1]),
                             cvt_pk(fmaf(st[0], cs[j][2], acc[m][j][2]) * st[1], fmaf(st[0], cs[j][3], acc[m][j][3]) * st[1])};
    }
  }
  char* base;
  int sm, sj, sel, half;
  if (ISV) { base = ws + WS_V + (size_t)(((m0 >> 4) + MB) * 2 + (g & 1)) * 9216 + ((ct - 6) * 192 + cb * 16 + r) * 16; sm = 18432; sj = 1024; sel = g >> 1; half = g >> 1; }
  else { base = ws + WS_Q + (size_t)(ct * 24 + cb * 2 + (g >> 1)) * 32768 + (m0 + MB * 16 + r) * 16; sm = 256; sj = 8 * 32768; sel = g & 1; half = g & 1; }
#pragma unroll
  for (int n = 0; n + 1 < NM * 3; n += 2) {
    u32x4 c;
#pragma unroll
    for (int e = 0; e < 2; ++e) {
      auto rr = ISV ? __builtin_amdgcn_permlane32_swap(o[n][e], o[n + 1][e], false, false) : __builtin_amdgcn_permlane16_swap(o[n][e], o[n + 1][e], false, false);
      c[e] = rr[0]; c[2 + e] = rr[1];
    }
    const int o0 = (n / 3) * sm + (n % 3) * sj, o1 = ((n + 1) / 3) * sm + ((n + 1) % 3) * sj;
    st16(base + (sel ? o1 : o0), c);
  }
  if (NM == 3) st8wt(base + 2 * sm + 2 * sj + half * 8, o[8]);
}

__global__ __launch_bounds__(1024) void k1_ln_qkv(const char* __restrict__ x, const char* __restrict__ wqkv, char* __restrict__ ws, const char* __restrict__ wo) {
  extern __shared__ __attribute__((aligned(16))) char smem[];
  STAMP_BEGIN
  lds_ptr lds = (lds_ptr)smem;
  const unsigned lds0 = (unsigned)(uintptr_t)lds;
  const int tid = threadIdx.x, lane = tid & 63, wid = __builtin_amdgcn_readfirstlane(tid >> 6);
  if (blockIdx.x >= 234) {
    const int sb = blockIdx.x - 234;
    if (sb == 18) {
      if (tid < 192) *(unsigned*)(ws + WS_CNT + tid * 128) = 0u;
      if (tid < 4) *(unsigned*)(ws + WS_ONES + tid * 4) = 0x3f803f80u;
    } else if (wid < 4) {
      const int wrow = (sb * 4 + wid) * 8 + (lane >> 3);
      float t = 0.f;
      u32x4 wv[9];
#pragma unroll
      for (int i = 0; i < 9; ++i) wv[i] = *(const u32x4*)(wo + (size_t)wrow * 1152 + (lane & 7) * 16 + i * 128);
#pragma unroll
      for (int i = 0; i < 9; ++i)
#pragma unroll
        for (int e = 0; e < 4; ++e) t = dot2(wv[i][e], 0x3f803f80u, t);
      t = sum8(t) * (1.f / 576.f);
      const unsigned h = cvt_pk(t, 0.f) & 0xffffu, l = cvt_pk(t - bf_lo(h), 0.f) & 0xffffu;
      if ((lane & 7) == 0) *(u32x4*)(ws + WS_WB + wrow * 16) = u32x4{h | (l << 16), 0u, 0u, 0u};
    }
    return;
  }
  const int tile = xcd_remap(blockIdx.x, 234);
  const int cgp = tile / 78, rem = tile - cgp * 78;
  const int mt = rem / 3, ct = cgp * 3 + (rem - mt * 3);
  const int m0 = min(mt * 80, SEQ - 80);
  const int sm = wid - 3;
  lds_ptr sa0 = lds + K1_X + sm * 2048 + ((lane >> 3) & 1) * 1024 + (lane & 7) * 128 + (((lane >> 4) ^ ((lane & 15) >> 1)) * 16);
  lds_ptr sa1 = lds + K1_X + sm * 2048 + ((lane >> 3) & 1) * 1024 + (lane & 7) * 128 + (((4 + (lane >> 4)) ^ ((lane & 15) >> 1)) * 16);
  f32x4 ssum = {0.f, 0.f, 0.f, 0.f}, sgram = {0.f, 0.f, 0.f, 0.f};
  bf16x8 sf0, sf1;
  auto stat_rd = [&](int st) { sf0 = lds_r128(sa0 + (st & 3) * 10240); sf1 = lds_r128(sa1 + (st & 3) * 10240); };
  auto stat_mm = [&]() {
    const bf16x8 ones = __builtin_bit_cast(bf16x8, u32x4{0x3f803f80u, 0x3f803f80u, 0x3f803f80u, 0x3f803f80u});
    ssum = MFMA16(ones, sf0, ssum); sgram = MFMA16(sf0, sf0, sgram);
    ssum = MFMA16(ones, sf1, ssum); sgram = MFMA16(sf1, sf1, sgram);
    pin(ssum); pin(sgram);
  };
  auto stat_fin = [&]() {
    const float q01 = (lane & 1) ? sgram[1] : sgram[0], q23 = (lane & 1) ? sgram[3] : sgram[2];
    const float mean = ssum[0] * (1.f / 576.f);
    const float var = fmaxf(((lane & 2) ? q23 : q01) * (1.f / 576.f) - mean * mean, 0.f);
    if ((lane >> 4) == ((lane & 15) >> 2)) {
      *(LDS_AS f32x2*)(lds + K1_ST + (sm * 16 + (lane & 15)) * 8) = f32x2{-mean, __builtin_amdgcn_rsqf(var + LN_EPS)};
      if (ct == 0) *(float*)(ws + WS_XM + (m0 + sm * 16 + (lane & 15)) * 4) = mean;
    }
    lds_bar();
  };
  if (wid < 3) {
    unsigned wv[4];
#pragma unroll
    for (int i = 0; i < 4; ++i) {
      const int rho = 8 * i + (lane >> 3);
      const int gk = ((rho >> 4) & 1) * 2 + ((rho >> 2) & 1), tk = (rho >> 3) & 1, qk = rho & 3;
      wv[i] = (8 * gk + 4 * tk + qk) * 3456 + (((lane & 7) ^ (((rho >> 1) & 3) << 1)) * 16);
    }
    const char* wsrc = wqkv + ct * 384 + wid * 128;
    const unsigned wdst = lds0 + K1_W + wid * 4096;
    for_seq<18>([&](auto KK) {
      constexpr int kk = KK;
#pragma unroll
      for (int n = (kk == 0 ? 0 : k1_nw(kk - 1)); n < k1_nw(kk); ++n)
#pragma unroll
        for (int i = 0; i < 4; ++i) glds16(wsrc + (size_t)n * 32 * 3456, wv[i], wdst + (n % 9) * K1_SLOT + i * 1024);
      wait_bar<(k1_nw(kk) - kk - 1) * 4>();
    });
    return;
  }
  if (wid < 8) {
    const unsigned v0 = (lane >> 3) * 1152 + (((lane & 7) ^ (lane >> 4)) * 16);
    const unsigned v1 = 9216 + (lane >> 3) * 1152 + (((lane & 7) ^ (4 + (lane >> 4))) * 16);
    const char* xs = x + (size_t)(m0 + sm * 16) * 1152;
    for_seq<18>([&](auto KK) {
      constexpr int kk = KK;
#pragma unroll
      for (int h = (kk == 0 ? 0 : k1_nx(kk - 1)); h < k1_nx(kk); ++h)
        glds16(xs + (h >> 1) * 128, (h & 1) ? v1 : v0, lds0 + K1_X + ((h >> 1) & 3) * 10240 + (sm * 2 + (h & 1)) * 1024);
      if constexpr (kk & 1) stat_rd(kk >> 1);
      else if constexpr (kk > 0) stat_mm();
      SB();
      wait_bar<k1_nx(kk) - 2 * (kk >> 1) - 2>();
    });
    stat_mm();
    stat_fin();
    return;
  }
  if (wid == 8) CODE_TOUCH(SYM_K1, LEN_K1, lds0 + K1_PF);
  if (wid == 9) CODE_TOUCH(SYM_K4, LEN_K4, lds0 + K1_PF + 256);
  if (wid >= 12) {
    const int ln = blockIdx.x * 148 + (wid - 12) * 37 + lane;
    if (lane < 37) {
      if (ln < 15552) touch_lines(wqkv, ln * 128, lds0 + K1_PF + 512);
      else if (ln < 33984) touch_lines(x, (ln - 15552) * 128, lds0 + K1_PF + 512);
    }
  }
  if (ct >= 6) {
    if (wid < 12) k1_consumer<true, false>(lds, wid - 8, lane, ws, ct, m0);
    else k1_consumer<true, true>(lds, wid - 12, lane, ws, ct, m0);
  } else {
    if (wid < 12) k1_consumer<false, false>(lds, wid - 8, lane, ws, ct, m0);
    else k1_consumer<false, true>(lds, wid - 12, lane, ws, ct, m0);
  }
  STAMP_ENDW(0, 512);
}

constexpr int K2_KM = 0, K2_V = 24576, K2_PF = 45056, K2_W = 65536, K2_SG = 98304;

#define MFMA32K8(a, b, c) __builtin_amdgcn_mfma_f32_32x32x8bf16_1k(a, b, c, 0, 0, 0)
template <class F>
DI void k2_core(lds_ptr lds, char* ws, int hd, int t, int wid, int lane, u32x4 q0u, u32x2 q1u, bool first, F&& hook) {
  const int hi = lane >> 5, l31 = lane & 31;
  const int dlo = ((18 * hd) & 7) >> 1;
#pragma unroll
  for (int e = 0; e < 4; ++e) q0u[e] = (4 * hi + e >= dlo) ? q0u[e] : 0u;
#pragma unroll
  for (int e = 0; e < 2; ++e) q1u[e] = (8 + 2 * hi + e < dlo + 9) ? q1u[e] : 0u;
  const bf16x8 q0 = __builtin_bit_cast(bf16x8, q0u);
  const s16x4 q1 = __builtin_bit_cast(s16x4, q1u);
  f32x16 S[9];
  lds_ptr kmp = lds + K2_KM + hi * 8192 + (wid * 32 + l31) * 16;
  lds_ptr kxp = lds + K2_KM + 16384 + (wid * 32 + l31) * 16 + hi * 8;
  const float NEG = -1e30f;
  const int dq = l31 - 4 * hi;
  float mx0 = NEG, mx1 = NEG;
  bf16x8 kf[3];
  s16x4 kx[3];
  auto rdk = [&](auto NN) {
    constexpr int n = NN, i = n == 0 ? 8 : n - 1;
    kf[n % 3] = lds_r128(kmp + i * 512);
    kx[n % 3] = *(LDS_AS const s16x4*)(kxp + i * 512);
  };
  auto post = [&](auto NN) {
    constexpr int n = NN, i = n == 0 ? 8 : n - 1;
    if constexpr (i < 8) {
      if constexpr (i == 0) {
#pragma unroll
        for (int r = 0; r < 16; ++r) S[0][r] = (((r & 3) + 8 * (r >> 2)) <= dq) ? S[8][r] : S[0][r];
      }
#pragma unroll
      for (int r = 0; r < 16; r += 4) {
        mx0 = fmaxf(fmaxf(mx0, S[i][r]), S[i][r + 1]);
        mx1 = fmaxf(fmaxf(mx1, S[i][r + 2]), S[i][r + 3]);
      }
    }
  };
  rdk(std::integral_constant<int, 0>{});
  rdk(std::integral_constant<int, 1>{});
  SB();
  for_seq<9>([&](auto NN) {
    constexpr int n = NN, i = n == 0 ? 8 : n - 1;
    const f32x16 z = {};
    S[i] = MFMA32(kf[n % 3], q0, z);
    SB();
    if constexpr (n + 2 < 9) { rdk(std::integral_constant<int, n + 2>{}); SB(); }
    S[i] = MFMA32K8(kx[n % 3], q1, S[i]);
    SB();
    if constexpr (n >= 1) { post(std::integral_constant<int, n - 1>{}); SB(); }
    hook(std::integral_constant<int, n>{}); SB();
  });
  post(std::integral_constant<int, 8>{});
  float mx = fmaxf(mx0, mx1);
  {
    auto rr = __builtin_amdgcn_permlane32_swap(__float_as_uint(mx), __float_as_uint(mx), false, false);
    mx = fmaxf(__uint_as_float(rr[0]), __uint_as_float(rr[1]));
  }
  s16x4 mq;
  {
    const float nm = -mx;
    const unsigned h1 = __float_as_uint(nm) & 0xffff0000u;
    const float r1 = nm - __uint_as_float(h1);
    const unsigned h2 = __float_as_uint(r1) & 0xffff0000u;
    const float r2 = r1 - __uint_as_float(h2);
    const u32x2 mu = {hi ? 0u : ((h1 >> 16) | h2), hi ? 0u : ((__float_as_uint(r2) >> 16) | 0xf1490000u)};
    mq = __builtin_bit_cast(s16x4, mu);
  }
  auto mk1 = [&](int i) { return __builtin_bit_cast(s16x4, u32x2{0x3f803f80u, (first && i > 0 && wid + i < 8) ? 0x3f803f80u : 0x00003f80u}); };
  S[1] = MFMA32K8(mk1(1), mq, S[1]);
  S[2] = MFMA32K8(mk1(2), mq, S[2]);
  SB();
  lds_ptr vp = lds + K2_V + wid * 2 * 592 + (l31 < 18 ? hi * 288 + l31 * 16 : 576);
  f32x16 O;
  const unsigned keep0 = first ? 0u : ~0u;
  bf16x8 vf[3];
  auto rdv = [&](auto EE) {
    constexpr int e = EE, blk = e < 14 ? e + 2 : ((e - 14) >> 1) + ((e & 1) ? 16 : 0);
    if constexpr (e < 18) vf[e % 3] = lds_r128(vp + blk * 592);
  };
  rdv(std::integral_constant<int, 0>{});
  rdv(std::integral_constant<int, 1>{});
  SB();
  for_seq<16>([&](auto HH) {
    constexpr int h = HH, i = ((h >> 1) + 1) & 7, s = h & 1;
    u32x4 P;
#pragma unroll
    for (int j = 0; j < 2; ++j) P[j] = cvt_pk(__builtin_amdgcn_exp2f(S[i][8 * s + 2 * j]), __builtin_amdgcn_exp2f(S[i][8 * s + 2 * j + 1]));
    if constexpr (i != 0 && s == 0 && i + 2 <= 8) {
      unsigned p0 = P[0], p1 = P[1];
      pin(p0); pin(p1);
      P[0] = p0; P[1] = p1;
      SB();
      S[(i + 2) & 7] = MFMA32K8(mk1((i + 2) & 7), mq, S[(i + 2) & 7]);
      SB();
    }
#pragma unroll
    for (int j = 2; j < 4; ++j) P[j] = cvt_pk(__builtin_amdgcn_exp2f(S[i][8 * s + 2 * j]), __builtin_amdgcn_exp2f(S[i][8 * s + 2 * j + 1]));
    if constexpr (i == 0) {
      constexpr int e = 14 + 2 * s;
      u32x4 P8, P0;
#pragma unroll
      for (int j = 0; j < 4; ++j) {
        const int r0 = 8 * s + 2 * j, r1 = r0 + 1;
        const unsigned mk = ((((r0 & 3) + 8 * (r0 >> 2)) <= dq) ? 0xffffu : 0u) | ((((r1 & 3) + 8 * (r1 >> 2)) <= dq) ? 0xffff0000u : 0u);
        P8[j] = P[j] & mk;
        P0[j] = P[j] & ~mk & keep0;
      }
      rdv(std::integral_constant<int, e + 2>{});
      SB();
      O = MFMA32(vf[e % 3], __builtin_bit_cast(bf16x8, P0), O);
      SB();
      rdv(std::integral_constant<int, e + 3>{});
      O = MFMA32(vf[(e + 1) % 3], __builtin_bit_cast(bf16x8, P8), O);
      SB();
    } else {
      rdv(std::integral_constant<int, h + 2>{});
      if constexpr (h < 9) hook(std::integral_constant<int, 9 + h>{});
      SB();
      if constexpr (h == 0) { const f32x16 z = {}; O = MFMA32(vf[0], __builtin_bit_cast(bf16x8, P), z); }
      else O = MFMA32(vf[h % 3], __builtin_bit_cast(bf16x8, P), O);
      SB();
    }
  });
  const float inv = __builtin_amdgcn_rcpf(O[10]);
  const int row = t * 32 + l31;
  char* ob = ws + WS_O + (size_t)(row >> 4) * 18 * 1024 + (row & 15) * 16;
  u32x4 o = {cvt_pk(O[0] * inv, O[1] * inv), cvt_pk(O[2] * inv, O[3] * inv), cvt_pk(O[4] * inv, O[5] * inv), cvt_pk(O[6] * inv, O[7] * inv)};
  st16(ob + (hd >> 1) * 1024 + (2 * (hd & 1) + hi) * 256, o);
  if (hi == 0) st4(ws + WS_OX + hd * 8192 + row * 4, cvt_pk(O[8] * inv, O[9] * inv));
}

DI void glds16v_if0(int c, const void* vaddr, unsigned lds_dst) {
  asm volatile("s_cmp_lg_u32 %2, 0\n\ts_cbranch_scc1 1f\n\ts_mov_b32 m0, %1\n\ts_nop 1\n\tglobal_load_lds_dwordx4 %0, off\n1:"
               ::"v"(vaddr), "s"(lds_dst), "s"(c) : "memory", "scc");
}
DI void poll_count(const unsigned* c, unsigned n) {
  for (int spin = 0; spin < (1 << 18); ++spin) {
    if (__hip_atomic_load(c, __ATOMIC_RELAXED, __HIP_MEMORY_SCOPE_AGENT) >= n) break;
    __builtin_amdgcn_s_sleep(1);
  }
}

template <class F>
DI void front_phase(lds_ptr lds, unsigned lds0, char* __restrict__ ws, const char* __restrict__ wo, const char* __restrict__ x,
                    const char* __restrict__ wg, const char* __restrict__ wu, F&& prefill) {
  const int tid = threadIdx.x, lane = tid & 63, wid = __builtin_amdgcn_readfirstlane(tid >> 6);
  const int hd = (blockIdx.x & 7) * 4 + ((blockIdx.x >> 3) & 3), qb = blockIdx.x >> 5;
  const int p3 = blockIdx.x & 7, m0 = p3 * 256 + ((blockIdx.x >> 3) & 3) * 64, cb = blockIdx.x >> 5;
  const int t = qb * 8 + wid;
  u32x4 q0;
  u32x2 q1;
  const char *wa, *wx;
  unsigned wst;
  {
    const int c0 = (18 * hd) >> 3;
    asm volatile("global_load_dwordx4 %0, %2, %4\n\tglobal_load_dwordx2 %1, %3, %4"
                 : "=&v"(q0), "=&v"(q1)
                 : "v"((lane >> 5) * 32768 + (lane & 31) * 16), "v"(65536 + (lane & 31) * 16 + (lane >> 5) * 8),
                   "s"(ws + WS_Q + (size_t)c0 * 32768 + t * 512)
                 : "memory");
    const int key0 = max(qb * 256 - 256 + wid * 64, 0);
#pragma unroll
    for (int i = 0; i < 3; ++i) glds16(ws + WS_K + (size_t)(c0 + i) * 32768 + key0 * 16, (qb == 0 && wid < 4) ? 0 : lane * 16, lds0 + K2_KM + i * 8192 + wid * 1024);
#pragma unroll
    for (int i = 0; i < 3; ++i) {
      const int pc = wid + 8 * i;
      if (pc < 19) {
        const int u = min(pc * 64 + lane, 32 * 37 - 1), bl = u / 37, w = u - bl * 37;
        const int blk = max(qb * 16 - 16 + bl, 0);
        const unsigned vo = w == 36 ? (unsigned)(WS_ONES - WS_V) : (blk * 2 + (w >= 18)) * 9216 + (hd * 18 + (w >= 18 ? w - 18 : w)) * 16;
        glds16(ws + WS_V, vo, lds0 + K2_V + pc * 1024);
      }
    }
    const int u = min(wid, 4) * 64 + lane, rho = u / 10, c = u - rho * 10;
    const int g_ = ((rho >> 4) & 1) * 2 + ((rho >> 2) & 1), t_ = (rho >> 3) & 1, q_ = rho & 3;
    const char* wbase = c < 9 ? wo + cb * 144 + c * 16 : ws + WS_WB;
    wst = c < 9 ? 1152 : 16;
    wa = wbase + (18 * (g_ >> 1) + 8 * t_ + 4 * (g_ & 1) + q_) * wst;
    wx = wbase + (18 * (4 * g_ + 2 * t_ + (q_ >> 1)) + 16 + (q_ & 1)) * wst;
    wst *= 36;
    pin(wa); pin(wx); pin(wst);
  }
  asm volatile("s_waitcnt vmcnt(0)\n\ts_barrier" : "+v"(q0), "+v"(q1)::"memory");
  STAMP_MARK(1, 2);
  k2_core(lds, ws, hd, t, wid, lane, q0, q1, qb == 0, [&](auto I) {
    if constexpr (I < 18) {
      glds16v_if0(wid / 5, I < 16 ? wa : wx, lds0 + K2_W + (I * 5 + wid) * 1024);
      if constexpr (I < 15) wa += wst;
      if constexpr (I == 16) wx += 8 * wst;
    }
    if constexpr (I == 17)
      touch_lines((wid & 1) ? wu : wg, ((blockIdx.x * 4 + (wid >> 1)) * 27 + min(lane, 26)) * 128, lds0 + K2_PF + wid * 256);
  });
  STAMP_MARK(1, 3);
  wait_bar<0>();
  unsigned* cnt = (unsigned*)(ws + WS_CNT);
  if (tid == 0) __hip_atomic_fetch_add(cnt + qb * 32, 1u, __ATOMIC_RELAXED, __HIP_MEMORY_SCOPE_AGENT);
  const int mi = wid >> 1, kg = wid & 1;
  const int g = lane >> 4, q = (lane & 15) >> 2, p = lane & 3, er = mi * 16 + (lane & 15);
  const bool ex = kg == 0 && g == 0;
  const u32x4 res = *(const u32x4*)(x + (size_t)(m0 + er) * 1152 + cb * 144 + (kg * 4 + g) * 16);
  const u32x4 res8 = *(const u32x4*)(x + (size_t)(m0 + er) * 1152 + cb * 144 + 128);
  const float xm = *(const float*)(ws + WS_XM + (m0 + er) * 4);
  lds_ptr wb = lds + K2_W + kg * 5120 + (16 * (g >> 1) + 4 * (g & 1) + q) * 160 + p * 8, wb2 = wb + 5 * 10240;
  pin(wb); pin(wb2);
  bf16x8 wf[9][5];
#pragma unroll
  for (int j = 0; j < 9; ++j)
#pragma unroll
    for (int n = 0; n < 5; ++n) {
      lds_ptr bp = (j < 5 ? wb + j * 10240 : wb2 + (j - 5) * 10240) + n * 32;
      wf[j][n] = cat8(lds_tr(bp), lds_tr(bp + 1280));
    }
  if (tid == 448) poll_count(cnt + p3 * 32, 32u);
  SB();
  lds_bar();
  SB();
  STAMP_MARK(1, 5);
  const __amdgpu_buffer_rsrc_t ors = __builtin_amdgcn_make_buffer_rsrc((void*)(ws + WS_O), 0, 128 * 18432 + 32 * 8192, 0x00020000);
  const int aoff = ((m0 >> 4) + mi) * 18432 + kg * 1024 + lane * 16;
  bf16x8 a[9];
#pragma unroll
  for (int i = 0; i < 8; ++i) a[i] = __builtin_bit_cast(bf16x8, __builtin_amdgcn_raw_buffer_load_b128(ors, aoff + i * 2048, 0, 16));
  {
    u32x4 ax;
    const int xo = 128 * 18432 + (kg * 16 + 4 * g) * 8192 + (m0 + er) * 4;
#pragma unroll
    for (int e = 0; e < 4; ++e) ax[e] = __builtin_amdgcn_raw_buffer_load_b32(ors, xo, e * 8192, 16);
    a[8] = __builtin_bit_cast(bf16x8, ax);
  }
  SB();
  prefill();
  SB();
  f32x4 acc[5];
#pragma unroll
  for (int n = 0; n < 5; ++n) acc[n] = f32x4{0.f, 0.f, 0.f, 0.f};
#pragma unroll
  for (int j = 0; j < 9; ++j)
#pragma unroll
    for (int n = 0; n < 5; ++n) acc[n] = MFMA16(wf[j][n], a[j], acc[n]);
  STAMP_MARK(1, 6);
  {
    lds_ptr cw = lds + K2_SG + (kg * 64 + er) * 336 + 16 * g;
#pragma unroll
    for (int n = 0; n < 5; ++n) *(LDS_AS f32x4*)(cw + n * 64) = acc[n];
  }
  lds_bar();
  {
    lds_ptr cr = lds + K2_SG + er * 336;
    const f32x2 ma = *(LDS_AS f32x2*)(cr + 288), mb = *(LDS_AS f32x2*)(cr + 64 * 336 + 288);
    const float mean = xm + ((ma[0] + ma[1]) + (mb[0] + mb[1]));
    const int row = m0 + er;
    u32x4 o[2];
#pragma unroll
    for (int k = 0; k < 2; ++k) {
      const int c = k ? 8 : kg * 4 + g;
      const u32x4 rs = k ? res8 : res;
      f32x4 a0 = *(LDS_AS f32x4*)(cr + c * 32), a1 = *(LDS_AS f32x4*)(cr + c * 32 + 16);
      a0 += *(LDS_AS f32x4*)(cr + 64 * 336 + c * 32) - mean; a1 += *(LDS_AS f32x4*)(cr + 64 * 336 + c * 32 + 16) - mean;
      o[k] = u32x4{cvt_pk(a0[0] + bf_lo(rs[0]), a0[1] + bf_hi(rs[0])), cvt_pk(a0[2] + bf_lo(rs[1]), a0[3] + bf_hi(rs[1])),
                   cvt_pk(a1[0] + bf_lo(rs[2]), a1[1] + bf_hi(rs[2])), cvt_pk(a1[2] + bf_lo(rs[3]), a1[3] + bf_hi(rs[3]))};
    }
    char* hp = ws + WS_H + (size_t)(row >> 4) * 18432 + cb * 2304 + (row & 15) * 16;
    st16(hp + (kg * 4 + g) * 256, o[0]);
    if (ex) st16(hp + 2048, o[1]);
    float s1 = 0.f, s2 = 0.f, t1 = 0.f, t2 = 0.f;
#pragma unroll
    for (int j = 0; j < 4; ++j) { s1 = dot2(o[0][j], 0x3f803f80u, s1); s2 = dot2(o[0][j], o[0][j], s2); t1 = dot2(o[1][j], 0x3f803f80u, t1); t2 = dot2(o[1][j], o[1][j], t2); }
    s1 += ex ? t1 : 0.f; s2 += ex ? t2 : 0.f;
    s1 = sum_g(s1); s2 = sum_g(s2);
    if (g == 0 || (g == 1 && kg == 0 && cb == 0)) st8wt(ws + WS_ST + (size_t)(g ? 16 : 2 * cb + kg) * 16384 + row * 8, u32x2{__float_as_uint(g ? mean : s1), __float_as_uint(s2)});
  }
}

constexpr int K4_SLOT = 18432, K4_PF = K4_SLOT * 8, K4_SY = 160000, K4_LDS = 160 * 1024;

constexpr int k4_frag(int i) { return i < 16 ? ((i >> 2) * 2 + (i & 1) + ((i & 2) ? 9 : 0)) : (i == 16 ? 8 : 17); }
constexpr int k4_foff(int f) { return (f >= 9 ? 9216 + (f - 9) * 32 : f * 32); }
constexpr int K4_T0 = 13 * 18;
constexpr int k4_slab(int G) { return G < K4_T0 ? G / 18 : (G - K4_T0 < 80 ? 13 + ((G - K4_T0) % 20) / 4 : 13 + (G - K4_T0 - 80) / 2); }
constexpr int k4_pos(int G) { return G < K4_T0 ? G % 18 : (G - K4_T0 < 80 ? ((G - K4_T0) / 20) * 4 + ((G - K4_T0) & 3) : 16 + ((G - K4_T0) & 1)); }
constexpr int k4_op(int G) {
  const int s = G / 18, i = G % 18;
  if (s > 12) return 0;
  if (s < 3) {
    const int pos[10] = {0, 2, 4, 6, 7, 9, 11, 13, 15, 16};
    for (int k = 0; k < 10; ++k) if (pos[k] == i) return 1 + (2 * s + 2 + k / 5) * 8 + k % 5;
    return 0;
  }
  if (s == 3) {
    const int pos[8] = {0, 2, 4, 6, 8, 11, 14, 16};
    for (int k = 0; k < 8; ++k) if (pos[k] == i) return k < 5 ? 1 + 8 * 8 + k : 1 + 9 * 8 + (k - 3);
    return 0;
  }
  if (i == 1) return 1 + (s + 5) * 8;
  if (i == 4) return 1 + (s + 5) * 8 + 1;
  if (s + 6 > 17) return 0;
  if (i == 8) return 1 + (s + 6) * 8 + 2;
  if (i == 13) return 1 + (s + 6) * 8 + 3;
  if (i == 16) return 1 + (s + 6) * 8 + 4;
  return 0;
}
constexpr int k4_allow(int G, int need) {
  int n = (need == 0) ? 2 : 0;
  for (int g = 0; g < G; ++g) {
    const int op = k4_op(g);
    if (op == 0) continue;
    const int sl = (op - 1) >> 3, kind = (op - 1) & 7;
    if (sl <= need) n = 0;
    else if (kind != 4) ++n;
  }
  return n;
}
constexpr int k4_need(int G) {
  if (G + 5 >= 324 || G == 0) return 0;
  int mx = 0;
  for (int g = 0; g < G + 5; ++g) mx = k4_slab(g) > mx ? k4_slab(g) : mx;
  return k4_slab(G + 5) > mx ? k4_slab(G + 5) : 0;
}

constexpr int d_off(int i) { return i < 6 ? 36864 + i * 9216 : (i < 10 ? (i - 6) * 9216 : (i < 17 ? 92160 + (i - 10) * 9216 : 36864)); }
constexpr int DT = 8;
constexpr int D_G0 = (18 - DT) * 9;
constexpr int d_i(int H) { return H < D_G0 ? H / 9 : (H - D_G0 < 8 * DT ? 18 - DT + ((H - D_G0) % (2 * DT)) / 2 : 18 - DT + (H - D_G0 - 8 * DT)); }
constexpr int d_f(int H) { return H < D_G0 ? H % 9 : (H - D_G0 < 8 * DT ? ((H - D_G0) / (2 * DT)) * 2 + ((H - D_G0) & 1) : 8); }
constexpr int d_first(int H) {
  for (int h = 0; h < H; ++h) if (d_i(h) == d_i(H)) return 0;
  return 1;
}
DI void st_part(int c, void* p, lds_ptr l, u32x4 v) {
  asm volatile("s_cmp_lg_u32 %3, 0\n\ts_cbranch_scc1 1f\n\tds_write_b128 %1, %2\n\ts_branch 2f\n1:\n\tglobal_store_dwordx4 %0, %2, off sc1\n2:\n\ts_nop 1"
               ::"v"(p), "v"(l), "v"(v), "s"(c) : "memory", "scc");
}
DI void ld2_sc1(bf16x8& d0, bf16x8& d1, unsigned o0, unsigned o1, const char* base) {
  asm volatile("global_load_dwordx4 %0, %2, %4 sc1\n\tglobal_load_dwordx4 %1, %3, %4 sc1" : "=&v"(d0), "=&v"(d1) : "v"(o0), "v"(o1), "s"(base) : "memory");
}
template <int N> DI void wait_vm2(bf16x8& d0, bf16x8& d1) { asm volatile("s_waitcnt vmcnt(%2)" : "+v"(d0), "+v"(d1) : "n"(N) : "memory"); }
DI void ld8(f32x2& d, unsigned o, const char* base) { asm volatile("global_load_dwordx2 %0, %1, %2 sc1" : "=v"(d) : "v"(o), "s"(base) : "memory"); }

__global__ __launch_bounds__(512, 2) void k4_gateup(const char* __restrict__ wgate, const char* __restrict__ wup, char* __restrict__ ws,
                                                    const char* __restrict__ wd, char* __restrict__ out, const char* __restrict__ wo, const char* __restrict__ x) {
  extern __shared__ __attribute__((aligned(16))) char smem[];
  STAMP_BEGIN
  lds_ptr lds = (lds_ptr)smem;
  const unsigned lds0 = (unsigned)(uintptr_t)lds;
  *(LDS_AS unsigned*)(lds + K4_SY + (threadIdx.x & 1) * 4) = 0u;
  const int wid = __builtin_amdgcn_readfirstlane(threadIdx.x >> 6);
  const int xcd = blockIdx.x & 7, idx = blockIdx.x >> 3;
  const int pn = 4 * (xcd >> 2) + (idx & 3), jt = 8 * (xcd & 3) + (idx >> 2);
  const int wm = wid & 3, wn = wid >> 2;
  const char* wsrc = (wn ? wup : wgate) + jt * 288;
  unsigned wvo[3];
  const unsigned wdst = lds0 + wn * 9216;
  auto issue_w = [&](int s, int i) {
    const unsigned so = (s & 7) * K4_SLOT;
    const char* wp = wsrc + (size_t)s * 32 * 9216;
    if (i < 2) glds16(wp, wvo[i], wdst + so + wm * 2048 + i * 1024);
    else glds16_if0(wm, wp, wvo[2], wdst + so + 8192);
  };
  front_phase(lds, lds0, ws, wo, x, wgate, wup, [&]() {
    int ln = threadIdx.x;
    pin(ln);
    ln &= 63;
#pragma unroll
    for (int i = 0; i < 3; ++i) {
      const int pc = i < 2 ? 2 * wm + i : 8;
      const int o = pc * 1024 + ln * 16;
      const int rho = o / 288, u = (o - rho * 288) >> 4;
      const int gk = ((rho >> 4) & 1) * 2 + ((rho >> 2) & 1), tk = (rho >> 3) & 1, qk = rho & 3;
      wvo[i] = (8 * gk + 4 * tk + qk) * 9216 + u * 16;
    }
    for_seq<2>([&](auto S) { issue_w(S, 0); issue_w(S, 1); issue_w(S, 2); });
  });
  int tid = threadIdx.x;
  pin(tid);
  const int lane = tid & 63;
  const char* asrc = ws + WS_H + (size_t)(pn * 16 + 2 * wid) * 18432;
  const unsigned avo = lane * 16, avo2 = lane * 16 + 18432;
  bf16x8 A[6][2];
  auto issue_a = [&](int s, bf16x8& dst, int m) {
    asm volatile("global_load_dwordx4 %0, %1, %2 sc1" : "=v"(dst) : "v"(m ? avo2 : avo), "s"(asrc + s * 1024) : "memory");
  };
  wait_vm<0>();
  unsigned* cnt3 = (unsigned*)(ws + WS_CNT + 20480);
  if (lds_arrive(lds + K4_SY + 4, lane) == 7 && lane == 0) __hip_atomic_fetch_add(cnt3 + (blockIdx.x & 7) * 32, 1u, __ATOMIC_RELAXED, __HIP_MEMORY_SCOPE_AGENT);
  if (tid == 0) poll_count(cnt3 + pn * 32, 32u);
  SB();
  raw_bar();
  STAMP_MARK(1, 7);
  for_seq<2>([&](auto S) { issue_a(S, A[S][0], 0); issue_a(S, A[S][1], 1); });
  f32x2 sp[8];
  const int g = lane >> 4, q = (lane & 15) >> 2, p = lane & 3, r = lane & 15;
  lds_ptr wb = lds + (16 * (g >> 1) + 4 * (g & 1) + q) * 288 + p * 8;
  lds_ptr wbs[3] = {wb, wb + 3 * K4_SLOT, wb + 6 * K4_SLOT};
  asm volatile("" : "+v"(wbs[1]), "+v"(wbs[2]));
  f32x4 acc[2][18];
  asm volatile("s_waitcnt vmcnt(%2)\n\ts_barrier" : "+v"(A[0][0]), "+v"(A[0][1]) : "n"(k4_allow(0, 0)) : "memory");
  STAMP_MARK(2, 2);
  STAMP_CYC0
  s16x4 Bl[6], Bh[6];
  for_seq<5>([&](auto I) { Bl[I] = lds_tr(wb + k4_foff(k4_frag(I))); Bh[I] = lds_tr(wb + k4_foff(k4_frag(I)) + 2304); });
  SB();
  char* hb = ws + WS_HID + (size_t)(pn * 16 + wid * 2) * (144 * 1024) + (size_t)(jt >> 2) * 18432;
  float c1[2];
  float sd[2];
  auto glu = [&](float ag, float au, int m) -> float {
    return au * __builtin_amdgcn_rcpf(fmaf(__builtin_amdgcn_exp2f(ag * c1[m]), sd[m], sd[m]));
  };
  u32x4 own[4][2];
  float eg[2][2];
  auto micro = [&](auto QQ, auto KK, auto JJ) {
    constexpr int Q = QQ, k = KK, j = JJ, m = k >> 2, d = k & 3, h = d >> 1, e = (d & 1) * 2, b = k & 1;
#pragma unroll
    for (int x = 0; x < 2; ++x) {
      if constexpr (j == 0) eg[b][x] = acc[m][2 * Q + h][e + x] * c1[m];
      if constexpr (j == 1) eg[b][x] = __builtin_amdgcn_exp2f(eg[b][x]);
      if constexpr (j == 2) eg[b][x] = fmaf(eg[b][x], sd[m], sd[m]);
      if constexpr (j == 3) eg[b][x] = __builtin_amdgcn_rcpf(eg[b][x]);
      if constexpr (j == 4) eg[b][x] *= acc[m][9 + 2 * Q + h][e + x];
      if constexpr (j < 5) pin(eg[b][x]);
    }
    if constexpr (j == 6) {
      own[Q][m][d] = cvt_pk(eg[b][0], eg[b][1]);
      if constexpr (d == 3) st16(hb + (size_t)m * (144 * 1024) + (4 * (jt & 3) + Q) * 1024 + lane * 16, own[Q][m]);
      else pin(own[Q][m]);
    }
  };
  auto gapops = [&](auto QQ, auto GG) {
    constexpr int gg = GG;
    if constexpr (gg >= 4 && gg < 36) micro(QQ, std::integral_constant<int, gg / 4 - 1>{}, std::integral_constant<int, 4 + gg % 4>{});
    if constexpr (gg >= 0 && gg < 32) micro(QQ, std::integral_constant<int, gg / 4>{}, std::integral_constant<int, gg % 4>{});
    SB();
  };
  unsigned dvo[2], dvl[2];
  const char* dsrc = wd + (size_t)(jt >> 2) * 576 * 1152 + (jt & 3) * 288;
  auto dvoff = [&]() {
    int ln = tid;
    asm volatile("" : "+v"(ln));
    ln &= 63;
#pragma unroll
    for (int i = 0; i < 2; ++i) {
      const int o = (i ? 8 : wid) * 1024 + ln * 16;
      const int rho = o / 288, u = (o - rho * 288) >> 4;
      const int gk = ((rho >> 4) & 1) * 2 + ((rho >> 2) & 1), tk = (rho >> 3) & 1, qk = rho & 3;
      dvo[i] = (16 * tk + 4 * gk + qk) * 1152 + u * 16;
      dvl[i] = (144 * (gk >> 1) + 128 + 8 * (gk & 1) + 4 * tk + qk) * 1152 + u * 16;
    }
  };
  auto issue_d = [&](auto II, int pc) {
    constexpr int i = II;
    const int k = (4 * (jt & 3) + i) & 15;
    const char* src = dsrc + (size_t)(i < 16 ? 144 * (k >> 2) + 32 * (k & 3) : 288 * (i - 16)) * 1152;
    if (pc == 0) glds16(src, i < 16 ? dvo[0] : dvl[0], lds0 + d_off(i) + wid * 1024);
    else glds16_if0(wid ^ (i & 7), src, i < 16 ? dvo[1] : dvl[1], lds0 + d_off(i) + 8192);
  };
  for_seq<18>([&](auto GH) { for_seq<18>([&](auto GL) {
    constexpr int G = GH * 18 + GL, sl = k4_slab(G), f = k4_frag(k4_pos(G)), rb = G % 6, nb = (G + 5) % 6, NG = G + 5;
    const bf16x8 b = cat8(Bl[rb], Bh[rb]);
    constexpr int need = k4_need(G);
    if constexpr (need != 0) {
      asm volatile("s_waitcnt vmcnt(%2)\n\ts_barrier" : "+v"(A[need % 6][0]), "+v"(A[need % 6][1]) : "n"(k4_allow(G, need)) : "memory");
      SB();
    }
    if constexpr (sl == 0) mfma16_zero(acc[0][f], b, A[0][0]); else mfma16_acc(acc[0][f], b, A[sl % 6][0]);
    SB();
    if constexpr (NG < 324) { Bl[nb] = lds_tr(wbs[(k4_slab(NG) & 7) / 3] + ((k4_slab(NG) & 7) % 3) * K4_SLOT + k4_foff(k4_frag(k4_pos(NG)))); SB(); }
    if constexpr (G >= K4_T0 + 20 && G < K4_T0 + 80 && (G - K4_T0) % 20 >= 1 && (G - K4_T0) % 20 < 19)
      gapops(std::integral_constant<int, (G - K4_T0) / 20 - 1>{}, std::integral_constant<int, 2 * ((G - K4_T0) % 20 - 1)>{});
    if constexpr (G >= K4_T0 + 81) {
      gapops(std::integral_constant<int, 3>{}, std::integral_constant<int, 4 * (G - K4_T0 - 81)>{});
      gapops(std::integral_constant<int, 3>{}, std::integral_constant<int, 4 * (G - K4_T0 - 81) + 1>{});
    }
    if constexpr (sl == 0) mfma16_zero(acc[1][f], b, A[0][1]); else mfma16_acc(acc[1][f], b, A[sl % 6][1]);
    SB();
    if constexpr (NG < 324) { Bh[nb] = lds_tr(wbs[(k4_slab(NG) & 7) / 3] + ((k4_slab(NG) & 7) % 3) * K4_SLOT + k4_foff(k4_frag(k4_pos(NG))) + 2304); SB(); }
    constexpr int op = k4_op(G);
    if constexpr (op != 0) {
      constexpr int os = (op - 1) >> 3, kind = (op - 1) & 7;
      if constexpr (kind < 2) issue_a(os, A[os % 6][kind], kind);
      else issue_w(os, kind - 2);
      SB();
    }
    if constexpr (G % 18 == 10 && (G / 18 == 5 || G / 18 == 7 || G / 18 == 9)) {
      constexpr int k = (G / 18 - 5) / 2;
      touch_if0((int)(blockIdx.x / 216) + (wid ^ (k + 1)), wd + (size_t)(blockIdx.x * 3 + k) * 8192, avo * 8, lds0 + K4_PF + k * 256);
      SB();
    }
    if constexpr (G >= 222 && G < 230) { ld8(sp[G - 222], (lane & 31) * 8 + (lane >> 5) * 8 * 16384, ws + WS_ST + (size_t)(pn * 256 + wid * 32) * 8 + (G - 222) * 16384); SB(); }
    if constexpr (G == K4_T0 + 11) {
      for_seq<8>([&](auto I) { pin(sp[I]); });
      const f32x2 t4 = ((sp[0] + sp[1]) + (sp[2] + sp[3])) + ((sp[4] + sp[5]) + (sp[6] + sp[7]));
      auto r1 = __builtin_amdgcn_permlane32_swap(__float_as_uint(t4[0]), __float_as_uint(t4[0]), false, false);
      auto r2 = __builtin_amdgcn_permlane32_swap(__float_as_uint(t4[1]), __float_as_uint(t4[1]), false, false);
      const float mean = (__uint_as_float(r1[0]) + __uint_as_float(r1[1])) * (1.f / 576.f);
      const float var = fmaxf((__uint_as_float(r2[0]) + __uint_as_float(r2[1])) * (1.f / 576.f) - mean * mean, 0.f) + LN_EPS;
      auto rv = __builtin_amdgcn_permlane16_swap(__float_as_uint(var), __float_as_uint(var), false, false);
      const float v0 = __uint_as_float(rv[0]), v1 = __uint_as_float(rv[1]);
      c1[0] = -1.4426950408889634f * __builtin_amdgcn_rsqf(v0); sd[0] = __builtin_amdgcn_sqrtf(v0);
      c1[1] = -1.4426950408889634f * __builtin_amdgcn_rsqf(v1); sd[1] = __builtin_amdgcn_sqrtf(v1);
      SB();
    }
    if constexpr (G >= K4_T0) {
      constexpr int t = G - K4_T0, u = t % 20;
      if constexpr (t == 11) { dvoff(); SB(); }
      if constexpr (t >= 12 && t < 24) { issue_d(std::integral_constant<int, (t - 12) / 2>{}, t & 1); SB(); }
      if constexpr (t >= 20 && t < 80 && u >= 1 && u < 19) gapops(std::integral_constant<int, t / 20 - 1>{}, std::integral_constant<int, 2 * (u - 1) + 1>{});
      if constexpr (t >= 81) {
        gapops(std::integral_constant<int, 3>{}, std::integral_constant<int, 4 * (t - 81) + 2>{});
        gapops(std::integral_constant<int, 3>{}, std::integral_constant<int, 4 * (t - 81) + 3>{});
      }
    }
  }); });
  {
    asm volatile("s_nop 7\n\ts_nop 1" : "+v"(acc[0][8]), "+v"(acc[1][8]), "+v"(acc[0][17]), "+v"(acc[1][17]));
#pragma unroll
    for (int m = 0; m < 2; ++m) {
      const f32x4 ag = acc[m][8], au = acc[m][17];
      u32x2 o = {cvt_pk(glu(ag[0], au[0], m), glu(ag[1], au[1], m)), cvt_pk(glu(ag[2], au[2], m), glu(ag[3], au[3], m))};
      st8wt(hb + (size_t)m * (144 * 1024) + (16 + ((jt >> 1) & 1)) * 1024 + (2 * (jt & 1) + (g >> 1)) * 256 + r * 16 + (g & 1) * 8, o);
    }
  }
  STAMP_MARK(2, 5);
  const int sb = jt >> 2, nq = jt & 3;
  unsigned* cntA = (unsigned*)(ws + WS_CNT + 8192) + (pn * 8 + sb) * 32;
  unsigned* cntB = (unsigned*)(ws + WS_CNT + 16384) + (pn * 4 + nq) * 32;
  wait_bar<10>();
  auto dptr = [&](auto HH) -> lds_ptr {
    constexpr int H = HH, o = d_off(d_i(H)) + d_f(H) * 32;
    return wbs[o / (3 * K4_SLOT)] + o % (3 * K4_SLOT);
  };
  for_seq<5>([&](auto I) { Bl[I] = lds_tr(dptr(I)); Bh[I] = lds_tr(dptr(I) + 2304); });
  SB();
  f32x4 dacc[2][9];
  bf16x8 DA[14][2];
  char* pb = ws + WS_PART + (size_t)(((pn * 4 + nq) * 8 + wid) * 8 + sb) * 9216 + lane * 16;
  for_seq<18>([&](auto HH) { for_seq<9>([&](auto HL) {
    constexpr int H = HH * 9 + HL, i = d_i(H), f = d_f(H), rb = H % 6, nb = (H + 5) % 6, NH = H + 5;
    if constexpr (H == 36) {
      if (tid == 0) poll_count(cntA, 4u);
      SB();
      wait_bar<0>();
      STAMP_MARK(2, 6);
      issue_d(std::integral_constant<int, 17>{}, 0); issue_d(std::integral_constant<int, 17>{}, 1);
      const unsigned ho = lane * 16, ho2 = lane * 16 + 144 * 1024;
      for_seq<14>([&](auto JJ) {
        constexpr int j = JJ;
        const int k = j < 12 ? ((4 * nq + 4 + j) & 15) : 4 + j;
        ld2_sc1(DA[j][0], DA[j][1], ho, ho2, hb + k * 1024);
      });
      SB();
    }
    if constexpr (i >= 4 && d_first(H)) {
      wait_vm2<2 * (17 - i)>(DA[i - 4][0], DA[i - 4][1]);
      SB();
    }
    if constexpr (H == D_G0) { raw_bar(); SB(); }
    const bf16x8 b = cat8(Bl[rb], Bh[rb]);
    bf16x8 a0, a1;
    if constexpr (i < 4) { a0 = __builtin_bit_cast(bf16x8, own[i][0]); a1 = __builtin_bit_cast(bf16x8, own[i][1]); }
    else { a0 = DA[i - 4][0]; a1 = DA[i - 4][1]; }
    if constexpr (i == 0) mfma16_zero(dacc[0][f], b, a0); else mfma16_acc(dacc[0][f], b, a0);
    SB();
    if constexpr (NH < 162) { Bl[nb] = lds_tr(dptr(std::integral_constant<int, NH>{})); SB(); }
    if constexpr (i == 0) mfma16_zero(dacc[1][f], b, a1); else mfma16_acc(dacc[1][f], b, a1);
    SB();
    if constexpr (NH < 162) { Bh[nb] = lds_tr(dptr(std::integral_constant<int, NH>{}) + 2304); SB(); }
    if constexpr (H < 22) { issue_d(std::integral_constant<int, 6 + H / 2>{}, H & 1); SB(); }
    if constexpr (H == 24) {
      wait_vm<11>();
      if (lds_arrive(lds + K4_SY, lane) == 7 && lane == 0) __hip_atomic_fetch_add(cntA, 1u, __ATOMIC_RELAXED, __HIP_MEMORY_SCOPE_AGENT);
      SB();
    }
    if constexpr (H >= D_G0 + 2 * DT && (((H - D_G0) % (2 * DT) == 2 || (H - D_G0) % (2 * DT) == 3))) {
      constexpr int pr = (H - D_G0) / (2 * DT) - 1, m = ((H - D_G0) % (2 * DT) == 3);
      const f32x4 x0 = dacc[m][2 * pr], x1 = dacc[m][2 * pr + 1];
      st_part(wid ^ sb, pb + (m * 4 + pr) * 1024, lds + lane * 16 + (m * 4 + pr) * 1024, u32x4{cvt_pk(x0[0], x0[1]), cvt_pk(x0[2], x0[3]), cvt_pk(x1[0], x1[1]), cvt_pk(x1[2], x1[3])});
      SB();
    }
  }); });
  {
    asm volatile("s_nop 7\n\ts_nop 1" : "+v"(dacc[0][8]), "+v"(dacc[1][8]));
    const f32x4 x0 = dacc[0][8], x1 = dacc[1][8];
    st_part(wid ^ sb, pb + 8192, lds + lane * 16 + 8192, u32x4{cvt_pk(x0[0], x0[1]), cvt_pk(x0[2], x0[3]), cvt_pk(x1[0], x1[1]), cvt_pk(x1[2], x1[3])});
  }
  wait_bar<0>();
  STAMP_MARK(2, 7);
  if (tid == 0) __hip_atomic_fetch_add(cntB, 1u, __ATOMIC_RELAXED, __HIP_MEMORY_SCOPE_AGENT);
  {
    const int nc = wid == 1 ? 2 : 1;
    const int rbase = pn * 256 + sb * 32;
    int row[2][2], col[2][2];
    row[0][0] = row[0][1] = rbase + (wid >> 2) * 16 + r; col[0][0] = nq * 144 + (wid & 3) * 32 + 4 * g; col[0][1] = col[0][0] + 16;
    row[1][0] = rbase + r; row[1][1] = rbase + 16 + r; col[1][0] = col[1][1] = nq * 144 + 128 + 4 * g;
    const __amdgpu_buffer_rsrc_t hrs = __builtin_amdgcn_make_buffer_rsrc((void*)(ws + WS_H), 0, (int)(WS_XM - WS_H), 0x00020000);
    u32x2 res[2][2];
    float hm[2][2];
#pragma unroll
    for (int c = 0; c < 2; ++c)
#pragma unroll
      for (int e = 0; e < 2; ++e)
      {
        res[c][e] = __builtin_amdgcn_raw_buffer_load_b64(hrs, (row[c][e] >> 4) * 18432 + (col[c][e] >> 3) * 256 + (row[c][e] & 15) * 16 + ((col[c][e] >> 2) & 1) * 8, 0, 16);
        hm[c][e] = __uint_as_float(__builtin_amdgcn_raw_buffer_load_b32(hrs, (int)(WS_HM - WS_H) + row[c][e] * 8, 0, 16));
      }
    if (tid == 0) poll_count(cntB, 8u);
    SB();
    raw_bar();
    SB();
    const __amdgpu_buffer_rsrc_t prs = __builtin_amdgcn_make_buffer_rsrc((void*)(ws + WS_PART + (size_t)((pn * 4 + nq) * 8 + sb) * 8 * 9216), 0, 8 * 9216, 0x00020000);
#pragma unroll
    for (int c = 0; c < 2; ++c) {
      if (c < nc) {
        u32x4 pv[8];
#pragma unroll
        for (int k = 0; k < 7; ++k) pv[k] = __builtin_amdgcn_raw_buffer_load_b128(prs, lane * 16 + (c ? 8192 : wid * 1024), ((sb + 1 + k) & 7) * 9216, 16);
        pv[7] = *(LDS_AS u32x4*)(lds + lane * 16 + (c ? 8192 : wid * 1024));
        float sum[8];
#pragma unroll
        for (int e = 0; e < 4; ++e) { sum[2 * e] = bf_lo(res[c][e >> 1][e & 1]) + hm[c][e >> 1]; sum[2 * e + 1] = bf_hi(res[c][e >> 1][e & 1]) + hm[c][e >> 1]; }
        float acc2[8];
#pragma unroll
        for (int e = 0; e < 8; ++e) acc2[e] = 0.f;
#pragma unroll
        for (int k = 0; k < 8; ++k)
#pragma unroll
          for (int e = 0; e < 4; ++e) { acc2[2 * e] += bf_lo(pv[k][e]); acc2[2 * e + 1] += bf_hi(pv[k][e]); }
        unsigned pk[4];
#pragma unroll
        for (int e = 0; e < 4; ++e) pk[e] = cvt_pk(sum[2 * e] + acc2[2 * e], sum[2 * e + 1] + acc2[2 * e + 1]);
        auto x0 = __builtin_amdgcn_permlane16_swap(pk[0], pk[2], false, false);
        auto x1 = __builtin_amdgcn_permlane16_swap(pk[1], pk[3], false, false);
        const int sel = g & 1;
        st16(out + (size_t)(row[c][0] + (c ? 16 * sel : 0)) * 1152 + (col[c][0] + (c ? -4 : 12) * sel) * 2, (u32x4{x0[0], x1[0], x0[1], x1[1]}));
      }
    }
  }
  STAMP_END(2);
  STAMP_END(1);
}

extern "C" void kernel_launch(void* const* d_in, const int* in_sizes, int n_in, void* d_out, int out_size, void* d_ws, size_t ws_size,
                              hipStream_t stream) {
  const char* x = (const char*)d_in[0];
  const char* wqkv = (const char*)d_in[1];
  const char* wo = (const char*)d_in[2];
  const char* wg = (const char*)d_in[4];
  const char* wu = (const char*)d_in[5];
  const char* wd = (const char*)d_in[6];
  char* ws = (char*)d_ws + WS_BASE(ws_size);
  (void)hipFuncSetAttribute((const void*)k1_ln_qkv, hipFuncAttributeMaxDynamicSharedMemorySize, K1_LDS);
  (void)hipFuncSetAttribute((const void*)k4_gateup, hipFuncAttributeMaxDynamicSharedMemorySize, K4_LDS);
  k1_ln_qkv<<<253, 1024, K1_LDS, stream>>>(x, wqkv, ws, wo);
  k4_gateup<<<256, 512, K4_LDS, stream>>>(wg, wu, ws, wd, (char*)d_out, wo, x);
}
```

```cpp
#include <hip/hip_runtime.h>
#include <hip/hip_bf16.h>
#include <stdint.h>
#include <utility>
#include <type_traits>

typedef __attribute__((ext_vector_type(8))) short bf16x8;
typedef __attribute__((ext_vector_type(4))) short s16x4;
typedef __attribute__((ext_vector_type(4))) float f32x4;
typedef __attribute__((ext_vector_type(16))) float f32x16;
typedef __attribute__((ext_vector_type(4))) unsigned u32x4;
typedef __attribute__((ext_vector_type(2))) unsigned u32x2;
typedef __attribute__((ext_vector_type(2))) __bf16 bf16x2_t;
typedef __attribute__((ext_vector_type(2))) float f32x2;
#define LDS_AS __attribute__((address_space(3)))
typedef LDS_AS char* lds_ptr;
#define DI __device__ __forceinline__

constexpr int SEQ = 2048;
constexpr float LN_EPS = 1e-5f;
constexpr float QSCALE = 0.23570226039551584f * 1.4426950408889634f;

constexpr size_t WS_Q = 0;
constexpr size_t WS_K = WS_Q + (size_t)72 * 32768;
constexpr size_t WS_V = WS_K + (size_t)72 * 32768;
constexpr size_t WS_ONES = WS_V + (size_t)256 * 9216;
constexpr size_t WS_O = WS_ONES + 1024;
constexpr size_t WS_OX = WS_O + (size_t)128 * 18 * 1024;
constexpr size_t WS_H = WS_OX + (size_t)32 * 8192;
constexpr size_t WS_ST = WS_H + (size_t)128 * 18 * 1024;
constexpr size_t WS_HM = WS_ST + (size_t)16 * 16384;
constexpr size_t WS_XM = WS_HM + 16384;
constexpr size_t WS_WB = WS_XM + 8192;
constexpr size_t WS_HID = WS_WB + 9216;
constexpr size_t WS_CNT = WS_HID + (size_t)128 * 144 * 1024;
constexpr size_t WS_PART = WS_CNT + 24576;
constexpr size_t WS_END = WS_PART + (size_t)2048 * 9216;
static_assert(WS_O % 1024 == 0 && WS_H % 1024 == 0 && WS_ST % 256 == 0 && WS_HID % 256 == 0, "align");

#define WS_BASE(sz) (((sz) - WS_END) & ~(size_t)0x1fffff)
#define STAMP_BEGIN
#define STAMP_MARK(k, i)
#define STAMP_MARKW(k, i, T)
#define STAMP_ENDW(k, T)
#define STAMP_CYC0
#define STAMP_CYC(k, i)
#define STAMP_END(k)

DI unsigned cvt_pk(float lo, float hi) {
  f32x2 v = {lo, hi};
  bf16x2_t r = __builtin_convertvector(v, bf16x2_t);
  return __builtin_bit_cast(unsigned, r);
}
DI float bf_lo(unsigned u) { return __builtin_bit_cast(float, u << 16); }
DI float bf_hi(unsigned u) { return __builtin_bit_cast(float, u & 0xffff0000u); }
DI float dot2(unsigned a, unsigned b, float c) {
  return __builtin_amdgcn_fdot2_f32_bf16(__builtin_bit_cast(bf16x2_t, a), __builtin_bit_cast(bf16x2_t, b), c, false);
}
template <int CTRL> DI float dpp_add(float v) {
  int t = __builtin_amdgcn_update_dpp(0, __builtin_bit_cast(int, v), CTRL, 0xf, 0xf, true);
  return v + __builtin_bit_cast(float, t);
}
DI float sum8(float v) {
  v = dpp_add<0xB1>(v);
  v = dpp_add<0x4E>(v);
  v = dpp_add<0x141>(v);
  return v;
}
DI float sum_g(float v) {
  auto a = __builtin_amdgcn_permlane16_swap(__float_as_uint(v), __float_as_uint(v), false, false);
  v = __uint_as_float(a[0]) + __uint_as_float(a[1]);
  auto b = __builtin_amdgcn_permlane32_swap(__float_as_uint(v), __float_as_uint(v), false, false);
  return __uint_as_float(b[0]) + __uint_as_float(b[1]);
}
DI void glds16(const void* sbase, unsigned voff, unsigned lds_dst) {
  asm volatile("s_mov_b32 m0, %2\n\ts_nop 3\n\tglobal_load_lds_dwordx4 %0, %1" ::"v"(voff), "s"(sbase), "s"(lds_dst) : "memory");
}
DI void st16(void* p, u32x4 v) { asm volatile("global_store_dwordx4 %0, %1, off sc1\n\ts_nop 1" ::"v"(p), "v"(v) : "memory"); }
DI void st8wt(void* p, u32x2 v) { asm volatile("global_store_dwordx2 %0, %1, off sc1" ::"v"(p), "v"(v) : "memory"); }
DI void st4(void* p, unsigned v) { asm volatile("global_store_dword %0, %1, off sc1" ::"v"(p), "v"(v) : "memory"); }
DI void glds16_if0(int c, const void* sbase, unsigned voff, unsigned lds_dst) {
  asm volatile("s_cmp_lg_u32 %3, 0\n\ts_cbranch_scc1 1f\n\ts_mov_b32 m0, %2\n\ts_nop 1\n\tglobal_load_lds_dwordx4 %0, %1\n1:"
               ::"v"(voff), "s"(sbase), "s"(lds_dst), "s"(c) : "memory", "scc");
}
DI void touch_if0(int c, const void* sbase, unsigned voff, unsigned lds_dst) {
  asm volatile("s_cmp_lg_u32 %3, 0\n\ts_cbranch_scc1 1f\n\ts_mov_b32 m0, %2\n\ts_nop 1\n\tglobal_load_lds_dword %0, %1\n1:"
               ::"v"(voff), "s"(sbase), "s"(lds_dst), "s"(c) : "memory", "scc");
}
DI void touch_lines(const char* base, unsigned voff, unsigned lds_dummy) {
  asm volatile("s_mov_b32 m0, %2\n\ts_nop 3\n\tglobal_load_lds_dword %0, %1" ::"v"(voff), "s"(base), "s"(lds_dummy) : "memory");
}
#define CODE_TOUCH(SYM, BYTES, LDS_DUMMY)                                                                          \
  do {                                                                                                             \
    const char* cp_;                                                                                               \
    asm volatile("s_getpc_b64 s[20:21]\n\ts_add_u32 s20, s20, " SYM "@rel32@lo+4\n\ts_addc_u32 s21, s21, " SYM     \
                 "@rel32@hi+12\n\ts_mov_b64 %0, s[20:21]" : "=s"(cp_)::"s20", "s21", "scc");                      \
    constexpr int nl_ = (BYTES) / 128, per_ = (nl_ + 28) / 29;                                                      \
    const int ln_ = (blockIdx.x >> 3) * per_ + lane;                                                               \
    if (lane < per_ && ln_ < nl_) touch_lines(cp_, ln_ * 128, LDS_DUMMY);                                          \
  } while (0)
#define SYM_K1 "_Z9k1_ln_qkvPKcS0_PcS0_"
#define SYM_K4 "_Z9k4_gateupPKcS0_PcS0_S1_S0_S0_"
constexpr int LEN_K1 = 20000, LEN_K4 = 38000;
template <int N> DI void wait_bar() { asm volatile("s_waitcnt vmcnt(%0) lgkmcnt(0)\n\ts_barrier" ::"n"(N) : "memory"); }
template <int N> DI void wait_vm() { asm volatile("s_waitcnt vmcnt(%0)" ::"n"(N) : "memory"); }
DI unsigned lds_arrive(lds_ptr c, int lane) {
  unsigned old = 0;
  if (lane == 0) old = __hip_atomic_fetch_add((LDS_AS unsigned*)c, 1u, __ATOMIC_RELAXED, __HIP_MEMORY_SCOPE_WORKGROUP);
  return __builtin_amdgcn_readfirstlane(old);
}
template <class T> DI void pin(T& v) { asm volatile("" : "+v"(v)); }
DI void raw_bar() { asm volatile("s_barrier" ::: "memory"); }
DI void lds_bar() { asm volatile("s_waitcnt lgkmcnt(0)\n\ts_barrier" ::: "memory"); }
DI bf16x8 lds_r128(lds_ptr p) { return *(LDS_AS const bf16x8*)p; }
typedef short v4i16_t __attribute__((ext_vector_type(4)));
DI s16x4 lds_tr(lds_ptr p) { return __builtin_amdgcn_ds_read_tr16_b64_v4i16((LDS_AS v4i16_t*)p); }
DI bf16x8 cat8(s16x4 a, s16x4 b) { return __builtin_shufflevector(a, b, 0, 1, 2, 3, 4, 5, 6, 7); }
#define SB() __builtin_amdgcn_sched_barrier(0)
#define MFMA16(a, b, c) __builtin_amdgcn_mfma_f32_16x16x32_bf16(a, b, c, 0, 0, 0)
#define MFMA32(a, b, c) __builtin_amdgcn_mfma_f32_32x32x16_bf16(a, b, c, 0, 0, 0)
DI void mfma16_acc(f32x4& c, bf16x8 a, bf16x8 b) { asm volatile("v_mfma_f32_16x16x32_bf16 %0, %1, %2, %0" : "+v"(c) : "v"(a), "v"(b)); }
DI void mfma16_zero(f32x4& c, bf16x8 a, bf16x8 b) { asm volatile("v_mfma_f32_16x16x32_bf16 %0, %1, %2, 0" : "=&v"(c) : "v"(a), "v"(b)); }
template <class F, int... I> DI void for_seq_impl(F&& f, std::integer_sequence<int, I...>) { (f(std::integral_constant<int, I>{}), ...); }
template <int N, class F> DI void for_seq(F&& f) { for_seq_impl(f, std::make_integer_sequence<int, N>{}); }
DI int xcd_remap(int b, int nwg) {
  int q = nwg >> 3, r = nwg & 7, x = b & 7, o = b >> 3;
  return (x < r ? x * (q + 1) : r * (q + 1) + (x - r) * q) + o;
}

constexpr int K1_X = 0, K1_W = 40960, K1_SLOT = 12288, K1_ST = K1_W + K1_SLOT * 9, K1_CS = K1_W, K1_PF = K1_ST + 1024, K1_LDS = K1_PF + 1024;
constexpr int cmin(int a, int b) { return a < b ? a : b; }
constexpr int k1_nw(int kk) { return cmin(18, cmin(3 + 2 * kk, kk + 7)); }
constexpr int k1_nx(int kk) { return cmin(18, cmin(3 + 2 * kk, kk + 5)); }

template <bool ISV, bool HI>
DI void k1_consumer(lds_ptr lds, int cb, int lane, char* ws, int ct, int m0) {
  const int g = lane >> 4, q = (lane & 15) >> 2, p = lane & 3, r = lane & 15;
  constexpr int NM = HI ? 2 : 3, MB = HI ? 3 : 0;
  lds_ptr a0 = lds + K1_X + MB * 2048 + (r >> 3) * 1024 + (r & 7) * 128 + ((g ^ (r >> 1)) * 16);
  lds_ptr a1 = lds + K1_X + MB * 2048 + (r >> 3) * 1024 + (r & 7) * 128 + (((4 + g) ^ (r >> 1)) * 16);
  const int rho0 = 16 * (g >> 1) + 4 * (g & 1) + q;
  lds_ptr wb = lds + K1_W + rho0 * 128 + ((cb ^ ((rho0 >> 1) & 3)) * 32) + p * 8;
  lds_ptr wb5 = wb + 5 * K1_SLOT;
  asm volatile("" : "+v"(wb), "+v"(wb5));
  const u32x4 onesu = {0x3f803f80u, 0x3f803f80u, 0x3f803f80u, 0x3f803f80u};
  const bf16x8 ones = __builtin_bit_cast(bf16x8, onesu);
  f32x4 acc[3][3];
#pragma unroll
  for (int m = 0; m < 3; ++m)
#pragma unroll
    for (int j = 0; j < 3; ++j) acc[m][j] = f32x4{0.f, 0.f, 0.f, 0.f};
  bf16x8 fA[2][3];
  s16x4 wl[2][3], wh[2][3];
  auto rd1 = [&](auto KK, auto II) {
    constexpr int kk = KK, i = II, bf = kk & 1, sl = kk % 9;
    if constexpr (i >= 2 && i <= 4) {
      if constexpr (i - 2 < NM) fA[bf][i - 2] = lds_r128(((kk & 1) ? a1 : a0) + ((kk >> 1) & 3) * 10240 + (i - 2) * 2048);
    } else {
      constexpr int j = i < 2 ? 0 : (i - 3) / 2, hh = i < 2 ? i : (i - 3) % 2;
      lds_ptr bp = (sl >= 5 ? wb5 + (sl - 5) * K1_SLOT : wb + sl * K1_SLOT) + j * 4096 + hh * 1024;
      if constexpr (hh == 0) wl[bf][j] = lds_tr(bp); else wh[bf][j] = lds_tr(bp);
    }
  };
  lds_bar();
  STAMP_MARKW(0, 2, 512);
  for_seq<9>([&](auto I) { rd1(std::integral_constant<int, 0>{}, I); });
  SB();
  for_seq<18>([&](auto KK) {
    constexpr int kk = KK, bf = kk & 1;
    if constexpr (kk < 17) { raw_bar(); SB(); }
    for_seq<9>([&](auto II) {
      constexpr int i = II, j = i / 3, m = i % 3;
      const bf16x8 wv = cat8(wl[bf][j], wh[bf][j]);
      const bf16x8 av = (m < NM) ? fA[bf][m] : ones;
      acc[m][j] = ISV ? MFMA16(av, wv, acc[m][j]) : MFMA16(wv, av, acc[m][j]);
      SB();
      if constexpr (kk < 17) { rd1(std::integral_constant<int, kk + 1>{}, II); SB(); }
    });
  });
  STAMP_MARKW(0, 4, 512);
  lds_ptr cx = lds + K1_CS + cb * 3072 + lane * 16;
  if (HI) {
#pragma unroll
    for (int j = 0; j < 3; ++j) *(LDS_AS f32x4*)(cx + j * 1024) = acc[2][j];
  }
  lds_bar();
  STAMP_MARKW(0, 5, 512);
  f32x4 cs[3];
#pragma unroll
  for (int j = 0; j < 3; ++j) cs[j] = HI ? acc[2][j] : *(LDS_AS f32x4*)(cx + j * 1024);
  u32x2 o[NM * 3];
  if (ISV) {
#pragma unroll
    for (int m = 0; m < NM; ++m) {
      const f32x4 s0 = *(LDS_AS f32x4*)(lds + K1_ST + ((MB + m) * 16 + 4 * g) * 8), s1 = *(LDS_AS f32x4*)(lds + K1_ST + ((MB + m) * 16 + 4 * g) * 8 + 16);
#pragma unroll
      for (int j = 0; j < 3; ++j) {
        const float c0 = cs[j][0];
        o[3 * m + j] = u32x2{cvt_pk(fmaf(s0[0], c0, acc[m][j][0]) * s0[1], fmaf(s0[2], c0, acc[m][j][1]) * s0[3]),
                             cvt_pk(fmaf(s1[0], c0, acc[m][j][2]) * s1[1], fmaf(s1[2], c0, acc[m][j][3]) * s1[3])};
      }
    }
  } else {
    const float sc = ct < 3 ? QSCALE : 1.f;
#pragma unroll
    for (int m = 0; m < NM; ++m) {
      f32x2 st = *(LDS_AS f32x2*)(lds + K1_ST + ((MB + m) * 16 + r) * 8);
      st[1] *= sc;
#pragma unroll
      for (int j = 0; j < 3; ++j)
        o[3 * m + j] = u32x2{cvt_pk(fmaf(st[0], cs[j][0], acc[m][j][0]) * st[1], fmaf(st[0], cs[j][1], acc[m][j][1]) * st[1]),
                             cvt_pk(fmaf(st[0], cs[j][2], acc[m][j][2]) * st[1], fmaf(st[0], cs[j][3], acc[m][j][3]) * st[1])};
    }
  }
  char* base;
  int sm, sj, sel, half;
  if (ISV) { base = ws + WS_V + (size_t)(((m0 >> 4) + MB) * 2 + (g & 1)) * 9216 + ((ct - 6) * 192 + cb * 16 + r) * 16; sm = 18432; sj = 1024; sel = g >> 1; half = g >> 1; }
  else { base = ws + WS_Q + (size_t)(ct * 24 + cb * 2 + (g >> 1)) * 32768 + (m0 + MB * 16 + r) * 16; sm = 256; sj = 8 * 32768; sel = g & 1; half = g & 1; }
#pragma unroll
  for (int n = 0; n + 1 < NM * 3; n += 2) {
    u32x4 c;
#pragma unroll
    for (int e = 0; e < 2; ++e) {
      auto rr = ISV ? __builtin_amdgcn_permlane32_swap(o[n][e], o[n + 1][e], false, false) : __builtin_amdgcn_permlane16_swap(o[n][e], o[n + 1][e], false, false);
      c[e] = rr[0]; c[2 + e] = rr[1];
    }
    const int o0 = (n / 3) * sm + (n % 3) * sj, o1 = ((n + 1) / 3) * sm + ((n + 1) % 3) * sj;
    st16(base + (sel ? o1 : o0), c);
  }
  if (NM == 3) st8wt(base + 2 * sm + 2 * sj + half * 8, o[8]);
}

__global__ __launch_bounds__(1024) void k1_ln_qkv(const char* __restrict__ x, const char* __restrict__ wqkv, char* __restrict__ ws, const char* __restrict__ wo) {
  extern __shared__ __attribute__((aligned(16))) char smem[];
  STAMP_BEGIN
  lds_ptr lds = (lds_ptr)smem;
  const unsigned lds0 = (unsigned)(uintptr_t)lds;
  const int tid = threadIdx.x, lane = tid & 63, wid = __builtin_amdgcn_readfirstlane(tid >> 6);
  if (blockIdx.x >= 234) {
    const int sb = blockIdx.x - 234;
    if (sb == 18) {
      if (tid < 192) *(unsigned*)(ws + WS_CNT + tid * 128) = 0u;
      if (tid < 4) *(unsigned*)(ws + WS_ONES + tid * 4) = 0x3f803f80u;
    } else if (wid < 4) {
      const int wrow = (sb * 4 + wid) * 8 + (lane >> 3);
      float t = 0.f;
      u32x4 wv[9];
#pragma unroll
      for (int i = 0; i < 9; ++i) wv[i] = *(const u32x4*)(wo + (size_t)wrow * 1152 + (lane & 7) * 16 + i * 128);
#pragma unroll
      for (int i = 0; i < 9; ++i)
#pragma unroll
        for (int e = 0; e < 4; ++e) t = dot2(wv[i][e], 0x3f803f80u, t);
      t = sum8(t) * (1.f / 576.f);
      const unsigned h = cvt_pk(t, 0.f) & 0xffffu, l = cvt_pk(t - bf_lo(h), 0.f) & 0xffffu;
      if ((lane & 7) == 0) *(u32x4*)(ws + WS_WB + wrow * 16) = u32x4{h | (l << 16), 0u, 0u, 0u};
    }
    return;
  }
  const int tile = xcd_remap(blockIdx.x, 234);
  const int cgp = tile / 78, rem = tile - cgp * 78;
  const int mt = rem / 3, ct = cgp * 3 + (rem - mt * 3);
  const int m0 = min(mt * 80, SEQ - 80);
  const int sm = wid - 3;
  lds_ptr sa0 = lds + K1_X + sm * 2048 + ((lane >> 3) & 1) * 1024 + (lane & 7) * 128 + (((lane >> 4) ^ ((lane & 15) >> 1)) * 16);
  lds_ptr sa1 = lds + K1_X + sm * 2048 + ((lane >> 3) & 1) * 1024 + (lane & 7) * 128 + (((4 + (lane >> 4)) ^ ((lane & 15) >> 1)) * 16);
  f32x4 ssum = {0.f, 0.f, 0.f, 0.f}, sgram = {0.f, 0.f, 0.f, 0.f};
  bf16x8 sf0, sf1;
  auto stat_rd = [&](int st) { sf0 = lds_r128(sa0 + (st & 3) * 10240); sf1 = lds_r128(sa1 + (st & 3) * 10240); };
  auto stat_mm = [&]() {
    const bf16x8 ones = __builtin_bit_cast(bf16x8, u32x4{0x3f803f80u, 0x3f803f80u, 0x3f803f80u, 0x3f803f80u});
    ssum = MFMA16(ones, sf0, ssum); sgram = MFMA16(sf0, sf0, sgram);
    ssum = MFMA16(ones, sf1, ssum); sgram = MFMA16(sf1, sf1, sgram);
    pin(ssum); pin(sgram);
  };
  auto stat_fin = [&]() {
    const float q01 = (lane & 1) ? sgram[1] : sgram[0], q23 = (lane & 1) ? sgram[3] : sgram[2];
    const float mean = ssum[0] * (1.f / 576.f);
    const float var = fmaxf(((lane & 2) ? q23 : q01) * (1.f / 576.f) - mean * mean, 0.f);
    if ((lane >> 4) == ((lane & 15) >> 2)) {
      *(LDS_AS f32x2*)(lds + K1_ST + (sm * 16 + (lane & 15)) * 8) = f32x2{-mean, __builtin_amdgcn_rsqf(var + LN_EPS)};
      if (ct == 0) *(float*)(ws + WS_XM + (m0 + sm * 16 + (lane & 15)) * 4) = mean;
    }
    lds_bar();
  };
  if (wid < 3) {
    unsigned wv[4];
#pragma unroll
    for (int i = 0; i < 4; ++i) {
      const int rho = 8 * i + (lane >> 3);
      const int gk = ((rho >> 4) & 1) * 2 + ((rho >> 2) & 1), tk = (rho >> 3) & 1, qk = rho & 3;
      wv[i] = (8 * gk + 4 * tk + qk) * 3456 + (((lane & 7) ^ (((rho >> 1) & 3) << 1)) * 16);
    }
    const char* wsrc = wqkv + ct * 384 + wid * 128;
    const unsigned wdst = lds0 + K1_W + wid * 4096;
    for_seq<18>([&](auto KK) {
      constexpr int kk = KK;
#pragma unroll
      for (int n = (kk == 0 ? 0 : k1_nw(kk - 1)); n < k1_nw(kk); ++n)
#pragma unroll
        for (int i = 0; i < 4; ++i) glds16(wsrc + (size_t)n * 32 * 3456, wv[i], wdst + (n % 9) * K1_SLOT + i * 1024);
      wait_bar<(k1_nw(kk) - kk - 1) * 4>();
    });
    return;
  }
  if (wid < 8) {
    const unsigned v0 = (lane >> 3) * 1152 + (((lane & 7) ^ (lane >> 4)) * 16);
    const unsigned v1 = 9216 + (lane >> 3) * 1152 + (((lane & 7) ^ (4 + (lane >> 4))) * 16);
    const char* xs = x + (size_t)(m0 + sm * 16) * 1152;
    for_seq<18>([&](auto KK) {
      constexpr int kk = KK;
#pragma unroll
      for (int h = (kk == 0 ? 0 : k1_nx(kk - 1)); h < k1_nx(kk); ++h)
        glds16(xs + (h >> 1) * 128, (h & 1) ? v1 : v0, lds0 + K1_X + ((h >> 1) & 3) * 10240 + (sm * 2 + (h & 1)) * 1024);
      if constexpr (kk & 1) stat_rd(kk >> 1);
      else if constexpr (kk > 0) stat_mm();
      SB();
      wait_bar<k1_nx(kk) - 2 * (kk >> 1) - 2>();
    });
    stat_mm();
    stat_fin();
    return;
  }
  if (wid == 8) CODE_TOUCH(SYM_K1, LEN_K1, lds0 + K1_PF);
  if (wid == 9) CODE_TOUCH(SYM_K4, LEN_K4, lds0 + K1_PF + 256);
  if (wid >= 12) {
    const int ln = blockIdx.x * 148 + (wid - 12) * 37 + lane;
    if (lane < 37) {
      if (ln < 15552) touch_lines(wqkv, ln * 128, lds0 + K1_PF + 512);
      else if (ln < 33984) touch_lines(x, (ln - 15552) * 128, lds0 + K1_PF + 512);
    }
  }
  if (ct >= 6) {
    if (wid < 12) k1_consumer<true, false>(lds, wid - 8, lane, ws, ct, m0);
    else k1_consumer<true, true>(lds, wid - 12, lane, ws, ct, m0);
  } else {
    if (wid < 12) k1_consumer<false, false>(lds, wid - 8, lane, ws, ct, m0);
    else k1_consumer<false, true>(lds, wid - 12, lane, ws, ct, m0);
  }
  STAMP_ENDW(0, 512);
}

constexpr int K2_KM = 0, K2_V = 24576, K2_PF = 45056, K2_W = 65536, K2_SG = 98304;

#define MFMA32K8(a, b, c) __builtin_amdgcn_mfma_f32_32x32x8bf16_1k(a, b, c, 0, 0, 0)
template <class F>
DI void k2_core(lds_ptr lds, char* ws, int hd, int t, int wid, int lane, u32x4 q0u, u32x2 q1u, bool first, F&& hook) {
  const int hi = lane >> 5, l31 = lane & 31;
  const int dlo = ((18 * hd) & 7) >> 1;
#pragma unroll
  for (int e = 0; e < 4; ++e) q0u[e] = (4 * hi + e >= dlo) ? q0u[e] : 0u;
#pragma unroll
  for (int e = 0; e < 2; ++e) q1u[e] = (8 + 2 * hi + e < dlo + 9) ? q1u[e] : 0u;
  const bf16x8 q0 = __builtin_bit_cast(bf16x8, q0u);
  const s16x4 q1 = __builtin_bit_cast(s16x4, q1u);
  f32x16 S[9];
  lds_ptr kmp = lds + K2_KM + hi * 8192 + (wid * 32 + l31) * 16;
  lds_ptr kxp = lds + K2_KM + 16384 + (wid * 32 + l31) * 16 + hi * 8;
  const float NEG = -1e30f;
  const int dq = l31 - 4 * hi;
  float mx0 = NEG, mx1 = NEG;
  bf16x8 kf[3];
  s16x4 kx[3];
  auto rdk = [&](auto NN) {
    constexpr int n = NN, i = n == 0 ? 8 : n - 1;
    kf[n % 3] = lds_r128(kmp + i * 512);
    kx[n % 3] = *(LDS_AS const s16x4*)(kxp + i * 512);
  };
  auto post = [&](auto NN) {
    constexpr int n = NN, i = n == 0 ? 8 : n - 1;
    if constexpr (i < 8) {
      if constexpr (i == 0) {
#pragma unroll
        for (int r = 0; r < 16; ++r) S[0][r] = (((r & 3) + 8 * (r >> 2)) <= dq) ? S[8][r] : S[0][r];
      }
#pragma unroll
      for (int r = 0; r < 16; r += 4) {
        mx0 = fmaxf(fmaxf(mx0, S[i][r]), S[i][r + 1]);
        mx1 = fmaxf(fmaxf(mx1, S[i][r + 2]), S[i][r + 3]);
      }
    }
  };
  rdk(std::integral_constant<int, 0>{});
  rdk(std::integral_constant<int, 1>{});
  SB();
  for_seq<9>([&](auto NN) {
    constexpr int n = NN, i = n == 0 ? 8 : n - 1;
    const f32x16 z = {};
    S[i] = MFMA32(kf[n % 3], q0, z);
    SB();
    if constexpr (n + 2 < 9) { rdk(std::integral_constant<int, n + 2>{}); SB(); }
    S[i] = MFMA32K8(kx[n % 3], q1, S[i]);
    SB();
    if constexpr (n >= 1) { post(std::integral_constant<int, n - 1>{}); SB(); }
    hook(std::integral_constant<int, n>{}); SB();
  });
  post(std::integral_constant<int, 8>{});
  float mx = fmaxf(mx0, mx1);
  {
    auto rr = __builtin_amdgcn_permlane32_swap(__float_as_uint(mx), __float_as_uint(mx), false, false);
    mx = fmaxf(__uint_as_float(rr[0]), __uint_as_float(rr[1]));
  }
  s16x4 mq;
  {
    const float nm = -mx;
    const unsigned h1 = __float_as_uint(nm) & 0xffff0000u;
    const float r1 = nm - __uint_as_float(h1);
    const unsigned h2 = __float_as_uint(r1) & 0xffff0000u;
    const float r2 = r1 - __uint_as_float(h2);
    const u32x2 mu = {hi ? 0u : ((h1 >> 16) | h2), hi ? 0u : ((__float_as_uint(r2) >> 16) | 0xf1490000u)};
    mq = __builtin_bit_cast(s16x4, mu);
  }
  auto mk1 = [&](int i) { return __builtin_bit_cast(s16x4, u32x2{0x3f803f80u, (first && i > 0 && wid + i < 8) ? 0x3f803f80u : 0x00003f80u}); };
  S[1] = MFMA32K8(mk1(1), mq, S[1]);
  S[2] = MFMA32K8(mk1(2), mq, S[2]);
  SB();
  lds_ptr vp = lds + K2_V + wid * 2 * 592 + (l31 < 18 ? hi * 288 + l31 * 16 : 576);
  f32x16 O;
  const unsigned keep0 = first ? 0u : ~0u;
  bf16x8 vf[3];
  auto rdv = [&](auto EE) {
    constexpr int e = EE, blk = e < 14 ? e + 2 : ((e - 14) >> 1) + ((e & 1) ? 16 : 0);
    if constexpr (e < 18) vf[e % 3] = lds_r128(vp + blk * 592);
  };
  rdv(std::integral_constant<int, 0>{});
  rdv(std::integral_constant<int, 1>{});
  SB();
  for_seq<16>([&](auto HH) {
    constexpr int h = HH, i = ((h >> 1) + 1) & 7, s = h & 1;
    u32x4 P;
#pragma unroll
    for (int j = 0; j < 2; ++j) P[j] = cvt_pk(__builtin_amdgcn_exp2f(S[i][8 * s + 2 * j]), __builtin_amdgcn_exp2f(S[i][8 * s + 2 * j + 1]));
    if constexpr (i != 0 && s == 0 && i + 2 <= 8) {
      unsigned p0 = P[0], p1 = P[1];
      pin(p0); pin(p1);
      P[0] = p0; P[1] = p1;
      SB();
      S[(i + 2) & 7] = MFMA32K8(mk1((i + 2) & 7), mq, S[(i + 2) & 7]);
      SB();
    }
#pragma unroll
    for (int j = 2; j < 4; ++j) P[j] = cvt_pk(__builtin_amdgcn_exp2f(S[i][8 * s + 2 * j]), __builtin_amdgcn_exp2f(S[i][8 * s + 2 * j + 1]));
    if constexpr (i == 0) {
      constexpr int e = 14 + 2 * s;
      u32x4 P8, P0;
#pragma unroll
      for (int j = 0; j < 4; ++j) {
        const int r0 = 8 * s + 2 * j, r1 = r0 + 1;
        const unsigned mk = ((((r0 & 3) + 8 * (r0 >> 2)) <= dq) ? 0xffffu : 0u) | ((((r1 & 3) + 8 * (r1 >> 2)) <= dq) ? 0xffff0000u : 0u);
        P8[j] = P[j] & mk;
        P0[j] = P[j] & ~mk & keep0;
      }
      rdv(std::integral_constant<int, e + 2>{});
      SB();
      O = MFMA32(vf[e % 3], __builtin_bit_cast(bf16x8, P0), O);
      SB();
      rdv(std::integral_constant<int, e + 3>{});
      O = MFMA32(vf[(e + 1) % 3], __builtin_bit_cast(bf16x8, P8), O);
      SB();
    } else {
      rdv(std::integral_constant<int, h + 2>{});
      if constexpr (h < 9) hook(std::integral_constant<int, 9 + h>{});
      SB();
      if constexpr (h == 0) { const f32x16 z = {}; O = MFMA32(vf[0], __builtin_bit_cast(bf16x8, P), z); }
      else O = MFMA32(vf[h % 3], __builtin_bit_cast(bf16x8, P), O);
      SB();
    }
  });
  const float inv = __builtin_amdgcn_rcpf(O[10]);
  const int row = t * 32 + l31;
  char* ob = ws + WS_O + (size_t)(row >> 4) * 18 * 1024 + (row & 15) * 16;
  u32x4 o = {cvt_pk(O[0] * inv, O[1] * inv), cvt_pk(O[2] * inv, O[3] * inv), cvt_pk(O[4] * inv, O[5] * inv), cvt_pk(O[6] * inv, O[7] * inv)};
  st16(ob + (hd >> 1) * 1024 + (2 * (hd & 1) + hi) * 256, o);
  if (hi == 0) st4(ws + WS_OX + hd * 8192 + row * 4, cvt_pk(O[8] * inv, O[9] * inv));
}

DI void glds16v_if0(int c, const void* vaddr, unsigned lds_dst) {
  asm volatile("s_cmp_lg_u32 %2, 0\n\ts_cbranch_scc1 1f\n\ts_mov_b32 m0, %1\n\ts_nop 1\n\tglobal_load_lds_dwordx4 %0, off\n1:"
               ::"v"(vaddr), "s"(lds_dst), "s"(c) : "memory", "scc");
}
DI void poll_count(const unsigned* c, unsigned n) {
  for (int spin = 0; spin < (1 << 18); ++spin) {
    if (__hip_atomic_load(c, __ATOMIC_RELAXED, __HIP_MEMORY_SCOPE_AGENT) >= n) break;
    __builtin_amdgcn_s_sleep(1);
  }
}

template <class F>
DI void front_phase(lds_ptr lds, unsigned lds0, char* __restrict__ ws, const char* __restrict__ wo, const char* __restrict__ x,
                    const char* __restrict__ wg, const char* __restrict__ wu, F&& prefill) {
  const int tid = threadIdx.x, lane = tid & 63, wid = __builtin_amdgcn_readfirstlane(tid >> 6);
  const int hd = (blockIdx.x & 7) * 4 + ((blockIdx.x >> 3) & 3), qb = blockIdx.x >> 5;
  const int p3 = blockIdx.x & 7, m0 = p3 * 256 + ((blockIdx.x >> 3) & 3) * 64, cb = blockIdx.x >> 5;
  const int t = qb * 8 + wid;
  u32x4 q0;
  u32x2 q1;
  const char *wa, *wx;
  unsigned wst;
  {
    const int c0 = (18 * hd) >> 3;
    asm volatile("global_load_dwordx4 %0, %2, %4\n\tglobal_load_dwordx2 %1, %3, %4"
                 : "=&v"(q0), "=&v"(q1)
                 : "v"((lane >> 5) * 32768 + (lane & 31) * 16), "v"(65536 + (lane & 31) * 16 + (lane >> 5) * 8),
                   "s"(ws + WS_Q + (size_t)c0 * 32768 + t * 512)
                 : "memory");
    const int key0 = max(qb * 256 - 256 + wid * 64, 0);
#pragma unroll
    for (int i = 0; i < 3; ++i) glds16(ws + WS_K + (size_t)(c0 + i) * 32768 + key0 * 16, (qb == 0 && wid < 4) ? 0 : lane * 16, lds0 + K2_KM + i * 8192 + wid * 1024);
#pragma unroll
    for (int i = 0; i < 3; ++i) {
      const int pc = wid + 8 * i;
      if (pc < 19) {
        const int u = min(pc * 64 + lane, 32 * 37 - 1), bl = u / 37, w = u - bl * 37;
        const int blk = max(qb * 16 - 16 + bl, 0);
        const unsigned vo = w == 36 ? (unsigned)(WS_ONES - WS_V) : (blk * 2 + (w >= 18)) * 9216 + (hd * 18 + (w >= 18 ? w - 18 : w)) * 16;
        glds16(ws + WS_V, vo, lds0 + K2_V + pc * 1024);
      }
    }
    const int u = min(wid, 4) * 64 + lane, rho = u / 10, c = u - rho * 10;
    const int g_ = ((rho >> 4) & 1) * 2 + ((rho >> 2) & 1), t_ = (rho >> 3) & 1, q_ = rho & 3;
    const char* wbase = c < 9 ? wo + cb * 144 + c * 16 : ws + WS_WB;
    wst = c < 9 ? 1152 : 16;
    wa = wbase + (18 * (g_ >> 1) + 8 * t_ + 4 * (g_ & 1) + q_) * wst;
    wx = wbase + (18 * (4 * g_ + 2 * t_ + (q_ >> 1)) + 16 + (q_ & 1)) * wst;
    wst *= 36;
    pin(wa); pin(wx); pin(wst);
  }
  asm volatile("s_waitcnt vmcnt(0)\n\ts_barrier" : "+v"(q0), "+v"(q1)::"memory");
  STAMP_MARK(1, 2);
  k2_core(lds, ws, hd, t, wid, lane, q0, q1, qb == 0, [&](auto I) {
    if constexpr (I < 18) {
      glds16v_if0(wid / 5, I < 16 ? wa : wx, lds0 + K2_W + (I * 5 + wid) * 1024);
      if constexpr (I < 15) wa += wst;
      if constexpr (I == 16) wx += 8 * wst;
    }
    if constexpr (I == 17)
      touch_lines((wid & 1) ? wu : wg, ((blockIdx.x * 4 + (wid >> 1)) * 27 + min(lane, 26)) * 128, lds0 + K2_PF + wid * 256);
  });
  STAMP_MARK(1, 3);
  wait_bar<0>();
  unsigned* cnt = (unsigned*)(ws + WS_CNT);
  if (tid == 0) __hip_atomic_fetch_add(cnt + qb * 32, 1u, __ATOMIC_RELAXED, __HIP_MEMORY_SCOPE_AGENT);
  const int mi = wid >> 1, kg = wid & 1;
  const int g = lane >> 4, q = (lane & 15) >> 2, p = lane & 3, er = mi * 16 + (lane & 15);
  const bool ex = kg == 0 && g == 0;
  const u32x4 res = *(const u32x4*)(x + (size_t)(m0 + er) * 1152 + cb * 144 + (kg * 4 + g) * 16);
  const u32x4 res8 = *(const u32x4*)(x + (size_t)(m0 + er) * 1152 + cb * 144 + 128);
  const float xm = *(const float*)(ws + WS_XM + (m0 + er) * 4);
  lds_ptr wb = lds + K2_W + kg * 5120 + (16 * (g >> 1) + 4 * (g & 1) + q) * 160 + p * 8, wb2 = wb + 5 * 10240;
  pin(wb); pin(wb2);
  bf16x8 wf[9][5];
#pragma unroll
  for (int j = 0; j < 9; ++j)
#pragma unroll
    for (int n = 0; n < 5; ++n) {
      lds_ptr bp = (j < 5 ? wb + j * 10240 : wb2 + (j - 5) * 10240) + n * 32;
      wf[j][n] = cat8(lds_tr(bp), lds_tr(bp + 1280));
    }
  if (tid == 448) poll_count(cnt + p3 * 32, 32u);
  SB();
  lds_bar();
  SB();
  STAMP_MARK(1, 5);
  const __amdgpu_buffer_rsrc_t ors = __builtin_amdgcn_make_buffer_rsrc((void*)(ws + WS_O), 0, 128 * 18432 + 32 * 8192, 0x00020000);
  const int aoff = ((m0 >> 4) + mi) * 18432 + kg * 1024 + lane * 16;
  bf16x8 a[9];
#pragma unroll
  for (int i = 0; i < 8; ++i) a[i] = __builtin_bit_cast(bf16x8, __builtin_amdgcn_raw_buffer_load_b128(ors, aoff + i * 2048, 0, 16));
  {
    u32x4 ax;
    const int xo = 128 * 18432 + (kg * 16 + 4 * g) * 8192 + (m0 + er) * 4;
#pragma unroll
    for (int e = 0; e < 4; ++e) ax[e] = __builtin_amdgcn_raw_buffer_load_b32(ors, xo, e * 8192, 16);
    a[8] = __builtin_bit_cast(bf16x8, ax);
  }
  SB();
  prefill();
  SB();
  f32x4 acc[5];
#pragma unroll
  for (int n = 0; n < 5; ++n) acc[n] = f32x4{0.f, 0.f, 0.f, 0.f};
#pragma unroll
  for (int j = 0; j < 9; ++j)
#pragma unroll
    for (int n = 0; n < 5; ++n) acc[n] = MFMA16(wf[j][n], a[j], acc[n]);
  STAMP_MARK(1, 6);
  {
    lds_ptr cw = lds + K2_SG + (kg * 64 + er) * 336 + 16 * g;
#pragma unroll
    for (int n = 0; n < 5; ++n) *(LDS_AS f32x4*)(cw + n * 64) = acc[n];
  }
  lds_bar();
  {
    lds_ptr cr = lds + K2_SG + er * 336;
    const f32x2 ma = *(LDS_AS f32x2*)(cr + 288), mb = *(LDS_AS f32x2*)(cr + 64 * 336 + 288);
    const float mean = xm + ((ma[0] + ma[1]) + (mb[0] + mb[1]));
    const int row = m0 + er;
    u32x4 o[2];
#pragma unroll
    for (int k = 0; k < 2; ++k) {
      const int c = k ? 8 : kg * 4 + g;
      const u32x4 rs = k ? res8 : res;
      f32x4 a0 = *(LDS_AS f32x4*)(cr + c * 32), a1 = *(LDS_AS f32x4*)(cr + c * 32 + 16);
      a0 += *(LDS_AS f32x4*)(cr + 64 * 336 + c * 32) - mean; a1 += *(LDS_AS f32x4*)(cr + 64 * 336 + c * 32 + 16) - mean;
      o[k] = u32x4{cvt_pk(a0[0] + bf_lo(rs[0]), a0[1] + bf_hi(rs[0])), cvt_pk(a0[2] + bf_lo(rs[1]), a0[3] + bf_hi(rs[1])),
                   cvt_pk(a1[0] + bf_lo(rs[2]), a1[1] + bf_hi(rs[2])), cvt_pk(a1[2] + bf_lo(rs[3]), a1[3] + bf_hi(rs[3]))};
    }
    char* hp = ws + WS_H + (size_t)(row >> 4) * 18432 + cb * 2304 + (row & 15) * 16;
    st16(hp + (kg * 4 + g) * 256, o[0]);
    if (ex) st16(hp + 2048, o[1]);
    float s1 = 0.f, s2 = 0.f, t1 = 0.f, t2 = 0.f;
#pragma unroll
    for (int j = 0; j < 4; ++j) { s1 = dot2(o[0][j], 0x3f803f80u, s1); s2 = dot2(o[0][j], o[0][j], s2); t1 = dot2(o[1][j], 0x3f803f80u, t1); t2 = dot2(o[1][j], o[1][j], t2); }
    s1 += ex ? t1 : 0.f; s2 += ex ? t2 : 0.f;
    s1 = sum_g(s1); s2 = sum_g(s2);
    if (g == 0 || (g == 1 && kg == 0 && cb == 0)) st8wt(ws + WS_ST + (size_t)(g ? 16 : 2 * cb + kg) * 16384 + row * 8, u32x2{__float_as_uint(g ? mean : s1), __float_as_uint(s2)});
  }
}

constexpr int K4_SLOT = 18432, K4_PF = K4_SLOT * 8, K4_SY = 160000, K4_LDS = 160 * 1024;

constexpr int k4_frag(int i) { return i < 16 ? ((i >> 2) * 2 + (i & 1) + ((i & 2) ? 9 : 0)) : (i == 16 ? 8 : 17); }
constexpr int k4_foff(int f) { return (f >= 9 ? 9216 + (f - 9) * 32 : f * 32); }
constexpr int K4_T0 = 13 * 18;
constexpr int k4_slab(int G) { return G < K4_T0 ? G / 18 : (G - K4_T0 < 80 ? 13 + ((G - K4_T0) % 20) / 4 : 13 + (G - K4_T0 - 80) / 2); }
constexpr int k4_pos(int G) { return G < K4_T0 ? G % 18 : (G - K4_T0 < 80 ? ((G - K4_T0) / 20) * 4 + ((G - K4_T0) & 3) : 16 + ((G - K4_T0) & 1)); }
constexpr int k4_op(int G) {
  const int s = G / 18, i = G % 18;
  if (s > 12) return 0;
  if (s < 3) {
    const int pos[10] = {0, 2, 4, 6, 7, 9, 11, 13, 15, 16};
    for (int k = 0; k < 10; ++k) if (pos[k] == i) return 1 + (2 * s + 2 + k / 5) * 8 + k % 5;
    return 0;
  }
  if (s == 3) {
    const int pos[8] = {0, 2, 4, 6, 8, 11, 14, 16};
    for (int k = 0; k < 8; ++k) if (pos[k] == i) return k < 5 ? 1 + 8 * 8 + k : 1 + 9 * 8 + (k - 3);
    return 0;
  }
  if (i == 1) return 1 + (s + 5) * 8;
  if (i == 4) return 1 + (s + 5) * 8 + 1;
  if (s + 6 > 17) return 0;
  if (i == 8) return 1 + (s + 6) * 8 + 2;
  if (i == 13) return 1 + (s + 6) * 8 + 3;
  if (i == 16) return 1 + (s + 6) * 8 + 4;
  return 0;
}
constexpr int k4_allow(int G, int need) {
  int n = (need == 0) ? 2 : 0;
  for (int g = 0; g < G; ++g) {
    const int op = k4_op(g);
    if (op == 0) continue;
    const int sl = (op - 1) >> 3, kind = (op - 1) & 7;
    if (sl <= need) n = 0;
    else if (kind != 4) ++n;
  }
  return n;
}
constexpr int k4_need(int G) {
  if (G + 5 >= 324 || G == 0) return 0;
  int mx = 0;
  for (int g = 0; g < G + 5; ++g) mx = k4_slab(g) > mx ? k4_slab(g) : mx;
  return k4_slab(G + 5) > mx ? k4_slab(G + 5) : 0;
}

constexpr int d_off(int i) { return i < 6 ? 36864 + i * 9216 : (i < 10 ? (i - 6) * 9216 : (i < 17 ? 92160 + (i - 10) * 9216 : 36864)); }
constexpr int DT = 8;
constexpr int D_G0 = (18 - DT) * 9;
constexpr int d_i(int H) { return H < D_G0 ? H / 9 : (H - D_G0 < 8 * DT ? 18 - DT + ((H - D_G0) % (2 * DT)) / 2 : 18 - DT + (H - D_G0 - 8 * DT)); }
constexpr int d_f(int H) { return H < D_G0 ? H % 9 : (H - D_G0 < 8 * DT ? ((H - D_G0) / (2 * DT)) * 2 + ((H - D_G0) & 1) : 8); }
constexpr int d_first(int H) {
  for (int h = 0; h < H; ++h) if (d_i(h) == d_i(H)) return 0;
  return 1;
}
DI void st_part(int c, void* p, lds_ptr l, u32x4 v) {
  asm volatile("s_cmp_lg_u32 %3, 0\n\ts_cbranch_scc1 1f\n\tds_write_b128 %1, %2\n\ts_branch 2f\n1:\n\tglobal_store_dwordx4 %0, %2, off sc1\n2:\n\ts_nop 1"
               ::"v"(p), "v"(l), "v"(v), "s"(c) : "memory", "scc");
}
DI void ld2_sc1(bf16x8& d0, bf16x8& d1, unsigned o0, unsigned o1, const char* base) {
  asm volatile("global_load_dwordx4 %0, %2, %4 sc1\n\tglobal_load_dwordx4 %1, %3, %4 sc1" : "=&v"(d0), "=&v"(d1) : "v"(o0), "v"(o1), "s"(base) : "memory");
}
template <int N> DI void wait_vm2(bf16x8& d0, bf16x8& d1) { asm volatile("s_waitcnt vmcnt(%2)" : "+v"(d0), "+v"(d1) : "n"(N) : "memory"); }
DI void ld8(f32x2& d, unsigned o, const char* base) { asm volatile("global_load_dwordx2 %0, %1, %2 sc1" : "=v"(d) : "v"(o), "s"(base) : "memory"); }

__global__ __launch_bounds__(512, 2) void k4_gateup(const char* __restrict__ wgate, const char* __restrict__ wup, char* __restrict__ ws,
                                                    const char* __restrict__ wd, char* __restrict__ out, const char* __restrict__ wo, const char* __restrict__ x) {
  extern __shared__ __attribute__((aligned(16))) char smem[];
  STAMP_BEGIN
  lds_ptr lds = (lds_ptr)smem;
  const unsigned lds0 = (unsigned)(uintptr_t)lds;
  *(LDS_AS unsigned*)(lds + K4_SY + (threadIdx.x & 1) * 4) = 0u;
  const int wid = __builtin_amdgcn_readfirstlane(threadIdx.x >> 6);
  const int xcd = blockIdx.x & 7, idx = blockIdx.x >> 3;
  const int pn = 4 * (xcd >> 2) + (idx & 3), jt = 8 * (xcd & 3) + (idx >> 2);
  const int wm = wid & 3, wn = wid >> 2;
  const char* wsrc = (wn ? wup : wgate) + jt * 288;
  unsigned wvo[3];
  const unsigned wdst = lds0 + wn * 9216;
  auto issue_w = [&](int s, int i) {
    const unsigned so = (s & 7) * K4_SLOT;
    const char* wp = wsrc + (size_t)s * 32 * 9216;
    if (i < 2) glds16(wp, wvo[i], wdst + so + wm * 2048 + i * 1024);
    else glds16_if0(wm, wp, wvo[2], wdst + so + 8192);
  };
  front_phase(lds, lds0, ws, wo, x, wgate, wup, [&]() {
    int ln = threadIdx.x;
    pin(ln);
    ln &= 63;
#pragma unroll
    for (int i = 0; i < 3; ++i) {
      const int pc = i < 2 ? 2 * wm + i : 8;
      const int o = pc * 1024 + ln * 16;
      const int rho = o / 288, u = (o - rho * 288) >> 4;
      const int gk = ((rho >> 4) & 1) * 2 + ((rho >> 2) & 1), tk = (rho >> 3) & 1, qk = rho & 3;
      wvo[i] = (8 * gk + 4 * tk + qk) * 9216 + u * 16;
    }
    for_seq<2>([&](auto S) { issue_w(S, 0); issue_w(S, 1); issue_w(S, 2); });
  });
  int tid = threadIdx.x;
  pin(tid);
  const int lane = tid & 63;
  const char* asrc = ws + WS_H + (size_t)(pn * 16 + 2 * wid) * 18432;
  const unsigned avo = lane * 16, avo2 = lane * 16 + 18432;
  bf16x8 A[6][2];
  auto issue_a = [&](int s, bf16x8& dst, int m) {
    asm volatile("global_load_dwordx4 %0, %1, %2 sc1" : "=v"(dst) : "v"(m ? avo2 : avo), "s"(asrc + s * 1024) : "memory");
  };
  wait_vm<0>();
  unsigned* cnt3 = (unsigned*)(ws + WS_CNT + 20480);
  if (lds_arrive(lds + K4_SY + 4, lane) == 7 && lane == 0) __hip_atomic_fetch_add(cnt3 + (blockIdx.x & 7) * 32, 1u, __ATOMIC_RELAXED, __HIP_MEMORY_SCOPE_AGENT);
  if (tid == 0) poll_count(cnt3 + pn * 32, 32u);
  SB();
  raw_bar();
  STAMP_MARK(1, 7);
  for_seq<2>([&](auto S) { issue_a(S, A[S][0], 0); issue_a(S, A[S][1], 1); });
  f32x2 sp[8];
  const int g = lane >> 4, q = (lane & 15) >> 2, p = lane & 3, r = lane & 15;
  lds_ptr wb = lds + (16 * (g >> 1) + 4 * (g & 1) + q) * 288 + p * 8;
  lds_ptr wbs[3] = {wb, wb + 3 * K4_SLOT, wb + 6 * K4_SLOT};
  asm volatile("" : "+v"(wbs[1]), "+v"(wbs[2]));
  f32x4 acc[2][18];
  asm volatile("s_waitcnt vmcnt(%2)\n\ts_barrier" : "+v"(A[0][0]), "+v"(A[0][1]) : "n"(k4_allow(0, 0)) : "memory");
  STAMP_MARK(2, 2);
  STAMP_CYC0
  s16x4 Bl[6], Bh[6];
  for_seq<5>([&](auto I) { Bl[I] = lds_tr(wb + k4_foff(k4_frag(I))); Bh[I] = lds_tr(wb + k4_foff(k4_frag(I)) + 2304); });
  SB();
  char* hb = ws + WS_HID + (size_t)(pn * 16 + wid * 2) * (144 * 1024) + (size_t)(jt >> 2) * 18432;
  float c1[2];
  float sd[2];
  auto glu = [&](float ag, float au, int m) -> float {
    return au * __builtin_amdgcn_rcpf(fmaf(__builtin_amdgcn_exp2f(ag * c1[m]), sd[m], sd[m]));
  };
  u32x4 own[4][2];
  float eg[2][2];
  auto micro = [&](auto QQ, auto KK, auto JJ) {
    constexpr int Q = QQ, k = KK, j = JJ, m = k >> 2, d = k & 3, h = d >> 1, e = (d & 1) * 2, b = k & 1;
#pragma unroll
    for (int x = 0; x < 2; ++x) {
      if constexpr (j == 0) eg[b][x] = acc[m][2 * Q + h][e + x] * c1[m];
      if constexpr (j == 1) eg[b][x] = __builtin_amdgcn_exp2f(eg[b][x]);
      if constexpr (j == 2) eg[b][x] = fmaf(eg[b][x], sd[m], sd[m]);
      if constexpr (j == 3) eg[b][x] = __builtin_amdgcn_rcpf(eg[b][x]);
      if constexpr (j == 4) eg[b][x] *= acc[m][9 + 2 * Q + h][e + x];
      if constexpr (j < 5) pin(eg[b][x]);
    }
    if constexpr (j == 6) {
      own[Q][m][d] = cvt_pk(eg[b][0], eg[b][1]);
      if constexpr (d == 3) st16(hb + (size_t)m * (144 * 1024) + (4 * (jt & 3) + Q) * 1024 + lane * 16, own[Q][m]);
      else pin(own[Q][m]);
    }
  };
  auto gapops = [&](auto QQ, auto GG) {
    constexpr int gg = GG;
    if constexpr (gg >= 4 && gg < 36) micro(QQ, std::integral_constant<int, gg / 4 - 1>{}, std::integral_constant<int, 4 + gg % 4>{});
    if constexpr (gg >= 0 && gg < 32) micro(QQ, std::integral_constant<int, gg / 4>{}, std::integral_constant<int, gg % 4>{});
    SB();
  };
  unsigned dvo[2], dvl[2];
  const char* dsrc = wd + (size_t)(jt >> 2) * 576 * 1152 + (jt & 3) * 288;
  auto dvoff = [&]() {
    int ln = tid;
    asm volatile("" : "+v"(ln));
    ln &= 63;
#pragma unroll
    for (int i = 0; i < 2; ++i) {
      const int o = (i ? 8 : wid) * 1024 + ln * 16;
      const int rho = o / 288, u = (o - rho * 288) >> 4;
      const int gk = ((rho >> 4) & 1) * 2 + ((rho >> 2) & 1), tk = (rho >> 3) & 1, qk = rho & 3;
      dvo[i] = (16 * tk + 4 * gk + qk) * 1152 + u * 16;
      dvl[i] = (144 * (gk >> 1) + 128 + 8 * (gk & 1) + 4 * tk + qk) * 1152 + u * 16;
    }
  };
  auto issue_d = [&](auto II, int pc) {
    constexpr int i = II;
    const int k = (4 * (jt & 3) + i) & 15;
    const char* src = dsrc + (size_t)(i < 16 ? 144 * (k >> 2) + 32 * (k & 3) : 288 * (i - 16)) * 1152;
    if (pc == 0) glds16(src, i < 16 ? dvo[0] : dvl[0], lds0 + d_off(i) + wid * 1024);
    else glds16_if0(wid ^ (i & 7), src, i < 16 ? dvo[1] : dvl[1], lds0 + d_off(i) + 8192);
  };
  for_seq<18>([&](auto GH) { for_seq<18>([&](auto GL) {
    constexpr int G = GH * 18 + GL, sl = k4_slab(G), f = k4_frag(k4_pos(G)), rb = G % 6, nb = (G + 5) % 6, NG = G + 5;
    const bf16x8 b = cat8(Bl[rb], Bh[rb]);
    constexpr int need = k4_need(G);
    if constexpr (need != 0) {
      asm volatile("s_waitcnt vmcnt(%2)\n\ts_barrier" : "+v"(A[need % 6][0]), "+v"(A[need % 6][1]) : "n"(k4_allow(G, need)) : "memory");
      SB();
    }
    if constexpr (sl == 0) mfma16_zero(acc[0][f], b, A[0][0]); else mfma16_acc(acc[0][f], b, A[sl % 6][0]);
    SB();
    if constexpr (NG < 324) { Bl[nb] = lds_tr(wbs[(k4_slab(NG) & 7) / 3] + ((k4_slab(NG) & 7) % 3) * K4_SLOT + k4_foff(k4_frag(k4_pos(NG)))); SB(); }
    if constexpr (G >= K4_T0 + 20 && G < K4_T0 + 80 && (G - K4_T0) % 20 >= 1 && (G - K4_T0) % 20 < 19)
      gapops(std::integral_constant<int, (G - K4_T0) / 20 - 1>{}, std::integral_constant<int, 2 * ((G - K4_T0) % 20 - 1)>{});
    if constexpr (G >= K4_T0 + 81) {
      gapops(std::integral_constant<int, 3>{}, std::integral_constant<int, 4 * (G - K4_T0 - 81)>{});
      gapops(std::integral_constant<int, 3>{}, std::integral_constant<int, 4 * (G - K4_T0 - 81) + 1>{});
    }
    if constexpr (sl == 0) mfma16_zero(acc[1][f], b, A[0][1]); else mfma16_acc(acc[1][f], b, A[sl % 6][1]);
    SB();
    if constexpr (NG < 324) { Bh[nb] = lds_tr(wbs[(k4_slab(NG) & 7) / 3] + ((k4_slab(NG) & 7) % 3) * K4_SLOT + k4_foff(k4_frag(k4_pos(NG))) + 2304); SB(); }
    constexpr int op = k4_op(G);
    if constexpr (op != 0) {
      constexpr int os = (op - 1) >> 3, kind = (op - 1) & 7;
      if constexpr (kind < 2) issue_a(os, A[os % 6][kind], kind);
      else issue_w(os, kind - 2);
      SB();
    }
    if constexpr (G % 18 == 10 && (G / 18 == 5 || G / 18 == 7 || G / 18 == 9)) {
      constexpr int k = (G / 18 - 5) / 2;
      touch_if0((int)(blockIdx.x / 216) + (wid ^ (k + 1)), wd + (size_t)(blockIdx.x * 3 + k) * 8192, avo * 8, lds0 + K4_PF + k * 256);
      SB();
    }
    if constexpr (G >= 222 && G < 230) { ld8(sp[G - 222], (lane & 31) * 8 + (lane >> 5) * 8 * 16384, ws + WS_ST + (size_t)(pn * 256 + wid * 32) * 8 + (G - 222) * 16384); SB(); }
    if constexpr (G == K4_T0 + 11) {
      for_seq<8>([&](auto I) { pin(sp[I]); });
      const f32x2 t4 = ((sp[0] + sp[1]) + (sp[2] + sp[3])) + ((sp[4] + sp[5]) + (sp[6] + sp[7]));
      auto r1 = __builtin_amdgcn_permlane32_swap(__float_as_uint(t4[0]), __float_as_uint(t4[0]), false, false);
      auto r2 = __builtin_amdgcn_permlane32_swap(__float_as_uint(t4[1]), __float_as_uint(t4[1]), false, false);
      const float mean = (__uint_as_float(r1[0]) + __uint_as_float(r1[1])) * (1.f / 576.f);
      const float var = fmaxf((__uint_as_float(r2[0]) + __uint_as_float(r2[1])) * (1.f / 576.f) - mean * mean, 0.f) + LN_EPS;
      auto rv = __builtin_amdgcn_permlane16_swap(__float_as_uint(var), __float_as_uint(var), false, false);
      const float v0 = __uint_as_float(rv[0]), v1 = __uint_as_float(rv[1]);
      c1[0] = -1.4426950408889634f * __builtin_amdgcn_rsqf(v0); sd[0] = __builtin_amdgcn_sqrtf(v0);
      c1[1] = -1.4426950408889634f * __builtin_amdgcn_rsqf(v1); sd[1] = __builtin_amdgcn_sqrtf(v1);
      SB();
    }
    if constexpr (G >= K4_T0) {
      constexpr int t = G - K4_T0, u = t % 20;
      if constexpr (t == 11) { dvoff(); SB(); }
      if constexpr (t >= 12 && t < 24) { issue_d(std::integral_constant<int, (t - 12) / 2>{}, t & 1); SB(); }
      if constexpr (t >= 20 && t < 80 && u >= 1 && u < 19) gapops(std::integral_constant<int, t / 20 - 1>{}, std::integral_constant<int, 2 * (u - 1) + 1>{});
      if constexpr (t >= 81) {
        gapops(std::integral_constant<int, 3>{}, std::integral_constant<int, 4 * (t - 81) + 2>{});
        gapops(std::integral_constant<int, 3>{}, std::integral_constant<int, 4 * (t - 81) + 3>{});
      }
    }
  }); });
  {
    asm volatile("s_nop 7\n\ts_nop 1" : "+v"(acc[0][8]), "+v"(acc[1][8]), "+v"(acc[0][17]), "+v"(acc[1][17]));
#pragma unroll
    for (int m = 0; m < 2; ++m) {
      const f32x4 ag = acc[m][8], au = acc[m][17];
      u32x2 o = {cvt_pk(glu(ag[0], au[0], m), glu(ag[1], au[1], m)), cvt_pk(glu(ag[2], au[2], m), glu(ag[3], au[3], m))};
      st8wt(hb + (size_t)m * (144 * 1024) + (16 + ((jt >> 1) & 1)) * 1024 + (2 * (jt & 1) + (g >> 1)) * 256 + r * 16 + (g & 1) * 8, o);
    }
  }
  STAMP_MARK(2, 5);
  const int sb = jt >> 2, nq = jt & 3;
  unsigned* cntA = (unsigned*)(ws + WS_CNT + 8192) + (pn * 8 + sb) * 32;
  unsigned* cntB = (unsigned*)(ws + WS_CNT + 16384) + (pn * 4 + nq) * 32;
  wait_bar<10>();
  auto dptr = [&](auto HH) -> lds_ptr {
    constexpr int H = HH, o = d_off(d_i(H)) + d_f(H) * 32;
    return wbs[o / (3 * K4_SLOT)] + o % (3 * K4_SLOT);
  };
  for_seq<5>([&](auto I) { Bl[I] = lds_tr(dptr(I)); Bh[I] = lds_tr(dptr(I) + 2304); });
  SB();
  f32x4 dacc[2][9];
  bf16x8 DA[14][2];
  char* pb = ws + WS_PART + (size_t)(((pn * 4 + nq) * 8 + wid) * 8 + sb) * 9216 + lane * 16;
  for_seq<18>([&](auto HH) { for_seq<9>([&](auto HL) {
    constexpr int H = HH * 9 + HL, i = d_i(H), f = d_f(H), rb = H % 6, nb = (H + 5) % 6, NH = H + 5;
    if constexpr (H == 36) {
      if (tid == 0) poll_count(cntA, 4u);
      SB();
      wait_bar<0>();
      STAMP_MARK(2, 6);
      issue_d(std::integral_constant<int, 17>{}, 0); issue_d(std::integral_constant<int, 17>{}, 1);
      const unsigned ho = lane * 16, ho2 = lane * 16 + 144 * 1024;
      for_seq<14>([&](auto JJ) {
        constexpr int j = JJ;
        const int k = j < 12 ? ((4 * nq + 4 + j) & 15) : 4 + j;
        ld2_sc1(DA[j][0], DA[j][1], ho, ho2, hb + k * 1024);
      });
      SB();
    }
    if constexpr (i >= 4 && d_first(H)) {
      wait_vm2<2 * (17 - i)>(DA[i - 4][0], DA[i - 4][1]);
      SB();
    }
    if constexpr (H == D_G0) { raw_bar(); SB(); }
    const bf16x8 b = cat8(Bl[rb], Bh[rb]);
    bf16x8 a0, a1;
    if constexpr (i < 4) { a0 = __builtin_bit_cast(bf16x8, own[i][0]); a1 = __builtin_bit_cast(bf16x8, own[i][1]); }
    else { a0 = DA[i - 4][0]; a1 = DA[i - 4][1]; }
    if constexpr (i == 0) mfma16_zero(dacc[0][f], b, a0); else mfma16_acc(dacc[0][f], b, a0);
    SB();
    if constexpr (NH < 162) { Bl[nb] = lds_tr(dptr(std::integral_constant<int, NH>{})); SB(); }
    if constexpr (i == 0) mfma16_zero(dacc[1][f], b, a1); else mfma16_acc(dacc[1][f], b, a1);
    SB();
    if constexpr (NH < 162) { Bh[nb] = lds_tr(dptr(std::integral_constant<int, NH>{}) + 2304); SB(); }
    if constexpr (H < 22) { issue_d(std::integral_constant<int, 6 + H / 2>{}, H & 1); SB(); }
    if constexpr (H == 24) {
      wait_vm<11>();
      if (lds_arrive(lds + K4_SY, lane) == 7 && lane == 0) __hip_atomic_fetch_add(cntA, 1u, __ATOMIC_RELAXED, __HIP_MEMORY_SCOPE_AGENT);
      SB();
    }
    if constexpr (H >= D_G0 + 2 * DT && (((H - D_G0) % (2 * DT) == 2 || (H - D_G0) % (2 * DT) == 3))) {
      constexpr int pr = (H - D_G0) / (2 * DT) - 1, m = ((H - D_G0) % (2 * DT) == 3);
      const f32x4 x0 = dacc[m][2 * pr], x1 = dacc[m][2 * pr + 1];
      st_part(wid ^ sb, pb + (m * 4 + pr) * 1024, lds + lane * 16 + (m * 4 + pr) * 1024, u32x4{cvt_pk(x0[0], x0[1]), cvt_pk(x0[2], x0[3]), cvt_pk(x1[0], x1[1]), cvt_pk(x1[2], x1[3])});
      SB();
    }
  }); });
  {
    asm volatile("s_nop 7\n\ts_nop 1" : "+v"(dacc[0][8]), "+v"(dacc[1][8]));
    const f32x4 x0 = dacc[0][8], x1 = dacc[1][8];
    st_part(wid ^ sb, pb + 8192, lds + lane * 16 + 8192, u32x4{cvt_pk(x0[0], x0[1]), cvt_pk(x0[2], x0[3]), cvt_pk(x1[0], x1[1]), cvt_pk(x1[2], x1[3])});
  }
  wait_bar<0>();
  STAMP_MARK(2, 7);
  if (tid == 0) __hip_atomic_fetch_add(cntB, 1u, __ATOMIC_RELAXED, __HIP_MEMORY_SCOPE_AGENT);
  {
    const int nc = wid == 1 ? 2 : 1;
    const int rbase = pn * 256 + sb * 32;
    int row[2][2], col[2][2];
    row[0][0] = row[0][1] = rbase + (wid >> 2) * 16 + r; col[0][0] = nq * 144 + (wid & 3) * 32 + 4 * g; col[0][1] = col[0][0] + 16;
    row[1][0] = rbase + r; row[1][1] = rbase + 16 + r; col[1][0] = col[1][1] = nq * 144 + 128 + 4 * g;
    const __amdgpu_buffer_rsrc_t hrs = __builtin_amdgcn_make_buffer_rsrc((void*)(ws + WS_H), 0, (int)(WS_XM - WS_H), 0x00020000);
    u32x2 res[2][2];
    float hm[2][2];
#pragma unroll
    for (int c = 0; c < 2; ++c)
      if (c < nc) {
#pragma unroll
        for (int e = 0; e < 2; ++e) {
          res[c][e] = __builtin_amdgcn_raw_buffer_load_b64(hrs, (row[c][e] >> 4) * 18432 + (col[c][e] >> 3) * 256 + (row[c][e] & 15) * 16 + ((col[c][e] >> 2) & 1) * 8, 0, 16);
          hm[c][e] = __uint_as_float(__builtin_amdgcn_raw_buffer_load_b32(hrs, (int)(WS_HM - WS_H) + row[c][e] * 8, 0, 16));
        }
      }
    if (tid == 0) poll_count(cntB, 8u);
    SB();
    raw_bar();
    SB();
    const __amdgpu_buffer_rsrc_t prs = __builtin_amdgcn_make_buffer_rsrc((void*)(ws + WS_PART + (size_t)((pn * 4 + nq) * 8 + sb) * 8 * 9216), 0, 8 * 9216, 0x00020000);
    u32x4 pvv[2][8];
#pragma unroll
    for (int c = 0; c < 2; ++c) {
      if (c < nc) {
#pragma unroll
        for (int k = 0; k < 7; ++k) pvv[c][k] = __builtin_amdgcn_raw_buffer_load_b128(prs, lane * 16 + (c ? 8192 : wid * 1024), ((sb + 1 + k) & 7) * 9216, 16);
        pvv[c][7] = *(LDS_AS u32x4*)(lds + lane * 16 + (c ? 8192 : wid * 1024));
      }
    }
    SB();
#pragma unroll
    for (int c = 0; c < 2; ++c) {
      if (c < nc) {
        const u32x4(&pv)[8] = pvv[c];
        float sum[8];
#pragma unroll
        for (int e = 0; e < 4; ++e) { sum[2 * e] = bf_lo(res[c][e >> 1][e & 1]) + hm[c][e >> 1]; sum[2 * e + 1] = bf_hi(res[c][e >> 1][e & 1]) + hm[c][e >> 1]; }
        float acc2[8];
#pragma unroll
        for (int e = 0; e < 8; ++e) acc2[e] = 0.f;
#pragma unroll
        for (int k = 0; k < 8; ++k)
#pragma unroll
          for (int e = 0; e < 4; ++e) { acc2[2 * e] += bf_lo(pv[k][e]); acc2[2 * e + 1] += bf_hi(pv[k][e]); }
        unsigned pk[4];
#pragma unroll
        for (int e = 0; e < 4; ++e) pk[e] = cvt_pk(sum[2 * e] + acc2[2 * e], sum[2 * e + 1] + acc2[2 * e + 1]);
        auto x0 = __builtin_amdgcn_permlane16_swap(pk[0], pk[2], false, false);
        auto x1 = __builtin_amdgcn_permlane16_swap(pk[1], pk[3], false, false);
        const int sel = g & 1;
        st16(out + (size_t)(row[c][0] + (c ? 16 * sel : 0)) * 1152 + (col[c][0] + (c ? -4 : 12) * sel) * 2, (u32x4{x0[0], x1[0], x0[1], x1[1]}));
      }
    }
  }
  STAMP_END(2);
  STAMP_END(1);
}

extern "C" void kernel_launch(void* const* d_in, const int* in_sizes, int n_in, void* d_out, int out_size, void* d_ws, size_t ws_size,
                              hipStream_t stream) {
  const char* x = (const char*)d_in[0];
  const char* wqkv = (const char*)d_in[1];
  const char* wo = (const char*)d_in[2];
  const char* wg = (const char*)d_in[4];
  const char* wu = (const char*)d_in[5];
  const char* wd = (const char*)d_in[6];
  char* ws = (char*)d_ws + WS_BASE(ws_size);
  (void)hipFuncSetAttribute((const void*)k1_ln_qkv, hipFuncAttributeMaxDynamicSharedMemorySize, K1_LDS);
  (void)hipFuncSetAttribute((const void*)k4_gateup, hipFuncAttributeMaxDynamicSharedMemorySize, K4_LDS);
  k1_ln_qkv<<<253, 1024, K1_LDS, stream>>>(x, wqkv, ws, wo);
  k4_gateup<<<256, 512, K4_LDS, stream>>>(wg, wu, ws, wd, (char*)d_out, wo, x);
}
```
